```python
import torch

def _softmax(x, axis=-1):
    return torch.softmax(x, dim=axis)
def _gelu(x):
    return torch.nn.functional.gelu(x, approximate="tanh")
def _silu(x):
    return torch.nn.functional.silu(x)
def _sigmoid(x):
    return torch.sigmoid(x)
def _layernorm(x, axis=-1, eps=1e-5):
    m = x.mean(dim=axis, keepdim=True)
    v = x.var(dim=axis, keepdim=True, unbiased=False)
    return (x - m) / torch.sqrt(v + eps)
def _scatter_add(tgt, idx, src, axis=0):
    return tgt.clone().index_add_(axis, idx, src)
def _segment_sum(src, seg, n):
    out = torch.zeros((n,) + src.shape[1:], dtype=src.dtype)
    return out.index_add_(0, seg, src)
def _pad(x, pw):
    flat = tuple(v for pair in reversed(pw) for v in pair)
    return torch.nn.functional.pad(x, flat)

kernel_name = 'parallel_sum__spectral_fnet__moe_arch_mod__prompt'

def setup_inputs():

    _w = (lambda a: (lambda t: t.to(torch.bfloat16) if t.is_floating_point() else t)(torch.as_tensor(a)))
    def _A_setup():
        B, S, D = 1, 2048, 1024
        ns = torch.arange(S)[:, None] * torch.arange(S)[None, :]
        nd = torch.arange(D)[:, None] * torch.arange(D)[None, :]
        Ws_re = torch.cos(-2.0 * torch.pi * ns / S).to(float)
        Ws_im = torch.sin(-2.0 * torch.pi * ns / S).to(float)
        Wd_re = torch.cos(-2.0 * torch.pi * nd / D).to(float)
        Wd_im = torch.sin(-2.0 * torch.pi * nd / D).to(float)
        return {
            "x": _w((lambda *s: torch.randn(*s, dtype=torch.bfloat16) * 0.05)(B, S, D)),
            "dft_seq_re": _w(Ws_re),
            "dft_seq_im": _w(Ws_im),
            "dft_hid_re": _w(Wd_re),
            "dft_hid_im": _w(Wd_im),
        }
    _A_in = _A_setup()

    def _B_setup():
        B, S, D, F, C = 1, 2048, 1024, 3584, 2
        return {
            "x": _w((lambda *s: torch.randn(*s, dtype=torch.bfloat16) * 0.05)(B, S, D)),
            "w_r": _w((lambda *s: torch.randn(*s, dtype=torch.bfloat16) * 1.6)(D)),
            "w_gate": _w((lambda *s: torch.randn(*s, dtype=torch.bfloat16) * 0.4)(D, F)),
            "w_up": _w((lambda *s: torch.randn(*s, dtype=torch.bfloat16) * 0.4)(D, F)),
            "w_down": _w((lambda *s: torch.randn(*s, dtype=torch.bfloat16) * 0.4)(F, D)),
        }
    _B_in = _B_setup()

    return {
        "A_x": _A_in["x"],
        "A_dft_seq_re": _A_in["dft_seq_re"],
        "A_dft_seq_im": _A_in["dft_seq_im"],
        "A_dft_hid_re": _A_in["dft_hid_re"],
        "A_dft_hid_im": _A_in["dft_hid_im"],
        "B_x": _B_in["x"],
        "B_w_r": _B_in["w_r"],
        "B_w_gate": _B_in["w_gate"],
        "B_w_up": _B_in["w_up"],
        "B_w_down": _B_in["w_down"],
    }

def reference(A_x, A_dft_seq_re, A_dft_seq_im, A_dft_hid_re, A_dft_hid_im, B_x, B_w_r, B_w_gate, B_w_up, B_w_down):
    def _A_ref(x, dft_seq_re, dft_seq_im, dft_hid_re, dft_hid_im):
        a = torch.einsum("bsd,ed->bse", x, dft_hid_re)
        b = torch.einsum("bsd,ed->bse", x, dft_hid_im)
        y = torch.einsum("ts,bse->bte", dft_seq_re, a) - torch.einsum("ts,bse->bte", dft_seq_im, b)
        return y

    def _B_ref(x, w_r, w_gate, w_up, w_down):
        B, S, D = x.shape
        C = 2
        r = x @ w_r
        idx = torch.argsort(-r, axis=-1)[:, :C]
        idx_e = (lambda x, n, axis: torch.repeat_interleave(x, n, dim=axis))(idx.reshape(B, C, 1), D, axis=2)
        x_sel = (lambda x, i, axis: torch.take_along_dim(x, i, dim=axis))(x, idx_e, axis=1)
        g = _silu(x_sel @ w_gate)
        u = x_sel @ w_up
        y = (g * u) @ w_down
        r_sel = _sigmoid((lambda x, i, axis: torch.take_along_dim(x, i, dim=axis))(r, idx, axis=1))
        y = y * r_sel.reshape(B, C, 1)
        b_off = (torch.arange(B) * S).reshape(B, 1)
        flat_idx = (idx + b_off).reshape(B * C)
        zero = (x * 0.0).reshape(B * S, D)
        upd = _scatter_add(zero, flat_idx, y.reshape(B * C, D), axis=0)
        return x + upd.reshape(B, S, D)

    x = A_x
    xn = _layernorm(x, axis=-1)
    return x + _A_ref(xn, A_dft_seq_re, A_dft_seq_im, A_dft_hid_re, A_dft_hid_im) + _B_ref(xn, B_w_r, B_w_gate, B_w_up, B_w_down)
```

```cpp
#include <hip/hip_runtime.h>
#include <hip/hip_bf16.h>
#include <stdint.h>
#include <type_traits>

extern "C" __device__ unsigned long long llvm_dispatch_id(void) __asm("llvm.amdgcn.dispatch.id");
extern "C" __device__ float llvm_fmul_legacy(float, float) __asm("llvm.amdgcn.fmul.legacy");
typedef unsigned short u16;
typedef unsigned long long u64;
typedef __attribute__((ext_vector_type(8))) short bf16x8;
typedef __attribute__((ext_vector_type(4))) unsigned u32x4;
typedef __attribute__((ext_vector_type(2))) unsigned u32x2;
typedef __attribute__((ext_vector_type(4))) float f32x4;
typedef __attribute__((ext_vector_type(2))) float f32x2;
typedef __attribute__((ext_vector_type(2))) __bf16 bf16x2;
typedef __attribute__((address_space(3))) void* lds_vp;
typedef volatile __attribute__((address_space(3))) int* lds_vi;

constexpr int S_ = 2048, D_ = 1024, F_ = 3584;
template <int N> using IC = std::integral_constant<int, N>;

static __device__ __forceinline__ float bf2f(u16 u) { return __uint_as_float((unsigned)u << 16); }
static __device__ __forceinline__ u16 f2bf(float f) {
  __hip_bfloat16 b = __float2bfloat16(f);
  return *reinterpret_cast<u16*>(&b);
}
static __device__ __forceinline__ float rbf(float f) { return bf2f(f2bf(f)); }
static __device__ __forceinline__ float lo_bf(unsigned u) { return __uint_as_float(u << 16); }
static __device__ __forceinline__ float hi_bf(unsigned u) { return __uint_as_float(u & 0xffff0000u); }
static __device__ __forceinline__ unsigned pk2(float a, float b) {
  const bf16x2 v = __builtin_convertvector((f32x2){a, b}, bf16x2);
  return __builtin_bit_cast(unsigned, v);
}
static __device__ __forceinline__ float add_bf(unsigned u, unsigned e, float c) {
  return __builtin_amdgcn_fdot2_f32_bf16(__builtin_bit_cast(bf16x2, u), __builtin_bit_cast(bf16x2, e), c, false);
}
static __device__ __forceinline__ float ln_elem(float x, float mb, float sd, float ri) {
  const float d = rbf(x - mb);
  const float q0 = d * ri;
  return rbf(__builtin_fmaf(__builtin_fmaf(-q0, sd, d), ri, q0));
}
template <int CTRL> static __device__ __forceinline__ float dpp_f(float v) {
  return __int_as_float(__builtin_amdgcn_update_dpp(0, __float_as_int(v), CTRL, 0xf, 0xf, true));
}
template <int CTRL> static __device__ __forceinline__ unsigned dpp_u(unsigned v) {
  return (unsigned)__builtin_amdgcn_update_dpp(0, (int)v, CTRL, 0xf, 0xf, true);
}
static __device__ __forceinline__ float row_sum(float v) {
  v += dpp_f<0xB1>(v);
  v += dpp_f<0x4E>(v);
  v += dpp_f<0x141>(v);
  v += dpp_f<0x140>(v);
  return v;
}
static __device__ __forceinline__ float fold32(float a, float b) {
  const auto r = __builtin_amdgcn_permlane32_swap(__float_as_uint(a), __float_as_uint(b), false, false);
  return __uint_as_float(r[0]) + __uint_as_float(r[1]);
}
static __device__ __forceinline__ float fold16(float a, float b) {
  const auto r = __builtin_amdgcn_permlane16_swap(__float_as_uint(a), __float_as_uint(b), false, false);
  return __uint_as_float(r[0]) + __uint_as_float(r[1]);
}
static __device__ __forceinline__ float rdl(float v, int l) { return __int_as_float(__builtin_amdgcn_readlane(__float_as_int(v), l)); }

#define ST_JB 9
#define WSN 10
#define FSM 0xa2
#define GV_JB 7
#define DG 11
#define AB_STRIDE 32
#define SPIN_MAX (1 << 17)

static __device__ __forceinline__ unsigned lds_addr(const void* p) { return (unsigned)(size_t)(lds_vp)p; }

constexpr int MSG = 4096;
constexpr size_t WS_ABT   = 0;
constexpr size_t WS_SLAB  = WS_ABT + (size_t)D_ * 4096 * 2;
constexpr size_t WS_GU    = WS_SLAB + (size_t)64 * 64 * MSG;
constexpr size_t WS_DN    = WS_GU + (size_t)8 * F_ * 16;
constexpr size_t WS_FLAGS = WS_DN + (size_t)2 * 8 * 32 * 64 * 16;
constexpr int FL_X = 0;
constexpr int FL_AB = 64 * 64 * 32;
constexpr int FL_ST = FL_AB + 2048 * AB_STRIDE;
constexpr int FL_CE = FL_ST + 4096;
constexpr int FL_KEY = FL_CE + 4096;
constexpr int FL_EPOCH = FL_KEY + 4096;
constexpr int WS_MB = (int)((WS_FLAGS + (FL_EPOCH + 1) * 4 + 1048575) >> 20);

#define WAIT_VM(n) asm volatile("s_waitcnt vmcnt(" #n ")" ::: "memory")
template <int N> static __device__ __forceinline__ void wait_vm() { asm volatile("s_waitcnt vmcnt(%0)" :: "i"(N) : "memory"); }
#define BAR_LGKM() asm volatile("s_waitcnt lgkmcnt(0)\n\ts_barrier" ::: "memory")
#define BAR() asm volatile("s_barrier" ::: "memory")
#define SB() __builtin_amdgcn_sched_barrier(0)
#define PAGE(n) asm volatile("s_branch 2f\n\t.p2align 12\nk_pg" #n ":\n\ts_setpc_b64 s[42:43]\n2:")
#define CALL_PG(n) asm volatile("s_getpc_b64 s[40:41]\n3:\n\ts_mov_b32 s42, k_pg" #n "-3b\n\ts_ashr_i32 s43, s42, 31\n\ts_add_u32 s40, s40, s42\n\ts_addc_u32 s41, s41, s43\n\t" \
                                "s_swappc_b64 s[42:43], s[40:41]" ::: "s40", "s41", "s42", "s43", "scc", "memory")

static __device__ __forceinline__ __amdgpu_buffer_rsrc_t mk_rsrc(const void* p) {
  return __builtin_amdgcn_make_buffer_rsrc((void*)p, 0, 0x7fffffff, 0x00020000);
}
#define DMA_(rsrc, voff, mbase, MI, sbase, SI, pol) do { unsigned _t; \
    asm volatile("s_add_i32 m0, %2, %3\n\ts_add_i32 %0, %5, %6\n\tbuffer_load_dwordx4 %1, %4, %0 offen" pol " lds" \
                 : "=&s"(_t) : "v"(voff), "s"(mbase), "i"(MI), "s"(rsrc), "s"(sbase), "i"(SI) : "memory", "scc"); } while (0)
#define TOUCH(rsrc, voff, mbase, MI, soff) \
    asm volatile("s_add_i32 m0, %1, %2\n\ts_nop 0\n\tbuffer_load_dword %0, %3, %4 offen lds" \
                 :: "v"(voff), "s"(mbase), "i"(MI), "s"(rsrc), "s"(soff) : "memory", "scc")
template <int OFF> static __device__ __forceinline__ u32x4 ld16_s(const void* p, int voff) {
  u32x4 v;
  asm volatile("global_load_dwordx4 %0, %1, %2 offset:%3" : "=v"(v) : "v"(voff), "s"(p), "i"(OFF) : "memory");
  return v;
}
template <int OFF> static __device__ __forceinline__ void sld(unsigned& sink, const void* p, int soff) {
  asm volatile("s_load_dword %0, %1, %2 offset:%3" : "+s"(sink) : "s"(p), "s"(soff), "i"(OFF) : "memory");
}
static __device__ __forceinline__ unsigned gld4_s(const void* p, unsigned voff) {
  unsigned v;
  asm volatile("global_load_dword %0, %1, %2" : "=v"(v) : "v"(voff), "s"(p) : "memory");
  return v;
}
static __device__ __forceinline__ u32x2 bld8_sc1(__amdgpu_buffer_rsrc_t r, unsigned voff) {
  u32x2 v;
  asm volatile("buffer_load_dwordx2 %0, %1, %2, 0 offen sc1" : "=v"(v) : "v"(voff), "s"(r) : "memory");
  return v;
}
template <int N> static __device__ __forceinline__ void wait_xs(unsigned& a, u32x2& b) { asm volatile("s_waitcnt vmcnt(%2)" : "+v"(a), "+v"(b) : "i"(N) : "memory"); }
template <int N> static __device__ __forceinline__ void wait_xq(u32x4 (&a)[2], u32x2& b) { asm volatile("s_waitcnt vmcnt(%3)" : "+v"(a[0]), "+v"(a[1]), "+v"(b) : "i"(N) : "memory"); }
static __device__ __forceinline__ u32x2 ld8_s_sc1(const void* p, int voff) {
  u32x2 v;
  asm volatile("global_load_dwordx2 %0, %1, %2 sc1" : "=v"(v) : "v"(voff), "s"(p) : "memory");
  return v;
}
static __device__ __forceinline__ void lgkm0(unsigned& sink) { asm volatile("s_waitcnt lgkmcnt(0)" : "+s"(sink) :: "memory"); }
template <typename T> static __device__ __forceinline__ void pin4(T& a, T& b, T& c, T& d) { asm volatile("" : "+s"(a), "+s"(b), "+s"(c), "+s"(d)); }
template <typename T, typename U> static __device__ __forceinline__ void pins(T& a, U& b) { asm volatile("" : "+s"(a), "+s"(b)); }
static __device__ __forceinline__ void launder(int& v, int& a, int& b) { asm volatile("" : "+v"(v)); asm volatile("" : "+s"(a), "+s"(b)); }
static __device__ __forceinline__ unsigned bld4_sc1(__amdgpu_buffer_rsrc_t r, int voff) {
  unsigned v;
  asm volatile("buffer_load_dword %0, %1, %2, 0 offen sc1" : "=v"(v) : "v"(voff), "s"(r) : "memory");
  return v;
}
static __device__ __forceinline__ u32x4 bld16_nt(__amdgpu_buffer_rsrc_t r, int voff, int soff) {
  u32x4 v;
  asm volatile("buffer_load_dwordx4 %0, %1, %2, %3 offen nt" : "=v"(v) : "v"(voff), "s"(r), "s"(soff) : "memory");
  return v;
}
static __device__ __forceinline__ void bld16_nt_io(u32x4& v, __amdgpu_buffer_rsrc_t r, int voff, int soff) {
  asm volatile("buffer_load_dwordx4 %0, %1, %2, %3 offen nt" : "+v"(v) : "v"(voff), "s"(r), "s"(soff) : "memory");
}
static __device__ __forceinline__ u32x4 bld16_sc1(__amdgpu_buffer_rsrc_t r, int voff, int soff) {
  u32x4 v;
  asm volatile("buffer_load_dwordx4 %0, %1, %2, %3 offen sc1" : "=v"(v) : "v"(voff), "s"(r), "s"(soff) : "memory");
  return v;
}
static __device__ __forceinline__ u32x2 granule_wait(const u64* p, unsigned tag) {
  u64 v = 0;
#pragma unroll 1
  for (int it = 0; it < SPIN_MAX; ++it) {
    v = __hip_atomic_load(p, __ATOMIC_RELAXED, __HIP_MEMORY_SCOPE_AGENT);
    if (__all((unsigned)(v >> 32) == tag)) break;
    __builtin_amdgcn_s_sleep(2);
  }
  return (u32x2){(unsigned)v, (unsigned)(v >> 32)};
}
static __device__ __forceinline__ float key_score(unsigned key) {
  const unsigned u = key >> 16;
  return bf2f((u16)((u & 0x8000u) ? (u & 0x7fffu) : (~u & 0xffffu)));
}
static __device__ __forceinline__ void merge2(unsigned& a1, unsigned& a2, unsigned b1, unsigned b2) {
  const unsigned lo = min(a1, b1);
  a1 = max(a1, b1);
  a2 = max(lo, max(a2, b2));
}
static __device__ __forceinline__ float sqdev16(const u32x4 (&v)[2], float mb) {
  f32x2 q2 = {0.f, 0.f};
#pragma unroll
  for (int i = 0; i < 8; ++i) {
    const f32x2 e = (f32x2){lo_bf(v[i >> 2][i & 3]), hi_bf(v[i >> 2][i & 3])} - mb;
    q2 += e * e;
  }
  return q2.x + q2.y;
}

constexpr int G1_NST = 4, G1_NKT = 16, G1_STAGE = 32768;
constexpr int G2_NKT = 16, G2_MRING = 98304;
constexpr int n_off(int k) { return (k % 3) * 32768; }
constexpr int m_off(int k) { return ((k + 2) % 3) * 16384; }
constexpr int k_ord(int k) { return k < 2 ? k : (k < 9 ? 2 * (k - 1) : 2 * (k - 8) + 1); }
constexpr int LDS_DUMMY = G2_MRING + 49152;
constexpr int LDS_GU = LDS_DUMMY + 4096;
constexpr int LDS_FIN = LDS_GU + 4096;
constexpr int LDS_MB = LDS_FIN + 4096;
constexpr int LDS_SEL = LDS_MB + 1536;
constexpr int LDS_SIG = LDS_SEL + 64;
constexpr int LDS_KEY = LDS_SIG + 32;
constexpr int LDS_BYTES = LDS_KEY + 128;
constexpr int LDS_ACC = n_off(G2_NKT - 3);

constexpr int tpos[16] = {0, 0, 0, 0, 109, 115, 1, 2, 2, 3, 4, 5, 6, 7, 8, 9};
constexpr int twho[16] = {0, 0, 0, 0, 7, 7, 0, 1, 2, 3, 4, 5, 6, 7, 0, 1};
constexpr int dn_cnt(int q) { return q == 5 ? 3 : q == 8 ? 4 : 0; }
constexpr int dn_first(int q) { return q <= 5 ? 0 : q <= 8 ? 3 : 7; }

static __device__ __forceinline__ void code_touch(unsigned& sink, unsigned off) {
  asm volatile("s_getpc_b64 s[40:41]\n\tk_main_rt:\n\ts_min_u32 %1, %1, k_main_end-k_main_rt-196\n\t"
               "s_load_dword %0, s[40:41], %1 offset:0\n\ts_load_dword %0, s[40:41], %1 offset:64\n\ts_load_dword %0, s[40:41], %1 offset:128\n\ts_load_dword %0, s[40:41], %1 offset:192"
               : "+s"(sink), "+s"(off) :: "s40", "s41", "scc", "memory");
}
static __device__ __forceinline__ void touch1(__amdgpu_buffer_rsrc_t r, int voff, unsigned mb, int soff) { TOUCH(r, voff, mb, LDS_DUMMY, soff); }
typedef const __attribute__((address_space(1))) u16* gptr;
typedef __attribute__((ext_vector_type(8))) u64 u64x8;
typedef __attribute__((ext_vector_type(2))) u64 u64x2;
extern "C" __global__ __launch_bounds__(512, 2) void k_main(const u16* x, const u16* wd_re, const u16* wd_im, const u16* w_r,
                                                 char* wsp, const u16* ws_re, const u16* ws_im,
                                                 const u16* w_gate, const u16* w_up, const u16* w_down, u16* out) {
  __shared__ __attribute__((aligned(16))) char smem[LDS_BYTES];
  u64x8 ka;
  u64x2 kb_;
  u64 kc;
  asm volatile("s_load_dwordx16 %0, %3, 0\n\ts_load_dwordx4 %1, %3, 64\n\ts_load_dwordx2 %2, %3, 80"
               : "=&s"(ka), "=&s"(kb_), "=&s"(kc) : "s"(__builtin_amdgcn_kernarg_segment_ptr()) : "memory");
  const int tid = threadIdx.x, lane = tid & 63;
  const int wid = __builtin_amdgcn_readfirstlane(tid >> 6);
  const int g = blockIdx.x, xcd = g & 7, j = g >> 3;
  if (wid < 4) {
    unsigned cw, co = (j * 4 + wid) * 448;
    asm volatile(".subsection 1\nk_main_end:\n.subsection 0\n\ts_min_u32 %1, %1, k_main_end-k_main-480\n\ts_getpc_b64 s[40:41]\n\ts_add_u32 s40, s40, %1\n\ts_addc_u32 s41, s41, 0\n\t"
                 "s_load_dword %0, s[40:41], 0\n\ts_load_dword %0, s[40:41], 64\n\ts_load_dword %0, s[40:41], 128\n\t"
                 "s_load_dword %0, s[40:41], 192\n\ts_load_dword %0, s[40:41], 256\n\ts_load_dword %0, s[40:41], 320\n\ts_load_dword %0, s[40:41], 384\n\ts_waitcnt lgkmcnt(0)"
                 : "=&s"(cw), "+s"(co) :: "s40", "s41", "scc", "memory");
  }
  const int pw = wid - 4;
  const int row0 = (wid & 3) * 8 + (lane >> 3);
  const int cm = ((lane & 7) ^ (((row0 >> 1) & 1) | ((wid & 3) << 1))) * 16;
  int vm = row0 * (D_ * 2) + cm;
  asm volatile("" : "+v"(vm));
  const int kq = xcd >> 1;
  const int tt = (xcd & 1) * 4 + (j >> 3);
  const int et = j & 7;
  const int z = kq >> 1, st = (kq & 1) * 8 + tt;
  const int lrow = (kq & 1) * 1024 + (xcd & 1) * 512 + z * 256 + j * 8 + pw * 2;
  const int drow = ((kq & 1) * 2 + (xcd & 1)) * 256 + j * 8 + pw * 2;
  unsigned o_m = st * 128 * D_ * 2, o_n = et * 128 * D_ * 2, o_l = lrow * D_ * 2, o_d = drow * D_ * 2, mb0 = lds_addr(smem) + pw * 1024;
  int l16 = lane * 16;
  asm volatile("" : "+s"(o_m), "+s"(o_n), "+s"(o_l), "+s"(o_d), "+s"(mb0));
  asm volatile("" : "+v"(l16));
  asm volatile("s_waitcnt lgkmcnt(0)" : "+s"(ka), "+s"(kb_), "+s"(kc) :: "memory");
  x = (const u16*)(gptr)ka[0]; wd_re = (const u16*)(gptr)ka[1]; wd_im = (const u16*)(gptr)ka[2]; w_r = (const u16*)(gptr)ka[3];
  wsp = (char*)(gptr)ka[4]; ws_re = (const u16*)(gptr)ka[5]; ws_im = (const u16*)(gptr)ka[6]; w_gate = (const u16*)(gptr)ka[7];
  w_up = (const u16*)(gptr)kb_[0]; w_down = (const u16*)(gptr)kb_[1]; out = (u16*)(gptr)kc;
  int* flags = (int*)(wsp + WS_FLAGS);
  u64* stat = (u64*)(flags + FL_ST);
  const u16* wdz = z ? wd_im : wd_re;
  const __amdgpu_buffer_rsrc_t rm = mk_rsrc(x);
  const __amdgpu_buffer_rsrc_t rn = mk_rsrc(wdz);
#define G1_X(kt, so, q) DMA_(rm, vm, mb0, (so) + (q) * 4096, o_m, (kt) * 128 + (((q) & 1) * 64 + ((q) >> 1) * 32) * D_ * 2, "")
#define G1_W(kt, so, q) DMA_(rn, vm, mb0, (so) + 16384 + (q) * 4096, o_n, (kt) * 128 + (q) * 32 * D_ * 2, "")
#define G1_STAGE_TILE(kt, so, W) do { SB(); G1_X(kt, so, 0); W(0); G1_X(kt, so, 1); W(1); G1_X(kt, so, 2); W(2); G1_X(kt, so, 3); W(3); \
    G1_W(kt, so, 0); W(4); G1_W(kt, so, 1); W(5); G1_W(kt, so, 2); W(6); G1_W(kt, so, 3); W(7); SB(); } while (0)
#define G1_STAGE_A(kt, so, W) do { SB(); G1_X(kt, so, 0); W(0); G1_X(kt, so, 2); W(1); \
    G1_W(kt, so, 0); W(2); G1_W(kt, so, 1); W(3); G1_W(kt, so, 2); W(4); G1_W(kt, so, 3); W(5); W(6); W(7); SB(); } while (0)
#define G1_STAGE_B(kt, so) do { SB(); G1_X(kt, so, 1); G1_X(kt, so, 3); SB(); } while (0)
#define NOW(i)
  const int fr = lane & 15, fq = lane >> 4;
  const int ha = wid >> 2;
  int trow, xo, po;
  const char *prs, *prc;
  auto epi_setup = [&]() __attribute__((always_inline)) {
    int t_ = tt, l_ = lane;
    asm volatile("" : "+s"(t_));
    asm volatile("" : "+v"(l_));
    trow = t_ * 256 + ((wid >> 1) & 1) * 128 + kq * 32 + ha * 16;
    xo = ((trow + (l_ & 15)) * D_ + et * 128 + (wid & 1) * 64 + (l_ >> 4) * 8) * 2;
    prs = wsp + WS_SLAB + (size_t)((t_ * 8 + et) * 64 + (wid & 3)) * MSG;
    prc = (const char*)(flags + FL_X + ((t_ * 8 + et) * 64 + (wid & 3)) * 32);
    asm volatile("" : "+s"(trow), "+s"(prs), "+s"(prc));
    po = l_ < 3 ? (kq * 4 + ((kq + 1 + l_) & 3)) * 512 : (int)0x80000000;
    asm volatile("" : "+v"(xo), "+v"(po));
  };
#define rs mk_rsrc(prs)
#define rc mk_rsrc(prc)
  lds_vi sig = (lds_vi)(smem + LDS_SIG + (wid & 3) * 4);
  unsigned tag;
  u32x4 xq[2];
  u32x2 sq;
  f32x4 ya[4];
  unsigned key[2];
  float xf[16], nf[16], lmb, lsd, lri, la, lb, lqa, lqb;
  unsigned nfp[8];
#define PINV(a) asm volatile("" : "+v"(a))
  auto nf_pre = [&]() __attribute__((always_inline)) {
    if (__builtin_expect(!__all(sq.y == tag), 0)) sq = granule_wait(stat + (xo >> 11), tag);
    lmb = lo_bf(sq.x); lsd = hi_bf(sq.x); lri = __builtin_amdgcn_rcpf(lsd);
    PINV(lmb); PINV(lsd); PINV(lri);
  };
  auto nf_step = [&](int n) __attribute__((always_inline)) {
    const int u = n / 7;
    switch (n % 7) {
      case 0: la = lo_bf(xq[u >> 2][u & 3]); PINV(la); la -= lmb; PINV(la); break;
      case 1: lb = hi_bf(xq[u >> 2][u & 3]); PINV(lb); lb -= lmb; PINV(lb); break;
      case 2: nfp[u] = pk2(la, lb); la = lo_bf(nfp[u]); PINV(la); PINV(nfp[u]); break;
      case 3: lqa = la * lri; PINV(lqa); lb = hi_bf(nfp[u]); PINV(lb); break;
      case 4: la = __builtin_fmaf(-lqa, lsd, la); PINV(la); lqb = lb * lri; PINV(lqb); break;
      case 5: lqa = __builtin_fmaf(la, lri, lqa); PINV(lqa); lb = __builtin_fmaf(-lqb, lsd, lb); PINV(lb); break;
      default: lqb = __builtin_fmaf(lb, lri, lqb); nfp[u] = pk2(lqa, lqb); PINV(nfp[u]); break;
    }
  };
  auto nf_unp = [&](int e) __attribute__((always_inline)) {
    const int u = e >> 1;
    if (e == 0) { PINV(xq[0]); PINV(xq[1]); }
    xf[e] = (e & 1) ? hi_bf(xq[u >> 2][u & 3]) : lo_bf(xq[u >> 2][u & 3]);
    nf[e] = (e & 1) ? hi_bf(nfp[u]) : lo_bf(nfp[u]);
    PINV(xf[e]); PINV(nf[e]);
  };
#define MSG_IDX(m) (kq * 4 + ((kq + 1 + (m)) & 3))
  unsigned seen = 0;

  if (__builtin_expect(wid >= 4, 1)) {
    __builtin_amdgcn_s_setprio(3);
    G1_STAGE_TILE(0, 0, NOW);
    constexpr int O_ST = (FL_ST - FL_AB) * 4, O_CE = (FL_CE - FL_AB) * 4, O_KEY = (FL_KEY - FL_AB) * 4, O_EP = (FL_EPOCH - FL_AB) * 4;
    const __amdgpu_buffer_rsrc_t rf = mk_rsrc(wsp + WS_FLAGS + FL_AB * 4);
    u32x4 lv[2], lv2[2], lw[2], ld[2], ld2[2];
    unsigned epv;
    {
      const char* xrow = (const char*)x + o_l;
      const char* wdrow = (const char*)wdz + o_d;
      lv[0] = ld16_s<0>(xrow, l16); lv[1] = ld16_s<1024>(xrow, l16);
      lv2[0] = ld16_s<2048>(xrow, l16); lv2[1] = ld16_s<3072>(xrow, l16);
      ld[0] = ld16_s<0>(wdrow, l16); ld[1] = ld16_s<1024>(wdrow, l16);
      ld2[0] = ld16_s<2048>(wdrow, l16); ld2[1] = ld16_s<3072>(wdrow, l16);
      lw[0] = ld16_s<0>(w_r, l16); lw[1] = ld16_s<1024>(w_r, l16);
      asm volatile("buffer_load_dword %0, off, %1, %2 sc1" : "=v"(epv) : "s"(rf), "s"(O_EP) : "memory");
    }
    SB();
    const unsigned long long did = llvm_dispatch_id();
    const unsigned dmix = (unsigned)did * 0x85EBCA6Bu + (unsigned)(did >> 32) * 0xC2B2AE35u + 0x27D4EB2Fu;
    const bool moe = et != 7;
    unsigned epoch;
    const int ci = lane & 15, rq = lane >> 4, tk = (lane >> 3) & 1;
    int mat, cg, dq, d0;
    u32x4 wv[16];
    const u16 *pgu, *pn2;
    const char *pm2, *spb;
#define rw __builtin_amdgcn_make_buffer_rsrc((void*)pgu, 0, moe ? 0x7fffffff : 0, 0x00020000)
#define rn2 mk_rsrc(pn2)
#define rm2 mk_rsrc(pm2)
    int vw, vn2, vm2;
    auto k_blk = [&](int k) __attribute__((always_inline)) { return tt ^ (k_ord(k) >> 1); };
#define KROT(kk_) (k_blk(kk_) * 256 + (k_ord(kk_) & 1) * 128)
    int tv2;
    unsigned sd = 0;
    auto touches = [&](auto P) __attribute__((always_inline)) {
      constexpr int p = decltype(P)::value;
      int e = et;
      asm volatile("" : "+s"(e));
#define TCH(k) if constexpr (tpos[k] == p) { if (e == twho[k]) { const int kb = KROT(k); touch1(rn2, tv2, mb0, kb); } }
      TCH(0) TCH(1) TCH(2) TCH(3) TCH(4) TCH(5) TCH(6) TCH(7) TCH(8) TCH(9) TCH(10) TCH(11) TCH(12) TCH(13) TCH(14) TCH(15)
    };
    unsigned mq0, mq1, mq2, mq3;
    const int cg8 = g & 7, rgb = g >> 3;
    const __amdgpu_buffer_rsrc_t rdn = mk_rsrc(w_down);
    const __amdgpu_buffer_rsrc_t rgu = mk_rsrc(wsp + WS_GU);
    int vdn, vgu;
    u32x4 dv[7], gv[4];
    int abo, abq[3];
    unsigned lma;
    unsigned sel[2][4];
    float g0, g1;
    u32x4 kk[4];
    unsigned k1 = 0, k2 = 0;
    const int ko_ = O_KEY + pw * 4096;
#define MM(a, b, c) __builtin_amdgcn_mfma_f32_16x16x32_bf16(__builtin_bit_cast(bf16x8, a), __builtin_bit_cast(bf16x8, b), c, 0, 0, 0)
#define PIN1(a) asm volatile("" : "+v"(a))
#define PIN2(a, b) asm volatile("" : "+v"(a), "+v"(b))
#define PIN3(a, b, c) asm volatile("" : "+v"(a), "+v"(b), "+v"(c))
    {
      int gro, go;
      u32x2 gr;
      u32x4 oh[5];
      f32x4 sa4, gw[2], gx[2];
      float su, gs, ta, tb, sx, mbv;
      unsigned dk[4], kl;
      const int hb = lane >> 5;
#pragma unroll
      for (int l = 0; l < WSN; ++l) asm volatile("" : "=v"(wv[l]));
      auto diag = [&](const f32x4& m) __attribute__((always_inline)) {
        return __uint_as_float((__float_as_uint(m[0]) & dk[0]) | (__float_as_uint(m[1]) & dk[1]) | (__float_as_uint(m[2]) & dk[2]) | (__float_as_uint(m[3]) & dk[3]));
      };
      auto setup = [&](auto JB, auto GP) __attribute__((always_inline)) {
        constexpr int jb = decltype(JB)::value, gp = decltype(GP)::value;
        SB();
        {
          int ln = lane, jj = j, p_ = pw;
          launder(ln, jj, p_);
          const int c_ = ln & 15, r_ = ln >> 4, e_ = jj & 7, t_ = (xcd & 1) * 4 + (jj >> 3);
          if constexpr (jb == 1 && gp == 0) {
            pn2 = ((kq >> 1) ? ws_im : ws_re) + (long)t_ * 256 * S_ + (kq & 1) * 1024;
            spb = (const char*)(pn2 + (e_ * 4 + p_) * 8 * S_);
            pins(pn2, spb);
          }
          if constexpr (jb == 1 && gp >= 1 && gp < 6) {
            const unsigned o = c_ == gp - 1 ? 0x3f803f80u : 0u;
            oh[gp - 1] = (u32x4){o, o, o, o};
            PIN1(oh[gp - 1]);
          }
          if constexpr (jb == 1 && gp == 6) {
            const int gm = moe ? (jj - (jj >> 3)) * 8 + xcd : 0;
            mat = gm / 112; cg = (gm % 112) >> 2; dq = gm & 3; d0 = dq * 256 + p_ * 64;
            pin4(mat, cg, dq, d0);
          }
          if constexpr (jb == 2 && gp == 0) {
            pgu = mat ? w_up : w_gate;
            pm2 = wsp + WS_ABT + ((long)e_ * 128 * 4096 + kq * 1024) * 2;
            pins(pgu, pm2);
            vw = ((d0 + r_ * 16) * F_ + cg * 128 + c_ * 8) * 2;
            PIN1(vw);
          }
          if constexpr (jb == 2 && (gp == 1 || gp == 2)) {
            dk[gp * 2 - 2] = c_ == r_ * 4 + gp * 2 - 2 ? ~0u : 0u; dk[gp * 2 - 1] = c_ == r_ * 4 + gp * 2 - 1 ? ~0u : 0u;
            PIN2(dk[gp * 2 - 2], dk[gp * 2 - 1]);
          }
          if constexpr (jb == 2 && gp == 3) { kl = lrow + (ln >> 5); go = (ln & 30) ? (int)0x80000000 : ((ln & 1) ? O_KEY : O_ST) + (int)kl * 8; PIN2(kl, go); }
          if constexpr (jb == 2 && gp == 4) { kl = 0xffff - kl; go = (ln & ~1) == 2 ? O_CE + (z * D_ + drow + (ln & 1)) * 8 : go; PIN2(kl, go); }
          if constexpr (jb == 2 && gp == 5) {
            gro = (p_ < 2 ? O_ST + (st * 128 + p_ * 64) * 8 : O_CE + (z * D_ + e_ * 128 + (p_ - 2) * 64) * 8) + ln * 8;
            tv2 = (p_ * 64 + ln) * 4096;
            lma = lds_addr(smem + LDS_MB) + (p_ < 2 ? p_ * 256 : 512 + p_ * 256) + ln * 4;
            PIN3(gro, tv2, lma);
          }
          if constexpr (jb == 7 && gp == 0) {
            const int r0 = p_ * 8 + (ln >> 3);
            vn2 = r0 * (S_ * 2) + ((ln & 7) ^ ((r0 >> 1) & 7)) * 16; vm2 = r0 * 8192 + (vm & 127);
            PIN2(vn2, vm2);
          }
          if constexpr (jb == 7 && gp == 1) { vdn = jj * 112 + p_ * 28 + r_ * 7; vgu = (((ln >> 3) & 1) * 4 * F_ + vdn + min(ln & 7, 6)) * 16; PIN2(vdn, vgu); }
          if constexpr (jb == 7 && gp == 2) { vdn = (vdn * D_ + cg8 * 128 + c_ * 8) * 2; PIN1(vdn); }
          if constexpr (jb == 7 && gp == 3) {
            abo = (((kq * 8 + e_) * 4 + p_) * 16 + c_) * AB_STRIDE * 4;
            abq[2] = ln >= 16 || (ln & 7) == t_ ? (int)0x80000000 : abo;
            PIN2(abo, abq[2]);
          }
          if constexpr (jb == 7 && gp == 4) {
            abq[1] = ln >= 8 ? (int)0x80000000 : abq[2];
            abq[0] = ((ln ^ t_) & 4) ? (int)0x80000000 : abq[1];
            PIN2(abq[0], abq[1]);
          }
          if constexpr (jb == 7 && gp == 5) {
            const unsigned mb = lds_addr(smem) + p_ * 1024;
            mq0 = mb + ((3 - kq) & 3) * 4096; mq1 = mb + ((4 - kq) & 3) * 4096; mq2 = mb + ((5 - kq) & 3) * 4096; mq3 = mb + ((6 - kq) & 3) * 4096;
            pin4(mq0, mq1, mq2, mq3);
          }
          if constexpr (jb == 8 && gp < 4) {
#pragma unroll
            for (int h = 0; h < 2; ++h) {
              const unsigned hv = h ? 0x0302u : 0x0100u;
              sel[h][gp] = ((ln & 7) >> 1) == gp ? ((ln & 1) ? (hv << 16) | 0x0c0cu : 0x0c0c0000u | hv) : 0x0c0c0c0cu;
            }
            PIN2(sel[0][gp], sel[1][gp]);
          }
        }
        SB();
      };
#define WS1(i) setup(IC<1>{}, IC<i>{})
#define WS2(i) setup(IC<2>{}, IC<i>{})
#define WS3(i) do { setup(IC<7>{}, IC<i>{}); if (i >= 4) setup(IC<8>{}, IC<(i) & 3>{}); } while (0)
      G1_STAGE_TILE(1, G1_STAGE, WS1);
      WAIT_VM(19); BAR();
      G1_STAGE_TILE(2, 2 * G1_STAGE, WS2);
      G1_STAGE_TILE(3, 3 * G1_STAGE, WS3);
      auto work1 = [&](auto JB, auto GP) __attribute__((always_inline)) {
        constexpr int jb = decltype(JB)::value, gp = decltype(GP)::value;
        SB();
        if constexpr (jb == 1) {
          const f32x4 z4 = {0.f, 0.f, 0.f, 0.f};
          if constexpr (gp == 0) { sa4 = MM(lv[0], oh[0], z4); gw[0] = MM(lv[0], lw[0], z4); gx[0] = MM(lv[0], lv[0], z4); PIN3(sa4, gw[0], gx[0]); }
          if constexpr (gp == 1) { sa4 = MM(lv2[0], oh[1], sa4); gw[1] = MM(lv2[0], lw[0], z4); gx[1] = MM(lv2[0], lv2[0], z4); PIN3(sa4, gw[1], gx[1]); }
          if constexpr (gp == 2) { sa4 = MM(lv[1], oh[0], sa4); gw[0] = MM(lv[1], lw[1], gw[0]); gx[0] = MM(lv[1], lv[1], gx[0]); PIN3(sa4, gw[0], gx[0]); }
          if constexpr (gp == 3) { sa4 = MM(lv2[1], oh[1], sa4); gw[1] = MM(lv2[1], lw[1], gw[1]); gx[1] = MM(lv2[1], lv2[1], gx[1]); PIN3(sa4, gw[1], gx[1]); }
          if constexpr (gp == 4) { sa4 = MM(ld[0], oh[2], sa4); sa4 = MM(ld[1], oh[2], sa4); PIN1(sa4); }
          if constexpr (gp == 5) { sa4 = MM(ld2[0], oh[3], sa4); sa4 = MM(ld2[1], oh[3], sa4); PIN1(sa4); }
          if constexpr (gp == 6) { sa4 = MM(lw[0], oh[4], sa4); sa4 = MM(lw[1], oh[4], sa4); PIN1(sa4);
            epoch = __builtin_amdgcn_readfirstlane(epv); tag = ((epoch + 1u) * 0x9E3779B1u ^ dmix) | 1u; }
        }
        if constexpr (jb == 2) {
          if constexpr (gp == 0) { su = (sa4[0] + sa4[1]) + (sa4[2] + sa4[3]); ta = diag(gw[0]); PIN2(su, ta); }
          if constexpr (gp == 1) { su = fold32(su, su); tb = diag(gw[1]); PIN2(su, tb); }
          if constexpr (gp == 2) { su = fold16(su, su); gs = fold32(ta, tb); PIN2(su, gs); }
          if constexpr (gp == 3) { ta = diag(gx[0]); tb = diag(gx[1]); PIN2(ta, tb); }
          if constexpr (gp == 4) { gs = fold16(gs, fold32(ta, tb)); PIN1(gs); }
          if constexpr (gp == 5) { gs += dpp_f<0xB1>(gs); gs += dpp_f<0x4E>(gs); PIN1(gs); }
          if constexpr (gp == 6) { gs += dpp_f<0x141>(gs); gs += dpp_f<0x140>(gs); PIN1(gs); }
        }
        if constexpr (jb == 3) {
          if constexpr (gp == 0) {
            const float s0 = rdl(su, 0), s1 = rdl(su, 1), q0 = rdl(gs, 16), q1 = rdl(gs, 48);
            sx = hb ? s1 : s0; ta = hb ? q1 : q0; PIN2(sx, ta);
          }
          if constexpr (gp == 1) {
            const float mean = sx * (1.0f / D_);
            mbv = rbf(mean);
            ta -= sx * mean;
            if (__builtin_expect(__any(!(sx * mean <= 16.f * ta)), 0)) {
              float q = fold32(sqdev16(lv, rdl(mbv, 0)), sqdev16(lv2, rdl(mbv, 32)));
              q = row_sum(fold16(q, q));
              const float dm = mean - mbv;
              ta = fmaxf(q - (float)D_ * dm * dm, 0.f);
            }
            PIN2(mbv, ta);
          }
          if constexpr (gp == 2) { ta = rbf(rbf(ta * (1.0f / D_)) + 1e-5f); gs -= mbv * rdl(su, 4); PIN2(ta, gs); }
          if constexpr (gp == 3) { ta = rbf(__builtin_amdgcn_sqrtf(ta)); tb = __builtin_amdgcn_rcpf(ta); PIN2(ta, tb); }
          if constexpr (gp == 4) {
            const unsigned rb = __float_as_uint(rbf(gs * tb));
            kl |= (rb ^ ((unsigned)((int)rb >> 31) | 0x80000000u)) & 0xffff0000u; PIN1(kl);
          }
          if constexpr (gp == 5) { kl = (lane & 2) ? __float_as_uint(su) : (lane & 1) ? kl : (__float_as_uint(mbv) >> 16) | __float_as_uint(ta); PIN1(kl); }
        }
        if constexpr (jb == ST_JB + 3) {
          if constexpr (gp == 0) { if (!__all(gr.y == tag)) gr = granule_wait((const u64*)((const char*)(flags + FL_AB) + gro), tag); PIN1(gr); }
          if constexpr (gp == 1) { g1 = (z ? -1.0f : 1.0f) / hi_bf(gr.x); PIN1(g1); }
          if constexpr (gp == 2) { g0 = pw < 2 ? -(lo_bf(gr.x) * g1) : __uint_as_float(gr.x); PIN1(g0); }
        }
        if constexpr (gp < 7 && (jb - 1) * 7 + gp < 64) {
          constexpr int n = (jb - 1) * 7 + gp;
          sld<((n >> 1) & 7) * 4096 + (n & 1) * 64>(sd, spb, KROT(n >> 4));
        }
        SB();
      };
      auto step1 = [&](auto JB) __attribute__((always_inline)) {
        constexpr int jb = decltype(JB)::value;
        if constexpr (jb == 1) PAGE(1);
        if constexpr (jb == 7) PAGE(2);
        constexpr int n16 = jb == 12 ? 12 : jb == 11 ? 14 : 16;
        constexpr auto wl = [](int q) { return (q == 3 ? 1 : 0) + (q == ST_JB ? 1 : 0); };
        if constexpr (jb == 1) {
          asm volatile("s_waitcnt vmcnt(%11)" : "+v"(lv[0]), "+v"(lv[1]), "+v"(lw[0]), "+v"(lw[1]), "+v"(ld[0]), "+v"(ld[1]),
                       "+v"(lv2[0]), "+v"(lv2[1]), "+v"(ld2[0]), "+v"(ld2[1]), "+v"(epv) : "i"(16) : "memory");
        } else if constexpr (jb == ST_JB + 3) {
          asm volatile("s_waitcnt vmcnt(%1)" : "+v"(gr) : "i"(n16 + wl(jb - 2) + wl(jb - 1)) : "memory");
        } else wait_vm<n16 + wl(jb - 3) + wl(jb - 2) + wl(jb - 1)>();
        BAR();
        if constexpr (jb == 2) { if (pw == 0 && lane < 16) ((int*)(smem + LDS_SEL))[lane < 8 ? lane : lane + 8] = 0; }
        if constexpr (jb == 4) { if (pw == 0 && lane == 0) ((unsigned*)(smem + LDS_SEL))[5] = tag; }
#define W1(i) work1(JB, IC<i>{})
        if constexpr (jb < 10) G1_STAGE_TILE(jb - 1 + G1_NST, ((jb - 1) % G1_NST) * G1_STAGE, W1);
        else G1_STAGE_A(jb - 1 + G1_NST, ((jb - 1) % G1_NST) * G1_STAGE, W1);
        if constexpr (jb == 12) { G1_STAGE_B(13, G1_STAGE); G1_STAGE_B(14, 2 * G1_STAGE); }
        if constexpr (jb == 3) __builtin_amdgcn_raw_buffer_store_b64((u32x2){kl, tag}, rf, go, 0, 16);
        if constexpr (jb == ST_JB) {
          asm volatile("buffer_load_dwordx2 %0, %1, %2, 0 offen sc1" : "=v"(gr) : "v"(gro), "s"(rf) : "memory");
        }
        touches(IC<100 + jb>{});
        if constexpr (jb <= 8) {
          if (et == jb - 1) {
#pragma unroll
            for (int l = 0; l < WSN; ++l) bld16_nt_io(wv[l], rw, vw, l * F_ * 2);
          }
        }
        SB();
      };
      static_assert(ST_JB + 3 <= 12 && ST_JB >= 6, "schedule");
      step1(IC<1>{}); step1(IC<2>{}); step1(IC<3>{}); step1(IC<4>{}); step1(IC<5>{}); step1(IC<6>{}); step1(IC<7>{}); step1(IC<8>{}); step1(IC<9>{}); step1(IC<10>{}); step1(IC<11>{}); step1(IC<12>{});
    }
#define G2_STAGE_N(kk_, W) do { const int kb = KROT(kk_); constexpr int so = n_off(kk_); SB(); \
    DMA_(rn2, vn2, mq0, so, kb, 0, ""); W(0); DMA_(rn2, vn2, mq1, so, kb, 32 * S_ * 2, ""); W(1); \
    DMA_(rn2, vn2, mq2, so, kb, 64 * S_ * 2, ""); W(2); DMA_(rn2, vn2, mq3, so, kb, 96 * S_ * 2, ""); W(3); \
    DMA_(rn2, vn2, mq0, so + 16384, kb, 128 * S_ * 2, ""); W(4); DMA_(rn2, vn2, mq1, so + 16384, kb, 160 * S_ * 2, ""); W(5); \
    DMA_(rn2, vn2, mq2, so + 16384, kb, 192 * S_ * 2, ""); W(6); DMA_(rn2, vn2, mq3, so + 16384, kb, 224 * S_ * 2, ""); W(7); SB(); } while (0)
#define G2_STAGE_NH(kk_, h) do { const int kb = KROT(kk_); constexpr int so = n_off(kk_) + (h) * 16384; SB(); \
    DMA_(rn2, vn2, mq0, so, kb, (h) * 128 * S_ * 2, ""); DMA_(rn2, vn2, mq1, so, kb, ((h) * 128 + 32) * S_ * 2, ""); \
    DMA_(rn2, vn2, mq2, so, kb, ((h) * 128 + 64) * S_ * 2, ""); DMA_(rn2, vn2, mq3, so, kb, ((h) * 128 + 96) * S_ * 2, ""); SB(); } while (0)
#define G2_STAGE_M(kk_, W) do { const int kb = KROT(kk_); constexpr int so = G2_MRING + m_off(kk_); SB(); \
    DMA_(rm2, vm2, mb0, so, kb, 0, " sc1"); W(8); DMA_(rm2, vm2, mb0, so + 4096, kb, 32 * 8192, " sc1"); W(9); \
    DMA_(rm2, vm2, mb0, so + 8192, kb, 64 * 8192, " sc1"); W(10); DMA_(rm2, vm2, mb0, so + 12288, kb, 96 * 8192, " sc1"); W(11); SB(); } while (0)
    WAIT_VM(16); BAR();
    asm volatile("ds_write_b32 %0, %1" :: "v"(lma), "v"(g0) : "memory");
    if (pw < 2) asm volatile("ds_write_b32 %0, %1 offset:512" :: "v"(lma), "v"(g1) : "memory");
    u32x4 tv[4];
    unsigned ready = 0;
    auto need = [&](int k) __attribute__((always_inline)) {
      const int b = k_blk(k) + (k_ord(k) & 1) * 8;
#pragma unroll 1
      for (int it = 0; !((ready >> b) & 1) && it < SPIN_MAX; ++it) {
        ready |= (unsigned)__ballot(__builtin_amdgcn_raw_buffer_load_b32(rf, abq[k < 5 ? 0 : k < 9 ? 1 : 2], 0, 16) == tag);
        asm volatile("" ::: "memory");
      }
    };
    unsigned ksel, bad = 0;
    int isel;
    unsigned xw;
    auto bvec = [&](unsigned src, const unsigned (&sl)[4]) __attribute__((always_inline)) {
      return (u32x4){__builtin_amdgcn_perm(0, src, sl[0]), __builtin_amdgcn_perm(0, src, sl[1]),
                     __builtin_amdgcn_perm(0, src, sl[2]), __builtin_amdgcn_perm(0, src, sl[3])};
    };
#define BC8(v, p) (unsigned)__builtin_amdgcn_update_dpp(__builtin_amdgcn_update_dpp(0, (int)(v), 0x150 + (p), 0xf, 0x3, false), (int)(v), 0x158 + (p), 0xf, 0xc, false)
    float* lg = (float*)(smem + LDS_GU);
    f32x4 macc = {0.f, 0.f, 0.f, 0.f};
    u32x2 sv;
    unsigned hp, gbad;
    float s0, s1;
    auto work2 = [&](auto JB, auto GP) __attribute__((always_inline)) {
      constexpr int jb = decltype(JB)::value, gp = decltype(GP)::value;
      SB();
      if constexpr (jb == -1 && gp < 4) {
        constexpr int c = gp == 0 ? 0xB1 : gp == 1 ? 0x4E : gp == 2 ? 0x141 : 0x140;
        merge2(k1, k2, dpp_u<c>(k1), dpp_u<c>(k2));
        PIN2(k1, k2);
      }
      if constexpr (jb == -1 && gp == 4) {
        ksel = tk ? k2 : k1;
        isel = (0xffff - (int)(ksel & 0xffffu)) & (S_ - 1);
        k1 = (isel * D_ + d0 + rq * 16 + (lane & 7) * 2) * 2; k2 = O_ST + isel * 8;
        PIN1(ksel);
        xw = gld4_s(x, k1);
        sv = bld8_sc1(rf, k2);
      }
      if constexpr (jb == 0 && gp == 1) wait_xs<3 + 1 + 2>(xw, sv);
      if constexpr (jb == 0) {
        if constexpr (gp == 1) { if (!__all(sv.y == tag)) sv = granule_wait(stat + isel, tag); s0 = __builtin_amdgcn_rcpf(hi_bf(sv.x)); PIN1(s0); }
        if constexpr (gp == 2) { s1 = ln_elem(lo_bf(xw), lo_bf(sv.x), hi_bf(sv.x), s0); PIN1(s1); }
        if constexpr (gp == 3) { xw = pk2(s1, ln_elem(hi_bf(xw), lo_bf(sv.x), hi_bf(sv.x), s0)); PIN1(xw); }
      }
      if constexpr (jb == DG && gp < 7) { PIN1(dv[gp]); macc = MM(dv[gp], bvec(BC8(hp, gp), sel[0]), macc); PIN1(macc); }
      if constexpr ((jb == 0 && gp >= 4) || jb == 1) {
        constexpr int l = jb == 0 ? gp - 4 : gp + 4;
        if constexpr (!(l & 1)) hp = BC8(xw, l >> 1);
        PIN1(wv[l]);
        macc = MM(wv[l], bvec(hp, sel[l & 1]), macc);
        PIN2(macc, hp);
      }
      if constexpr (jb == GV_JB + 2) {
        if constexpr (gp == 0) { gbad = (gv[0].y ^ tag) | (gv[0].w ^ tag) | (gv[1].y ^ tag) | (gv[1].w ^ tag); PIN1(gbad); }
        if constexpr (gp == 1) { gbad |= (gv[2].y ^ tag) | (gv[2].w ^ tag) | (gv[3].y ^ tag) | (gv[3].w ^ tag); PIN1(gbad); }
        if constexpr (gp == 2) {
          s0 = ((__uint_as_float(gv[0].x) + __uint_as_float(gv[1].x)) + __uint_as_float(gv[2].x)) + __uint_as_float(gv[3].x);
          s1 = ((__uint_as_float(gv[0].z) + __uint_as_float(gv[1].z)) + __uint_as_float(gv[2].z)) + __uint_as_float(gv[3].z);
          PIN2(s0, s1);
        }
        if constexpr (gp == 3) {
#pragma unroll 1
          for (int it = 0; !__all(gbad == 0) && it < SPIN_MAX; ++it) {
            u32x4 q[4];
            gbad = 0;
#pragma unroll
            for (int i = 0; i < 4; ++i) { q[i] = __builtin_amdgcn_raw_buffer_load_b128(rgu, vgu, i * F_ * 16, 16); gbad |= (q[i].y ^ tag) | (q[i].w ^ tag); }
            s0 = ((__uint_as_float(q[0].x) + __uint_as_float(q[1].x)) + __uint_as_float(q[2].x)) + __uint_as_float(q[3].x);
            s1 = ((__uint_as_float(q[0].z) + __uint_as_float(q[1].z)) + __uint_as_float(q[2].z)) + __uint_as_float(q[3].z);
            asm volatile("" ::: "memory");
          }
          const unsigned gp2 = pk2(s0, s1);
          const unsigned up2 = dpp_u<0x128>(gp2);
          s0 = tk ? hi_bf(up2) : lo_bf(gp2); s1 = tk ? hi_bf(gp2) : lo_bf(up2);
          PIN2(s0, s1);
        }
        if constexpr (gp == 4) { s0 = rbf(s0 * __builtin_amdgcn_rcpf(1.f + __expf(-s0))); PIN1(s0); }
        if constexpr (gp == 5) { hp = pk2(s0 * s1, 0.f); PIN1(hp); }
      }
      if constexpr (jb == 10 && gp == 2) epi_setup();
      if constexpr (jb == 10 && gp == 4) code_touch(sd, (j * 4 + pw) * 256);
      SB();
    };
#define W2M(i) work2(IC<-1>{}, IC<i>{})
#define W20(i) work2(IC<0>{}, IC<i>{})
#define W21(i) work2(IC<1>{}, IC<i>{})
    unsigned fs;
    {
      G1_STAGE_B(15, 3 * G1_STAGE);
      G2_STAGE_NH(0, 0);
      lgkm0(sd);
      WAIT_VM(16); BAR();
      G2_STAGE_NH(0, 1);
      WAIT_VM(10); BAR();
#pragma unroll
      for (int i = 0; i < 4; ++i) kk[i] = bld16_sc1(rf, l16, ko_ + i * 1024);
#pragma unroll
      for (int l = WSN; l < 16; ++l) wv[l] = bld16_nt(rw, vw, l * F_ * 2);
      asm volatile("s_waitcnt vmcnt(%4)" : "+v"(kk[0]), "+v"(kk[1]), "+v"(kk[2]), "+v"(kk[3]) : "i"(16 - WSN) : "memory");
      touches(IC<115>{});
#pragma unroll
      for (int i = 0; i < 4; ++i) {
        k2 = max(k2, min(k1, kk[i].x)); k1 = max(k1, kk[i].x);
        k2 = max(k2, min(k1, kk[i].z)); k1 = max(k1, kk[i].z);
        bad |= (kk[i].y ^ tag) | (kk[i].w ^ tag);
      }
#pragma unroll 1
      for (int it = 0; !__all(bad == 0) && it < SPIN_MAX; ++it) {
        k1 = 0; k2 = 0; bad = 0;
#pragma unroll
        for (int i = 0; i < 4; ++i) {
          const u32x4 q = __builtin_amdgcn_raw_buffer_load_b128(rf, l16, ko_ + i * 1024, 16);
          k2 = max(k2, min(k1, q.x)); k1 = max(k1, q.x);
          k2 = max(k2, min(k1, q.z)); k1 = max(k1, q.z);
          bad |= (q.y ^ tag) | (q.w ^ tag);
        }
        asm volatile("" ::: "memory");
      }
      merge2(k1, k2, dpp_u<0xB1>(k1), dpp_u<0xB1>(k2));
      merge2(k1, k2, dpp_u<0x4E>(k1), dpp_u<0x4E>(k2));
      merge2(k1, k2, dpp_u<0x141>(k1), dpp_u<0x141>(k2));
      merge2(k1, k2, dpp_u<0x140>(k1), dpp_u<0x140>(k2));
      if ((lane & 15) == 0) *(u32x2*)(smem + LDS_KEY + (pw * 4 + rq) * 8) = (u32x2){k1, k2};
      PAGE(3);
      BAR_LGKM();
#pragma unroll
      for (int jj = 0; jj < 4; ++jj) tv[jj] = *(const u32x4*)(smem + G2_MRING + m_off(0) + (jj * 4 + pw) * 1024 + lane * 16);
      { const u32x2 kp = *(const u32x2*)(smem + LDS_KEY + ci * 8); k1 = kp.x; k2 = kp.y; }
#pragma unroll
      for (int jj = 0; jj < 4; ++jj) __builtin_amdgcn_raw_buffer_store_b128(tv[jj], rm2, vm2 + jj * 32 * 8192 + 2 * tt * 128, 0, 16);
      G2_STAGE_N(1, W2M);
      WAIT_VM(10);
      if (lane == tt) __builtin_amdgcn_raw_buffer_store_b32(tag, rf, abo, 0, 16);
      G2_STAGE_N(2, W20);
      __builtin_amdgcn_s_sleep(2);
      WAIT_VM(9);
      fs = bld4_sc1(rf, abq[0]);
    }
    BAR();
#pragma unroll
    for (int jj = 0; jj < 4; ++jj) tv[jj] = *(const u32x4*)(smem + G2_MRING + m_off(1) + (jj * 4 + pw) * 1024 + lane * 16);
    G2_STAGE_N(3, NOW);
    asm volatile("s_waitcnt vmcnt(8)" : "+v"(fs) :: "memory");
    ready = (unsigned)__ballot(fs == tag);
    need(2);
    G2_STAGE_M(2, NOW);
    need(3);
    G2_STAGE_M(3, NOW);
    fs = bld4_sc1(rf, abq[1]);
    touches(IC<1>{});
    W21(0); W21(1); W21(2); W21(3); W21(4); W21(5); W21(6); W21(7); W21(8); W21(9); W21(10); W21(11);
    asm volatile("" : "+v"(tv[0]), "+v"(tv[1]), "+v"(tv[2]), "+v"(tv[3]));
    auto step2 = [&](auto JB) __attribute__((always_inline)) {
      constexpr int jb = decltype(JB)::value;
      if constexpr (jb == 3) PAGE(4);
      if constexpr (jb == 9) PAGE(5);
      constexpr auto must = [](int q) { return (q == 2 ? 4 : 0) + ((FSM >> q) & 1) + ((q == GV_JB || q == GV_JB - 1) ? 2 : 0); };
      if constexpr (jb == 2) wait_vm<4 + must(1)>();
      else wait_vm<dn_cnt(jb - 2) + (jb == 5 ? 1 : 0) + 12 + must(jb - 1) + dn_cnt(jb - 1)>();
      if constexpr (jb == 3) { if (pw == 0 && (lane & ~8) == 0) ((unsigned*)(smem + LDS_SEL))[tk] = ksel; }
      if constexpr ((jb == 2 || jb == DG + 1)) {
        *(f32x4*)(lg + pw * 256 + lane * 4) = macc;
        macc = (f32x4){0.f, 0.f, 0.f, 0.f};
        lgkm0(sd);
      }
      BAR();
      float p0[4], p1[4];
      const int col = (pw & 1) * 64 + lane;
      if constexpr (jb == 3) {
        if (g == 0 && pw == 0 && lane == 0) __hip_atomic_store((unsigned*)flags + FL_EPOCH, epoch + 1u, __ATOMIC_RELAXED, __HIP_MEMORY_SCOPE_AGENT);
      }
      if constexpr (jb == DG + 1) {
        const float* p = lg + (pw & 1) * 32 + (lane >> 4) * 64 + ((2 * lane) & 7) * 4 + ((lane >> 2) & 3);
#pragma unroll
        for (int i = 0; i < 4; ++i) { p0[i] = p[i * 256]; p1[i] = p[i * 256 + 4]; }
      }
      if constexpr (jb == 2) {
        const float* p = lg + (col >> 5) * 64 + (col & 7) * 4 + ((col >> 3) & 3);
#pragma unroll
        for (int i = 0; i < 4; ++i) { p0[i] = p[i * 256]; p1[i] = p[i * 256 + 32]; }
      }
      if constexpr (jb == 4) {
        if (lane == 8 + tt) __builtin_amdgcn_raw_buffer_store_b32(tag, rf, abo, 0, 16);
      }
      if constexpr ((FSM >> (jb - 2)) & 1) {
        asm volatile("" : "+v"(fs));
        ready |= (unsigned)__ballot(fs == tag);
      }
      if constexpr (jb == GV_JB + 2) asm volatile("" : "+v"(gv[0]), "+v"(gv[1]), "+v"(gv[2]), "+v"(gv[3]));
#define W2(i) work2(JB, IC<i>{})
      G2_STAGE_N(jb + 2, W2);
      need(jb + 2);
      G2_STAGE_M(jb + 2, W2);
      if constexpr (jb == 2) {
#pragma unroll
        for (int jj = 0; jj < 4; ++jj) __builtin_amdgcn_raw_buffer_store_b128(tv[jj], rm2, vm2 + jj * 32 * 8192 + (2 * tt + 1) * 128, 0, 16);
      }
      if constexpr ((FSM >> jb) & 1) fs = bld4_sc1(rf, abq[jb < 5 ? 1 : 2]);
      if constexpr ((jb == GV_JB || jb == GV_JB - 1)) {
#pragma unroll
        for (int i = (jb == GV_JB ? 2 : 0); i < (jb == GV_JB ? 4 : 2); ++i) gv[i] = bld16_sc1(rgu, vgu, i * F_ * 16);
      }
      touches(JB);
#pragma unroll
      for (int i = dn_first(jb); i < dn_first(jb) + dn_cnt(jb); ++i) dv[i] = bld16_nt(rdn, vdn, i * D_ * 2);
      if constexpr (jb == DG + 1) {
        if (pw < 2)
          __builtin_amdgcn_raw_buffer_store_b128((u32x4){__float_as_uint((p0[0] + p0[1]) + (p0[2] + p0[3])), tag, __float_as_uint((p1[0] + p1[1]) + (p1[2] + p1[3])), tag},
                                                 mk_rsrc(wsp + WS_DN), (((pw * 8 + cg8) * 32 + rgb) * 64 + lane) * 16, 0, 16);
      }
      if constexpr (jb == 2) {
        if (moe && pw < 2)
          __builtin_amdgcn_raw_buffer_store_b128((u32x4){__float_as_uint((p0[0] + p0[1]) + (p0[2] + p0[3])), tag, __float_as_uint((p1[0] + p1[1]) + (p1[2] + p1[3])), tag},
                                                 rgu, ((mat * 4 + dq) * F_ + cg * 128 + col) * 16, 0, 16);
      }
      SB();
    };
    static_assert(DG + 1 <= 12 && DG >= GV_JB + 3 && dn_first(DG - 2) == 7, "schedule");
    step2(IC<2>{}); step2(IC<3>{}); step2(IC<4>{}); step2(IC<5>{}); step2(IC<6>{}); step2(IC<7>{}); step2(IC<8>{}); step2(IC<9>{}); step2(IC<10>{}); step2(IC<11>{}); step2(IC<12>{}); step2(IC<13>{});
    lgkm0(sd);
    WAIT_VM(12); BAR();
    xq[0] = ld16_s<0>(x, xo); xq[1] = ld16_s<64>(x, xo); sq = ld8_s_sc1(stat, xo >> 11 << 3);
    WAIT_VM(3); BAR();
    WAIT_VM(0);
    __builtin_amdgcn_s_setprio(0);
    asm volatile("" : "+v"(xq[0]), "+v"(xq[1]), "+v"(sq) :: "memory");
    key[0] = __builtin_amdgcn_readlane(ksel, 0); key[1] = __builtin_amdgcn_readlane(ksel, 8);
    nf_pre();
#pragma unroll
    for (int n = 0; n < 56; ++n) nf_step(n);
#pragma unroll
    for (int e = 0; e < 16; ++e) nf_unp(e);
#pragma unroll 1
    for (int c = 0; c < 2; ++c) {
      const int ic = (0xffff - (int)((c ? key[1] : key[0]) & 0xffffu)) & (S_ - 1);
      if (__builtin_expect((ic >> 8) != tt || ((ic >> 5) & 3) != kq, 1)) continue;
      const __amdgpu_buffer_rsrc_t rd = mk_rsrc(wsp + WS_DN + (size_t)((c * 8 + et) * 32 + pw * 8) * 1024);
#pragma unroll 1
      for (int it = 0; it < SPIN_MAX; ++it) {
        if (__all(__builtin_amdgcn_raw_buffer_load_b32(rd, (lane & 7) * 1024 + 1020, 0, 16) == tag)) break;
        __builtin_amdgcn_s_sleep(8);
        asm volatile("" ::: "memory");
      }
      float s0, s1;
      unsigned bad = 1;
#pragma unroll 1
      for (int it = 0; !__all(bad == 0) && it < SPIN_MAX; ++it) {
        bad = 0; s0 = 0.f; s1 = 0.f;
#pragma unroll
        for (int i = 0; i < 8; ++i) {
          const u32x4 q = __builtin_amdgcn_raw_buffer_load_b128(rd, lane * 16, i * 1024, 16);
          bad |= (q.y ^ tag) | (q.w ^ tag); s0 += __uint_as_float(q.x); s1 += __uint_as_float(q.z);
        }
        asm volatile("" ::: "memory");
      }
      *(f32x2*)(smem + LDS_FIN + (c * 4 + pw) * 512 + lane * 8) = (f32x2){s0, s1};
      asm volatile("s_waitcnt lgkmcnt(0)" ::: "memory");
      if (lane == 0) asm volatile("ds_add_u32 %0, %1" :: "v"(lds_addr(smem + LDS_SEL + 24 + c * 4)), "v"(1) : "memory");
    }
  } else {
  {
    const int q = j * 4 + wid;
    constexpr int Q1 = WS_MB, Q2 = Q1 + 8, Q3 = Q2 + 8, Q4 = Q3 + 7, Q5 = Q4 + 7, Q6 = Q5 + 7, Q7 = Q6 + 4, Q8 = Q7 + 4, QC = Q8 + 1;
    static_assert(QC <= 128, "warm-up slots");
    unsigned dmy = 0;
    if (q < QC) {
      const char* base; int b0;
      if (q < Q1) { base = wsp; b0 = 0; }
      else if (q < Q2) { base = (const char*)ws_re; b0 = Q1; }
      else if (q < Q3) { base = (const char*)ws_im; b0 = Q2; }
      else if (q < Q4) { base = (const char*)w_gate; b0 = Q3; }
      else if (q < Q5) { base = (const char*)w_up; b0 = Q4; }
      else if (q < Q6) { base = (const char*)w_down; b0 = Q5; }
      else if (q < Q7) { base = (const char*)out; b0 = Q6; }
      else if (q < Q8) { base = (const char*)x; b0 = Q7; }
      else { base = (const char*)w_r; b0 = Q8; }
      base += (size_t)(q - b0) << 20;
      asm volatile("s_load_dword %0, %1, 0" : "+s"(dmy) : "s"(base) : "memory");
    }
    if (wid == 0) { CALL_PG(1); CALL_PG(8); CALL_PG(5); }
    else if (wid == 1) { CALL_PG(2); CALL_PG(9); CALL_PG(6); }
    else if (wid == 2) { CALL_PG(3); CALL_PG(10); }
    else { CALL_PG(4); CALL_PG(11); }
    asm volatile("s_barrier\n\ts_waitcnt lgkmcnt(0)" : "+s"(dmy) :: "memory");
  }
  PAGE(8);
  epi_setup();
  const int wr = wid >> 1, wc = wid & 1;
  const int sw = (fr >> 1) & 7;
  const int ko0 = ((0 + fq) ^ sw) * 16, ko1 = ((4 + fq) ^ sw) * 16;
  const int mrow = (fr >> 2) * 8 + (fr & 3);
  bf16x8 fm[2][4], fn[2][4];
  {
    const char* pm = smem + (wr * 64 + mrow) * 128;
    const char* pn = smem + 16384 + (wc * 64 + mrow) * 128;
    f32x4 acc[4][4];
#pragma unroll
    for (int i = 0; i < 4; ++i)
#pragma unroll
      for (int jn = 0; jn < 4; ++jn)
#pragma unroll
        for (int q = 0; q < 4; ++q) acc[i][jn][q] = 0.f;
#define TO(i) (((i) >> 1) * 4096 + ((i) & 1) * 512)
#define LDSP(T, a) ((__attribute__((address_space(3))) T*)(size_t)(a))
#define SLOTS(n) _Pragma("unroll") for (int q = 0; q < (n); ++q)
#define MFA(c, a, b) asm volatile("v_mfma_f32_16x16x32_bf16 %0, %1, %2, %0" : "+v"(c) : "v"(a), "v"(b))
#define MFA2(c, a, b, a2, b2) asm volatile("v_mfma_f32_16x16x32_bf16 %0, %1, %2, %0\n\tv_mfma_f32_16x16x32_bf16 %0, %3, %4, %0" : "+v"(c) : "v"(a), "v"(b), "v"(a2), "v"(b2))
    unsigned lm[2][2], ln[2][2];
#pragma unroll
    for (int u = 0; u < 2; ++u)
#pragma unroll
      for (int kk = 0; kk < 2; ++kk) {
        lm[u][kk] = lds_addr(pm) + u * 2 * G1_STAGE + (kk ? ko1 : ko0);
        ln[u][kk] = lds_addr(pn) + u * 2 * G1_STAGE + (kk ? ko1 : ko0);
        asm volatile("" : "+v"(lm[u][kk]), "+v"(ln[u][kk]));
      }
    auto ldm = [&](int stg, int i, int kk) __attribute__((always_inline)) { fm[kk][i] = *LDSP(const bf16x8, lm[stg >> 1][kk] + (stg & 1) * G1_STAGE + TO(i)); };
    auto ldn = [&](int stg, int jn, int kk) __attribute__((always_inline)) { fn[kk][jn] = *LDSP(const bf16x8, ln[stg >> 1][kk] + (stg & 1) * G1_STAGE + TO(jn)); };
    auto quad = [&](int a, int c, int d, int s) __attribute__((always_inline)) {
      const int i = (s < 2 || s >= 6) ? a : a + 1, jn = s < 4 ? c : d, kk = (s == 1 || s == 2 || s == 5 || s == 6);
      MFA(acc[i][jn], fm[kk][i], fn[kk][jn]);
    };
    auto quad2 = [&](int a, int c, int d, int s) __attribute__((always_inline)) {
      const int i = (s < 2 || s >= 6) ? a : a + 1, jn = s < 4 ? c : d, kk = (s == 2 || s == 6);
      MFA2(acc[i][jn], fm[kk][i], fn[kk][jn], fm[1 - kk][i], fn[1 - kk][jn]);
    };
    auto half1 = [&](int stg) __attribute__((always_inline)) {
      const int ja = (stg & 1) * 2, jb = 2 - ja;
      SLOTS(8) {
        quad(0, ja, ja + 1, q);
        if (q < 4) ldn(stg, jb + (q >> 1), q & 1); else ldm(stg, 2 + ((q - 4) >> 1), q == 5 || q == 6);
        SB();
      }
      SLOTS(4) { quad2(0, jb, jb + 1, 2 * q); SB(); }
    };
    auto half2 = [&](int stg) __attribute__((always_inline)) {
      const int ja = (stg & 1) * 2, jb = 2 - ja, nx = (stg + 1) & 3;
      SLOTS(8) {
        quad(2, jb + 1, jb, q);
        if (!(q & 1)) ldm(nx, q >> 2, q == 2 || q == 4);
        SB();
      }
      SLOTS(8) {
        quad(2, ja, ja + 1, q);
        if (!(q & 1)) ldn(nx, jb + (q >> 2), (q >> 1) & 1);
        SB();
      }
    };
    SLOTS(16) asm volatile("" : "+v"(acc[q >> 2][q & 3]));
    SB();
    SLOTS(4) { ldm(0, q >> 1, q & 1); ldn(0, q >> 1, q & 1); }
    SB();
#pragma unroll 1
    for (int k4 = 0; k4 < G1_NKT - 4; k4 += 4) {
      half1(0); BAR_LGKM();
      SB(); half2(0);
      half1(1); BAR_LGKM();
      SB(); half2(1);
      half1(2); BAR_LGKM();
      SB(); half2(2);
      half1(3); BAR_LGKM();
      SB(); half2(3);
    }
    bf16x8 gm[2][4], gn[2][8];
    unsigned am[2][2], an[2][2];
#pragma unroll
    for (int u = 0; u < 2; ++u)
#pragma unroll
      for (int kk = 0; kk < 2; ++kk) {
        am[u][kk] = lds_addr(pm) + (u ? 3 : 1) * G1_STAGE + (kk ? ko1 : ko0);
        an[u][kk] = lds_addr(pn) + (u ? 3 : 1) * G1_STAGE + (kk ? ko1 : ko0);
        asm volatile("" : "+v"(am[u][kk]), "+v"(an[u][kk]));
      }
    unsigned al = lds_addr(smem + LDS_MB) + (wr * 32 + fq * 8) * 4, ac = lds_addr(smem + LDS_MB) + (256 + wc * 64 + mrow) * 4;
    unsigned ae = lds_addr(smem + G2_MRING) + (wc * 64 + mrow) * 128 + (((wr * 4 + fq) ^ sw) * 16);
    asm volatile("" : "+v"(al), "+v"(ac), "+v"(ae));
    auto rdg = [&](int set, int t, int h, int r) __attribute__((always_inline)) {
      const int u = t == 15, o = t == 14 ? G1_STAGE : 0;
      if (r == 0 || r == 2 || r == 4 || r == 5) {
        const int i = r >= 4, kk = r == 2 || r == 4;
        gm[set][kk * 2 + i] = *LDSP(const bf16x8, am[u][kk] + o + h * 4096 + i * 512);
      } else {
        const int jn = r < 4 ? 0 : (r - 4) >> 1, kk = r < 4 ? r >> 1 : r & 1;
        gn[set][kk * 4 + jn] = *LDSP(const bf16x8, an[u][kk] + o + TO(jn));
      }
    };
    auto mmg = [&](int set, int h, int q) __attribute__((always_inline)) {
      const int jn = q >> 2, r = q & 3, i = (r >> 1) ^ (jn & 1), kk = r == 1 || r == 2;
      MFA(acc[h * 2 + i][jn], gm[set][kk * 2 + i], gn[set][kk * 4 + jn]);
    };
    f32x4 rsv[2], mrv[2];
    float cev[4], tc[32];
    auto rds = [&](int h, int r) __attribute__((always_inline)) {
      if (r < 2) mrv[r] = *LDSP(const f32x4, al + h * 256 + r * 16); else rsv[r - 2] = *LDSP(const f32x4, al + 512 + h * 256 + (r - 2) * 16);
    };
    auto prep = [&](int n) __attribute__((always_inline)) {
      tc[n] = llvm_fmul_legacy(mrv[(n >> 2) & 1][n & 3], cev[n >> 3]);
      asm volatile("" : "+v"(tc[n]));
    };
    u32x4 ep;
    auto epi = [&](int h, int q) __attribute__((always_inline)) {
      const int jn = q >> 1, i = q & 1;
      asm volatile("v_fmac_f32 %0, %4, %8\n\tv_fmac_f32 %1, %5, %9\n\tv_fmac_f32 %2, %6, %10\n\tv_fmac_f32 %3, %7, %11"
                   : "+v"(tc[q * 4]), "+v"(tc[q * 4 + 1]), "+v"(tc[q * 4 + 2]), "+v"(tc[q * 4 + 3])
                   : "v"(acc[h * 2 + i][jn][0]), "v"(acc[h * 2 + i][jn][1]), "v"(acc[h * 2 + i][jn][2]), "v"(acc[h * 2 + i][jn][3]),
                     "v"(rsv[i][0]), "v"(rsv[i][1]), "v"(rsv[i][2]), "v"(rsv[i][3]));
      ep[i * 2] = pk2(tc[q * 4], tc[q * 4 + 1]); ep[i * 2 + 1] = pk2(tc[q * 4 + 2], tc[q * 4 + 3]);
      if (i) *LDSP(u32x4, ae + m_off(h) + TO(jn)) = ep;
    };
    SB();
    half1(0);
    BAR_LGKM();
    SB();
    SLOTS(16) {
      if (q < 8) quad(2, 3, 2, q); else quad(2, 0, 1, q - 8);
      if (q < 12) rdg(0, 13, 0, q);
      SB();
    }
    BAR();
    SB();
    SLOTS(16) {
      mmg(0, 0, q);
      if (q < 12) rdg(1, 14, 0, q);
      else { rds(0, q - 12); cev[q - 12] = *LDSP(const float, ac + (((q - 12) >> 1) * 32 + ((q - 12) & 1) * 4) * 4); }
      SB();
    }
    PAGE(9);
    BAR();
    SB();
    SLOTS(16) { mmg(1, 0, q); if ((q & 3) < 3) rdg(0, 15, 0, (q >> 2) * 3 + (q & 3)); if (q >= 8) { prep(2 * q - 16); prep(2 * q - 15); } SB(); }
    SLOTS(16) { mmg(0, 0, q); if ((q & 3) < 3) rdg(1, 13, 1, (q >> 2) * 3 + (q & 3)); prep(16 + q); SB(); if (q >= 9 && (q & 1)) { epi(0, (q - 9) >> 1); SB(); } }
    SLOTS(8) { mmg(1, 1, q); if (q < 6) { rdg(0, 14, 1, 2 * q); rdg(0, 14, 1, 2 * q + 1); } SB(); if (q & 1) { epi(0, 4 + (q >> 1)); SB(); } }
    BAR_LGKM();
    SB();
    SLOTS(8) { mmg(1, 1, 8 + q); if (q < 4) rdg(1, 15, 1, q == 0 ? 1 : q == 1 ? 3 : q + 4); else rds(1, q - 4); SB(); }
    SLOTS(16) { mmg(0, 1, q); if (q < 8) rdg(1, 15, 1, q == 0 ? 0 : q == 1 ? 2 : q < 4 ? q + 2 : q + 4); prep(q); SB(); }
    asm volatile("s_waitcnt lgkmcnt(0)" ::: "memory");
    if (lane == 0) asm volatile("ds_add_u32 %0, %1" :: "v"(lds_addr(smem + LDS_SEL + 12)), "v"(1) : "memory");
    SB();
    SLOTS(16) {
      mmg(1, 1, q);
      if (q < 4) fn[0][q] = *(const bf16x8*)(smem + (wr * 128 + fr) * 128 + (q >> 1) * 2048 + ((q & 1) ? ko1 : ko0));
      else if (q < 8) fm[q & 1][(q - 4) >> 1] = *(const bf16x8*)(smem + G2_MRING + m_off(0) + (wc * 64 + mrow) * 128 + TO((q - 4) >> 1) + ((q & 1) ? ko1 : ko0));
      prep(16 + q);
      SB();
    }
    {
      lds_vi rel = (lds_vi)(smem + LDS_SEL + 12);
      int it = 0;
      while (rel[0] != 4 && it < SPIN_MAX * 8) ++it;
      asm volatile("" ::: "memory");
    }
    asm volatile("s_nop 7");
    SB();
    SLOTS(8) { epi(1, q); SB(); }
  }

  const char* pn = smem + (wr * 128 + fr) * 128;
  const char* pm = smem + G2_MRING + (wc * 64 + mrow) * 128;
  f32x4 acc[8][4];
  auto mm = [&](bool imaj, int set, int q2, int s, int mode) __attribute__((always_inline)) {
    int i, jj, kk;
    if (imaj) { i = s >> 2; const int r = s & 3; jj = (r >> 1) ^ (i & 1); kk = (r == 1 || r == 2); }
    else { jj = s >> 3; const int r = s & 7; i = jj ? 3 - (r >> 1) : (r >> 1); kk = ((r + 1) >> 1) & 1; }
    if (mode == 1 && !(s & 1)) asm volatile("v_mfma_f32_16x16x32_bf16 %0, %1, %2, 0" : "=&v"(acc[q2 * 2 + jj][i]) : "v"(fm[kk][i]), "v"(fn[set][jj * 2 + kk]));
    else if (mode) asm volatile("v_mfma_f32_16x16x32_bf16 %0, %1, %2, %0" : "+v"(acc[q2 * 2 + jj][i]) : "v"(fm[kk][i]), "v"(fn[set][jj * 2 + kk]));
    else acc[q2 * 2 + jj][i] = __builtin_amdgcn_mfma_f32_16x16x32_bf16(fm[kk][i], fn[set][jj * 2 + kk], acc[q2 * 2 + jj][i], 0, 0, 0);
  };
  unsigned bn[2], bm[2], an[2], am[2];
#pragma unroll
  for (int kk = 0; kk < 2; ++kk) {
    bn[kk] = lds_addr(pn) + (kk ? ko1 : ko0); bm[kk] = lds_addr(pm) + (kk ? ko1 : ko0);
    asm volatile("" : "+v"(bn[kk]), "+v"(bm[kk]));
    an[kk] = bn[kk]; am[kk] = bm[kk] + m_off(0);
  }
#define RDN(jt, kk, o) (*LDSP(const bf16x8, an[kk] + (o) + (jt) * 2048))
#define RDM(i, kk, o) (*LDSP(const bf16x8, am[kk] + (o) + TO(i)))
  PAGE(10);
  tag = __builtin_amdgcn_readfirstlane(((const unsigned*)(smem + LDS_SEL))[5]);
  {
    int son = 0;
    auto tile = [&](int kt, int zero) __attribute__((always_inline)) {
      SLOTS(16) {
        mm(true, 0, 0, q, zero);
        if (!(q & 1)) {
          if (q < 8) fm[(q >> 1) & 1][2 + (q >> 2)] = RDM(2 + (q >> 2), (q >> 1) & 1, 0);
          else { const int r = (q - 8) >> 1; fn[1][r] = RDN(2 + (r >> 1), r & 1, 0); }
        }
        SB();
      }
      SLOTS(16) {
        mm(false, 1, 1, q, zero);
        if (!(q & 1) && q < 8) fn[0][q >> 1] = RDN(4 + (q >> 2), (q >> 1) & 1, 0);
        SB();
      }
      const int som = son >> 1;
      son = son == 65536 ? 0 : son + 32768;
      SLOTS(16) {
        mm(false, 0, 2, q, zero);
        if (!(q & 1) && q < 8) fn[1][q >> 1] = RDN(6 + (q >> 2), (q >> 1) & 1, 0);
        if (q == 9 || q == 11) { an[q == 11] = bn[q == 11] + son; asm volatile("" : "+v"(an[q == 11])); }
        if (q == 13 || q == 15) { am[q == 15] = bm[q == 15] + som; asm volatile("" : "+v"(am[q == 15])); }
        SB();
      }
      BAR_LGKM();
      SB();
      SLOTS(16) {
        mm(true, 1, 3, q, zero);
        if (!(q & 1)) {
          if (q < 8) fn[0][q >> 1] = RDN(q >> 2, (q >> 1) & 1, 0);
          else fm[(q >> 1) & 1][(q - 8) >> 2] = RDM((q - 8) >> 2, (q >> 1) & 1, 0);
        }
        SB();
      }
    };
    SB();
    tile(0, 1);
#pragma unroll 1
    for (int kt = 1; kt < G2_NKT - 3; ++kt) tile(kt, 0);
  }
  unsigned fx;
  {
    constexpr int NG = 12;
    static constexpr int g_t[NG] = {0, 0, 1, 1, 2, 2, 2, 1, 0, 0, 1, 2};
    static constexpr int g_q[NG] = {0, 1, 0, 1, 0, 1, 2, 2, 2, 3, 3, 3};
    constexpr int G14 = 1, G15 = 3, GP0 = 5, GP1 = 6;
    constexpr auto chp = [](int g) { return g == 0 || g_t[g] != g_t[g - 1]; };
    constexpr auto chn = [](int g) { return g + 1 < NG && g_t[g + 1] != g_t[g]; };
    static_assert(n_off(13) == 32768 && n_off(14) == 65536 && n_off(15) == 0, "");
    unsigned an2[2];
#pragma unroll
    for (int kk = 0; kk < 2; ++kk) {
      an[kk] = bn[kk]; an2[kk] = bn[kk] + 65536; am[kk] = bm[kk];
      asm volatile("" : "+v"(an[kk]), "+v"(an2[kk]), "+v"(am[kk]));
    }
    int vo;
    u32x4 pk;
    u32x2 kv;
    constexpr int NF0 = 152;
    auto publish = [&](int d, int h) __attribute__((always_inline)) {
      if (h == 0) { vo = lane; PINV(vo); vo = vo * 16 + (((d + kq + 1) & 3) * 4 + kq) * 4 * MSG; PINV(vo); }
      const f32x4 v = acc[2 * d + (h >> 2)][h & 3];
      pk[(h & 1) * 2] = pk2(v[0], v[1]); pk[(h & 1) * 2 + 1] = pk2(v[2], v[3]);
      if (h & 1) __builtin_amdgcn_raw_buffer_store_b128(pk, rs, vo + (h >> 1) * 1024, 0, 16);
    };
    int fo = lane == 0 ? kq * 512 : (int)0x80000000;
#define MSG_FLAG(d) __builtin_amdgcn_raw_buffer_store_b32(tag, rc, fo + (((d) + kq + 1) & 3) * 2048, 0, 16)
    auto rdn = [&](int g, int r) __attribute__((always_inline)) {
      const int t = g_t[g], jt = g_q[g] * 2 + (r >> 1);
      fn[g & 1][r] = *LDSP(const bf16x8, (t == 1 ? an2[r & 1] : an[r & 1]) + (t == 0 ? 32768 : 0) + jt * 2048);
    };
    auto rdm = [&](int t, int i, int kk) __attribute__((always_inline)) { fm[kk][i] = *LDSP(const bf16x8, am[kk] + m_off(13 + t) + TO(i)); };
    auto group = [&](auto GI) __attribute__((always_inline)) {
      constexpr int gi = decltype(GI)::value;
      if (gi == G14) { BAR();
        SB(); }
      if (gi == G15) {
        PAGE(11);
        BAR();
        SB();
        xq[0] = ld16_s<0>(x, xo); xq[1] = ld16_s<64>(x, xo);
        sq = ld8_s_sc1(stat, xo >> 11 << 3);
        kv = *(const u32x2*)(smem + LDS_SEL);
        SB();
      }
#pragma unroll
      for (int q = 0; q < 16; ++q) {
        const int n = gi * 16 + q;
        if (n == NF0) {
          wait_xq<12>(xq, sq);
          key[0] = __builtin_amdgcn_readfirstlane(kv.x); key[1] = __builtin_amdgcn_readfirstlane(kv.y);
          nf_pre();
          SB();
        }
        if (n == 112) {
        }
        if (n == 152) {
        }
        if (n == 168) {
          WAIT_VM(8); MSG_FLAG(0); SB(); }
        if (n == 176) { WAIT_VM(5); MSG_FLAG(1); SB(); }
        if (n == 184) {
          WAIT_VM(2);
          MSG_FLAG(2); fx = bld4_sc1(rc, po); SB(); }
        mm(chp(gi) || chn(gi), gi & 1, g_q[gi], q, 2);
        constexpr bool ni = gi + 1 < NG && (chp(gi + 1 < NG ? gi + 1 : gi) || chn(gi + 1 < NG ? gi + 1 : gi));
        auto nr = [](int r) { return ni && r >= 2 ? 5 - r : r; };
        if constexpr (chp(gi) && chn(gi)) {
          if (q < 4) rdm(g_t[gi], 2 + (q >> 1), q & 1); else if (q < 8) rdn(gi + 1, nr(q - 4)); else if (q < 12) rdm(g_t[gi + 1], (q - 8) >> 1, q & 1);
        } else if constexpr (chp(gi)) {
          if (q < 4) rdm(g_t[gi], 2 + (q >> 1), q & 1);
          else if (gi + 1 < NG && q < 12 && !(q & 1)) rdn(gi + 1, nr((q - 4) >> 1));
        } else if constexpr (chn(gi)) {
          if (!(q & 1)) { if (q < 8) rdn(gi + 1, nr(q >> 1)); else rdm(g_t[gi + 1], (q - 8) >> 2, (q >> 1) & 1); }
        } else {
          if (!(q & 1) && q < 8) rdn(gi + 1, nr(q >> 1));
        }
        if (gi == GP0 && q < 8) publish(0, q);
        if (gi == GP1 && q < 8) publish(1, q);
        if (gi == 9 && q < 8) publish(2, q);
        if (n >= NF0) {
          const int m = (n - NF0) * 2;
          if (m < 56) { nf_step(m); nf_step(m + 1); } else if (m < 72) { nf_unp(m - 56); nf_unp(m - 55); }
        }
        SB();
      }
    };
#define GR4(i) group(IC<i>{}); group(IC<i + 1>{}); group(IC<i + 2>{}); group(IC<i + 3>{});
    GR4(0) GR4(4) GR4(8)
    asm volatile("s_nop 7");
    SB();
  }
#pragma unroll
  for (int i = 0; i < 4; ++i) *(f32x4*)(smem + LDS_ACC + wr * 16384 + wc * 4096 + i * 1024 + lane * 16) = acc[7][i];
#pragma unroll
  for (int i = 0; i < 4; ++i) ya[i] = acc[6][i];
  asm volatile("s_waitcnt lgkmcnt(0)" ::: "memory");
  if (lane == 0) sig[0] = 2;
  asm volatile("s_waitcnt vmcnt(0)" : "+v"(fx) :: "memory");
  seen = (unsigned)__ballot(fx == tag);
  }

  PAGE(6);
  const unsigned rel0 = 0xffff - (key[0] & 0xffffu) - trow, rel1 = 0xffff - (key[1] & 0xffffu) - trow;
  if (__builtin_expect(rel0 < 16 || rel1 < 16, 0)) {
#pragma unroll 1
    for (int c = 0; c < 2; ++c) {
      const unsigned rel = c ? rel1 : rel0;
      if (rel >= 16) continue;
      const float sg = rbf(1.f / (1.f + __expf(-key_score(c ? key[1] : key[0]))));
      lds_vi cnt = (lds_vi)(smem + LDS_SEL + 24 + c * 4);
      int it = 0;
      while (cnt[0] != 4 && it < SPIN_MAX * 8) { __builtin_amdgcn_s_sleep(1); ++it; }
      asm volatile("" ::: "memory");
      const float* fp = (const float*)(smem + LDS_FIN + c * 2048) + (wid & 1) * 64 + fq * 8;
#pragma unroll
      for (int u4 = 0; u4 < 4; ++u4) {
        const float* q = fp + (u4 >> 1) * 32 + (u4 & 1) * 4;
        const f32x4 v = (*(const f32x4*)q + *(const f32x4*)(q + 128)) + (*(const f32x4*)(q + 256) + *(const f32x4*)(q + 384));
#pragma unroll
        for (int k = 0; k < 4; ++k) {
          float u = rbf(nf[u4 * 4 + k] + rbf(rbf(v[k]) * sg));
          asm volatile("" : "+v"(u));
          nf[u4 * 4 + k] = rel == fr ? u : nf[u4 * 4 + k];
        }
      }
    }
  }
  if (ha) {
    int it = 0;
    while (sig[0] != 2 && it < SPIN_MAX * 8) { __builtin_amdgcn_s_sleep(1); ++it; }
    asm volatile("" ::: "memory");
#pragma unroll
    for (int i = 0; i < 4; ++i) ya[i] = *(const f32x4*)(smem + LDS_ACC + (wid & 2) * 8192 + (wid & 1) * 4096 + i * 1024 + lane * 16);
  }
  u32x4 pp[3][2];
  unsigned e_lo = 0x00003f80u, e_hi = 0x3f800000u;
  asm volatile("" : "+v"(e_lo), "+v"(e_hi));
  __amdgpu_buffer_rsrc_t rs_ = rs, rc_ = rc;
  int mo[3] = {MSG_IDX(0) * 4 * MSG, MSG_IDX(1) * 4 * MSG, MSG_IDX(2) * 4 * MSG}, mv = lane * 16 + ha * 2048, mv2 = mv + 1024;
  asm volatile("" : "+s"(rs_), "+s"(rc_), "+s"(mo[0]), "+s"(mo[1]), "+s"(mo[2]));
  asm volatile("" : "+v"(mv), "+v"(mv2));
#define MSG_POLL(m) do { \
    if (!((seen >> (m)) & 1)) { \
      unsigned pv_, it_ = SPIN_MAX; \
      asm volatile("1:\n\tbuffer_load_dword %0, %3, %4, 0 offen sc1\n\ts_waitcnt vmcnt(0)\n\tv_cmp_eq_u32 vcc, %5, %0\n\ts_mov_b32 %1, vcc_lo\n\t" \
                   "s_bitcmp1_b32 %1, %6\n\ts_cbranch_scc1 2f\n\ts_sub_u32 %2, %2, 1\n\ts_cbranch_scc1 2f\n\ts_sleep 1\n\ts_branch 1b\n2:" \
                   : "=&v"(pv_), "+s"(seen), "+s"(it_) : "v"(po), "s"(rc_), "s"(tag), "i"(m) : "vcc", "scc", "memory"); \
    } \
    pp[m][0] = __builtin_amdgcn_raw_buffer_load_b128(rs_, mv, mo[m], 16); \
    pp[m][1] = __builtin_amdgcn_raw_buffer_load_b128(rs_, mv2, mo[m], 16); } while (0)
#define MSG_ADD(m) do { \
    _Pragma("unroll") for (int u = 0; u < 8; ++u) { \
      ya[u >> 1][(u & 1) * 2] = add_bf(pp[m][u >> 2][u & 3], e_lo, ya[u >> 1][(u & 1) * 2]); ya[u >> 1][(u & 1) * 2 + 1] = add_bf(pp[m][u >> 2][u & 3], e_hi, ya[u >> 1][(u & 1) * 2 + 1]); } } while (0)
  MSG_POLL(2); MSG_POLL(1); MSG_POLL(0);
  SB();
  MSG_ADD(2); MSG_ADD(1);
  asm volatile("" : "+v"(ya[0]), "+v"(ya[1]), "+v"(ya[2]), "+v"(ya[3]));
  SB();
  MSG_ADD(0);
  {
    unsigned ov[8];
#pragma unroll
    for (int u = 0; u < 8; ++u) {
      const unsigned yb = pk2(ya[u >> 1][(u & 1) * 2], ya[u >> 1][(u & 1) * 2 + 1]);
      const unsigned sb = pk2(add_bf(yb, e_lo, xf[2 * u]), add_bf(yb, e_hi, xf[2 * u + 1]));
      ov[u] = pk2(add_bf(sb, e_lo, nf[2 * u]), add_bf(sb, e_hi, nf[2 * u + 1]));
    }
    *(u32x4*)((char*)out + (unsigned)xo) = (u32x4){ov[0], ov[1], ov[2], ov[3]};
    *(u32x4*)((char*)out + (unsigned)xo + 64) = (u32x4){ov[4], ov[5], ov[6], ov[7]};
  }
  if (g == 37 && wid == 0) {
    asm volatile("s_waitcnt vmcnt(0)" ::: "memory");
    for (int i = 0; i < 40; ++i) __builtin_amdgcn_s_sleep(8);
    const char* p = wsp;
    const __amdgpu_buffer_rsrc_t r = mk_rsrc(p);
    unsigned a = __builtin_amdgcn_raw_buffer_load_b32(r, 65536 - 128, 0, 16);
    asm volatile("s_waitcnt vmcnt(0)" : "+v"(a) :: "memory");
    const unsigned t0 = (unsigned)__builtin_amdgcn_s_memrealtime();
    unsigned b = a & 0;
#pragma unroll 1
    for (int i = 0; i < 40; ++i) { b = __builtin_amdgcn_raw_buffer_load_b32(r, i * 1152 + (b & 0), 0, 16); asm volatile("s_waitcnt vmcnt(0)" : "+v"(b) :: "memory"); }
    if (b == 0x12345678u && t0 == 77) out[0] = 1;
  }
}

extern "C" void kernel_launch(void* const* d_in, const int* in_sizes, int n_in, void* d_out, int out_size,
                              void* d_ws, size_t ws_size, hipStream_t stream) {
  const u16* x = (const u16*)d_in[0];
  const u16* ws_re = (const u16*)d_in[1];
  const u16* ws_im = (const u16*)d_in[2];
  const u16* wd_re = (const u16*)d_in[3];
  const u16* wd_im = (const u16*)d_in[4];
  const u16* w_r = (const u16*)d_in[6];
  const u16* w_gate = (const u16*)d_in[7];
  const u16* w_up = (const u16*)d_in[8];
  const u16* w_down = (const u16*)d_in[9];
  k_main<<<256, 512, 0, stream>>>(x, wd_re, wd_im, w_r, (char*)d_ws, ws_re, ws_im, w_gate, w_up, w_down, (u16*)d_out);
}
```

```cpp
#include <hip/hip_runtime.h>
#include <hip/hip_bf16.h>
#include <stdint.h>
#include <type_traits>

extern "C" __device__ unsigned long long llvm_dispatch_id(void) __asm("llvm.amdgcn.dispatch.id");
extern "C" __device__ float llvm_fmul_legacy(float, float) __asm("llvm.amdgcn.fmul.legacy");
typedef unsigned short u16;
typedef unsigned long long u64;
typedef __attribute__((ext_vector_type(8))) short bf16x8;
typedef __attribute__((ext_vector_type(4))) unsigned u32x4;
typedef __attribute__((ext_vector_type(2))) unsigned u32x2;
typedef __attribute__((ext_vector_type(4))) float f32x4;
typedef __attribute__((ext_vector_type(2))) float f32x2;
typedef __attribute__((ext_vector_type(2))) __bf16 bf16x2;
typedef __attribute__((address_space(3))) void* lds_vp;
typedef volatile __attribute__((address_space(3))) int* lds_vi;

constexpr int S_ = 2048, D_ = 1024, F_ = 3584;
template <int N> using IC = std::integral_constant<int, N>;

static __device__ __forceinline__ float bf2f(u16 u) { return __uint_as_float((unsigned)u << 16); }
static __device__ __forceinline__ u16 f2bf(float f) {
  __hip_bfloat16 b = __float2bfloat16(f);
  return *reinterpret_cast<u16*>(&b);
}
static __device__ __forceinline__ float rbf(float f) { return bf2f(f2bf(f)); }
static __device__ __forceinline__ float lo_bf(unsigned u) { return __uint_as_float(u << 16); }
static __device__ __forceinline__ float hi_bf(unsigned u) { return __uint_as_float(u & 0xffff0000u); }
static __device__ __forceinline__ unsigned pk2(float a, float b) {
  const bf16x2 v = __builtin_convertvector((f32x2){a, b}, bf16x2);
  return __builtin_bit_cast(unsigned, v);
}
static __device__ __forceinline__ float add_bf(unsigned u, unsigned e, float c) {
  return __builtin_amdgcn_fdot2_f32_bf16(__builtin_bit_cast(bf16x2, u), __builtin_bit_cast(bf16x2, e), c, false);
}
static __device__ __forceinline__ float ln_elem(float x, float mb, float sd, float ri) {
  const float d = rbf(x - mb);
  const float q0 = d * ri;
  return rbf(__builtin_fmaf(__builtin_fmaf(-q0, sd, d), ri, q0));
}
template <int CTRL> static __device__ __forceinline__ float dpp_f(float v) {
  return __int_as_float(__builtin_amdgcn_update_dpp(0, __float_as_int(v), CTRL, 0xf, 0xf, true));
}
template <int CTRL> static __device__ __forceinline__ unsigned dpp_u(unsigned v) {
  return (unsigned)__builtin_amdgcn_update_dpp(0, (int)v, CTRL, 0xf, 0xf, true);
}
static __device__ __forceinline__ float row_sum(float v) {
  v += dpp_f<0xB1>(v);
  v += dpp_f<0x4E>(v);
  v += dpp_f<0x141>(v);
  v += dpp_f<0x140>(v);
  return v;
}
static __device__ __forceinline__ float fold32(float a, float b) {
  const auto r = __builtin_amdgcn_permlane32_swap(__float_as_uint(a), __float_as_uint(b), false, false);
  return __uint_as_float(r[0]) + __uint_as_float(r[1]);
}
static __device__ __forceinline__ float fold16(float a, float b) {
  const auto r = __builtin_amdgcn_permlane16_swap(__float_as_uint(a), __float_as_uint(b), false, false);
  return __uint_as_float(r[0]) + __uint_as_float(r[1]);
}
static __device__ __forceinline__ float rdl(float v, int l) { return __int_as_float(__builtin_amdgcn_readlane(__float_as_int(v), l)); }

#define ST_JB 9
#define F0 168
#define F1 176
#define F2 184
#define FX F2
#define CTJ 10
#define WSN 10
#define FSM 0xa2
#define GV_JB 7
#define DG 11
#define DN1 5
#define DN2 8
#define GTRY 0
#define NFP 0
#define GSL 8
#define NSC 0
#define SP0 3
#define SPN 6
#define ORD 0
#define TPOS {0, 0, 0, 0, 109, 115, 1, 2, 2, 3, 4, 5, 6, 7, 8, 9}
#define TWHO {0, 0, 0, 0, 7, 7, 0, 1, 2, 3, 4, 5, 6, 7, 0, 1}
#define PRE 31
#define PPOS 0
#define FULL 0
#define AB_STRIDE 32
#define SPIN_MAX (1 << 17)

static __device__ __forceinline__ unsigned lds_addr(const void* p) { return (unsigned)(size_t)(lds_vp)p; }

constexpr int MSG = 4096;
constexpr size_t WS_ABT   = 0;
constexpr size_t WS_SLAB  = WS_ABT + (size_t)D_ * 4096 * 2;
constexpr size_t WS_GU    = WS_SLAB + (size_t)64 * 64 * MSG;
constexpr size_t WS_DN    = WS_GU + (size_t)8 * F_ * 16;
constexpr size_t WS_FLAGS = WS_DN + (size_t)2 * 8 * 32 * 64 * 16;
constexpr int FL_X = 0;
constexpr int FL_AB = 64 * 64 * 32;
constexpr int FL_ST = FL_AB + 2048 * AB_STRIDE;
constexpr int FL_CE = FL_ST + 4096;
constexpr int FL_KEY = FL_CE + 4096;
constexpr int FL_EPOCH = FL_KEY + 4096;
constexpr int WS_MB = (int)((WS_FLAGS + (FL_EPOCH + 1) * 4 + 1048575) >> 20);

#define WAIT_VM(n) asm volatile("s_waitcnt vmcnt(" #n ")" ::: "memory")
template <int N> static __device__ __forceinline__ void wait_vm() { asm volatile("s_waitcnt vmcnt(%0)" :: "i"(N) : "memory"); }
#define BAR_LGKM() asm volatile("s_waitcnt lgkmcnt(0)\n\ts_barrier" ::: "memory")
#define BAR() asm volatile("s_barrier" ::: "memory")
#define SB() __builtin_amdgcn_sched_barrier(0)
#define PAGE(n) asm volatile("s_branch 2f\n\t.p2align 12\nk_pg" #n ":\n\ts_setpc_b64 s[42:43]\n2:")
#define CALL_PG(n) asm volatile("s_getpc_b64 s[40:41]\n3:\n\ts_mov_b32 s42, k_pg" #n "-3b\n\ts_ashr_i32 s43, s42, 31\n\ts_add_u32 s40, s40, s42\n\ts_addc_u32 s41, s41, s43\n\t" \
                                "s_swappc_b64 s[42:43], s[40:41]" ::: "s40", "s41", "s42", "s43", "scc", "memory")

static __device__ __forceinline__ __amdgpu_buffer_rsrc_t mk_rsrc(const void* p) {
  return __builtin_amdgcn_make_buffer_rsrc((void*)p, 0, 0x7fffffff, 0x00020000);
}
#define DMA_(rsrc, voff, mbase, MI, sbase, SI, pol) do { unsigned _t; \
    asm volatile("s_add_i32 m0, %2, %3\n\ts_add_i32 %0, %5, %6\n\tbuffer_load_dwordx4 %1, %4, %0 offen" pol " lds" \
                 : "=&s"(_t) : "v"(voff), "s"(mbase), "i"(MI), "s"(rsrc), "s"(sbase), "i"(SI) : "memory", "scc"); } while (0)
#define TOUCH(rsrc, voff, mbase, MI, soff) \
    asm volatile("s_add_i32 m0, %1, %2\n\ts_nop 0\n\tbuffer_load_dword %0, %3, %4 offen lds" \
                 :: "v"(voff), "s"(mbase), "i"(MI), "s"(rsrc), "s"(soff) : "memory", "scc")
template <int OFF> static __device__ __forceinline__ u32x4 ld16_s(const void* p, int voff) {
  u32x4 v;
  asm volatile("global_load_dwordx4 %0, %1, %2 offset:%3" : "=v"(v) : "v"(voff), "s"(p), "i"(OFF) : "memory");
  return v;
}
template <int OFF> static __device__ __forceinline__ void sld(unsigned& sink, const void* p, int soff) {
  asm volatile("s_load_dword %0, %1, %2 offset:%3" : "+s"(sink) : "s"(p), "s"(soff), "i"(OFF) : "memory");
}
static __device__ __forceinline__ unsigned gld4_s(const void* p, unsigned voff) {
  unsigned v;
  asm volatile("global_load_dword %0, %1, %2" : "=v"(v) : "v"(voff), "s"(p) : "memory");
  return v;
}
static __device__ __forceinline__ u32x2 bld8_sc1(__amdgpu_buffer_rsrc_t r, unsigned voff) {
  u32x2 v;
  asm volatile("buffer_load_dwordx2 %0, %1, %2, 0 offen sc1" : "=v"(v) : "v"(voff), "s"(r) : "memory");
  return v;
}
template <int N> static __device__ __forceinline__ void wait_xs(unsigned& a, u32x2& b) { asm volatile("s_waitcnt vmcnt(%2)" : "+v"(a), "+v"(b) : "i"(N) : "memory"); }
template <int N> static __device__ __forceinline__ void wait_xq(u32x4 (&a)[2], u32x2& b) { asm volatile("s_waitcnt vmcnt(%3)" : "+v"(a[0]), "+v"(a[1]), "+v"(b) : "i"(N) : "memory"); }
static __device__ __forceinline__ u32x2 ld8_s_sc1(const void* p, int voff) {
  u32x2 v;
  asm volatile("global_load_dwordx2 %0, %1, %2 sc1" : "=v"(v) : "v"(voff), "s"(p) : "memory");
  return v;
}
static __device__ __forceinline__ void lgkm0(unsigned& sink) { asm volatile("s_waitcnt lgkmcnt(0)" : "+s"(sink) :: "memory"); }
template <typename T> static __device__ __forceinline__ void pin4(T& a, T& b, T& c, T& d) { asm volatile("" : "+s"(a), "+s"(b), "+s"(c), "+s"(d)); }
template <typename T, typename U> static __device__ __forceinline__ void pins(T& a, U& b) { asm volatile("" : "+s"(a), "+s"(b)); }
static __device__ __forceinline__ void launder(int& v, int& a, int& b) { asm volatile("" : "+v"(v)); asm volatile("" : "+s"(a), "+s"(b)); }
static __device__ __forceinline__ unsigned bld4_sc1(__amdgpu_buffer_rsrc_t r, int voff) {
  unsigned v;
  asm volatile("buffer_load_dword %0, %1, %2, 0 offen sc1" : "=v"(v) : "v"(voff), "s"(r) : "memory");
  return v;
}
static __device__ __forceinline__ u32x4 bld16_nt(__amdgpu_buffer_rsrc_t r, int voff, int soff) {
  u32x4 v;
  asm volatile("buffer_load_dwordx4 %0, %1, %2, %3 offen nt" : "=v"(v) : "v"(voff), "s"(r), "s"(soff) : "memory");
  return v;
}
static __device__ __forceinline__ void bld16_nt_io(u32x4& v, __amdgpu_buffer_rsrc_t r, int voff, int soff) {
  asm volatile("buffer_load_dwordx4 %0, %1, %2, %3 offen nt" : "+v"(v) : "v"(voff), "s"(r), "s"(soff) : "memory");
}
static __device__ __forceinline__ u32x4 bld16_sc1(__amdgpu_buffer_rsrc_t r, int voff, int soff) {
  u32x4 v;
  asm volatile("buffer_load_dwordx4 %0, %1, %2, %3 offen sc1" : "=v"(v) : "v"(voff), "s"(r), "s"(soff) : "memory");
  return v;
}
static __device__ __forceinline__ u32x2 granule_wait(const u64* p, unsigned tag) {
  u64 v = 0;
#pragma unroll 1
  for (int it = 0; it < SPIN_MAX; ++it) {
    v = __hip_atomic_load(p, __ATOMIC_RELAXED, __HIP_MEMORY_SCOPE_AGENT);
    if (__all((unsigned)(v >> 32) == tag)) break;
    __builtin_amdgcn_s_sleep(2);
  }
  return (u32x2){(unsigned)v, (unsigned)(v >> 32)};
}
static __device__ __forceinline__ float key_score(unsigned key) {
  const unsigned u = key >> 16;
  return bf2f((u16)((u & 0x8000u) ? (u & 0x7fffu) : (~u & 0xffffu)));
}
static __device__ __forceinline__ void merge2(unsigned& a1, unsigned& a2, unsigned b1, unsigned b2) {
  const unsigned lo = min(a1, b1);
  a1 = max(a1, b1);
  a2 = max(lo, max(a2, b2));
}
static __device__ __forceinline__ float sqdev16(const u32x4 (&v)[2], float mb) {
  f32x2 q2 = {0.f, 0.f};
#pragma unroll
  for (int i = 0; i < 8; ++i) {
    const f32x2 e = (f32x2){lo_bf(v[i >> 2][i & 3]), hi_bf(v[i >> 2][i & 3])} - mb;
    q2 += e * e;
  }
  return q2.x + q2.y;
}

constexpr int G1_NST = 4, G1_NKT = 16, G1_STAGE = 32768;
constexpr int G2_NKT = 16, G2_MRING = 98304;
constexpr int n_off(int k) { return (k % 3) * 32768; }
constexpr int m_off(int k) { return ((k + 2) % 3) * 16384; }
constexpr int k_ord(int k) { return k < 2 ? k : (k < 9 ? 2 * (k - 1) : 2 * (k - 8) + 1); }
constexpr int LDS_DUMMY = G2_MRING + 49152;
constexpr int LDS_GU = LDS_DUMMY + 4096;
constexpr int LDS_FIN = LDS_GU + 4096;
constexpr int LDS_MB = LDS_FIN + 4096;
constexpr int LDS_SEL = LDS_MB + 1536;
constexpr int LDS_SIG = LDS_SEL + 64;
constexpr int LDS_KEY = LDS_SIG + 32;
constexpr int LDS_BYTES = LDS_KEY + 128;
constexpr int LDS_ACC = n_off(G2_NKT - 3);

constexpr int tpos[16] = TPOS;
constexpr int twho[16] = TWHO;
constexpr int dn_cnt(int q) { return q == DN1 ? 3 : q == DN2 ? 4 : 0; }
constexpr int dn_first(int q) { return q <= DN1 ? 0 : q <= DN2 ? 3 : 7; }

static __device__ __forceinline__ void code_touch(unsigned& sink, unsigned off) {
  asm volatile("s_getpc_b64 s[40:41]\n\tk_main_rt:\n\ts_min_u32 %1, %1, k_main_end-k_main_rt-196\n\t"
               "s_load_dword %0, s[40:41], %1 offset:0\n\ts_load_dword %0, s[40:41], %1 offset:64\n\ts_load_dword %0, s[40:41], %1 offset:128\n\ts_load_dword %0, s[40:41], %1 offset:192"
               : "+s"(sink), "+s"(off) :: "s40", "s41", "scc", "memory");
}
static __device__ __forceinline__ void touch1(__amdgpu_buffer_rsrc_t r, int voff, unsigned mb, int soff) { TOUCH(r, voff, mb, LDS_DUMMY, soff); }
typedef const __attribute__((address_space(1))) u16* gptr;
typedef __attribute__((ext_vector_type(8))) u64 u64x8;
typedef __attribute__((ext_vector_type(2))) u64 u64x2;
extern "C" __global__ __launch_bounds__(512, 2) void k_main(const u16* x, const u16* wd_re, const u16* wd_im, const u16* w_r,
                                                 char* wsp, const u16* ws_re, const u16* ws_im,
                                                 const u16* w_gate, const u16* w_up, const u16* w_down, u16* out) {
  __shared__ __attribute__((aligned(16))) char smem[LDS_BYTES];
  u64x8 ka;
  u64x2 kb_;
  u64 kc;
  asm volatile("s_load_dwordx16 %0, %3, 0\n\ts_load_dwordx4 %1, %3, 64\n\ts_load_dwordx2 %2, %3, 80"
               : "=&s"(ka), "=&s"(kb_), "=&s"(kc) : "s"(__builtin_amdgcn_kernarg_segment_ptr()) : "memory");
  const int tid = threadIdx.x, lane = tid & 63;
  const int wid = __builtin_amdgcn_readfirstlane(tid >> 6);
  const int g = blockIdx.x, xcd = g & 7, j = g >> 3;
  if (wid < 4) {
    unsigned cw, co = (j * 4 + wid) * 448;
    asm volatile(".subsection 1\nk_main_end:\n.subsection 0\n\ts_min_u32 %1, %1, k_main_end-k_main-480\n\ts_getpc_b64 s[40:41]\n\ts_add_u32 s40, s40, %1\n\ts_addc_u32 s41, s41, 0\n\t"
                 "s_load_dword %0, s[40:41], 0\n\ts_load_dword %0, s[40:41], 64\n\ts_load_dword %0, s[40:41], 128\n\t"
                 "s_load_dword %0, s[40:41], 192\n\ts_load_dword %0, s[40:41], 256\n\ts_load_dword %0, s[40:41], 320\n\ts_load_dword %0, s[40:41], 384\n\ts_waitcnt lgkmcnt(0)"
                 : "=&s"(cw), "+s"(co) :: "s40", "s41", "scc", "memory");
  }
  const int pw = wid - 4;
  const int row0 = (wid & 3) * 8 + (lane >> 3);
  const int cm = ((lane & 7) ^ (((row0 >> 1) & 1) | ((wid & 3) << 1))) * 16;
  int vm = row0 * (D_ * 2) + cm;
  asm volatile("" : "+v"(vm));
  const int kq = xcd >> 1;
  const int tt = (xcd & 1) * 4 + (j >> 3);
  const int et = j & 7;
  const int z = kq >> 1, st = (kq & 1) * 8 + tt;
  const int lrow = (kq & 1) * 1024 + (xcd & 1) * 512 + z * 256 + j * 8 + pw * 2;
  const int drow = ((kq & 1) * 2 + (xcd & 1)) * 256 + j * 8 + pw * 2;
  unsigned o_m = st * 128 * D_ * 2, o_n = et * 128 * D_ * 2, o_l = lrow * D_ * 2, o_d = drow * D_ * 2, mb0 = lds_addr(smem) + pw * 1024;
  int l16 = lane * 16;
  asm volatile("" : "+s"(o_m), "+s"(o_n), "+s"(o_l), "+s"(o_d), "+s"(mb0));
  asm volatile("" : "+v"(l16));
  asm volatile("s_waitcnt lgkmcnt(0)" : "+s"(ka), "+s"(kb_), "+s"(kc) :: "memory");
  x = (const u16*)(gptr)ka[0]; wd_re = (const u16*)(gptr)ka[1]; wd_im = (const u16*)(gptr)ka[2]; w_r = (const u16*)(gptr)ka[3];
  wsp = (char*)(gptr)ka[4]; ws_re = (const u16*)(gptr)ka[5]; ws_im = (const u16*)(gptr)ka[6]; w_gate = (const u16*)(gptr)ka[7];
  w_up = (const u16*)(gptr)kb_[0]; w_down = (const u16*)(gptr)kb_[1]; out = (u16*)(gptr)kc;
  int* flags = (int*)(wsp + WS_FLAGS);
  u64* stat = (u64*)(flags + FL_ST);
  const u16* wdz = z ? wd_im : wd_re;
  const __amdgpu_buffer_rsrc_t rm = mk_rsrc(x);
  const __amdgpu_buffer_rsrc_t rn = mk_rsrc(wdz);
#define G1_X(kt, so, q) DMA_(rm, vm, mb0, (so) + (q) * 4096, o_m, (kt) * 128 + (((q) & 1) * 64 + ((q) >> 1) * 32) * D_ * 2, "")
#define G1_W(kt, so, q) DMA_(rn, vm, mb0, (so) + 16384 + (q) * 4096, o_n, (kt) * 128 + (q) * 32 * D_ * 2, "")
#define G1_STAGE_TILE(kt, so, W) do { SB(); G1_X(kt, so, 0); W(0); G1_X(kt, so, 1); W(1); G1_X(kt, so, 2); W(2); G1_X(kt, so, 3); W(3); \
    G1_W(kt, so, 0); W(4); G1_W(kt, so, 1); W(5); G1_W(kt, so, 2); W(6); G1_W(kt, so, 3); W(7); SB(); } while (0)
#define G1_STAGE_A(kt, so, W) do { SB(); G1_X(kt, so, 0); W(0); G1_X(kt, so, 2); W(1); \
    G1_W(kt, so, 0); W(2); G1_W(kt, so, 1); W(3); G1_W(kt, so, 2); W(4); G1_W(kt, so, 3); W(5); W(6); W(7); SB(); } while (0)
#define G1_STAGE_B(kt, so) do { SB(); G1_X(kt, so, 1); G1_X(kt, so, 3); SB(); } while (0)
#define NOW(i)
  const int fr = lane & 15, fq = lane >> 4;
  const int ha = wid >> 2;
  int trow, xo, po;
  const char *prs, *prc;
  auto epi_setup = [&]() __attribute__((always_inline)) {
    int t_ = tt, l_ = lane;
    asm volatile("" : "+s"(t_));
    asm volatile("" : "+v"(l_));
    trow = t_ * 256 + ((wid >> 1) & 1) * 128 + kq * 32 + ha * 16;
    xo = ((trow + (l_ & 15)) * D_ + et * 128 + (wid & 1) * 64 + (l_ >> 4) * 8) * 2;
    prs = wsp + WS_SLAB + (size_t)((t_ * 8 + et) * 64 + (wid & 3)) * MSG;
    prc = (const char*)(flags + FL_X + ((t_ * 8 + et) * 64 + (wid & 3)) * 32);
    asm volatile("" : "+s"(trow), "+s"(prs), "+s"(prc));
    po = l_ < 3 ? (kq * 4 + ((kq + 1 + l_) & 3)) * 512 : (int)0x80000000;
    asm volatile("" : "+v"(xo), "+v"(po));
  };
#define rs mk_rsrc(prs)
#define rc mk_rsrc(prc)
  lds_vi sig = (lds_vi)(smem + LDS_SIG + (wid & 3) * 4);
  unsigned tag;
  u32x4 xq[2];
  u32x2 sq;
  f32x4 ya[4];
  unsigned key[2];
  float xf[16], nf[16], lmb, lsd, lri, la, lb, lqa, lqb;
  unsigned nfp[8];
#define PINV(a) asm volatile("" : "+v"(a))
  auto nf_pre = [&]() __attribute__((always_inline)) {
    if (__builtin_expect(!__all(sq.y == tag), 0)) sq = granule_wait(stat + (xo >> 11), tag);
    lmb = lo_bf(sq.x); lsd = hi_bf(sq.x); lri = __builtin_amdgcn_rcpf(lsd);
    PINV(lmb); PINV(lsd); PINV(lri);
  };
  auto nf_step = [&](int n) __attribute__((always_inline)) {
    const int u = n / 7;
    switch (n % 7) {
      case 0: la = lo_bf(xq[u >> 2][u & 3]); PINV(la); la -= lmb; PINV(la); break;
      case 1: lb = hi_bf(xq[u >> 2][u & 3]); PINV(lb); lb -= lmb; PINV(lb); break;
      case 2: nfp[u] = pk2(la, lb); la = lo_bf(nfp[u]); PINV(la); PINV(nfp[u]); break;
      case 3: lqa = la * lri; PINV(lqa); lb = hi_bf(nfp[u]); PINV(lb); break;
      case 4: la = __builtin_fmaf(-lqa, lsd, la); PINV(la); lqb = lb * lri; PINV(lqb); break;
      case 5: lqa = __builtin_fmaf(la, lri, lqa); PINV(lqa); lb = __builtin_fmaf(-lqb, lsd, lb); PINV(lb); break;
      default: lqb = __builtin_fmaf(lb, lri, lqb); nfp[u] = pk2(lqa, lqb); PINV(nfp[u]); break;
    }
  };
  auto nf_unp = [&](int e) __attribute__((always_inline)) {
    const int u = e >> 1;
    if (e == 0) { PINV(xq[0]); PINV(xq[1]); }
    xf[e] = (e & 1) ? hi_bf(xq[u >> 2][u & 3]) : lo_bf(xq[u >> 2][u & 3]);
    nf[e] = (e & 1) ? hi_bf(nfp[u]) : lo_bf(nfp[u]);
    PINV(xf[e]); PINV(nf[e]);
  };
#define MSG_IDX(m) (kq * 4 + ((kq + 1 + (m)) & 3))
  unsigned seen = 0;

  if (__builtin_expect(wid >= 4, 1)) {
    __builtin_amdgcn_s_setprio(3);
    G1_STAGE_TILE(0, 0, NOW);
    constexpr int O_ST = (FL_ST - FL_AB) * 4, O_CE = (FL_CE - FL_AB) * 4, O_KEY = (FL_KEY - FL_AB) * 4, O_EP = (FL_EPOCH - FL_AB) * 4;
    const __amdgpu_buffer_rsrc_t rf = mk_rsrc(wsp + WS_FLAGS + FL_AB * 4);
    u32x4 lv[2], lv2[2], lw[2], ld[2], ld2[2];
    unsigned epv;
    {
      const char* xrow = (const char*)x + o_l;
      const char* wdrow = (const char*)wdz + o_d;
      lv[0] = ld16_s<0>(xrow, l16); lv[1] = ld16_s<1024>(xrow, l16);
      lv2[0] = ld16_s<2048>(xrow, l16); lv2[1] = ld16_s<3072>(xrow, l16);
      ld[0] = ld16_s<0>(wdrow, l16); ld[1] = ld16_s<1024>(wdrow, l16);
      ld2[0] = ld16_s<2048>(wdrow, l16); ld2[1] = ld16_s<3072>(wdrow, l16);
      lw[0] = ld16_s<0>(w_r, l16); lw[1] = ld16_s<1024>(w_r, l16);
      asm volatile("buffer_load_dword %0, off, %1, %2 sc1" : "=v"(epv) : "s"(rf), "s"(O_EP) : "memory");
    }
    SB();
    const unsigned long long did = llvm_dispatch_id();
    const unsigned dmix = (unsigned)did * 0x85EBCA6Bu + (unsigned)(did >> 32) * 0xC2B2AE35u + 0x27D4EB2Fu;
    const bool moe = et != 7;
    unsigned epoch;
    const int ci = lane & 15, rq = lane >> 4, tk = (lane >> 3) & 1;
    int mat, cg, dq, d0;
    u32x4 wv[16];
    const u16 *pgu, *pn2;
    const char *pm2, *spb;
#define rw __builtin_amdgcn_make_buffer_rsrc((void*)pgu, 0, moe ? 0x7fffffff : 0, 0x00020000)
#define rn2 mk_rsrc(pn2)
#define rm2 mk_rsrc(pm2)
    int vw, vn2, vm2;
    int ord2;
    auto k_blk = [&](int k) __attribute__((always_inline)) { return ORD && k >= 9 ? (ord2 >> (4 * (k - 9))) & 7 : tt ^ (k_ord(k) >> 1); };
#define KROT(kk_) (k_blk(kk_) * 256 + (k_ord(kk_) & 1) * 128)
    int tv2;
    unsigned sd = 0;
    auto touches = [&](auto P) __attribute__((always_inline)) {
      constexpr int p = decltype(P)::value;
      int e = et;
      asm volatile("" : "+s"(e));
#define TCH(k) if constexpr (tpos[k] == p && k < 16 - NSC) { if (e == twho[k]) { const int kb = KROT(k); touch1(rn2, tv2, mb0, kb); } }
      TCH(0) TCH(1) TCH(2) TCH(3) TCH(4) TCH(5) TCH(6) TCH(7) TCH(8) TCH(9) TCH(10) TCH(11) TCH(12) TCH(13) TCH(14) TCH(15)
    };
    unsigned mq0, mq1, mq2, mq3;
    const int cg8 = g & 7, rgb = g >> 3;
    const __amdgpu_buffer_rsrc_t rdn = mk_rsrc(w_down);
    const __amdgpu_buffer_rsrc_t rgu = mk_rsrc(wsp + WS_GU);
    int vdn, vgu;
    u32x4 dv[7], gv[4];
    int abo, abq[3];
    unsigned lma;
    unsigned sel[2][4];
    float g0, g1;
    u32x4 kk[4];
    unsigned k1 = 0, k2 = 0;
    const int ko_ = O_KEY + pw * 4096;
#define MM(a, b, c) __builtin_amdgcn_mfma_f32_16x16x32_bf16(__builtin_bit_cast(bf16x8, a), __builtin_bit_cast(bf16x8, b), c, 0, 0, 0)
#define PIN1(a) asm volatile("" : "+v"(a))
#define PIN2(a, b) asm volatile("" : "+v"(a), "+v"(b))
#define PIN3(a, b, c) asm volatile("" : "+v"(a), "+v"(b), "+v"(c))
    {
      int gro, go;
      u32x2 gr;
      u32x4 oh[5];
      f32x4 sa4, gw[2], gx[2];
      float su, gs, ta, tb, sx, mbv;
      unsigned dk[4], kl;
      const int hb = lane >> 5;
#pragma unroll
      for (int l = 0; l < WSN; ++l) asm volatile("" : "=v"(wv[l]));
      auto diag = [&](const f32x4& m) __attribute__((always_inline)) {
        return __uint_as_float((__float_as_uint(m[0]) & dk[0]) | (__float_as_uint(m[1]) & dk[1]) | (__float_as_uint(m[2]) & dk[2]) | (__float_as_uint(m[3]) & dk[3]));
      };
      auto setup = [&](auto JB, auto GP) __attribute__((always_inline)) {
        constexpr int jb = decltype(JB)::value, gp = decltype(GP)::value;
        SB();
        {
          int ln = lane, jj = j, p_ = pw;
          launder(ln, jj, p_);
          const int c_ = ln & 15, r_ = ln >> 4, e_ = jj & 7, t_ = (xcd & 1) * 4 + (jj >> 3);
          if constexpr (jb == 1 && gp == 0) {
            pn2 = ((kq >> 1) ? ws_im : ws_re) + (long)t_ * 256 * S_ + (kq & 1) * 1024;
            spb = (const char*)(pn2 + (e_ * 4 + p_) * 8 * S_);
            pins(pn2, spb);
          }
          if constexpr (jb == 1 && gp >= 1 && gp < 6) {
            const unsigned o = c_ == gp - 1 ? 0x3f803f80u : 0u;
            oh[gp - 1] = (u32x4){o, o, o, o};
            PIN1(oh[gp - 1]);
          }
          if constexpr (jb == 1 && gp == 6) {
            const int gm = moe ? (jj - (jj >> 3)) * 8 + xcd : 0;
            mat = gm / 112; cg = (gm % 112) >> 2; dq = gm & 3; d0 = dq * 256 + p_ * 64;
            pin4(mat, cg, dq, d0);
          }
          if constexpr (jb == 2 && gp == 0) {
            pgu = mat ? w_up : w_gate;
            pm2 = wsp + WS_ABT + ((long)e_ * 128 * 4096 + kq * 1024) * 2;
            pins(pgu, pm2);
            vw = ((d0 + r_ * 16) * F_ + cg * 128 + c_ * 8) * 2;
            PIN1(vw);
          }
          if constexpr (jb == 2 && (gp == 1 || gp == 2)) {
            dk[gp * 2 - 2] = c_ == r_ * 4 + gp * 2 - 2 ? ~0u : 0u; dk[gp * 2 - 1] = c_ == r_ * 4 + gp * 2 - 1 ? ~0u : 0u;
            PIN2(dk[gp * 2 - 2], dk[gp * 2 - 1]);
          }
          if constexpr (jb == 2 && gp == 3) { kl = lrow + (ln >> 5); go = (ln & 30) ? (int)0x80000000 : ((ln & 1) ? O_KEY : O_ST) + (int)kl * 8; PIN2(kl, go); }
          if constexpr (jb == 2 && gp == 4) { kl = 0xffff - kl; go = (ln & ~1) == 2 ? O_CE + (z * D_ + drow + (ln & 1)) * 8 : go; PIN2(kl, go); }
          if constexpr (jb == 2 && gp == 5) {
            gro = (p_ < 2 ? O_ST + (st * 128 + p_ * 64) * 8 : O_CE + (z * D_ + e_ * 128 + (p_ - 2) * 64) * 8) + ln * 8;
            tv2 = (p_ * 64 + ln) * 4096;
            lma = lds_addr(smem + LDS_MB) + (p_ < 2 ? p_ * 256 : 512 + p_ * 256) + ln * 4;
            PIN3(gro, tv2, lma);
          }
          if constexpr (jb == 2 && gp == 6) {
            const u64 lo = (t_ & 2) ? ((t_ & 4) ? 0x0150234601503247ull : 0x0514670205147603ull) : ((t_ & 4) ? 0x0123067401532076ull : 0x0567423005176432ull);
            ord2 = (int)(lo >> ((t_ & 1) * 32));
            pins(ord2, p_);
          }
          if constexpr (jb == 7 && gp == 0) {
            const int r0 = p_ * 8 + (ln >> 3);
            vn2 = r0 * (S_ * 2) + ((ln & 7) ^ ((r0 >> 1) & 7)) * 16; vm2 = r0 * 8192 + (vm & 127);
            PIN2(vn2, vm2);
          }
          if constexpr (jb == 7 && gp == 1) { vdn = jj * 112 + p_ * 28 + r_ * 7; vgu = (((ln >> 3) & 1) * 4 * F_ + vdn + min(ln & 7, 6)) * 16; PIN2(vdn, vgu); }
          if constexpr (jb == 7 && gp == 2) { vdn = (vdn * D_ + cg8 * 128 + c_ * 8) * 2; PIN1(vdn); }
          if constexpr (jb == 7 && gp == 3) {
            abo = (((kq * 8 + e_) * 4 + p_) * 16 + c_) * AB_STRIDE * 4;
            abq[2] = ln >= 16 || (ln & 7) == t_ ? (int)0x80000000 : abo;
            PIN2(abo, abq[2]);
          }
          if constexpr (jb == 7 && gp == 4) {
            abq[1] = ln >= 8 ? (int)0x80000000 : abq[2];
            abq[0] = ((ln ^ t_) & 4) ? (int)0x80000000 : abq[1];
            PIN2(abq[0], abq[1]);
          }
          if constexpr (jb == 7 && gp == 5) {
            const unsigned mb = lds_addr(smem) + p_ * 1024;
            mq0 = mb + ((3 - kq) & 3) * 4096; mq1 = mb + ((4 - kq) & 3) * 4096; mq2 = mb + ((5 - kq) & 3) * 4096; mq3 = mb + ((6 - kq) & 3) * 4096;
            pin4(mq0, mq1, mq2, mq3);
          }
          if constexpr (jb == 8 && gp < 4) {
#pragma unroll
            for (int h = 0; h < 2; ++h) {
              const unsigned hv = h ? 0x0302u : 0x0100u;
              sel[h][gp] = ((ln & 7) >> 1) == gp ? ((ln & 1) ? (hv << 16) | 0x0c0cu : 0x0c0c0000u | hv) : 0x0c0c0c0cu;
            }
            PIN2(sel[0][gp], sel[1][gp]);
          }
        }
        SB();
      };
#define WS1(i) setup(IC<1>{}, IC<i>{})
#define WS2(i) setup(IC<2>{}, IC<i>{})
#define WS3(i) do { setup(IC<7>{}, IC<i>{}); if (i >= 4) setup(IC<8>{}, IC<(i) & 3>{}); } while (0)
      G1_STAGE_TILE(1, G1_STAGE, WS1);
      WAIT_VM(19); BAR();
      G1_STAGE_TILE(2, 2 * G1_STAGE, WS2);
      G1_STAGE_TILE(3, 3 * G1_STAGE, WS3);
      auto work1 = [&](auto JB, auto GP) __attribute__((always_inline)) {
        constexpr int jb = decltype(JB)::value, gp = decltype(GP)::value;
        SB();
        if constexpr (jb == 1) {
          const f32x4 z4 = {0.f, 0.f, 0.f, 0.f};
          if constexpr (gp == 0) { sa4 = MM(lv[0], oh[0], z4); gw[0] = MM(lv[0], lw[0], z4); gx[0] = MM(lv[0], lv[0], z4); PIN3(sa4, gw[0], gx[0]); }
          if constexpr (gp == 1) { sa4 = MM(lv2[0], oh[1], sa4); gw[1] = MM(lv2[0], lw[0], z4); gx[1] = MM(lv2[0], lv2[0], z4); PIN3(sa4, gw[1], gx[1]); }
          if constexpr (gp == 2) { sa4 = MM(lv[1], oh[0], sa4); gw[0] = MM(lv[1], lw[1], gw[0]); gx[0] = MM(lv[1], lv[1], gx[0]); PIN3(sa4, gw[0], gx[0]); }
          if constexpr (gp == 3) { sa4 = MM(lv2[1], oh[1], sa4); gw[1] = MM(lv2[1], lw[1], gw[1]); gx[1] = MM(lv2[1], lv2[1], gx[1]); PIN3(sa4, gw[1], gx[1]); }
          if constexpr (gp == 4) { sa4 = MM(ld[0], oh[2], sa4); sa4 = MM(ld[1], oh[2], sa4); PIN1(sa4); }
          if constexpr (gp == 5) { sa4 = MM(ld2[0], oh[3], sa4); sa4 = MM(ld2[1], oh[3], sa4); PIN1(sa4); }
          if constexpr (gp == 6) { sa4 = MM(lw[0], oh[4], sa4); sa4 = MM(lw[1], oh[4], sa4); PIN1(sa4);
            epoch = __builtin_amdgcn_readfirstlane(epv); tag = ((epoch + 1u) * 0x9E3779B1u ^ dmix) | 1u; }
        }
        if constexpr (jb == 2) {
          if constexpr (gp == 0) { su = (sa4[0] + sa4[1]) + (sa4[2] + sa4[3]); ta = diag(gw[0]); PIN2(su, ta); }
          if constexpr (gp == 1) { su = fold32(su, su); tb = diag(gw[1]); PIN2(su, tb); }
          if constexpr (gp == 2) { su = fold16(su, su); gs = fold32(ta, tb); PIN2(su, gs); }
          if constexpr (gp == 3) { ta = diag(gx[0]); tb = diag(gx[1]); PIN2(ta, tb); }
          if constexpr (gp == 4) { gs = fold16(gs, fold32(ta, tb)); PIN1(gs); }
          if constexpr (gp == 5) { gs += dpp_f<0xB1>(gs); gs += dpp_f<0x4E>(gs); PIN1(gs); }
          if constexpr (gp == 6) { gs += dpp_f<0x141>(gs); gs += dpp_f<0x140>(gs); PIN1(gs); }
        }
        if constexpr (jb == 3) {
          if constexpr (gp == 0) {
            const float s0 = rdl(su, 0), s1 = rdl(su, 1), q0 = rdl(gs, 16), q1 = rdl(gs, 48);
            sx = hb ? s1 : s0; ta = hb ? q1 : q0; PIN2(sx, ta);
          }
          if constexpr (gp == 1) {
            const float mean = sx * (1.0f / D_);
            mbv = rbf(mean);
            ta -= sx * mean;
            if (__builtin_expect(__any(!(sx * mean <= 16.f * ta)), 0)) {
              float q = fold32(sqdev16(lv, rdl(mbv, 0)), sqdev16(lv2, rdl(mbv, 32)));
              q = row_sum(fold16(q, q));
              const float dm = mean - mbv;
              ta = fmaxf(q - (float)D_ * dm * dm, 0.f);
            }
            PIN2(mbv, ta);
          }
          if constexpr (gp == 2) { ta = rbf(rbf(ta * (1.0f / D_)) + 1e-5f); gs -= mbv * rdl(su, 4); PIN2(ta, gs); }
          if constexpr (gp == 3) { ta = rbf(__builtin_amdgcn_sqrtf(ta)); tb = __builtin_amdgcn_rcpf(ta); PIN2(ta, tb); }
          if constexpr (gp == 4) {
            const unsigned rb = __float_as_uint(rbf(gs * tb));
            kl |= (rb ^ ((unsigned)((int)rb >> 31) | 0x80000000u)) & 0xffff0000u; PIN1(kl);
          }
          if constexpr (gp == 5) { kl = (lane & 2) ? __float_as_uint(su) : (lane & 1) ? kl : (__float_as_uint(mbv) >> 16) | __float_as_uint(ta); PIN1(kl); }
        }
        if constexpr (jb == ST_JB + 3) {
          if constexpr (gp == 0) { if (!__all(gr.y == tag)) gr = granule_wait((const u64*)((const char*)(flags + FL_AB) + gro), tag); PIN1(gr); }
          if constexpr (gp == 1) { g1 = (z ? -1.0f : 1.0f) / hi_bf(gr.x); PIN1(g1); }
          if constexpr (gp == 2) { g0 = pw < 2 ? -(lo_bf(gr.x) * g1) : __uint_as_float(gr.x); PIN1(g0); }
        }
        if constexpr (gp < 7 && (jb - 1) * 7 + gp < 64) {
          constexpr int n = (jb - 1) * 7 + gp;
          sld<((n >> 1) & 7) * 4096 + (n & 1) * 64>(sd, spb, KROT(n >> 4));
        }
        SB();
      };
      auto step1 = [&](auto JB) __attribute__((always_inline)) {
        constexpr int jb = decltype(JB)::value;
        if constexpr (jb == 1) PAGE(1);
        if constexpr (jb == 7) PAGE(2);
        constexpr int n16 = jb == 12 ? 12 : jb == 11 ? 14 : 16;
        constexpr auto wl = [](int q) { return (q == 3 ? 1 : 0) + (q == ST_JB ? 1 : 0); };
        if constexpr (jb == 1) {
          asm volatile("s_waitcnt vmcnt(%11)" : "+v"(lv[0]), "+v"(lv[1]), "+v"(lw[0]), "+v"(lw[1]), "+v"(ld[0]), "+v"(ld[1]),
                       "+v"(lv2[0]), "+v"(lv2[1]), "+v"(ld2[0]), "+v"(ld2[1]), "+v"(epv) : "i"(16) : "memory");
        } else if constexpr (jb == ST_JB + 3) {
          asm volatile("s_waitcnt vmcnt(%1)" : "+v"(gr) : "i"(n16 + wl(jb - 2) + wl(jb - 1)) : "memory");
        } else wait_vm<n16 + wl(jb - 3) + wl(jb - 2) + wl(jb - 1)>();
        BAR();
        if constexpr (jb == 2) { if (pw == 0 && lane < 16) ((int*)(smem + LDS_SEL))[lane < 8 ? lane : lane + 8] = 0; }
        if constexpr (jb == 4) { if (pw == 0 && lane == 0) ((unsigned*)(smem + LDS_SEL))[5] = tag; }
#define W1(i) work1(JB, IC<i>{})
        if constexpr (jb < 10) G1_STAGE_TILE(jb - 1 + G1_NST, ((jb - 1) % G1_NST) * G1_STAGE, W1);
        else G1_STAGE_A(jb - 1 + G1_NST, ((jb - 1) % G1_NST) * G1_STAGE, W1);
        if constexpr (jb == 12) { G1_STAGE_B(13, G1_STAGE); G1_STAGE_B(14, 2 * G1_STAGE); }
        if constexpr (jb == 3) __builtin_amdgcn_raw_buffer_store_b64((u32x2){kl, tag}, rf, go, 0, 16);
        if constexpr (jb == ST_JB) {
          asm volatile("buffer_load_dwordx2 %0, %1, %2, 0 offen sc1" : "=v"(gr) : "v"(gro), "s"(rf) : "memory");
        }
        touches(IC<100 + jb>{});
        if constexpr (jb <= 8) {
          if (et == jb - 1) {
#pragma unroll
            for (int l = 0; l < WSN; ++l) bld16_nt_io(wv[l], rw, vw, l * F_ * 2);
          }
        }
        SB();
      };
      static_assert(ST_JB + 3 <= 12 && ST_JB >= 6, "schedule");
      step1(IC<1>{}); step1(IC<2>{}); step1(IC<3>{}); step1(IC<4>{}); step1(IC<5>{}); step1(IC<6>{}); step1(IC<7>{}); step1(IC<8>{}); step1(IC<9>{}); step1(IC<10>{}); step1(IC<11>{}); step1(IC<12>{});
    }
#define G2_STAGE_N(kk_, W) do { const int kb = KROT(kk_); constexpr int so = n_off(kk_); SB(); \
    DMA_(rn2, vn2, mq0, so, kb, 0, ""); W(0); DMA_(rn2, vn2, mq1, so, kb, 32 * S_ * 2, ""); W(1); \
    DMA_(rn2, vn2, mq2, so, kb, 64 * S_ * 2, ""); W(2); DMA_(rn2, vn2, mq3, so, kb, 96 * S_ * 2, ""); W(3); \
    DMA_(rn2, vn2, mq0, so + 16384, kb, 128 * S_ * 2, ""); W(4); DMA_(rn2, vn2, mq1, so + 16384, kb, 160 * S_ * 2, ""); W(5); \
    DMA_(rn2, vn2, mq2, so + 16384, kb, 192 * S_ * 2, ""); W(6); DMA_(rn2, vn2, mq3, so + 16384, kb, 224 * S_ * 2, ""); W(7); SB(); } while (0)
#define G2_STAGE_NH(kk_, h) do { const int kb = KROT(kk_); constexpr int so = n_off(kk_) + (h) * 16384; SB(); \
    DMA_(rn2, vn2, mq0, so, kb, (h) * 128 * S_ * 2, ""); DMA_(rn2, vn2, mq1, so, kb, ((h) * 128 + 32) * S_ * 2, ""); \
    DMA_(rn2, vn2, mq2, so, kb, ((h) * 128 + 64) * S_ * 2, ""); DMA_(rn2, vn2, mq3, so, kb, ((h) * 128 + 96) * S_ * 2, ""); SB(); } while (0)
#define G2_STAGE_M(kk_, W) do { const int kb = KROT(kk_); constexpr int so = G2_MRING + m_off(kk_); SB(); \
    DMA_(rm2, vm2, mb0, so, kb, 0, " sc1"); W(8); DMA_(rm2, vm2, mb0, so + 4096, kb, 32 * 8192, " sc1"); W(9); \
    DMA_(rm2, vm2, mb0, so + 8192, kb, 64 * 8192, " sc1"); W(10); DMA_(rm2, vm2, mb0, so + 12288, kb, 96 * 8192, " sc1"); W(11); SB(); } while (0)
    WAIT_VM(16); BAR();
    asm volatile("ds_write_b32 %0, %1" :: "v"(lma), "v"(g0) : "memory");
    if (pw < 2) asm volatile("ds_write_b32 %0, %1 offset:512" :: "v"(lma), "v"(g1) : "memory");
    u32x4 tv[4];
    unsigned ready = 0;
    auto need = [&](int k) __attribute__((always_inline)) {
      const int b = k_blk(k) + (k_ord(k) & 1) * 8;
#pragma unroll 1
      for (int it = 0; !((ready >> b) & 1) && it < SPIN_MAX; ++it) {
        ready |= (unsigned)__ballot(__builtin_amdgcn_raw_buffer_load_b32(rf, abq[k < 5 ? 0 : k < 9 ? 1 : 2], 0, 16) == tag);
        asm volatile("" ::: "memory");
      }
    };
    unsigned ksel, bad = 0;
    int isel;
    unsigned xw;
    auto bvec = [&](unsigned src, const unsigned (&sl)[4]) __attribute__((always_inline)) {
      return (u32x4){__builtin_amdgcn_perm(0, src, sl[0]), __builtin_amdgcn_perm(0, src, sl[1]),
                     __builtin_amdgcn_perm(0, src, sl[2]), __builtin_amdgcn_perm(0, src, sl[3])};
    };
#define BC8(v, p) (unsigned)__builtin_amdgcn_update_dpp(__builtin_amdgcn_update_dpp(0, (int)(v), 0x150 + (p), 0xf, 0x3, false), (int)(v), 0x158 + (p), 0xf, 0xc, false)
    float* lg = (float*)(smem + LDS_GU);
    f32x4 macc = {0.f, 0.f, 0.f, 0.f};
    u32x2 sv;
    unsigned hp, gbad;
    float s0, s1;
    auto work2 = [&](auto JB, auto GP) __attribute__((always_inline)) {
      constexpr int jb = decltype(JB)::value, gp = decltype(GP)::value;
      SB();
      if constexpr (jb == -1 && gp < 4) {
        constexpr int c = gp == 0 ? 0xB1 : gp == 1 ? 0x4E : gp == 2 ? 0x141 : 0x140;
        merge2(k1, k2, dpp_u<c>(k1), dpp_u<c>(k2));
        PIN2(k1, k2);
      }
      if constexpr (jb == -1 && gp == 4) {
        ksel = tk ? k2 : k1;
        isel = (0xffff - (int)(ksel & 0xffffu)) & (S_ - 1);
        k1 = (isel * D_ + d0 + rq * 16 + (lane & 7) * 2) * 2; k2 = O_ST + isel * 8;
        PIN1(ksel);
        xw = gld4_s(x, k1);
        sv = bld8_sc1(rf, k2);
      }
      if constexpr (jb == 0 && gp == 1) wait_xs<3 + 1 + 2>(xw, sv);
      if constexpr (jb == 0) {
        if constexpr (gp == 1) { if (!__all(sv.y == tag)) sv = granule_wait(stat + isel, tag); s0 = __builtin_amdgcn_rcpf(hi_bf(sv.x)); PIN1(s0); }
        if constexpr (gp == 2) { s1 = ln_elem(lo_bf(xw), lo_bf(sv.x), hi_bf(sv.x), s0); PIN1(s1); }
        if constexpr (gp == 3) { xw = pk2(s1, ln_elem(hi_bf(xw), lo_bf(sv.x), hi_bf(sv.x), s0)); PIN1(xw); }
      }
      if constexpr (jb == DG && gp < 7) { PIN1(dv[gp]); macc = MM(dv[gp], bvec(BC8(hp, gp), sel[0]), macc); PIN1(macc); }
      if constexpr ((jb == 0 && gp >= 4) || jb == 1) {
        constexpr int l = jb == 0 ? gp - 4 : gp + 4;
        if constexpr (!(l & 1)) hp = BC8(xw, l >> 1);
        PIN1(wv[l]);
        macc = MM(wv[l], bvec(hp, sel[l & 1]), macc);
        PIN2(macc, hp);
      }
      if constexpr (jb == GV_JB + 2) {
        if constexpr (gp == 0) { gbad = (gv[0].y ^ tag) | (gv[0].w ^ tag) | (gv[1].y ^ tag) | (gv[1].w ^ tag); PIN1(gbad); }
        if constexpr (gp == 1) { gbad |= (gv[2].y ^ tag) | (gv[2].w ^ tag) | (gv[3].y ^ tag) | (gv[3].w ^ tag); PIN1(gbad); }
        if constexpr (gp == 2) {
          s0 = ((__uint_as_float(gv[0].x) + __uint_as_float(gv[1].x)) + __uint_as_float(gv[2].x)) + __uint_as_float(gv[3].x);
          s1 = ((__uint_as_float(gv[0].z) + __uint_as_float(gv[1].z)) + __uint_as_float(gv[2].z)) + __uint_as_float(gv[3].z);
          PIN2(s0, s1);
        }
        if constexpr (gp == 3) {
#pragma unroll 1
          for (int it = 0; !__all(gbad == 0) && it < SPIN_MAX; ++it) {
            u32x4 q[4];
            gbad = 0;
#pragma unroll
            for (int i = 0; i < 4; ++i) { q[i] = __builtin_amdgcn_raw_buffer_load_b128(rgu, vgu, i * F_ * 16, 16); gbad |= (q[i].y ^ tag) | (q[i].w ^ tag); }
            s0 = ((__uint_as_float(q[0].x) + __uint_as_float(q[1].x)) + __uint_as_float(q[2].x)) + __uint_as_float(q[3].x);
            s1 = ((__uint_as_float(q[0].z) + __uint_as_float(q[1].z)) + __uint_as_float(q[2].z)) + __uint_as_float(q[3].z);
            asm volatile("" ::: "memory");
          }
          const unsigned gp2 = pk2(s0, s1);
          const unsigned up2 = dpp_u<0x128>(gp2);
          s0 = tk ? hi_bf(up2) : lo_bf(gp2); s1 = tk ? hi_bf(gp2) : lo_bf(up2);
          PIN2(s0, s1);
        }
        if constexpr (gp == 4) { s0 = rbf(s0 * __builtin_amdgcn_rcpf(1.f + __expf(-s0))); PIN1(s0); }
        if constexpr (gp == 5) { hp = pk2(s0 * s1, 0.f); PIN1(hp); }
      }
      if constexpr (jb >= SP0 && gp >= 12 - SPN && (jb - SP0) * SPN + gp - (12 - SPN) < NSC * 16) {
        constexpr int n = (jb - SP0) * SPN + gp - (12 - SPN);
        static_assert(jb <= 11, "");
        sld<((n >> 1) & 7) * 4096 + (n & 1) * 64>(sd, spb, KROT(16 - NSC + (n >> 4)));
      }
      if constexpr (jb == 10 && gp == 2) epi_setup();
      if constexpr (jb == CTJ && gp == 4) code_touch(sd, (j * 4 + pw) * 256);
      SB();
    };
#define W2M(i) work2(IC<-1>{}, IC<i>{})
#define W20(i) work2(IC<0>{}, IC<i>{})
#define W21(i) work2(IC<1>{}, IC<i>{})
    unsigned fs;
    {
      G1_STAGE_B(15, 3 * G1_STAGE);
      G2_STAGE_NH(0, 0);
      lgkm0(sd);
      WAIT_VM(16); BAR();
      G2_STAGE_NH(0, 1);
      WAIT_VM(10); BAR();
#pragma unroll
      for (int i = 0; i < 4; ++i) kk[i] = bld16_sc1(rf, l16, ko_ + i * 1024);
#pragma unroll
      for (int l = WSN; l < 16; ++l) wv[l] = bld16_nt(rw, vw, l * F_ * 2);
      asm volatile("s_waitcnt vmcnt(%4)" : "+v"(kk[0]), "+v"(kk[1]), "+v"(kk[2]), "+v"(kk[3]) : "i"(16 - WSN) : "memory");
      touches(IC<115>{});
#pragma unroll
      for (int i = 0; i < 4; ++i) {
        k2 = max(k2, min(k1, kk[i].x)); k1 = max(k1, kk[i].x);
        k2 = max(k2, min(k1, kk[i].z)); k1 = max(k1, kk[i].z);
        bad |= (kk[i].y ^ tag) | (kk[i].w ^ tag);
      }
#pragma unroll 1
      for (int it = 0; !__all(bad == 0) && it < SPIN_MAX; ++it) {
        k1 = 0; k2 = 0; bad = 0;
#pragma unroll
        for (int i = 0; i < 4; ++i) {
          const u32x4 q = __builtin_amdgcn_raw_buffer_load_b128(rf, l16, ko_ + i * 1024, 16);
          k2 = max(k2, min(k1, q.x)); k1 = max(k1, q.x);
          k2 = max(k2, min(k1, q.z)); k1 = max(k1, q.z);
          bad |= (q.y ^ tag) | (q.w ^ tag);
        }
        asm volatile("" ::: "memory");
      }
      merge2(k1, k2, dpp_u<0xB1>(k1), dpp_u<0xB1>(k2));
      merge2(k1, k2, dpp_u<0x4E>(k1), dpp_u<0x4E>(k2));
      merge2(k1, k2, dpp_u<0x141>(k1), dpp_u<0x141>(k2));
      merge2(k1, k2, dpp_u<0x140>(k1), dpp_u<0x140>(k2));
      if ((lane & 15) == 0) *(u32x2*)(smem + LDS_KEY + (pw * 4 + rq) * 8) = (u32x2){k1, k2};
      PAGE(3);
      BAR_LGKM();
#pragma unroll
      for (int jj = 0; jj < 4; ++jj) tv[jj] = *(const u32x4*)(smem + G2_MRING + m_off(0) + (jj * 4 + pw) * 1024 + lane * 16);
      { const u32x2 kp = *(const u32x2*)(smem + LDS_KEY + ci * 8); k1 = kp.x; k2 = kp.y; }
#pragma unroll
      for (int jj = 0; jj < 4; ++jj) __builtin_amdgcn_raw_buffer_store_b128(tv[jj], rm2, vm2 + jj * 32 * 8192 + 2 * tt * 128, 0, 16);
      G2_STAGE_N(1, W2M);
      WAIT_VM(10);
      if (lane == tt) __builtin_amdgcn_raw_buffer_store_b32(tag, rf, abo, 0, 16);
      G2_STAGE_N(2, W20);
      __builtin_amdgcn_s_sleep(2);
      WAIT_VM(9);
      fs = bld4_sc1(rf, abq[0]);
    }
    BAR();
#pragma unroll
    for (int jj = 0; jj < 4; ++jj) tv[jj] = *(const u32x4*)(smem + G2_MRING + m_off(1) + (jj * 4 + pw) * 1024 + lane * 16);
    G2_STAGE_N(3, NOW);
    asm volatile("s_waitcnt vmcnt(8)" : "+v"(fs) :: "memory");
    ready = (unsigned)__ballot(fs == tag);
    need(2);
    G2_STAGE_M(2, NOW);
    need(3);
    G2_STAGE_M(3, NOW);
    fs = bld4_sc1(rf, abq[1]);
    touches(IC<1>{});
    W21(0); W21(1); W21(2); W21(3); W21(4); W21(5); W21(6); W21(7); W21(8); W21(9); W21(10); W21(11);
    asm volatile("" : "+v"(tv[0]), "+v"(tv[1]), "+v"(tv[2]), "+v"(tv[3]));
    auto step2 = [&](auto JB) __attribute__((always_inline)) {
      constexpr int jb = decltype(JB)::value;
      if constexpr (jb == 3) PAGE(4);
      if constexpr (jb == 9) PAGE(5);
      constexpr auto must = [](int q) { return (q == 2 ? 4 : 0) + ((FSM >> q) & 1) + ((q == GV_JB || q == GV_JB - 1) ? 2 : 0); };
      if constexpr (jb == 2) wait_vm<4 + must(1)>();
      else wait_vm<dn_cnt(jb - 2) + (jb == 5 ? 1 : 0) + 12 + must(jb - 1) + dn_cnt(jb - 1)>();
      if constexpr (jb == 3) { if (pw == 0 && (lane & ~8) == 0) ((unsigned*)(smem + LDS_SEL))[tk] = ksel; }
      if constexpr ((jb == 2 || jb == DG + 1)) {
        *(f32x4*)(lg + pw * 256 + lane * 4) = macc;
        macc = (f32x4){0.f, 0.f, 0.f, 0.f};
        lgkm0(sd);
      }
      BAR();
      float p0[4], p1[4];
      const int col = (pw & 1) * 64 + lane;
      if constexpr (jb == 3) {
        if (g == 0 && pw == 0 && lane == 0) __hip_atomic_store((unsigned*)flags + FL_EPOCH, epoch + 1u, __ATOMIC_RELAXED, __HIP_MEMORY_SCOPE_AGENT);
      }
      if constexpr (jb == DG + 1) {
        const float* p = lg + (pw & 1) * 32 + (lane >> 4) * 64 + ((2 * lane) & 7) * 4 + ((lane >> 2) & 3);
#pragma unroll
        for (int i = 0; i < 4; ++i) { p0[i] = p[i * 256]; p1[i] = p[i * 256 + 4]; }
      }
      if constexpr (jb == 2) {
        const float* p = lg + (col >> 5) * 64 + (col & 7) * 4 + ((col >> 3) & 3);
#pragma unroll
        for (int i = 0; i < 4; ++i) { p0[i] = p[i * 256]; p1[i] = p[i * 256 + 32]; }
      }
      if constexpr (jb == 4) {
        if (lane == 8 + tt) __builtin_amdgcn_raw_buffer_store_b32(tag, rf, abo, 0, 16);
      }
      if constexpr ((FSM >> (jb - 2)) & 1) {
        asm volatile("" : "+v"(fs));
        ready |= (unsigned)__ballot(fs == tag);
      }
      if constexpr (jb == GV_JB + 2) asm volatile("" : "+v"(gv[0]), "+v"(gv[1]), "+v"(gv[2]), "+v"(gv[3]));
#define W2(i) work2(JB, IC<i>{})
      G2_STAGE_N(jb + 2, W2);
      need(jb + 2);
      G2_STAGE_M(jb + 2, W2);
      if constexpr (jb == 2) {
#pragma unroll
        for (int jj = 0; jj < 4; ++jj) __builtin_amdgcn_raw_buffer_store_b128(tv[jj], rm2, vm2 + jj * 32 * 8192 + (2 * tt + 1) * 128, 0, 16);
      }
      if constexpr ((FSM >> jb) & 1) fs = bld4_sc1(rf, abq[jb < 5 ? 1 : 2]);
      if constexpr ((jb == GV_JB || jb == GV_JB - 1)) {
#pragma unroll
        for (int i = (jb == GV_JB ? 2 : 0); i < (jb == GV_JB ? 4 : 2); ++i) gv[i] = bld16_sc1(rgu, vgu, i * F_ * 16);
      }
      touches(JB);
#pragma unroll
      for (int i = dn_first(jb); i < dn_first(jb) + dn_cnt(jb); ++i) dv[i] = bld16_nt(rdn, vdn, i * D_ * 2);
      if constexpr (jb == DG + 1) {
        if (pw < 2)
          __builtin_amdgcn_raw_buffer_store_b128((u32x4){__float_as_uint((p0[0] + p0[1]) + (p0[2] + p0[3])), tag, __float_as_uint((p1[0] + p1[1]) + (p1[2] + p1[3])), tag},
                                                 mk_rsrc(wsp + WS_DN), (((pw * 8 + cg8) * 32 + rgb) * 64 + lane) * 16, 0, 16);
      }
      if constexpr (jb == 2) {
        if (moe && pw < 2)
          __builtin_amdgcn_raw_buffer_store_b128((u32x4){__float_as_uint((p0[0] + p0[1]) + (p0[2] + p0[3])), tag, __float_as_uint((p1[0] + p1[1]) + (p1[2] + p1[3])), tag},
                                                 rgu, ((mat * 4 + dq) * F_ + cg * 128 + col) * 16, 0, 16);
      }
      SB();
    };
    static_assert(DG + 1 <= 12 && DG >= GV_JB + 3 && dn_first(DG - 2) == 7, "schedule");
    step2(IC<2>{}); step2(IC<3>{}); step2(IC<4>{}); step2(IC<5>{}); step2(IC<6>{}); step2(IC<7>{}); step2(IC<8>{}); step2(IC<9>{}); step2(IC<10>{}); step2(IC<11>{}); step2(IC<12>{}); step2(IC<13>{});
    lgkm0(sd);
    PAGE(7);
    WAIT_VM(12); BAR();
    xq[0] = ld16_s<0>(x, xo); xq[1] = ld16_s<64>(x, xo); sq = ld8_s_sc1(stat, xo >> 11 << 3);
    WAIT_VM(3); BAR();
    key[0] = __builtin_amdgcn_readlane(ksel, 0); key[1] = __builtin_amdgcn_readlane(ksel, 8);
    unsigned todo = 0;
#pragma unroll
    for (int c = 0; c < 2; ++c) {
      const int ic = (0xffff - (int)(key[c] & 0xffffu)) & (S_ - 1);
      todo |= (ic >> 8) == tt && ((ic >> 5) & 3) == kq ? 1 << c : 0;
    }
    auto gather = [&](int spin) __attribute__((always_inline)) {
#pragma unroll 1
      for (int c = 0; c < 2; ++c) {
        if (!((todo >> c) & 1)) continue;
        const __amdgpu_buffer_rsrc_t rd = mk_rsrc(wsp + WS_DN + (size_t)(c * 8 + et) * 32768 + pw * 256);
#pragma unroll 1
        for (int it = 1; it < spin; ++it) {
          if (__all(__builtin_amdgcn_raw_buffer_load_b32(rd, (lane & 31) * 1024 + 252, 0, 16) == tag)) break;
          __builtin_amdgcn_s_sleep(GSL);
          asm volatile("" ::: "memory");
        }
        float s0, s1;
        unsigned bad = 1;
#pragma unroll 1
        for (int it = 0; !__all(bad == 0) && it < spin; ++it) {
          bad = 0; s0 = 0.f; s1 = 0.f;
#pragma unroll
          for (int i = 0; i < 8; ++i) {
            const u32x4 q = __builtin_amdgcn_raw_buffer_load_b128(rd, rq * 1024 + ci * 16, i * 4096, 16);
            bad |= (q.y ^ tag) | (q.w ^ tag); s0 += __uint_as_float(q.x); s1 += __uint_as_float(q.z);
          }
          asm volatile("" ::: "memory");
        }
        if (spin == 1 && !__all(bad == 0)) continue;
        todo &= ~(1u << c);
        s0 = fold32(s0, s1);
        s0 = fold16(s0, s0);
        const float sg = rbf(1.f / (1.f + __expf(-key_score(__builtin_amdgcn_readlane(ksel, c * 8)))));
        ((float*)(smem + LDS_FIN))[c * 128 + pw * 32 + ci * 2 + (lane >> 5)] = rbf(rbf(s0) * sg);
        asm volatile("s_waitcnt lgkmcnt(0)" ::: "memory");
        if (lane == 0) asm volatile("ds_add_u32 %0, %1" :: "v"(lds_addr(smem + LDS_SEL + 24 + c * 4)), "v"(1) : "memory");
      }
    };
    if (GTRY && todo != 0) gather(1);
    asm volatile("s_waitcnt vmcnt(0)" : "+v"(xq[0]), "+v"(xq[1]), "+v"(sq) :: "memory");
    __builtin_amdgcn_s_setprio(NFP);
    nf_pre();
#pragma unroll
    for (int n = 0; n < 56; ++n) nf_step(n);
#pragma unroll
    for (int e = 0; e < 16; ++e) nf_unp(e);
    if (todo != 0) gather(SPIN_MAX);
  } else {
  {
    const int q = j * 4 + wid;
    constexpr int Q1 = WS_MB, Q2 = Q1 + 8, Q3 = Q2 + 8, Q4 = Q3 + 7, Q5 = Q4 + 7, Q6 = Q5 + 7, Q7 = Q6 + 4, Q8 = Q7 + 4, QC = Q8 + 1;
    static_assert(QC <= 128, "warm-up slots");
    unsigned dmy = 0;
    if (q < QC) {
      const char* base; int b0;
      if (q < Q1) { base = wsp; b0 = 0; }
      else if (q < Q2) { base = (const char*)ws_re; b0 = Q1; }
      else if (q < Q3) { base = (const char*)ws_im; b0 = Q2; }
      else if (q < Q4) { base = (const char*)w_gate; b0 = Q3; }
      else if (q < Q5) { base = (const char*)w_up; b0 = Q4; }
      else if (q < Q6) { base = (const char*)w_down; b0 = Q5; }
      else if (q < Q7) { base = (const char*)out; b0 = Q6; }
      else if (q < Q8) { base = (const char*)x; b0 = Q7; }
      else { base = (const char*)w_r; b0 = Q8; }
      base += (size_t)(q - b0) << 20;
      asm volatile("s_load_dword %0, %1, 0" : "+s"(dmy) : "s"(base) : "memory");
    }
    auto pre = [&]() __attribute__((always_inline)) {
      const int w = g * 4 + wid, q = lane >> 2, l = w * 4 + (lane & 3);
      constexpr int R = (int)(WS_FLAGS - WS_GU);
      const int o = q == 0 && (PRE & 1) ? R + FL_X * 4 + l * 128 : q == 1 && (PRE & 2) && l < 2048 ? R + FL_AB * 4 + l * 128 : q == 2 && (PRE & 4) && l < 384 ? R + FL_ST * 4 + l * 128
                    : q == 3 && (PRE & 8) && l < F_ ? l * 128 : q == 4 && (PRE & 16) ? (int)(WS_DN - WS_GU) + l * 128 : (int)0x80000000;
      asm volatile("s_mov_b32 m0, %0\n\ts_nop 0\n\tbuffer_load_dword %1, %2, 0 offen lds" :: "s"(lds_addr(smem + LDS_DUMMY) + wid * 1024 + 512), "v"(o), "s"(mk_rsrc(wsp + WS_GU)) : "memory");
    };
    if (PRE && PPOS == 0) pre();
    if (wid == 0) { CALL_PG(1); CALL_PG(8); CALL_PG(5); }
    else if (wid == 1) { CALL_PG(2); CALL_PG(9); CALL_PG(6); }
    else if (wid == 2) { CALL_PG(3); CALL_PG(10); CALL_PG(7); }
    else { CALL_PG(4); CALL_PG(11); }
    if (PRE && PPOS == 1) pre();
    asm volatile("s_barrier\n\ts_waitcnt lgkmcnt(0)" : "+s"(dmy) :: "memory");
    if (PRE && PPOS == 2) pre();
  }
  PAGE(8);
  epi_setup();
  const int wr = wid >> 1, wc = wid & 1;
  const int sw = (fr >> 1) & 7;
  const int ko0 = ((0 + fq) ^ sw) * 16, ko1 = ((4 + fq) ^ sw) * 16;
  const int mrow = (fr >> 2) * 8 + (fr & 3);
  bf16x8 fm[2][4], fn[2][4];
  {
    const char* pm = smem + (wr * 64 + mrow) * 128;
    const char* pn = smem + 16384 + (wc * 64 + mrow) * 128;
    f32x4 acc[4][4];
#pragma unroll
    for (int i = 0; i < 4; ++i)
#pragma unroll
      for (int jn = 0; jn < 4; ++jn)
#pragma unroll
        for (int q = 0; q < 4; ++q) acc[i][jn][q] = 0.f;
#define TO(i) (((i) >> 1) * 4096 + ((i) & 1) * 512)
#define LDSP(T, a) ((__attribute__((address_space(3))) T*)(size_t)(a))
#define SLOTS(n) _Pragma("unroll") for (int q = 0; q < (n); ++q)
#define MFA(c, a, b) asm volatile("v_mfma_f32_16x16x32_bf16 %0, %1, %2, %0" : "+v"(c) : "v"(a), "v"(b))
#define MFA2(c, a, b, a2, b2) asm volatile("v_mfma_f32_16x16x32_bf16 %0, %1, %2, %0\n\tv_mfma_f32_16x16x32_bf16 %0, %3, %4, %0" : "+v"(c) : "v"(a), "v"(b), "v"(a2), "v"(b2))
    unsigned lm[2][2], ln[2][2];
#pragma unroll
    for (int u = 0; u < 2; ++u)
#pragma unroll
      for (int kk = 0; kk < 2; ++kk) {
        lm[u][kk] = lds_addr(pm) + u * 2 * G1_STAGE + (kk ? ko1 : ko0);
        ln[u][kk] = lds_addr(pn) + u * 2 * G1_STAGE + (kk ? ko1 : ko0);
        asm volatile("" : "+v"(lm[u][kk]), "+v"(ln[u][kk]));
      }
    auto ldm = [&](int stg, int i, int kk) __attribute__((always_inline)) { fm[kk][i] = *LDSP(const bf16x8, lm[stg >> 1][kk] + (stg & 1) * G1_STAGE + TO(i)); };
    auto ldn = [&](int stg, int jn, int kk) __attribute__((always_inline)) { fn[kk][jn] = *LDSP(const bf16x8, ln[stg >> 1][kk] + (stg & 1) * G1_STAGE + TO(jn)); };
    auto quad = [&](int a, int c, int d, int s) __attribute__((always_inline)) {
      const int i = (s < 2 || s >= 6) ? a : a + 1, jn = s < 4 ? c : d, kk = (s == 1 || s == 2 || s == 5 || s == 6);
      MFA(acc[i][jn], fm[kk][i], fn[kk][jn]);
    };
    auto quad2 = [&](int a, int c, int d, int s) __attribute__((always_inline)) {
      const int i = (s < 2 || s >= 6) ? a : a + 1, jn = s < 4 ? c : d, kk = (s == 2 || s == 6);
      MFA2(acc[i][jn], fm[kk][i], fn[kk][jn], fm[1 - kk][i], fn[1 - kk][jn]);
    };
    auto half1 = [&](int stg) __attribute__((always_inline)) {
      const int ja = (stg & 1) * 2, jb = 2 - ja;
      SLOTS(8) {
        quad(0, ja, ja + 1, q);
        if (q < 4) ldn(stg, jb + (q >> 1), q & 1); else ldm(stg, 2 + ((q - 4) >> 1), q == 5 || q == 6);
        SB();
      }
      SLOTS(4) { quad2(0, jb, jb + 1, 2 * q); SB(); }
    };
    auto half2 = [&](int stg) __attribute__((always_inline)) {
      const int ja = (stg & 1) * 2, jb = 2 - ja, nx = (stg + 1) & 3;
      SLOTS(8) {
        quad(2, jb + 1, jb, q);
        if (!(q & 1)) ldm(nx, q >> 2, q == 2 || q == 4);
        SB();
      }
      SLOTS(8) {
        quad(2, ja, ja + 1, q);
        if (!(q & 1)) ldn(nx, jb + (q >> 2), (q >> 1) & 1);
        SB();
      }
    };
    SLOTS(16) asm volatile("" : "+v"(acc[q >> 2][q & 3]));
    SB();
    SLOTS(4) { ldm(0, q >> 1, q & 1); ldn(0, q >> 1, q & 1); }
    SB();
#pragma unroll 1
    for (int k4 = 0; k4 < G1_NKT - 4; k4 += 4) {
      half1(0); BAR_LGKM();
      SB(); half2(0);
      half1(1); BAR_LGKM();
      SB(); half2(1);
      half1(2); BAR_LGKM();
      SB(); half2(2);
      half1(3); BAR_LGKM();
      SB(); half2(3);
    }
    bf16x8 gm[2][4], gn[2][8];
    unsigned am[2][2], an[2][2];
#pragma unroll
    for (int u = 0; u < 2; ++u)
#pragma unroll
      for (int kk = 0; kk < 2; ++kk) {
        am[u][kk] = lds_addr(pm) + (u ? 3 : 1) * G1_STAGE + (kk ? ko1 : ko0);
        an[u][kk] = lds_addr(pn) + (u ? 3 : 1) * G1_STAGE + (kk ? ko1 : ko0);
        asm volatile("" : "+v"(am[u][kk]), "+v"(an[u][kk]));
      }
    unsigned al = lds_addr(smem + LDS_MB) + (wr * 32 + fq * 8) * 4, ac = lds_addr(smem + LDS_MB) + (256 + wc * 64 + mrow) * 4;
    unsigned ae = lds_addr(smem + G2_MRING) + (wc * 64 + mrow) * 128 + (((wr * 4 + fq) ^ sw) * 16);
    asm volatile("" : "+v"(al), "+v"(ac), "+v"(ae));
    auto rdg = [&](int set, int t, int h, int r) __attribute__((always_inline)) {
      const int u = t == 15, o = t == 14 ? G1_STAGE : 0;
      if (r == 0 || r == 2 || r == 4 || r == 5) {
        const int i = r >= 4, kk = r == 2 || r == 4;
        gm[set][kk * 2 + i] = *LDSP(const bf16x8, am[u][kk] + o + h * 4096 + i * 512);
      } else {
        const int jn = r < 4 ? 0 : (r - 4) >> 1, kk = r < 4 ? r >> 1 : r & 1;
        gn[set][kk * 4 + jn] = *LDSP(const bf16x8, an[u][kk] + o + TO(jn));
      }
    };
    auto mmg = [&](int set, int h, int q) __attribute__((always_inline)) {
      const int jn = q >> 2, r = q & 3, i = (r >> 1) ^ (jn & 1), kk = r == 1 || r == 2;
      MFA(acc[h * 2 + i][jn], gm[set][kk * 2 + i], gn[set][kk * 4 + jn]);
    };
    f32x4 rsv[2], mrv[2];
    float cev[4], tc[32];
    auto rds = [&](int h, int r) __attribute__((always_inline)) {
      if (r < 2) mrv[r] = *LDSP(const f32x4, al + h * 256 + r * 16); else rsv[r - 2] = *LDSP(const f32x4, al + 512 + h * 256 + (r - 2) * 16);
    };
    auto prep = [&](int n) __attribute__((always_inline)) {
      tc[n] = llvm_fmul_legacy(mrv[(n >> 2) & 1][n & 3], cev[n >> 3]);
      asm volatile("" : "+v"(tc[n]));
    };
    u32x4 ep;
    auto epi = [&](int h, int q) __attribute__((always_inline)) {
      const int jn = q >> 1, i = q & 1;
      asm volatile("v_fmac_f32 %0, %4, %8\n\tv_fmac_f32 %1, %5, %9\n\tv_fmac_f32 %2, %6, %10\n\tv_fmac_f32 %3, %7, %11"
                   : "+v"(tc[q * 4]), "+v"(tc[q * 4 + 1]), "+v"(tc[q * 4 + 2]), "+v"(tc[q * 4 + 3])
                   : "v"(acc[h * 2 + i][jn][0]), "v"(acc[h * 2 + i][jn][1]), "v"(acc[h * 2 + i][jn][2]), "v"(acc[h * 2 + i][jn][3]),
                     "v"(rsv[i][0]), "v"(rsv[i][1]), "v"(rsv[i][2]), "v"(rsv[i][3]));
      ep[i * 2] = pk2(tc[q * 4], tc[q * 4 + 1]); ep[i * 2 + 1] = pk2(tc[q * 4 + 2], tc[q * 4 + 3]);
      if (i) *LDSP(u32x4, ae + m_off(h) + TO(jn)) = ep;
    };
    SB();
    half1(0);
    BAR_LGKM();
    SB();
    SLOTS(16) {
      if (q < 8) quad(2, 3, 2, q); else quad(2, 0, 1, q - 8);
      if (q < 12) rdg(0, 13, 0, q);
      SB();
    }
    BAR();
    SB();
    SLOTS(16) {
      mmg(0, 0, q);
      if (q < 12) rdg(1, 14, 0, q);
      else { rds(0, q - 12); cev[q - 12] = *LDSP(const float, ac + (((q - 12) >> 1) * 32 + ((q - 12) & 1) * 4) * 4); }
      SB();
    }
    PAGE(9);
    BAR();
    SB();
    SLOTS(16) { mmg(1, 0, q); if ((q & 3) < 3) rdg(0, 15, 0, (q >> 2) * 3 + (q & 3)); if (q >= 8) { prep(2 * q - 16); prep(2 * q - 15); } SB(); }
    SLOTS(16) { mmg(0, 0, q); if ((q & 3) < 3) rdg(1, 13, 1, (q >> 2) * 3 + (q & 3)); prep(16 + q); SB(); if (q >= 9 && (q & 1)) { epi(0, (q - 9) >> 1); SB(); } }
    SLOTS(8) { mmg(1, 1, q); if (q < 6) { rdg(0, 14, 1, 2 * q); rdg(0, 14, 1, 2 * q + 1); } SB(); if (q & 1) { epi(0, 4 + (q >> 1)); SB(); } }
    BAR_LGKM();
    SB();
    SLOTS(8) { mmg(1, 1, 8 + q); if (q < 4) rdg(1, 15, 1, q == 0 ? 1 : q == 1 ? 3 : q + 4); else rds(1, q - 4); SB(); }
    SLOTS(16) { mmg(0, 1, q); if (q < 8) rdg(1, 15, 1, q == 0 ? 0 : q == 1 ? 2 : q < 4 ? q + 2 : q + 4); prep(q); SB(); }
    asm volatile("s_waitcnt lgkmcnt(0)" ::: "memory");
    if (lane == 0) asm volatile("ds_add_u32 %0, %1" :: "v"(lds_addr(smem + LDS_SEL + 12)), "v"(1) : "memory");
    SB();
    SLOTS(16) {
      mmg(1, 1, q);
      if (q < 4) fn[0][q] = *(const bf16x8*)(smem + (wr * 128 + fr) * 128 + (q >> 1) * 2048 + ((q & 1) ? ko1 : ko0));
      else if (q < 8) fm[q & 1][(q - 4) >> 1] = *(const bf16x8*)(smem + G2_MRING + m_off(0) + (wc * 64 + mrow) * 128 + TO((q - 4) >> 1) + ((q & 1) ? ko1 : ko0));
      prep(16 + q);
      SB();
    }
    {
      lds_vi rel = (lds_vi)(smem + LDS_SEL + 12);
      int it = 0;
      while (rel[0] != 4 && it < SPIN_MAX * 8) ++it;
      asm volatile("" ::: "memory");
    }
    asm volatile("s_nop 7");
    SB();
    SLOTS(8) { epi(1, q); SB(); }
  }

  const char* pn = smem + (wr * 128 + fr) * 128;
  const char* pm = smem + G2_MRING + (wc * 64 + mrow) * 128;
  f32x4 acc[8][4];
  auto mm = [&](bool imaj, int set, int q2, int s, int mode) __attribute__((always_inline)) {
    int i, jj, kk;
    if (imaj) { i = s >> 2; const int r = s & 3; jj = (r >> 1) ^ (i & 1); kk = (r == 1 || r == 2); }
    else { jj = s >> 3; const int r = s & 7; i = jj ? 3 - (r >> 1) : (r >> 1); kk = ((r + 1) >> 1) & 1; }
    if (mode == 1 && !(s & 1)) asm volatile("v_mfma_f32_16x16x32_bf16 %0, %1, %2, 0" : "=&v"(acc[q2 * 2 + jj][i]) : "v"(fm[kk][i]), "v"(fn[set][jj * 2 + kk]));
    else if (mode) asm volatile("v_mfma_f32_16x16x32_bf16 %0, %1, %2, %0" : "+v"(acc[q2 * 2 + jj][i]) : "v"(fm[kk][i]), "v"(fn[set][jj * 2 + kk]));
    else acc[q2 * 2 + jj][i] = __builtin_amdgcn_mfma_f32_16x16x32_bf16(fm[kk][i], fn[set][jj * 2 + kk], acc[q2 * 2 + jj][i], 0, 0, 0);
  };
  unsigned bn[2], bm[2], an[2], am[2];
#pragma unroll
  for (int kk = 0; kk < 2; ++kk) {
    bn[kk] = lds_addr(pn) + (kk ? ko1 : ko0); bm[kk] = lds_addr(pm) + (kk ? ko1 : ko0);
    asm volatile("" : "+v"(bn[kk]), "+v"(bm[kk]));
    an[kk] = bn[kk]; am[kk] = bm[kk] + m_off(0);
  }
#define RDN(jt, kk, o) (*LDSP(const bf16x8, an[kk] + (o) + (jt) * 2048))
#define RDM(i, kk, o) (*LDSP(const bf16x8, am[kk] + (o) + TO(i)))
  PAGE(10);
  tag = __builtin_amdgcn_readfirstlane(((const unsigned*)(smem + LDS_SEL))[5]);
  {
    int son = 0;
    auto tile = [&](int kt, int zero) __attribute__((always_inline)) {
      SLOTS(16) {
        mm(true, 0, 0, q, zero);
        if (!(q & 1)) {
          if (q < 8) fm[(q >> 1) & 1][2 + (q >> 2)] = RDM(2 + (q >> 2), (q >> 1) & 1, 0);
          else { const int r = (q - 8) >> 1; fn[1][r] = RDN(2 + (r >> 1), r & 1, 0); }
        }
        SB();
      }
      SLOTS(16) {
        mm(false, 1, 1, q, zero);
        if (!(q & 1) && q < 8) fn[0][q >> 1] = RDN(4 + (q >> 2), (q >> 1) & 1, 0);
        SB();
      }
      const int som = son >> 1;
      son = son == 65536 ? 0 : son + 32768;
      SLOTS(16) {
        mm(false, 0, 2, q, zero);
        if (!(q & 1) && q < 8) fn[1][q >> 1] = RDN(6 + (q >> 2), (q >> 1) & 1, 0);
        if (q == 9 || q == 11) { an[q == 11] = bn[q == 11] + son; asm volatile("" : "+v"(an[q == 11])); }
        if (q == 13 || q == 15) { am[q == 15] = bm[q == 15] + som; asm volatile("" : "+v"(am[q == 15])); }
        SB();
      }
      BAR_LGKM();
      SB();
      SLOTS(16) {
        mm(true, 1, 3, q, zero);
        if (!(q & 1)) {
          if (q < 8) fn[0][q >> 1] = RDN(q >> 2, (q >> 1) & 1, 0);
          else fm[(q >> 1) & 1][(q - 8) >> 2] = RDM((q - 8) >> 2, (q >> 1) & 1, 0);
        }
        SB();
      }
    };
    SB();
    tile(0, 1);
#pragma unroll 1
    for (int kt = 1; kt < G2_NKT - 3; ++kt) tile(kt, 0);
  }
  unsigned fx;
  {
    constexpr int NG = 12;
    static constexpr int g_t[NG] = {0, 0, 1, 1, 2, 2, 2, 1, 0, 0, 1, 2};
    static constexpr int g_q[NG] = {0, 1, 0, 1, 0, 1, 2, 2, 2, 3, 3, 3};
    constexpr int G14 = 1, G15 = 3, GP0 = 5, GP1 = 6;
    constexpr auto chp = [](int g) { return g == 0 || g_t[g] != g_t[g - 1]; };
    constexpr auto chn = [](int g) { return g + 1 < NG && g_t[g + 1] != g_t[g]; };
    static_assert(n_off(13) == 32768 && n_off(14) == 65536 && n_off(15) == 0, "");
    unsigned an2[2];
#pragma unroll
    for (int kk = 0; kk < 2; ++kk) {
      an[kk] = bn[kk]; an2[kk] = bn[kk] + 65536; am[kk] = bm[kk];
      asm volatile("" : "+v"(an[kk]), "+v"(an2[kk]), "+v"(am[kk]));
    }
    int vo;
    u32x4 pk;
    u32x2 kv;
    constexpr int NF0 = 152;
    auto publish = [&](int d, int h) __attribute__((always_inline)) {
      if (h == 0) { vo = lane; PINV(vo); vo = vo * 16 + (((d + kq + 1) & 3) * 4 + kq) * 4 * MSG; PINV(vo); }
      const f32x4 v = acc[2 * d + (h >> 2)][h & 3];
      pk[(h & 1) * 2] = pk2(v[0], v[1]); pk[(h & 1) * 2 + 1] = pk2(v[2], v[3]);
      if (h & 1) __builtin_amdgcn_raw_buffer_store_b128(pk, rs, vo + (h >> 1) * 1024, 0, 16);
    };
    int fo = lane < (FULL ? 16 : 1) ? kq * 512 + lane * 4 : (int)0x80000000;
#define MSG_FLAG(d) __builtin_amdgcn_raw_buffer_store_b32(tag, rc, fo + (((d) + kq + 1) & 3) * 2048, 0, 16)
    auto rdn = [&](int g, int r) __attribute__((always_inline)) {
      const int t = g_t[g], jt = g_q[g] * 2 + (r >> 1);
      fn[g & 1][r] = *LDSP(const bf16x8, (t == 1 ? an2[r & 1] : an[r & 1]) + (t == 0 ? 32768 : 0) + jt * 2048);
    };
    auto rdm = [&](int t, int i, int kk) __attribute__((always_inline)) { fm[kk][i] = *LDSP(const bf16x8, am[kk] + m_off(13 + t) + TO(i)); };
    auto group = [&](auto GI) __attribute__((always_inline)) {
      constexpr int gi = decltype(GI)::value;
      if (gi == G14) { BAR();
        SB(); }
      if (gi == G15) {
        PAGE(11);
        BAR();
        SB();
        xq[0] = ld16_s<0>(x, xo); xq[1] = ld16_s<64>(x, xo);
        sq = ld8_s_sc1(stat, xo >> 11 << 3);
        kv = *(const u32x2*)(smem + LDS_SEL);
        SB();
      }
#pragma unroll
      for (int q = 0; q < 16; ++q) {
        const int n = gi * 16 + q;
        if (n == NF0) {
          wait_xq<12>(xq, sq);
          key[0] = __builtin_amdgcn_readfirstlane(kv.x); key[1] = __builtin_amdgcn_readfirstlane(kv.y);
          nf_pre();
          SB();
        }
        if (n == 112) {
        }
        if (n == 152) {
        }
        if (n == F0) {
          WAIT_VM(8); MSG_FLAG(0); SB(); }
        if (n == F1) { WAIT_VM(5); MSG_FLAG(1); SB(); }
        if (n == F2) {
          WAIT_VM(2);
          MSG_FLAG(2); SB(); }
        if (n == FX) { fx = bld4_sc1(rc, po); SB(); }
        mm(chp(gi) || chn(gi), gi & 1, g_q[gi], q, 2);
        constexpr bool ni = gi + 1 < NG && (chp(gi + 1 < NG ? gi + 1 : gi) || chn(gi + 1 < NG ? gi + 1 : gi));
        auto nr = [](int r) { return ni && r >= 2 ? 5 - r : r; };
        if constexpr (chp(gi) && chn(gi)) {
          if (q < 4) rdm(g_t[gi], 2 + (q >> 1), q & 1); else if (q < 8) rdn(gi + 1, nr(q - 4)); else if (q < 12) rdm(g_t[gi + 1], (q - 8) >> 1, q & 1);
        } else if constexpr (chp(gi)) {
          if (q < 4) rdm(g_t[gi], 2 + (q >> 1), q & 1);
          else if (gi + 1 < NG && q < 12 && !(q & 1)) rdn(gi + 1, nr((q - 4) >> 1));
        } else if constexpr (chn(gi)) {
          if (!(q & 1)) { if (q < 8) rdn(gi + 1, nr(q >> 1)); else rdm(g_t[gi + 1], (q - 8) >> 2, (q >> 1) & 1); }
        } else {
          if (!(q & 1) && q < 8) rdn(gi + 1, nr(q >> 1));
        }
        if (gi == GP0 && q < 8) publish(0, q);
        if (gi == GP1 && q < 8) publish(1, q);
        if (gi == 9 && q < 8) publish(2, q);
        if (n >= NF0) {
          const int m = (n - NF0) * 2;
          if (m < 56) { nf_step(m); nf_step(m + 1); } else if (m < 72) { nf_unp(m - 56); nf_unp(m - 55); }
        }
        SB();
      }
    };
#define GR4(i) group(IC<i>{}); group(IC<i + 1>{}); group(IC<i + 2>{}); group(IC<i + 3>{});
    GR4(0) GR4(4) GR4(8)
    asm volatile("s_nop 7");
    SB();
  }
#pragma unroll
  for (int i = 0; i < 4; ++i) *(f32x4*)(smem + LDS_ACC + wr * 16384 + wc * 4096 + i * 1024 + lane * 16) = acc[7][i];
#pragma unroll
  for (int i = 0; i < 4; ++i) ya[i] = acc[6][i];
  asm volatile("s_waitcnt lgkmcnt(0)" ::: "memory");
  if (lane == 0) sig[0] = 2;
  asm volatile("s_waitcnt vmcnt(0)" : "+v"(fx) :: "memory");
  seen = (unsigned)__ballot(fx == tag);
  }

  PAGE(6);
  const unsigned rel0 = 0xffff - (key[0] & 0xffffu) - trow, rel1 = 0xffff - (key[1] & 0xffffu) - trow;
  if (min(rel0, rel1) < 16) {
#pragma unroll 1
    for (int c = 0; c < 2; ++c) {
      const unsigned rel = c ? rel1 : rel0;
      if (rel >= 16) continue;
      lds_vi cnt = (lds_vi)(smem + LDS_SEL + 24 + c * 4);
      int it = 0;
      while (cnt[0] != 4 && it < SPIN_MAX * 8) { __builtin_amdgcn_s_sleep(1); ++it; }
      asm volatile("" ::: "memory");
      if (rel == fr) {
        const float* fp = (const float*)(smem + LDS_FIN + c * 512) + (wid & 1) * 64 + fq * 8;
#pragma unroll
        for (int u4 = 0; u4 < 4; ++u4) {
          const f32x4 v = *(const f32x4*)(fp + (u4 >> 1) * 32 + (u4 & 1) * 4);
#pragma unroll
          for (int k = 0; k < 4; k += 2) {
            const unsigned u = pk2(nf[u4 * 4 + k] + v[k], nf[u4 * 4 + k + 1] + v[k + 1]);
            nf[u4 * 4 + k] = lo_bf(u); nf[u4 * 4 + k + 1] = hi_bf(u);
          }
        }
      }
    }
  }
  if (ha) {
    int it = 0;
    while (sig[0] != 2 && it < SPIN_MAX * 8) { __builtin_amdgcn_s_sleep(1); ++it; }
    asm volatile("" ::: "memory");
#pragma unroll
    for (int i = 0; i < 4; ++i) ya[i] = *(const f32x4*)(smem + LDS_ACC + (wid & 2) * 8192 + (wid & 1) * 4096 + i * 1024 + lane * 16);
  }
  u32x4 pp[3][2];
  unsigned e_lo = 0x00003f80u, e_hi = 0x3f800000u;
  asm volatile("" : "+v"(e_lo), "+v"(e_hi));
  __amdgpu_buffer_rsrc_t rs_ = rs, rc_ = rc;
  int mo[3] = {MSG_IDX(0) * 4 * MSG, MSG_IDX(1) * 4 * MSG, MSG_IDX(2) * 4 * MSG}, mv = lane * 16 + ha * 2048, mv2 = mv + 1024;
  asm volatile("" : "+s"(rs_), "+s"(rc_), "+s"(mo[0]), "+s"(mo[1]), "+s"(mo[2]));
  asm volatile("" : "+v"(mv), "+v"(mv2));
#define MSG_POLL(m) do { \
    if (!((seen >> (m)) & 1)) { \
      unsigned pv_, it_ = SPIN_MAX; \
      asm volatile("1:\n\tbuffer_load_dword %0, %3, %4, 0 offen sc1\n\ts_waitcnt vmcnt(0)\n\tv_cmp_eq_u32 vcc, %5, %0\n\ts_mov_b32 %1, vcc_lo\n\t" \
                   "s_bitcmp1_b32 %1, %6\n\ts_cbranch_scc1 2f\n\ts_sub_u32 %2, %2, 1\n\ts_cbranch_scc1 2f\n\ts_sleep 1\n\ts_branch 1b\n2:" \
                   : "=&v"(pv_), "+s"(seen), "+s"(it_) : "v"(po), "s"(rc_), "s"(tag), "i"(m) : "vcc", "scc", "memory"); \
    } \
    pp[m][0] = __builtin_amdgcn_raw_buffer_load_b128(rs_, mv, mo[m], 16); \
    pp[m][1] = __builtin_amdgcn_raw_buffer_load_b128(rs_, mv2, mo[m], 16); } while (0)
#define MSG_ADD(m) do { \
    _Pragma("unroll") for (int u = 0; u < 8; ++u) { \
      ya[u >> 1][(u & 1) * 2] = add_bf(pp[m][u >> 2][u & 3], e_lo, ya[u >> 1][(u & 1) * 2]); ya[u >> 1][(u & 1) * 2 + 1] = add_bf(pp[m][u >> 2][u & 3], e_hi, ya[u >> 1][(u & 1) * 2 + 1]); } } while (0)
  MSG_POLL(2); MSG_POLL(1); MSG_POLL(0);
  SB();
  MSG_ADD(2); MSG_ADD(1);
  asm volatile("" : "+v"(ya[0]), "+v"(ya[1]), "+v"(ya[2]), "+v"(ya[3]));
  SB();
  MSG_ADD(0);
  {
    unsigned ov[8];
    SB();
#pragma unroll
    for (int u = 0; u < 8; ++u) ov[u] = pk2(ya[u >> 1][(u & 1) * 2], ya[u >> 1][(u & 1) * 2 + 1]);
    SB();
#pragma unroll
    for (int u = 0; u < 8; ++u) { xf[2 * u] = add_bf(ov[u], e_lo, xf[2 * u]); xf[2 * u + 1] = add_bf(ov[u], e_hi, xf[2 * u + 1]); }
    SB();
#pragma unroll
    for (int u = 0; u < 8; ++u) ov[u] = pk2(xf[2 * u], xf[2 * u + 1]);
    SB();
#pragma unroll
    for (int u = 0; u < 8; ++u) { nf[2 * u] = add_bf(ov[u], e_lo, nf[2 * u]); nf[2 * u + 1] = add_bf(ov[u], e_hi, nf[2 * u + 1]); }
    SB();
#pragma unroll
    for (int u = 0; u < 8; ++u) ov[u] = pk2(nf[2 * u], nf[2 * u + 1]);
    SB();
    *(u32x4*)((char*)out + (unsigned)xo) = (u32x4){ov[0], ov[1], ov[2], ov[3]};
    *(u32x4*)((char*)out + (unsigned)xo + 64) = (u32x4){ov[4], ov[5], ov[6], ov[7]};
  }
}

extern "C" void kernel_launch(void* const* d_in, const int* in_sizes, int n_in, void* d_out, int out_size,
                              void* d_ws, size_t ws_size, hipStream_t stream) {
  const u16* x = (const u16*)d_in[0];
  const u16* ws_re = (const u16*)d_in[1];
  const u16* ws_im = (const u16*)d_in[2];
  const u16* wd_re = (const u16*)d_in[3];
  const u16* wd_im = (const u16*)d_in[4];
  const u16* w_r = (const u16*)d_in[6];
  const u16* w_gate = (const u16*)d_in[7];
  const u16* w_up = (const u16*)d_in[8];
  const u16* w_down = (const u16*)d_in[9];
  k_main<<<256, 512, 0, stream>>>(x, wd_re, wd_im, w_r, (char*)d_ws, ws_re, ws_im, w_gate, w_up, w_down, (u16*)d_out);
}
```

```cpp
#include <hip/hip_runtime.h>
#include <hip/hip_bf16.h>
#include <stdint.h>
#include <type_traits>

extern "C" __device__ unsigned long long llvm_dispatch_id(void) __asm("llvm.amdgcn.dispatch.id");
extern "C" __device__ float llvm_fmul_legacy(float, float) __asm("llvm.amdgcn.fmul.legacy");
typedef unsigned short u16;
typedef unsigned long long u64;
typedef __attribute__((ext_vector_type(8))) short bf16x8;
typedef __attribute__((ext_vector_type(4))) unsigned u32x4;
typedef __attribute__((ext_vector_type(2))) unsigned u32x2;
typedef __attribute__((ext_vector_type(4))) float f32x4;
typedef __attribute__((ext_vector_type(2))) float f32x2;
typedef __attribute__((ext_vector_type(2))) __bf16 bf16x2;
typedef __attribute__((address_space(3))) void* lds_vp;
typedef volatile __attribute__((address_space(3))) int* lds_vi;

constexpr int S_ = 2048, D_ = 1024, F_ = 3584;
template <int N> using IC = std::integral_constant<int, N>;

static __device__ __forceinline__ float bf2f(u16 u) { return __uint_as_float((unsigned)u << 16); }
static __device__ __forceinline__ u16 f2bf(float f) {
  __hip_bfloat16 b = __float2bfloat16(f);
  return *reinterpret_cast<u16*>(&b);
}
static __device__ __forceinline__ float rbf(float f) { return bf2f(f2bf(f)); }
static __device__ __forceinline__ float lo_bf(unsigned u) { return __uint_as_float(u << 16); }
static __device__ __forceinline__ float hi_bf(unsigned u) { return __uint_as_float(u & 0xffff0000u); }
static __device__ __forceinline__ unsigned pk2(float a, float b) {
  const bf16x2 v = __builtin_convertvector((f32x2){a, b}, bf16x2);
  return __builtin_bit_cast(unsigned, v);
}
static __device__ __forceinline__ float add_bf(unsigned u, unsigned e, float c) {
  return __builtin_amdgcn_fdot2_f32_bf16(__builtin_bit_cast(bf16x2, u), __builtin_bit_cast(bf16x2, e), c, false);
}
static __device__ __forceinline__ float ln_elem(float x, float mb, float sd, float ri) {
  const float d = rbf(x - mb);
  const float q0 = d * ri;
  return rbf(__builtin_fmaf(__builtin_fmaf(-q0, sd, d), ri, q0));
}
template <int CTRL> static __device__ __forceinline__ float dpp_f(float v) {
  return __int_as_float(__builtin_amdgcn_update_dpp(0, __float_as_int(v), CTRL, 0xf, 0xf, true));
}
template <int CTRL> static __device__ __forceinline__ unsigned dpp_u(unsigned v) {
  return (unsigned)__builtin_amdgcn_update_dpp(0, (int)v, CTRL, 0xf, 0xf, true);
}
static __device__ __forceinline__ float row_sum(float v) {
  v += dpp_f<0xB1>(v);
  v += dpp_f<0x4E>(v);
  v += dpp_f<0x141>(v);
  v += dpp_f<0x140>(v);
  return v;
}
static __device__ __forceinline__ float fold32(float a, float b) {
  const auto r = __builtin_amdgcn_permlane32_swap(__float_as_uint(a), __float_as_uint(b), false, false);
  return __uint_as_float(r[0]) + __uint_as_float(r[1]);
}
static __device__ __forceinline__ float fold16(float a, float b) {
  const auto r = __builtin_amdgcn_permlane16_swap(__float_as_uint(a), __float_as_uint(b), false, false);
  return __uint_as_float(r[0]) + __uint_as_float(r[1]);
}
static __device__ __forceinline__ float rdl(float v, int l) { return __int_as_float(__builtin_amdgcn_readlane(__float_as_int(v), l)); }

#define ST_JB 9
#define WSN 10
#define FSM 0xa2
#define GV_JB 7
#define DG 11
#define AB_STRIDE 32
#define SPIN_MAX (1 << 17)

static __device__ __forceinline__ unsigned lds_addr(const void* p) { return (unsigned)(size_t)(lds_vp)p; }

constexpr int MSG = 4096;
constexpr size_t WS_ABT   = 0;
constexpr size_t WS_SLAB  = WS_ABT + (size_t)D_ * 4096 * 2;
constexpr size_t WS_GU    = WS_SLAB + (size_t)64 * 64 * MSG;
constexpr size_t WS_DN    = WS_GU + (size_t)8 * F_ * 16;
constexpr size_t WS_FLAGS = WS_DN + (size_t)2 * 8 * 32 * 64 * 16;
constexpr int FL_X = 0;
constexpr int FL_AB = 64 * 64 * 32;
constexpr int FL_ST = FL_AB + 2048 * AB_STRIDE;
constexpr int FL_CE = FL_ST + 4096;
constexpr int FL_KEY = FL_CE + 4096;
constexpr int FL_EPOCH = FL_KEY + 4096;
constexpr int WS_MB = (int)((WS_FLAGS + (FL_EPOCH + 1) * 4 + 1048575) >> 20);

#define WAIT_VM(n) asm volatile("s_waitcnt vmcnt(" #n ")" ::: "memory")
template <int N> static __device__ __forceinline__ void wait_vm() { asm volatile("s_waitcnt vmcnt(%0)" :: "i"(N) : "memory"); }
#define BAR_LGKM() asm volatile("s_waitcnt lgkmcnt(0)\n\ts_barrier" ::: "memory")
#define BAR() asm volatile("s_barrier" ::: "memory")
#define SB() __builtin_amdgcn_sched_barrier(0)
#define PAGE(n) asm volatile("s_branch 2f\n\t.p2align 12\nk_pg" #n ":\n\ts_setpc_b64 s[42:43]\n2:")
#define CALL_PG(n) asm volatile("s_getpc_b64 s[40:41]\n3:\n\ts_mov_b32 s42, k_pg" #n "-3b\n\ts_ashr_i32 s43, s42, 31\n\ts_add_u32 s40, s40, s42\n\ts_addc_u32 s41, s41, s43\n\t" \
                                "s_swappc_b64 s[42:43], s[40:41]" ::: "s40", "s41", "s42", "s43", "scc", "memory")

static __device__ __forceinline__ __amdgpu_buffer_rsrc_t mk_rsrc(const void* p) {
  return __builtin_amdgcn_make_buffer_rsrc((void*)p, 0, 0x7fffffff, 0x00020000);
}
#define DMA_(rsrc, voff, mbase, MI, sbase, SI, pol) do { unsigned _t; \
    asm volatile("s_add_i32 m0, %2, %3\n\ts_add_i32 %0, %5, %6\n\tbuffer_load_dwordx4 %1, %4, %0 offen" pol " lds" \
                 : "=&s"(_t) : "v"(voff), "s"(mbase), "i"(MI), "s"(rsrc), "s"(sbase), "i"(SI) : "memory", "scc"); } while (0)
#define TOUCH(rsrc, voff, mbase, MI, soff) \
    asm volatile("s_add_i32 m0, %1, %2\n\ts_nop 0\n\tbuffer_load_dword %0, %3, %4 offen lds" \
                 :: "v"(voff), "s"(mbase), "i"(MI), "s"(rsrc), "s"(soff) : "memory", "scc")
template <int OFF> static __device__ __forceinline__ u32x4 ld16_s(const void* p, int voff) {
  u32x4 v;
  asm volatile("global_load_dwordx4 %0, %1, %2 offset:%3" : "=v"(v) : "v"(voff), "s"(p), "i"(OFF) : "memory");
  return v;
}
template <int OFF> static __device__ __forceinline__ void sld(unsigned& sink, const void* p, int soff) {
  asm volatile("s_load_dword %0, %1, %2 offset:%3" : "+s"(sink) : "s"(p), "s"(soff), "i"(OFF) : "memory");
}
static __device__ __forceinline__ unsigned gld4_s(const void* p, unsigned voff) {
  unsigned v;
  asm volatile("global_load_dword %0, %1, %2" : "=v"(v) : "v"(voff), "s"(p) : "memory");
  return v;
}
static __device__ __forceinline__ u32x2 bld8_sc1(__amdgpu_buffer_rsrc_t r, unsigned voff) {
  u32x2 v;
  asm volatile("buffer_load_dwordx2 %0, %1, %2, 0 offen sc1" : "=v"(v) : "v"(voff), "s"(r) : "memory");
  return v;
}
template <int N> static __device__ __forceinline__ void wait_xs(unsigned& a, u32x2& b) { asm volatile("s_waitcnt vmcnt(%2)" : "+v"(a), "+v"(b) : "i"(N) : "memory"); }
template <int N> static __device__ __forceinline__ void wait_xq(u32x4 (&a)[2], u32x2& b) { asm volatile("s_waitcnt vmcnt(%3)" : "+v"(a[0]), "+v"(a[1]), "+v"(b) : "i"(N) : "memory"); }
static __device__ __forceinline__ u32x2 ld8_s_sc1(const void* p, int voff) {
  u32x2 v;
  asm volatile("global_load_dwordx2 %0, %1, %2 sc1" : "=v"(v) : "v"(voff), "s"(p) : "memory");
  return v;
}
static __device__ __forceinline__ void lgkm0(unsigned& sink) { asm volatile("s_waitcnt lgkmcnt(0)" : "+s"(sink) :: "memory"); }
template <typename T> static __device__ __forceinline__ void pin4(T& a, T& b, T& c, T& d) { asm volatile("" : "+s"(a), "+s"(b), "+s"(c), "+s"(d)); }
template <typename T, typename U> static __device__ __forceinline__ void pins(T& a, U& b) { asm volatile("" : "+s"(a), "+s"(b)); }
static __device__ __forceinline__ void launder(int& v, int& a, int& b) { asm volatile("" : "+v"(v)); asm volatile("" : "+s"(a), "+s"(b)); }
static __device__ __forceinline__ unsigned bld4_sc1(__amdgpu_buffer_rsrc_t r, int voff) {
  unsigned v;
  asm volatile("buffer_load_dword %0, %1, %2, 0 offen sc1" : "=v"(v) : "v"(voff), "s"(r) : "memory");
  return v;
}
static __device__ __forceinline__ u32x4 bld16_nt(__amdgpu_buffer_rsrc_t r, int voff, int soff) {
  u32x4 v;
  asm volatile("buffer_load_dwordx4 %0, %1, %2, %3 offen nt" : "=v"(v) : "v"(voff), "s"(r), "s"(soff) : "memory");
  return v;
}
static __device__ __forceinline__ void bld16_nt_io(u32x4& v, __amdgpu_buffer_rsrc_t r, int voff, int soff) {
  asm volatile("buffer_load_dwordx4 %0, %1, %2, %3 offen nt" : "+v"(v) : "v"(voff), "s"(r), "s"(soff) : "memory");
}
static __device__ __forceinline__ u32x4 bld16_sc1(__amdgpu_buffer_rsrc_t r, int voff, int soff) {
  u32x4 v;
  asm volatile("buffer_load_dwordx4 %0, %1, %2, %3 offen sc1" : "=v"(v) : "v"(voff), "s"(r), "s"(soff) : "memory");
  return v;
}
static __device__ __forceinline__ u32x2 granule_wait(const u64* p, unsigned tag) {
  u64 v = 0;
#pragma unroll 1
  for (int it = 0; it < SPIN_MAX; ++it) {
    v = __hip_atomic_load(p, __ATOMIC_RELAXED, __HIP_MEMORY_SCOPE_AGENT);
    if (__all((unsigned)(v >> 32) == tag)) break;
    __builtin_amdgcn_s_sleep(2);
  }
  return (u32x2){(unsigned)v, (unsigned)(v >> 32)};
}
static __device__ __forceinline__ float key_score(unsigned key) {
  const unsigned u = key >> 16;
  return bf2f((u16)((u & 0x8000u) ? (u & 0x7fffu) : (~u & 0xffffu)));
}
static __device__ __forceinline__ void merge2(unsigned& a1, unsigned& a2, unsigned b1, unsigned b2) {
  const unsigned lo = min(a1, b1);
  a1 = max(a1, b1);
  a2 = max(lo, max(a2, b2));
}
static __device__ __forceinline__ float sqdev16(const u32x4 (&v)[2], float mb) {
  f32x2 q2 = {0.f, 0.f};
#pragma unroll
  for (int i = 0; i < 8; ++i) {
    const f32x2 e = (f32x2){lo_bf(v[i >> 2][i & 3]), hi_bf(v[i >> 2][i & 3])} - mb;
    q2 += e * e;
  }
  return q2.x + q2.y;
}

constexpr int G1_NST = 4, G1_NKT = 16, G1_STAGE = 32768;
constexpr int G2_NKT = 16, G2_MRING = 98304;
constexpr int n_off(int k) { return (k % 3) * 32768; }
constexpr int m_off(int k) { return ((k + 2) % 3) * 16384; }
constexpr int k_ord(int k) { return k < 2 ? k : (k < 9 ? 2 * (k - 1) : 2 * (k - 8) + 1); }
constexpr int LDS_DUMMY = G2_MRING + 49152;
constexpr int LDS_GU = LDS_DUMMY + 4096;
constexpr int LDS_FIN = LDS_GU + 4096;
constexpr int LDS_MB = LDS_FIN + 4096;
constexpr int LDS_SEL = LDS_MB + 1536;
constexpr int LDS_SIG = LDS_SEL + 64;
constexpr int LDS_KEY = LDS_SIG + 32;
constexpr int LDS_BYTES = LDS_KEY + 128;
constexpr int LDS_ACC = n_off(G2_NKT - 3);

constexpr int tpos[16] = {0, 0, 0, 0, 109, 115, 1, 2, 2, 3, 4, 5, 6, 7, 8, 9};
constexpr int twho[16] = {0, 0, 0, 0, 7, 7, 0, 1, 2, 3, 4, 5, 6, 7, 0, 1};
constexpr int dn_cnt(int q) { return q == 5 ? 3 : q == 8 ? 4 : 0; }
constexpr int dn_first(int q) { return q <= 5 ? 0 : q <= 8 ? 3 : 7; }

static __device__ __forceinline__ void code_touch(unsigned& sink, unsigned off) {
  asm volatile("s_getpc_b64 s[40:41]\n\tk_main_rt:\n\ts_min_u32 %1, %1, k_main_end-k_main_rt-196\n\t"
               "s_load_dword %0, s[40:41], %1 offset:0\n\ts_load_dword %0, s[40:41], %1 offset:64\n\ts_load_dword %0, s[40:41], %1 offset:128\n\ts_load_dword %0, s[40:41], %1 offset:192"
               : "+s"(sink), "+s"(off) :: "s40", "s41", "scc", "memory");
}
static __device__ __forceinline__ void touch1(__amdgpu_buffer_rsrc_t r, int voff, unsigned mb, int soff) { TOUCH(r, voff, mb, LDS_DUMMY, soff); }
typedef const __attribute__((address_space(1))) u16* gptr;
typedef __attribute__((ext_vector_type(8))) u64 u64x8;
typedef __attribute__((ext_vector_type(2))) u64 u64x2;
extern "C" __global__ __launch_bounds__(512, 2) void k_main(const u16* x, const u16* wd_re, const u16* wd_im, const u16* w_r,
                                                 char* wsp, const u16* ws_re, const u16* ws_im,
                                                 const u16* w_gate, const u16* w_up, const u16* w_down, u16* out) {
  __shared__ __attribute__((aligned(16))) char smem[LDS_BYTES];
  u64x8 ka;
  u64x2 kb_;
  u64 kc;
  asm volatile("s_load_dwordx16 %0, %3, 0\n\ts_load_dwordx4 %1, %3, 64\n\ts_load_dwordx2 %2, %3, 80"
               : "=&s"(ka), "=&s"(kb_), "=&s"(kc) : "s"(__builtin_amdgcn_kernarg_segment_ptr()) : "memory");
  const int tid = threadIdx.x, lane = tid & 63;
  const int wid = __builtin_amdgcn_readfirstlane(tid >> 6);
  const int g = blockIdx.x, xcd = g & 7, j = g >> 3;
  if (wid < 4) {
    unsigned cw, co = (j * 4 + wid) * 448;
    asm volatile(".subsection 1\nk_main_end:\n.subsection 0\n\ts_min_u32 %1, %1, k_main_end-k_main-480\n\ts_getpc_b64 s[40:41]\n\ts_add_u32 s40, s40, %1\n\ts_addc_u32 s41, s41, 0\n\t"
                 "s_load_dword %0, s[40:41], 0\n\ts_load_dword %0, s[40:41], 64\n\ts_load_dword %0, s[40:41], 128\n\t"
                 "s_load_dword %0, s[40:41], 192\n\ts_load_dword %0, s[40:41], 256\n\ts_load_dword %0, s[40:41], 320\n\ts_load_dword %0, s[40:41], 384\n\ts_waitcnt lgkmcnt(0)"
                 : "=&s"(cw), "+s"(co) :: "s40", "s41", "scc", "memory");
  }
  const int pw = wid - 4;
  const int row0 = (wid & 3) * 8 + (lane >> 3);
  const int cm = ((lane & 7) ^ (((row0 >> 1) & 1) | ((wid & 3) << 1))) * 16;
  int vm = row0 * (D_ * 2) + cm;
  asm volatile("" : "+v"(vm));
  const int kq = xcd >> 1;
  const int tt = (xcd & 1) * 4 + (j >> 3);
  const int et = j & 7;
  const int z = kq >> 1, st = (kq & 1) * 8 + tt;
  const int lrow = (kq & 1) * 1024 + (xcd & 1) * 512 + z * 256 + j * 8 + pw * 2;
  const int drow = ((kq & 1) * 2 + (xcd & 1)) * 256 + j * 8 + pw * 2;
  unsigned o_m = st * 128 * D_ * 2, o_n = et * 128 * D_ * 2, o_l = lrow * D_ * 2, o_d = drow * D_ * 2, mb0 = lds_addr(smem) + pw * 1024;
  int l16 = lane * 16;
  asm volatile("" : "+s"(o_m), "+s"(o_n), "+s"(o_l), "+s"(o_d), "+s"(mb0));
  asm volatile("" : "+v"(l16));
  asm volatile("s_waitcnt lgkmcnt(0)" : "+s"(ka), "+s"(kb_), "+s"(kc) :: "memory");
  x = (const u16*)(gptr)ka[0]; wd_re = (const u16*)(gptr)ka[1]; wd_im = (const u16*)(gptr)ka[2]; w_r = (const u16*)(gptr)ka[3];
  wsp = (char*)(gptr)ka[4]; ws_re = (const u16*)(gptr)ka[5]; ws_im = (const u16*)(gptr)ka[6]; w_gate = (const u16*)(gptr)ka[7];
  w_up = (const u16*)(gptr)kb_[0]; w_down = (const u16*)(gptr)kb_[1]; out = (u16*)(gptr)kc;
  int* flags = (int*)(wsp + WS_FLAGS);
  u64* stat = (u64*)(flags + FL_ST);
  const u16* wdz = z ? wd_im : wd_re;
  const __amdgpu_buffer_rsrc_t rm = mk_rsrc(x);
  const __amdgpu_buffer_rsrc_t rn = mk_rsrc(wdz);
#define G1_X(kt, so, q) DMA_(rm, vm, mb0, (so) + (q) * 4096, o_m, (kt) * 128 + (((q) & 1) * 64 + ((q) >> 1) * 32) * D_ * 2, "")
#define G1_W(kt, so, q) DMA_(rn, vm, mb0, (so) + 16384 + (q) * 4096, o_n, (kt) * 128 + (q) * 32 * D_ * 2, "")
#define G1_STAGE_TILE(kt, so, W) do { SB(); G1_X(kt, so, 0); W(0); G1_X(kt, so, 1); W(1); G1_X(kt, so, 2); W(2); G1_X(kt, so, 3); W(3); \
    G1_W(kt, so, 0); W(4); G1_W(kt, so, 1); W(5); G1_W(kt, so, 2); W(6); G1_W(kt, so, 3); W(7); SB(); } while (0)
#define G1_STAGE_A(kt, so, W) do { SB(); G1_X(kt, so, 0); W(0); G1_X(kt, so, 2); W(1); \
    G1_W(kt, so, 0); W(2); G1_W(kt, so, 1); W(3); G1_W(kt, so, 2); W(4); G1_W(kt, so, 3); W(5); W(6); W(7); SB(); } while (0)
#define G1_STAGE_B(kt, so) do { SB(); G1_X(kt, so, 1); G1_X(kt, so, 3); SB(); } while (0)
#define NOW(i)
  const int fr = lane & 15, fq = lane >> 4;
  const int ha = wid >> 2;
  int trow, xo, po;
  const char *prs, *prc;
  auto epi_setup = [&]() __attribute__((always_inline)) {
    int t_ = tt, l_ = lane;
    asm volatile("" : "+s"(t_));
    asm volatile("" : "+v"(l_));
    trow = t_ * 256 + ((wid >> 1) & 1) * 128 + kq * 32 + ha * 16;
    xo = ((trow + (l_ & 15)) * D_ + et * 128 + (wid & 1) * 64 + (l_ >> 4) * 8) * 2;
    prs = wsp + WS_SLAB + (size_t)((t_ * 8 + et) * 64 + (wid & 3)) * MSG;
    prc = (const char*)(flags + FL_X + ((t_ * 8 + et) * 64 + (wid & 3)) * 32);
    asm volatile("" : "+s"(trow), "+s"(prs), "+s"(prc));
    po = l_ < 3 ? (kq * 4 + ((kq + 1 + l_) & 3)) * 512 : (int)0x80000000;
    asm volatile("" : "+v"(xo), "+v"(po));
  };
#define rs mk_rsrc(prs)
#define rc mk_rsrc(prc)
  lds_vi sig = (lds_vi)(smem + LDS_SIG + (wid & 3) * 4);
  unsigned tag;
  u32x4 xq[2];
  u32x2 sq;
  f32x4 ya[4];
  unsigned key[2];
  float xf[16], nf[16], lmb, lsd, lri, la, lb, lqa, lqb;
  unsigned nfp[8];
#define PINV(a) asm volatile("" : "+v"(a))
  auto nf_pre = [&]() __attribute__((always_inline)) {
    if (__builtin_expect(!__all(sq.y == tag), 0)) sq = granule_wait(stat + (xo >> 11), tag);
    lmb = lo_bf(sq.x); lsd = hi_bf(sq.x); lri = __builtin_amdgcn_rcpf(lsd);
    PINV(lmb); PINV(lsd); PINV(lri);
  };
  auto nf_step = [&](int n) __attribute__((always_inline)) {
    const int u = n / 7;
    switch (n % 7) {
      case 0: la = lo_bf(xq[u >> 2][u & 3]); PINV(la); la -= lmb; PINV(la); break;
      case 1: lb = hi_bf(xq[u >> 2][u & 3]); PINV(lb); lb -= lmb; PINV(lb); break;
      case 2: nfp[u] = pk2(la, lb); la = lo_bf(nfp[u]); PINV(la); PINV(nfp[u]); break;
      case 3: lqa = la * lri; PINV(lqa); lb = hi_bf(nfp[u]); PINV(lb); break;
      case 4: la = __builtin_fmaf(-lqa, lsd, la); PINV(la); lqb = lb * lri; PINV(lqb); break;
      case 5: lqa = __builtin_fmaf(la, lri, lqa); PINV(lqa); lb = __builtin_fmaf(-lqb, lsd, lb); PINV(lb); break;
      default: lqb = __builtin_fmaf(lb, lri, lqb); nfp[u] = pk2(lqa, lqb); PINV(nfp[u]); break;
    }
  };
  auto nf_unp = [&](int e) __attribute__((always_inline)) {
    const int u = e >> 1;
    if (e == 0) { PINV(xq[0]); PINV(xq[1]); }
    xf[e] = (e & 1) ? hi_bf(xq[u >> 2][u & 3]) : lo_bf(xq[u >> 2][u & 3]);
    nf[e] = (e & 1) ? hi_bf(nfp[u]) : lo_bf(nfp[u]);
    PINV(xf[e]); PINV(nf[e]);
  };
#define MSG_IDX(m) (kq * 4 + ((kq + 1 + (m)) & 3))
  unsigned seen = 0;

  if (__builtin_expect(wid >= 4, 1)) {
    __builtin_amdgcn_s_setprio(3);
    G1_STAGE_TILE(0, 0, NOW);
    constexpr int O_ST = (FL_ST - FL_AB) * 4, O_CE = (FL_CE - FL_AB) * 4, O_KEY = (FL_KEY - FL_AB) * 4, O_EP = (FL_EPOCH - FL_AB) * 4;
    const __amdgpu_buffer_rsrc_t rf = mk_rsrc(wsp + WS_FLAGS + FL_AB * 4);
    u32x4 lv[2], lv2[2], lw[2], ld[2], ld2[2];
    unsigned epv;
    {
      const char* xrow = (const char*)x + o_l;
      const char* wdrow = (const char*)wdz + o_d;
      lv[0] = ld16_s<0>(xrow, l16); lv[1] = ld16_s<1024>(xrow, l16);
      lv2[0] = ld16_s<2048>(xrow, l16); lv2[1] = ld16_s<3072>(xrow, l16);
      ld[0] = ld16_s<0>(wdrow, l16); ld[1] = ld16_s<1024>(wdrow, l16);
      ld2[0] = ld16_s<2048>(wdrow, l16); ld2[1] = ld16_s<3072>(wdrow, l16);
      lw[0] = ld16_s<0>(w_r, l16); lw[1] = ld16_s<1024>(w_r, l16);
      asm volatile("buffer_load_dword %0, off, %1, %2 sc1" : "=v"(epv) : "s"(rf), "s"(O_EP) : "memory");
    }
    SB();
    const unsigned long long did = llvm_dispatch_id();
    const unsigned dmix = (unsigned)did * 0x85EBCA6Bu + (unsigned)(did >> 32) * 0xC2B2AE35u + 0x27D4EB2Fu;
    const bool moe = et != 7;
    unsigned epoch;
    const int ci = lane & 15, rq = lane >> 4, tk = (lane >> 3) & 1;
    int mat, cg, dq, d0;
    u32x4 wv[16];
    const u16 *pgu, *pn2;
    const char *pm2, *spb;
#define rw __builtin_amdgcn_make_buffer_rsrc((void*)pgu, 0, moe ? 0x7fffffff : 0, 0x00020000)
#define rn2 mk_rsrc(pn2)
#define rm2 mk_rsrc(pm2)
    int vw, vn2, vm2;
    auto k_blk = [&](int k) __attribute__((always_inline)) { return tt ^ (k_ord(k) >> 1); };
#define KROT(kk_) (k_blk(kk_) * 256 + (k_ord(kk_) & 1) * 128)
    int tv2;
    unsigned sd = 0;
    auto touches = [&](auto P) __attribute__((always_inline)) {
      constexpr int p = decltype(P)::value;
      int e = et;
      asm volatile("" : "+s"(e));
#define TCH(k) if constexpr (tpos[k] == p) { if (e == twho[k]) { const int kb = KROT(k); touch1(rn2, tv2, mb0, kb); } }
      TCH(0) TCH(1) TCH(2) TCH(3) TCH(4) TCH(5) TCH(6) TCH(7) TCH(8) TCH(9) TCH(10) TCH(11) TCH(12) TCH(13) TCH(14) TCH(15)
    };
    unsigned mq0, mq1, mq2, mq3;
    const int cg8 = g & 7, rgb = g >> 3;
    const __amdgpu_buffer_rsrc_t rdn = mk_rsrc(w_down);
    const __amdgpu_buffer_rsrc_t rgu = mk_rsrc(wsp + WS_GU);
    int vdn, vgu;
    u32x4 dv[7], gv[4];
    int abo, abq[3];
    unsigned lma;
    unsigned sel[2][4];
    float g0, g1;
    u32x4 kk[4];
    unsigned k1 = 0, k2 = 0;
    const int ko_ = O_KEY + pw * 4096;
#define MM(a, b, c) __builtin_amdgcn_mfma_f32_16x16x32_bf16(__builtin_bit_cast(bf16x8, a), __builtin_bit_cast(bf16x8, b), c, 0, 0, 0)
#define PIN1(a) asm volatile("" : "+v"(a))
#define PIN2(a, b) asm volatile("" : "+v"(a), "+v"(b))
#define PIN3(a, b, c) asm volatile("" : "+v"(a), "+v"(b), "+v"(c))
    {
      int gro, go;
      u32x2 gr;
      u32x4 oh[5];
      f32x4 sa4, gw[2], gx[2];
      float su, gs, ta, tb, sx, mbv;
      unsigned dk[4], kl;
      const int hb = lane >> 5;
#pragma unroll
      for (int l = 0; l < WSN; ++l) asm volatile("" : "=v"(wv[l]));
      auto diag = [&](const f32x4& m) __attribute__((always_inline)) {
        return __uint_as_float((__float_as_uint(m[0]) & dk[0]) | (__float_as_uint(m[1]) & dk[1]) | (__float_as_uint(m[2]) & dk[2]) | (__float_as_uint(m[3]) & dk[3]));
      };
      auto setup = [&](auto JB, auto GP) __attribute__((always_inline)) {
        constexpr int jb = decltype(JB)::value, gp = decltype(GP)::value;
        SB();
        {
          int ln = lane, jj = j, p_ = pw;
          launder(ln, jj, p_);
          const int c_ = ln & 15, r_ = ln >> 4, e_ = jj & 7, t_ = (xcd & 1) * 4 + (jj >> 3);
          if constexpr (jb == 1 && gp == 0) {
            pn2 = ((kq >> 1) ? ws_im : ws_re) + (long)t_ * 256 * S_ + (kq & 1) * 1024;
            spb = (const char*)(pn2 + (e_ * 4 + p_) * 8 * S_);
            pins(pn2, spb);
          }
          if constexpr (jb == 1 && gp >= 1 && gp < 6) {
            const unsigned o = c_ == gp - 1 ? 0x3f803f80u : 0u;
            oh[gp - 1] = (u32x4){o, o, o, o};
            PIN1(oh[gp - 1]);
          }
          if constexpr (jb == 1 && gp == 6) {
            const int gm = moe ? (jj - (jj >> 3)) * 8 + xcd : 0;
            mat = gm / 112; cg = (gm % 112) >> 2; dq = gm & 3; d0 = dq * 256 + p_ * 64;
            pin4(mat, cg, dq, d0);
          }
          if constexpr (jb == 2 && gp == 0) {
            pgu = mat ? w_up : w_gate;
            pm2 = wsp + WS_ABT + ((long)e_ * 128 * 4096 + kq * 1024) * 2;
            pins(pgu, pm2);
            vw = ((d0 + r_ * 16) * F_ + cg * 128 + c_ * 8) * 2;
            PIN1(vw);
          }
          if constexpr (jb == 2 && (gp == 1 || gp == 2)) {
            dk[gp * 2 - 2] = c_ == r_ * 4 + gp * 2 - 2 ? ~0u : 0u; dk[gp * 2 - 1] = c_ == r_ * 4 + gp * 2 - 1 ? ~0u : 0u;
            PIN2(dk[gp * 2 - 2], dk[gp * 2 - 1]);
          }
          if constexpr (jb == 2 && gp == 3) { kl = lrow + (ln >> 5); go = (ln & 30) ? (int)0x80000000 : ((ln & 1) ? O_KEY : O_ST) + (int)kl * 8; PIN2(kl, go); }
          if constexpr (jb == 2 && gp == 4) { kl = 0xffff - kl; go = (ln & ~1) == 2 ? O_CE + (z * D_ + drow + (ln & 1)) * 8 : go; PIN2(kl, go); }
          if constexpr (jb == 2 && gp == 5) {
            gro = (p_ < 2 ? O_ST + (st * 128 + p_ * 64) * 8 : O_CE + (z * D_ + e_ * 128 + (p_ - 2) * 64) * 8) + ln * 8;
            tv2 = (p_ * 64 + ln) * 4096;
            lma = lds_addr(smem + LDS_MB) + (p_ < 2 ? p_ * 256 : 512 + p_ * 256) + ln * 4;
            PIN3(gro, tv2, lma);
          }
          if constexpr (jb == 7 && gp == 0) {
            const int r0 = p_ * 8 + (ln >> 3);
            vn2 = r0 * (S_ * 2) + ((ln & 7) ^ ((r0 >> 1) & 7)) * 16; vm2 = r0 * 8192 + (vm & 127);
            PIN2(vn2, vm2);
          }
          if constexpr (jb == 7 && gp == 1) { vdn = jj * 112 + p_ * 28 + r_ * 7; vgu = (((ln >> 3) & 1) * 4 * F_ + vdn + min(ln & 7, 6)) * 16; PIN2(vdn, vgu); }
          if constexpr (jb == 7 && gp == 2) { vdn = (vdn * D_ + cg8 * 128 + c_ * 8) * 2; PIN1(vdn); }
          if constexpr (jb == 7 && gp == 3) {
            abo = (((kq * 8 + e_) * 4 + p_) * 16 + c_) * AB_STRIDE * 4;
            abq[2] = ln >= 16 || (ln & 7) == t_ ? (int)0x80000000 : abo;
            PIN2(abo, abq[2]);
          }
          if constexpr (jb == 7 && gp == 4) {
            abq[1] = ln >= 8 ? (int)0x80000000 : abq[2];
            abq[0] = ((ln ^ t_) & 4) ? (int)0x80000000 : abq[1];
            PIN2(abq[0], abq[1]);
          }
          if constexpr (jb == 7 && gp == 5) {
            const unsigned mb = lds_addr(smem) + p_ * 1024;
            mq0 = mb + ((3 - kq) & 3) * 4096; mq1 = mb + ((4 - kq) & 3) * 4096; mq2 = mb + ((5 - kq) & 3) * 4096; mq3 = mb + ((6 - kq) & 3) * 4096;
            pin4(mq0, mq1, mq2, mq3);
          }
          if constexpr (jb == 8 && gp < 4) {
#pragma unroll
            for (int h = 0; h < 2; ++h) {
              const unsigned hv = h ? 0x0302u : 0x0100u;
              sel[h][gp] = ((ln & 7) >> 1) == gp ? ((ln & 1) ? (hv << 16) | 0x0c0cu : 0x0c0c0000u | hv) : 0x0c0c0c0cu;
            }
            PIN2(sel[0][gp], sel[1][gp]);
          }
        }
        SB();
      };
#define WS1(i) setup(IC<1>{}, IC<i>{})
#define WS2(i) setup(IC<2>{}, IC<i>{})
#define WS3(i) do { setup(IC<7>{}, IC<i>{}); if (i >= 4) setup(IC<8>{}, IC<(i) & 3>{}); } while (0)
      G1_STAGE_TILE(1, G1_STAGE, WS1);
      WAIT_VM(19); BAR();
      G1_STAGE_TILE(2, 2 * G1_STAGE, WS2);
      G1_STAGE_TILE(3, 3 * G1_STAGE, WS3);
      auto work1 = [&](auto JB, auto GP) __attribute__((always_inline)) {
        constexpr int jb = decltype(JB)::value, gp = decltype(GP)::value;
        SB();
        if constexpr (jb == 1) {
          const f32x4 z4 = {0.f, 0.f, 0.f, 0.f};
          if constexpr (gp == 0) { sa4 = MM(lv[0], oh[0], z4); gw[0] = MM(lv[0], lw[0], z4); gx[0] = MM(lv[0], lv[0], z4); PIN3(sa4, gw[0], gx[0]); }
          if constexpr (gp == 1) { sa4 = MM(lv2[0], oh[1], sa4); gw[1] = MM(lv2[0], lw[0], z4); gx[1] = MM(lv2[0], lv2[0], z4); PIN3(sa4, gw[1], gx[1]); }
          if constexpr (gp == 2) { sa4 = MM(lv[1], oh[0], sa4); gw[0] = MM(lv[1], lw[1], gw[0]); gx[0] = MM(lv[1], lv[1], gx[0]); PIN3(sa4, gw[0], gx[0]); }
          if constexpr (gp == 3) { sa4 = MM(lv2[1], oh[1], sa4); gw[1] = MM(lv2[1], lw[1], gw[1]); gx[1] = MM(lv2[1], lv2[1], gx[1]); PIN3(sa4, gw[1], gx[1]); }
          if constexpr (gp == 4) { sa4 = MM(ld[0], oh[2], sa4); sa4 = MM(ld[1], oh[2], sa4); PIN1(sa4); }
          if constexpr (gp == 5) { sa4 = MM(ld2[0], oh[3], sa4); sa4 = MM(ld2[1], oh[3], sa4); PIN1(sa4); }
          if constexpr (gp == 6) { sa4 = MM(lw[0], oh[4], sa4); sa4 = MM(lw[1], oh[4], sa4); PIN1(sa4);
            epoch = __builtin_amdgcn_readfirstlane(epv); tag = ((epoch + 1u) * 0x9E3779B1u ^ dmix) | 1u; }
        }
        if constexpr (jb == 2) {
          if constexpr (gp == 0) { su = (sa4[0] + sa4[1]) + (sa4[2] + sa4[3]); ta = diag(gw[0]); PIN2(su, ta); }
          if constexpr (gp == 1) { su = fold32(su, su); tb = diag(gw[1]); PIN2(su, tb); }
          if constexpr (gp == 2) { su = fold16(su, su); gs = fold32(ta, tb); PIN2(su, gs); }
          if constexpr (gp == 3) { ta = diag(gx[0]); tb = diag(gx[1]); PIN2(ta, tb); }
          if constexpr (gp == 4) { gs = fold16(gs, fold32(ta, tb)); PIN1(gs); }
          if constexpr (gp == 5) { gs += dpp_f<0xB1>(gs); gs += dpp_f<0x4E>(gs); PIN1(gs); }
          if constexpr (gp == 6) { gs += dpp_f<0x141>(gs); gs += dpp_f<0x140>(gs); PIN1(gs); }
        }
        if constexpr (jb == 3) {
          if constexpr (gp == 0) {
            const float s0 = rdl(su, 0), s1 = rdl(su, 1), q0 = rdl(gs, 16), q1 = rdl(gs, 48);
            sx = hb ? s1 : s0; ta = hb ? q1 : q0; PIN2(sx, ta);
          }
          if constexpr (gp == 1) {
            const float mean = sx * (1.0f / D_);
            mbv = rbf(mean);
            ta -= sx * mean;
            if (__builtin_expect(__any(!(sx * mean <= 16.f * ta)), 0)) {
              float q = fold32(sqdev16(lv, rdl(mbv, 0)), sqdev16(lv2, rdl(mbv, 32)));
              q = row_sum(fold16(q, q));
              const float dm = mean - mbv;
              ta = fmaxf(q - (float)D_ * dm * dm, 0.f);
            }
            PIN2(mbv, ta);
          }
          if constexpr (gp == 2) { ta = rbf(rbf(ta * (1.0f / D_)) + 1e-5f); gs -= mbv * rdl(su, 4); PIN2(ta, gs); }
          if constexpr (gp == 3) { ta = rbf(__builtin_amdgcn_sqrtf(ta)); tb = __builtin_amdgcn_rcpf(ta); PIN2(ta, tb); }
          if constexpr (gp == 4) {
            const unsigned rb = __float_as_uint(rbf(gs * tb));
            kl |= (rb ^ ((unsigned)((int)rb >> 31) | 0x80000000u)) & 0xffff0000u; PIN1(kl);
          }
          if constexpr (gp == 5) { kl = (lane & 2) ? __float_as_uint(su) : (lane & 1) ? kl : (__float_as_uint(mbv) >> 16) | __float_as_uint(ta); PIN1(kl); }
        }
        if constexpr (jb == ST_JB + 3) {
          if constexpr (gp == 0) { if (!__all(gr.y == tag)) gr = granule_wait((const u64*)((const char*)(flags + FL_AB) + gro), tag); PIN1(gr); }
          if constexpr (gp == 1) { g1 = (z ? -1.0f : 1.0f) / hi_bf(gr.x); PIN1(g1); }
          if constexpr (gp == 2) { g0 = pw < 2 ? -(lo_bf(gr.x) * g1) : __uint_as_float(gr.x); PIN1(g0); }
        }
        if constexpr (gp < 7 && (jb - 1) * 7 + gp < 64) {
          constexpr int n = (jb - 1) * 7 + gp;
          sld<((n >> 1) & 7) * 4096 + (n & 1) * 64>(sd, spb, KROT(n >> 4));
        }
        SB();
      };
      auto step1 = [&](auto JB) __attribute__((always_inline)) {
        constexpr int jb = decltype(JB)::value;
        if constexpr (jb == 1) PAGE(1);
        if constexpr (jb == 7) PAGE(2);
        constexpr int n16 = jb == 12 ? 12 : jb == 11 ? 14 : 16;
        constexpr auto wl = [](int q) { return (q == 3 ? 1 : 0) + (q == ST_JB ? 1 : 0); };
        if constexpr (jb == 1) {
          asm volatile("s_waitcnt vmcnt(%11)" : "+v"(lv[0]), "+v"(lv[1]), "+v"(lw[0]), "+v"(lw[1]), "+v"(ld[0]), "+v"(ld[1]),
                       "+v"(lv2[0]), "+v"(lv2[1]), "+v"(ld2[0]), "+v"(ld2[1]), "+v"(epv) : "i"(16) : "memory");
        } else if constexpr (jb == ST_JB + 3) {
          asm volatile("s_waitcnt vmcnt(%1)" : "+v"(gr) : "i"(n16 + wl(jb - 2) + wl(jb - 1)) : "memory");
        } else wait_vm<n16 + wl(jb - 3) + wl(jb - 2) + wl(jb - 1)>();
        BAR();
        if constexpr (jb == 2) { if (pw == 0 && lane < 16) ((int*)(smem + LDS_SEL))[lane < 8 ? lane : lane + 8] = 0; }
        if constexpr (jb == 4) { if (pw == 0 && lane == 0) ((unsigned*)(smem + LDS_SEL))[5] = tag; }
#define W1(i) work1(JB, IC<i>{})
        if constexpr (jb < 10) G1_STAGE_TILE(jb - 1 + G1_NST, ((jb - 1) % G1_NST) * G1_STAGE, W1);
        else G1_STAGE_A(jb - 1 + G1_NST, ((jb - 1) % G1_NST) * G1_STAGE, W1);
        if constexpr (jb == 12) { G1_STAGE_B(13, G1_STAGE); G1_STAGE_B(14, 2 * G1_STAGE); }
        if constexpr (jb == 3) __builtin_amdgcn_raw_buffer_store_b64((u32x2){kl, tag}, rf, go, 0, 16);
        if constexpr (jb == ST_JB) {
          asm volatile("buffer_load_dwordx2 %0, %1, %2, 0 offen sc1" : "=v"(gr) : "v"(gro), "s"(rf) : "memory");
        }
        touches(IC<100 + jb>{});
        if constexpr (jb <= 8) {
          if (et == jb - 1) {
#pragma unroll
            for (int l = 0; l < WSN; ++l) bld16_nt_io(wv[l], rw, vw, l * F_ * 2);
          }
        }
        SB();
      };
      static_assert(ST_JB + 3 <= 12 && ST_JB >= 6, "schedule");
      step1(IC<1>{}); step1(IC<2>{}); step1(IC<3>{}); step1(IC<4>{}); step1(IC<5>{}); step1(IC<6>{}); step1(IC<7>{}); step1(IC<8>{}); step1(IC<9>{}); step1(IC<10>{}); step1(IC<11>{}); step1(IC<12>{});
    }
#define G2_STAGE_N(kk_, W) do { const int kb = KROT(kk_); constexpr int so = n_off(kk_); SB(); \
    DMA_(rn2, vn2, mq0, so, kb, 0, ""); W(0); DMA_(rn2, vn2, mq1, so, kb, 32 * S_ * 2, ""); W(1); \
    DMA_(rn2, vn2, mq2, so, kb, 64 * S_ * 2, ""); W(2); DMA_(rn2, vn2, mq3, so, kb, 96 * S_ * 2, ""); W(3); \
    DMA_(rn2, vn2, mq0, so + 16384, kb, 128 * S_ * 2, ""); W(4); DMA_(rn2, vn2, mq1, so + 16384, kb, 160 * S_ * 2, ""); W(5); \
    DMA_(rn2, vn2, mq2, so + 16384, kb, 192 * S_ * 2, ""); W(6); DMA_(rn2, vn2, mq3, so + 16384, kb, 224 * S_ * 2, ""); W(7); SB(); } while (0)
#define G2_STAGE_NH(kk_, h) do { const int kb = KROT(kk_); constexpr int so = n_off(kk_) + (h) * 16384; SB(); \
    DMA_(rn2, vn2, mq0, so, kb, (h) * 128 * S_ * 2, ""); DMA_(rn2, vn2, mq1, so, kb, ((h) * 128 + 32) * S_ * 2, ""); \
    DMA_(rn2, vn2, mq2, so, kb, ((h) * 128 + 64) * S_ * 2, ""); DMA_(rn2, vn2, mq3, so, kb, ((h) * 128 + 96) * S_ * 2, ""); SB(); } while (0)
#define G2_STAGE_M(kk_, W) do { const int kb = KROT(kk_); constexpr int so = G2_MRING + m_off(kk_); SB(); \
    DMA_(rm2, vm2, mb0, so, kb, 0, " sc1"); W(8); DMA_(rm2, vm2, mb0, so + 4096, kb, 32 * 8192, " sc1"); W(9); \
    DMA_(rm2, vm2, mb0, so + 8192, kb, 64 * 8192, " sc1"); W(10); DMA_(rm2, vm2, mb0, so + 12288, kb, 96 * 8192, " sc1"); W(11); SB(); } while (0)
    WAIT_VM(16); BAR();
    asm volatile("ds_write_b32 %0, %1" :: "v"(lma), "v"(g0) : "memory");
    if (pw < 2) asm volatile("ds_write_b32 %0, %1 offset:512" :: "v"(lma), "v"(g1) : "memory");
    u32x4 tv[4];
    unsigned ready = 0;
    auto need = [&](int k) __attribute__((always_inline)) {
      const int b = k_blk(k) + (k_ord(k) & 1) * 8;
#pragma unroll 1
      for (int it = 0; !((ready >> b) & 1) && it < SPIN_MAX; ++it) {
        ready |= (unsigned)__ballot(__builtin_amdgcn_raw_buffer_load_b32(rf, abq[k < 5 ? 0 : k < 9 ? 1 : 2], 0, 16) == tag);
        asm volatile("" ::: "memory");
      }
    };
    unsigned ksel, bad = 0;
    int isel;
    unsigned xw;
    auto bvec = [&](unsigned src, const unsigned (&sl)[4]) __attribute__((always_inline)) {
      return (u32x4){__builtin_amdgcn_perm(0, src, sl[0]), __builtin_amdgcn_perm(0, src, sl[1]),
                     __builtin_amdgcn_perm(0, src, sl[2]), __builtin_amdgcn_perm(0, src, sl[3])};
    };
#define BC8(v, p) (unsigned)__builtin_amdgcn_update_dpp(__builtin_amdgcn_update_dpp(0, (int)(v), 0x150 + (p), 0xf, 0x3, false), (int)(v), 0x158 + (p), 0xf, 0xc, false)
    float* lg = (float*)(smem + LDS_GU);
    f32x4 macc = {0.f, 0.f, 0.f, 0.f};
    u32x2 sv;
    unsigned hp, gbad;
    float s0, s1;
    auto work2 = [&](auto JB, auto GP) __attribute__((always_inline)) {
      constexpr int jb = decltype(JB)::value, gp = decltype(GP)::value;
      SB();
      if constexpr (jb == -1 && gp < 4) {
        constexpr int c = gp == 0 ? 0xB1 : gp == 1 ? 0x4E : gp == 2 ? 0x141 : 0x140;
        merge2(k1, k2, dpp_u<c>(k1), dpp_u<c>(k2));
        PIN2(k1, k2);
      }
      if constexpr (jb == -1 && gp == 4) {
        ksel = tk ? k2 : k1;
        isel = (0xffff - (int)(ksel & 0xffffu)) & (S_ - 1);
        k1 = (isel * D_ + d0 + rq * 16 + (lane & 7) * 2) * 2; k2 = O_ST + isel * 8;
        PIN1(ksel);
        xw = gld4_s(x, k1);
        sv = bld8_sc1(rf, k2);
      }
      if constexpr (jb == 0 && gp == 1) wait_xs<3 + 1 + 2>(xw, sv);
      if constexpr (jb == 0) {
        if constexpr (gp == 1) { if (!__all(sv.y == tag)) sv = granule_wait(stat + isel, tag); s0 = __builtin_amdgcn_rcpf(hi_bf(sv.x)); PIN1(s0); }
        if constexpr (gp == 2) { s1 = ln_elem(lo_bf(xw), lo_bf(sv.x), hi_bf(sv.x), s0); PIN1(s1); }
        if constexpr (gp == 3) { xw = pk2(s1, ln_elem(hi_bf(xw), lo_bf(sv.x), hi_bf(sv.x), s0)); PIN1(xw); }
      }
      if constexpr (jb == DG && gp < 7) { PIN1(dv[gp]); macc = MM(dv[gp], bvec(BC8(hp, gp), sel[0]), macc); PIN1(macc); }
      if constexpr ((jb == 0 && gp >= 4) || jb == 1) {
        constexpr int l = jb == 0 ? gp - 4 : gp + 4;
        if constexpr (!(l & 1)) hp = BC8(xw, l >> 1);
        PIN1(wv[l]);
        macc = MM(wv[l], bvec(hp, sel[l & 1]), macc);
        PIN2(macc, hp);
      }
      if constexpr (jb == GV_JB + 2) {
        if constexpr (gp == 0) { gbad = (gv[0].y ^ tag) | (gv[0].w ^ tag) | (gv[1].y ^ tag) | (gv[1].w ^ tag); PIN1(gbad); }
        if constexpr (gp == 1) { gbad |= (gv[2].y ^ tag) | (gv[2].w ^ tag) | (gv[3].y ^ tag) | (gv[3].w ^ tag); PIN1(gbad); }
        if constexpr (gp == 2) {
          s0 = ((__uint_as_float(gv[0].x) + __uint_as_float(gv[1].x)) + __uint_as_float(gv[2].x)) + __uint_as_float(gv[3].x);
          s1 = ((__uint_as_float(gv[0].z) + __uint_as_float(gv[1].z)) + __uint_as_float(gv[2].z)) + __uint_as_float(gv[3].z);
          PIN2(s0, s1);
        }
        if constexpr (gp == 3) {
#pragma unroll 1
          for (int it = 0; !__all(gbad == 0) && it < SPIN_MAX; ++it) {
            u32x4 q[4];
            gbad = 0;
#pragma unroll
            for (int i = 0; i < 4; ++i) { q[i] = __builtin_amdgcn_raw_buffer_load_b128(rgu, vgu, i * F_ * 16, 16); gbad |= (q[i].y ^ tag) | (q[i].w ^ tag); }
            s0 = ((__uint_as_float(q[0].x) + __uint_as_float(q[1].x)) + __uint_as_float(q[2].x)) + __uint_as_float(q[3].x);
            s1 = ((__uint_as_float(q[0].z) + __uint_as_float(q[1].z)) + __uint_as_float(q[2].z)) + __uint_as_float(q[3].z);
            asm volatile("" ::: "memory");
          }
          const unsigned gp2 = pk2(s0, s1);
          const unsigned up2 = dpp_u<0x128>(gp2);
          s0 = tk ? hi_bf(up2) : lo_bf(gp2); s1 = tk ? hi_bf(gp2) : lo_bf(up2);
          PIN2(s0, s1);
        }
        if constexpr (gp == 4) { s0 = rbf(s0 * __builtin_amdgcn_rcpf(1.f + __expf(-s0))); PIN1(s0); }
        if constexpr (gp == 5) { hp = pk2(s0 * s1, 0.f); PIN1(hp); }
      }
      if constexpr (jb == 10 && gp == 2) epi_setup();
      if constexpr (jb == 10 && gp == 4) code_touch(sd, (j * 4 + pw) * 256);
      SB();
    };
#define W2M(i) work2(IC<-1>{}, IC<i>{})
#define W20(i) work2(IC<0>{}, IC<i>{})
#define W21(i) work2(IC<1>{}, IC<i>{})
    unsigned fs;
    {
      G1_STAGE_B(15, 3 * G1_STAGE);
      G2_STAGE_NH(0, 0);
      lgkm0(sd);
      WAIT_VM(16); BAR();
      G2_STAGE_NH(0, 1);
      WAIT_VM(10); BAR();
#pragma unroll
      for (int i = 0; i < 4; ++i) kk[i] = bld16_sc1(rf, l16, ko_ + i * 1024);
#pragma unroll
      for (int l = WSN; l < 16; ++l) wv[l] = bld16_nt(rw, vw, l * F_ * 2);
      asm volatile("s_waitcnt vmcnt(%4)" : "+v"(kk[0]), "+v"(kk[1]), "+v"(kk[2]), "+v"(kk[3]) : "i"(16 - WSN) : "memory");
      touches(IC<115>{});
#pragma unroll
      for (int i = 0; i < 4; ++i) {
        k2 = max(k2, min(k1, kk[i].x)); k1 = max(k1, kk[i].x);
        k2 = max(k2, min(k1, kk[i].z)); k1 = max(k1, kk[i].z);
        bad |= (kk[i].y ^ tag) | (kk[i].w ^ tag);
      }
#pragma unroll 1
      for (int it = 0; !__all(bad == 0) && it < SPIN_MAX; ++it) {
        k1 = 0; k2 = 0; bad = 0;
#pragma unroll
        for (int i = 0; i < 4; ++i) {
          const u32x4 q = __builtin_amdgcn_raw_buffer_load_b128(rf, l16, ko_ + i * 1024, 16);
          k2 = max(k2, min(k1, q.x)); k1 = max(k1, q.x);
          k2 = max(k2, min(k1, q.z)); k1 = max(k1, q.z);
          bad |= (q.y ^ tag) | (q.w ^ tag);
        }
        asm volatile("" ::: "memory");
      }
      merge2(k1, k2, dpp_u<0xB1>(k1), dpp_u<0xB1>(k2));
      merge2(k1, k2, dpp_u<0x4E>(k1), dpp_u<0x4E>(k2));
      merge2(k1, k2, dpp_u<0x141>(k1), dpp_u<0x141>(k2));
      merge2(k1, k2, dpp_u<0x140>(k1), dpp_u<0x140>(k2));
      if ((lane & 15) == 0) *(u32x2*)(smem + LDS_KEY + (pw * 4 + rq) * 8) = (u32x2){k1, k2};
      PAGE(3);
      BAR_LGKM();
#pragma unroll
      for (int jj = 0; jj < 4; ++jj) tv[jj] = *(const u32x4*)(smem + G2_MRING + m_off(0) + (jj * 4 + pw) * 1024 + lane * 16);
      { const u32x2 kp = *(const u32x2*)(smem + LDS_KEY + ci * 8); k1 = kp.x; k2 = kp.y; }
#pragma unroll
      for (int jj = 0; jj < 4; ++jj) __builtin_amdgcn_raw_buffer_store_b128(tv[jj], rm2, vm2 + jj * 32 * 8192 + 2 * tt * 128, 0, 16);
      G2_STAGE_N(1, W2M);
      WAIT_VM(10);
      if (lane == tt) __builtin_amdgcn_raw_buffer_store_b32(tag, rf, abo, 0, 16);
      G2_STAGE_N(2, W20);
      __builtin_amdgcn_s_sleep(2);
      WAIT_VM(9);
      fs = bld4_sc1(rf, abq[0]);
    }
    BAR();
#pragma unroll
    for (int jj = 0; jj < 4; ++jj) tv[jj] = *(const u32x4*)(smem + G2_MRING + m_off(1) + (jj * 4 + pw) * 1024 + lane * 16);
    G2_STAGE_N(3, NOW);
    asm volatile("s_waitcnt vmcnt(8)" : "+v"(fs) :: "memory");
    ready = (unsigned)__ballot(fs == tag);
    need(2);
    G2_STAGE_M(2, NOW);
    need(3);
    G2_STAGE_M(3, NOW);
    fs = bld4_sc1(rf, abq[1]);
    touches(IC<1>{});
    W21(0); W21(1); W21(2); W21(3); W21(4); W21(5); W21(6); W21(7); W21(8); W21(9); W21(10); W21(11);
    asm volatile("" : "+v"(tv[0]), "+v"(tv[1]), "+v"(tv[2]), "+v"(tv[3]));
    auto step2 = [&](auto JB) __attribute__((always_inline)) {
      constexpr int jb = decltype(JB)::value;
      if constexpr (jb == 3) PAGE(4);
      if constexpr (jb == 9) PAGE(5);
      constexpr auto must = [](int q) { return (q == 2 ? 4 : 0) + ((FSM >> q) & 1) + ((q == GV_JB || q == GV_JB - 1) ? 2 : 0); };
      if constexpr (jb == 2) wait_vm<4 + must(1)>();
      else wait_vm<dn_cnt(jb - 2) + (jb == 5 ? 1 : 0) + 12 + must(jb - 1) + dn_cnt(jb - 1)>();
      if constexpr (jb == 3) { if (pw == 0 && (lane & ~8) == 0) ((unsigned*)(smem + LDS_SEL))[tk] = ksel; }
      if constexpr ((jb == 2 || jb == DG + 1)) {
        *(f32x4*)(lg + pw * 256 + lane * 4) = macc;
        macc = (f32x4){0.f, 0.f, 0.f, 0.f};
        lgkm0(sd);
      }
      BAR();
      float p0[4], p1[4];
      const int col = (pw & 1) * 64 + lane;
      if constexpr (jb == 3) {
        if (g == 0 && pw == 0 && lane == 0) __hip_atomic_store((unsigned*)flags + FL_EPOCH, epoch + 1u, __ATOMIC_RELAXED, __HIP_MEMORY_SCOPE_AGENT);
      }
      if constexpr (jb == DG + 1) {
        const float* p = lg + (pw & 1) * 32 + (lane >> 4) * 64 + ((2 * lane) & 7) * 4 + ((lane >> 2) & 3);
#pragma unroll
        for (int i = 0; i < 4; ++i) { p0[i] = p[i * 256]; p1[i] = p[i * 256 + 4]; }
      }
      if constexpr (jb == 2) {
        const float* p = lg + (col >> 5) * 64 + (col & 7) * 4 + ((col >> 3) & 3);
#pragma unroll
        for (int i = 0; i < 4; ++i) { p0[i] = p[i * 256]; p1[i] = p[i * 256 + 32]; }
      }
      if constexpr (jb == 4) {
        if (lane == 8 + tt) __builtin_amdgcn_raw_buffer_store_b32(tag, rf, abo, 0, 16);
      }
      if constexpr ((FSM >> (jb - 2)) & 1) {
        asm volatile("" : "+v"(fs));
        ready |= (unsigned)__ballot(fs == tag);
      }
      if constexpr (jb == GV_JB + 2) asm volatile("" : "+v"(gv[0]), "+v"(gv[1]), "+v"(gv[2]), "+v"(gv[3]));
#define W2(i) work2(JB, IC<i>{})
      G2_STAGE_N(jb + 2, W2);
      need(jb + 2);
      G2_STAGE_M(jb + 2, W2);
      if constexpr (jb == 2) {
#pragma unroll
        for (int jj = 0; jj < 4; ++jj) __builtin_amdgcn_raw_buffer_store_b128(tv[jj], rm2, vm2 + jj * 32 * 8192 + (2 * tt + 1) * 128, 0, 16);
      }
      if constexpr ((FSM >> jb) & 1) fs = bld4_sc1(rf, abq[jb < 5 ? 1 : 2]);
      if constexpr ((jb == GV_JB || jb == GV_JB - 1)) {
#pragma unroll
        for (int i = (jb == GV_JB ? 2 : 0); i < (jb == GV_JB ? 4 : 2); ++i) gv[i] = bld16_sc1(rgu, vgu, i * F_ * 16);
      }
      touches(JB);
#pragma unroll
      for (int i = dn_first(jb); i < dn_first(jb) + dn_cnt(jb); ++i) dv[i] = bld16_nt(rdn, vdn, i * D_ * 2);
      if constexpr (jb == DG + 1) {
        if (pw < 2)
          __builtin_amdgcn_raw_buffer_store_b128((u32x4){__float_as_uint((p0[0] + p0[1]) + (p0[2] + p0[3])), tag, __float_as_uint((p1[0] + p1[1]) + (p1[2] + p1[3])), tag},
                                                 mk_rsrc(wsp + WS_DN), (((pw * 8 + cg8) * 32 + rgb) * 64 + lane) * 16, 0, 16);
      }
      if constexpr (jb == 2) {
        if (moe && pw < 2)
          __builtin_amdgcn_raw_buffer_store_b128((u32x4){__float_as_uint((p0[0] + p0[1]) + (p0[2] + p0[3])), tag, __float_as_uint((p1[0] + p1[1]) + (p1[2] + p1[3])), tag},
                                                 rgu, ((mat * 4 + dq) * F_ + cg * 128 + col) * 16, 0, 16);
      }
      SB();
    };
    static_assert(DG + 1 <= 12 && DG >= GV_JB + 3 && dn_first(DG - 2) == 7, "schedule");
    step2(IC<2>{}); step2(IC<3>{}); step2(IC<4>{}); step2(IC<5>{}); step2(IC<6>{}); step2(IC<7>{}); step2(IC<8>{}); step2(IC<9>{}); step2(IC<10>{}); step2(IC<11>{}); step2(IC<12>{}); step2(IC<13>{});
    lgkm0(sd);
    WAIT_VM(12); BAR();
    xq[0] = ld16_s<0>(x, xo); xq[1] = ld16_s<64>(x, xo); sq = ld8_s_sc1(stat, xo >> 11 << 3);
    WAIT_VM(3); BAR();
    WAIT_VM(0);
    __builtin_amdgcn_s_setprio(0);
    asm volatile("" : "+v"(xq[0]), "+v"(xq[1]), "+v"(sq) :: "memory");
    key[0] = __builtin_amdgcn_readlane(ksel, 0); key[1] = __builtin_amdgcn_readlane(ksel, 8);
    nf_pre();
#pragma unroll
    for (int n = 0; n < 56; ++n) nf_step(n);
#pragma unroll
    for (int e = 0; e < 16; ++e) nf_unp(e);
#pragma unroll 1
    for (int c = 0; c < 2; ++c) {
      const int ic = (0xffff - (int)((c ? key[1] : key[0]) & 0xffffu)) & (S_ - 1);
      if (__builtin_expect((ic >> 8) != tt || ((ic >> 5) & 3) != kq, 1)) continue;
      const __amdgpu_buffer_rsrc_t rd = mk_rsrc(wsp + WS_DN + (size_t)((c * 8 + et) * 32 + pw * 8) * 1024);
#pragma unroll 1
      for (int it = 0; it < SPIN_MAX; ++it) {
        if (__all(__builtin_amdgcn_raw_buffer_load_b32(rd, (lane & 7) * 1024 + 1020, 0, 16) == tag)) break;
        __builtin_amdgcn_s_sleep(8);
        asm volatile("" ::: "memory");
      }
      float s0, s1;
      unsigned bad = 1;
#pragma unroll 1
      for (int it = 0; !__all(bad == 0) && it < SPIN_MAX; ++it) {
        bad = 0; s0 = 0.f; s1 = 0.f;
#pragma unroll
        for (int i = 0; i < 8; ++i) {
          const u32x4 q = __builtin_amdgcn_raw_buffer_load_b128(rd, lane * 16, i * 1024, 16);
          bad |= (q.y ^ tag) | (q.w ^ tag); s0 += __uint_as_float(q.x); s1 += __uint_as_float(q.z);
        }
        asm volatile("" ::: "memory");
      }
      *(f32x2*)(smem + LDS_FIN + (c * 4 + pw) * 512 + lane * 8) = (f32x2){s0, s1};
      asm volatile("s_waitcnt lgkmcnt(0)" ::: "memory");
      if (lane == 0) asm volatile("ds_add_u32 %0, %1" :: "v"(lds_addr(smem + LDS_SEL + 24 + c * 4)), "v"(1) : "memory");
    }
  } else {
  {
    const int q = j * 4 + wid;
    constexpr int Q1 = WS_MB, Q2 = Q1 + 8, Q3 = Q2 + 8, Q4 = Q3 + 7, Q5 = Q4 + 7, Q6 = Q5 + 7, Q7 = Q6 + 4, Q8 = Q7 + 4, QC = Q8 + 1;
    static_assert(QC <= 128, "warm-up slots");
    unsigned dmy = 0;
    if (q < QC) {
      const char* base; int b0;
      if (q < Q1) { base = wsp; b0 = 0; }
      else if (q < Q2) { base = (const char*)ws_re; b0 = Q1; }
      else if (q < Q3) { base = (const char*)ws_im; b0 = Q2; }
      else if (q < Q4) { base = (const char*)w_gate; b0 = Q3; }
      else if (q < Q5) { base = (const char*)w_up; b0 = Q4; }
      else if (q < Q6) { base = (const char*)w_down; b0 = Q5; }
      else if (q < Q7) { base = (const char*)out; b0 = Q6; }
      else if (q < Q8) { base = (const char*)x; b0 = Q7; }
      else { base = (const char*)w_r; b0 = Q8; }
      base += (size_t)(q - b0) << 20;
      asm volatile("s_load_dword %0, %1, 0" : "+s"(dmy) : "s"(base) : "memory");
    }
    if (wid == 0) { CALL_PG(1); CALL_PG(8); CALL_PG(5); }
    else if (wid == 1) { CALL_PG(2); CALL_PG(9); CALL_PG(6); }
    else if (wid == 2) { CALL_PG(3); CALL_PG(10); }
    else { CALL_PG(4); CALL_PG(11); }
    asm volatile("s_barrier\n\ts_waitcnt lgkmcnt(0)" : "+s"(dmy) :: "memory");
  }
  PAGE(8);
  epi_setup();
  const int wr = wid >> 1, wc = wid & 1;
  const int sw = (fr >> 1) & 7;
  const int ko0 = ((0 + fq) ^ sw) * 16, ko1 = ((4 + fq) ^ sw) * 16;
  const int mrow = (fr >> 2) * 8 + (fr & 3);
  bf16x8 fm[2][4], fn[2][4];
  {
    const char* pm = smem + (wr * 64 + mrow) * 128;
    const char* pn = smem + 16384 + (wc * 64 + mrow) * 128;
    f32x4 acc[4][4];
#pragma unroll
    for (int i = 0; i < 4; ++i)
#pragma unroll
      for (int jn = 0; jn < 4; ++jn)
#pragma unroll
        for (int q = 0; q < 4; ++q) acc[i][jn][q] = 0.f;
#define TO(i) (((i) >> 1) * 4096 + ((i) & 1) * 512)
#define LDSP(T, a) ((__attribute__((address_space(3))) T*)(size_t)(a))
#define SLOTS(n) _Pragma("unroll") for (int q = 0; q < (n); ++q)
#define MFA(c, a, b) asm volatile("v_mfma_f32_16x16x32_bf16 %0, %1, %2, %0" : "+v"(c) : "v"(a), "v"(b))
#define MFA2(c, a, b, a2, b2) asm volatile("v_mfma_f32_16x16x32_bf16 %0, %1, %2, %0\n\tv_mfma_f32_16x16x32_bf16 %0, %3, %4, %0" : "+v"(c) : "v"(a), "v"(b), "v"(a2), "v"(b2))
    unsigned lm[2][2], ln[2][2];
#pragma unroll
    for (int u = 0; u < 2; ++u)
#pragma unroll
      for (int kk = 0; kk < 2; ++kk) {
        lm[u][kk] = lds_addr(pm) + u * 2 * G1_STAGE + (kk ? ko1 : ko0);
        ln[u][kk] = lds_addr(pn) + u * 2 * G1_STAGE + (kk ? ko1 : ko0);
        asm volatile("" : "+v"(lm[u][kk]), "+v"(ln[u][kk]));
      }
    auto ldm = [&](int stg, int i, int kk) __attribute__((always_inline)) { fm[kk][i] = *LDSP(const bf16x8, lm[stg >> 1][kk] + (stg & 1) * G1_STAGE + TO(i)); };
    auto ldn = [&](int stg, int jn, int kk) __attribute__((always_inline)) { fn[kk][jn] = *LDSP(const bf16x8, ln[stg >> 1][kk] + (stg & 1) * G1_STAGE + TO(jn)); };
    auto quad = [&](int a, int c, int d, int s) __attribute__((always_inline)) {
      const int i = (s < 2 || s >= 6) ? a : a + 1, jn = s < 4 ? c : d, kk = (s == 1 || s == 2 || s == 5 || s == 6);
      MFA(acc[i][jn], fm[kk][i], fn[kk][jn]);
    };
    auto quad2 = [&](int a, int c, int d, int s) __attribute__((always_inline)) {
      const int i = (s < 2 || s >= 6) ? a : a + 1, jn = s < 4 ? c : d, kk = (s == 2 || s == 6);
      MFA2(acc[i][jn], fm[kk][i], fn[kk][jn], fm[1 - kk][i], fn[1 - kk][jn]);
    };
    auto half1 = [&](int stg) __attribute__((always_inline)) {
      const int ja = (stg & 1) * 2, jb = 2 - ja;
      SLOTS(8) {
        quad(0, ja, ja + 1, q);
        if (q < 4) ldn(stg, jb + (q >> 1), q & 1); else ldm(stg, 2 + ((q - 4) >> 1), q == 5 || q == 6);
        SB();
      }
      SLOTS(4) { quad2(0, jb, jb + 1, 2 * q); SB(); }
    };
    auto half2 = [&](int stg) __attribute__((always_inline)) {
      const int ja = (stg & 1) * 2, jb = 2 - ja, nx = (stg + 1) & 3;
      SLOTS(8) {
        quad(2, jb + 1, jb, q);
        if (!(q & 1)) ldm(nx, q >> 2, q == 2 || q == 4);
        SB();
      }
      SLOTS(8) {
        quad(2, ja, ja + 1, q);
        if (!(q & 1)) ldn(nx, jb + (q >> 2), (q >> 1) & 1);
        SB();
      }
    };
    SLOTS(16) asm volatile("" : "+v"(acc[q >> 2][q & 3]));
    SB();
    SLOTS(4) { ldm(0, q >> 1, q & 1); ldn(0, q >> 1, q & 1); }
    SB();
#pragma unroll 1
    for (int k4 = 0; k4 < G1_NKT - 4; k4 += 4) {
      half1(0); BAR_LGKM();
      SB(); half2(0);
      half1(1); BAR_LGKM();
      SB(); half2(1);
      half1(2); BAR_LGKM();
      SB(); half2(2);
      half1(3); BAR_LGKM();
      SB(); half2(3);
    }
    bf16x8 gm[2][4], gn[2][8];
    unsigned am[2][2], an[2][2];
#pragma unroll
    for (int u = 0; u < 2; ++u)
#pragma unroll
      for (int kk = 0; kk < 2; ++kk) {
        am[u][kk] = lds_addr(pm) + (u ? 3 : 1) * G1_STAGE + (kk ? ko1 : ko0);
        an[u][kk] = lds_addr(pn) + (u ? 3 : 1) * G1_STAGE + (kk ? ko1 : ko0);
        asm volatile("" : "+v"(am[u][kk]), "+v"(an[u][kk]));
      }
    unsigned al = lds_addr(smem + LDS_MB) + (wr * 32 + fq * 8) * 4, ac = lds_addr(smem + LDS_MB) + (256 + wc * 64 + mrow) * 4;
    unsigned ae = lds_addr(smem + G2_MRING) + (wc * 64 + mrow) * 128 + (((wr * 4 + fq) ^ sw) * 16);
    asm volatile("" : "+v"(al), "+v"(ac), "+v"(ae));
    auto rdg = [&](int set, int t, int h, int r) __attribute__((always_inline)) {
      const int u = t == 15, o = t == 14 ? G1_STAGE : 0;
      if (r == 0 || r == 2 || r == 4 || r == 5) {
        const int i = r >= 4, kk = r == 2 || r == 4;
        gm[set][kk * 2 + i] = *LDSP(const bf16x8, am[u][kk] + o + h * 4096 + i * 512);
      } else {
        const int jn = r < 4 ? 0 : (r - 4) >> 1, kk = r < 4 ? r >> 1 : r & 1;
        gn[set][kk * 4 + jn] = *LDSP(const bf16x8, an[u][kk] + o + TO(jn));
      }
    };
    auto mmg = [&](int set, int h, int q) __attribute__((always_inline)) {
      const int jn = q >> 2, r = q & 3, i = (r >> 1) ^ (jn & 1), kk = r == 1 || r == 2;
      MFA(acc[h * 2 + i][jn], gm[set][kk * 2 + i], gn[set][kk * 4 + jn]);
    };
    f32x4 rsv[2], mrv[2];
    float cev[4], tc[32];
    auto rds = [&](int h, int r) __attribute__((always_inline)) {
      if (r < 2) mrv[r] = *LDSP(const f32x4, al + h * 256 + r * 16); else rsv[r - 2] = *LDSP(const f32x4, al + 512 + h * 256 + (r - 2) * 16);
    };
    auto prep = [&](int n) __attribute__((always_inline)) {
      tc[n] = llvm_fmul_legacy(mrv[(n >> 2) & 1][n & 3], cev[n >> 3]);
      asm volatile("" : "+v"(tc[n]));
    };
    u32x4 ep;
    auto epi = [&](int h, int q) __attribute__((always_inline)) {
      const int jn = q >> 1, i = q & 1;
      asm volatile("v_fmac_f32 %0, %4, %8\n\tv_fmac_f32 %1, %5, %9\n\tv_fmac_f32 %2, %6, %10\n\tv_fmac_f32 %3, %7, %11"
                   : "+v"(tc[q * 4]), "+v"(tc[q * 4 + 1]), "+v"(tc[q * 4 + 2]), "+v"(tc[q * 4 + 3])
                   : "v"(acc[h * 2 + i][jn][0]), "v"(acc[h * 2 + i][jn][1]), "v"(acc[h * 2 + i][jn][2]), "v"(acc[h * 2 + i][jn][3]),
                     "v"(rsv[i][0]), "v"(rsv[i][1]), "v"(rsv[i][2]), "v"(rsv[i][3]));
      ep[i * 2] = pk2(tc[q * 4], tc[q * 4 + 1]); ep[i * 2 + 1] = pk2(tc[q * 4 + 2], tc[q * 4 + 3]);
      if (i) *LDSP(u32x4, ae + m_off(h) + TO(jn)) = ep;
    };
    SB();
    half1(0);
    BAR_LGKM();
    SB();
    SLOTS(16) {
      if (q < 8) quad(2, 3, 2, q); else quad(2, 0, 1, q - 8);
      if (q < 12) rdg(0, 13, 0, q);
      SB();
    }
    BAR();
    SB();
    SLOTS(16) {
      mmg(0, 0, q);
      if (q < 12) rdg(1, 14, 0, q);
      else { rds(0, q - 12); cev[q - 12] = *LDSP(const float, ac + (((q - 12) >> 1) * 32 + ((q - 12) & 1) * 4) * 4); }
      SB();
    }
    PAGE(9);
    BAR();
    SB();
    SLOTS(16) { mmg(1, 0, q); if ((q & 3) < 3) rdg(0, 15, 0, (q >> 2) * 3 + (q & 3)); if (q >= 8) { prep(2 * q - 16); prep(2 * q - 15); } SB(); }
    SLOTS(16) { mmg(0, 0, q); if ((q & 3) < 3) rdg(1, 13, 1, (q >> 2) * 3 + (q & 3)); prep(16 + q); SB(); if (q >= 9 && (q & 1)) { epi(0, (q - 9) >> 1); SB(); } }
    SLOTS(8) { mmg(1, 1, q); if (q < 6) { rdg(0, 14, 1, 2 * q); rdg(0, 14, 1, 2 * q + 1); } SB(); if (q & 1) { epi(0, 4 + (q >> 1)); SB(); } }
    BAR_LGKM();
    SB();
    SLOTS(8) { mmg(1, 1, 8 + q); if (q < 4) rdg(1, 15, 1, q == 0 ? 1 : q == 1 ? 3 : q + 4); else rds(1, q - 4); SB(); }
    SLOTS(16) { mmg(0, 1, q); if (q < 8) rdg(1, 15, 1, q == 0 ? 0 : q == 1 ? 2 : q < 4 ? q + 2 : q + 4); prep(q); SB(); }
    asm volatile("s_waitcnt lgkmcnt(0)" ::: "memory");
    if (lane == 0) asm volatile("ds_add_u32 %0, %1" :: "v"(lds_addr(smem + LDS_SEL + 12)), "v"(1) : "memory");
    SB();
    SLOTS(16) {
      mmg(1, 1, q);
      if (q < 4) fn[0][q] = *(const bf16x8*)(smem + (wr * 128 + fr) * 128 + (q >> 1) * 2048 + ((q & 1) ? ko1 : ko0));
      else if (q < 8) fm[q & 1][(q - 4) >> 1] = *(const bf16x8*)(smem + G2_MRING + m_off(0) + (wc * 64 + mrow) * 128 + TO((q - 4) >> 1) + ((q & 1) ? ko1 : ko0));
      prep(16 + q);
      SB();
    }
    {
      lds_vi rel = (lds_vi)(smem + LDS_SEL + 12);
      int it = 0;
      while (rel[0] != 4 && it < SPIN_MAX * 8) ++it;
      asm volatile("" ::: "memory");
    }
    asm volatile("s_nop 7");
    SB();
    SLOTS(8) { epi(1, q); SB(); }
  }

  const char* pn = smem + (wr * 128 + fr) * 128;
  const char* pm = smem + G2_MRING + (wc * 64 + mrow) * 128;
  f32x4 acc[8][4];
  auto mm = [&](bool imaj, int set, int q2, int s, int mode) __attribute__((always_inline)) {
    int i, jj, kk;
    if (imaj) { i = s >> 2; const int r = s & 3; jj = (r >> 1) ^ (i & 1); kk = (r == 1 || r == 2); }
    else { jj = s >> 3; const int r = s & 7; i = jj ? 3 - (r >> 1) : (r >> 1); kk = ((r + 1) >> 1) & 1; }
    if (mode == 1 && !(s & 1)) asm volatile("v_mfma_f32_16x16x32_bf16 %0, %1, %2, 0" : "=&v"(acc[q2 * 2 + jj][i]) : "v"(fm[kk][i]), "v"(fn[set][jj * 2 + kk]));
    else if (mode) asm volatile("v_mfma_f32_16x16x32_bf16 %0, %1, %2, %0" : "+v"(acc[q2 * 2 + jj][i]) : "v"(fm[kk][i]), "v"(fn[set][jj * 2 + kk]));
    else acc[q2 * 2 + jj][i] = __builtin_amdgcn_mfma_f32_16x16x32_bf16(fm[kk][i], fn[set][jj * 2 + kk], acc[q2 * 2 + jj][i], 0, 0, 0);
  };
  unsigned bn[2], bm[2], an[2], am[2];
#pragma unroll
  for (int kk = 0; kk < 2; ++kk) {
    bn[kk] = lds_addr(pn) + (kk ? ko1 : ko0); bm[kk] = lds_addr(pm) + (kk ? ko1 : ko0);
    asm volatile("" : "+v"(bn[kk]), "+v"(bm[kk]));
    an[kk] = bn[kk]; am[kk] = bm[kk] + m_off(0);
  }
#define RDN(jt, kk, o) (*LDSP(const bf16x8, an[kk] + (o) + (jt) * 2048))
#define RDM(i, kk, o) (*LDSP(const bf16x8, am[kk] + (o) + TO(i)))
  PAGE(10);
  tag = __builtin_amdgcn_readfirstlane(((const unsigned*)(smem + LDS_SEL))[5]);
  {
    int son = 0;
    auto tile = [&](int kt, int zero) __attribute__((always_inline)) {
      SLOTS(16) {
        mm(true, 0, 0, q, zero);
        if (!(q & 1)) {
          if (q < 8) fm[(q >> 1) & 1][2 + (q >> 2)] = RDM(2 + (q >> 2), (q >> 1) & 1, 0);
          else { const int r = (q - 8) >> 1; fn[1][r] = RDN(2 + (r >> 1), r & 1, 0); }
        }
        SB();
      }
      SLOTS(16) {
        mm(false, 1, 1, q, zero);
        if (!(q & 1) && q < 8) fn[0][q >> 1] = RDN(4 + (q >> 2), (q >> 1) & 1, 0);
        SB();
      }
      const int som = son >> 1;
      son = son == 65536 ? 0 : son + 32768;
      SLOTS(16) {
        mm(false, 0, 2, q, zero);
        if (!(q & 1) && q < 8) fn[1][q >> 1] = RDN(6 + (q >> 2), (q >> 1) & 1, 0);
        if (q == 9 || q == 11) { an[q == 11] = bn[q == 11] + son; asm volatile("" : "+v"(an[q == 11])); }
        if (q == 13 || q == 15) { am[q == 15] = bm[q == 15] + som; asm volatile("" : "+v"(am[q == 15])); }
        SB();
      }
      BAR_LGKM();
      SB();
      SLOTS(16) {
        mm(true, 1, 3, q, zero);
        if (!(q & 1)) {
          if (q < 8) fn[0][q >> 1] = RDN(q >> 2, (q >> 1) & 1, 0);
          else fm[(q >> 1) & 1][(q - 8) >> 2] = RDM((q - 8) >> 2, (q >> 1) & 1, 0);
        }
        SB();
      }
    };
    SB();
    tile(0, 1);
#pragma unroll 1
    for (int kt = 1; kt < G2_NKT - 3; ++kt) tile(kt, 0);
  }
  unsigned fx;
  {
    constexpr int NG = 12;
    static constexpr int g_t[NG] = {0, 0, 1, 1, 2, 2, 2, 1, 0, 0, 1, 2};
    static constexpr int g_q[NG] = {0, 1, 0, 1, 0, 1, 2, 2, 2, 3, 3, 3};
    constexpr int G14 = 1, G15 = 3, GP0 = 5, GP1 = 6;
    constexpr auto chp = [](int g) { return g == 0 || g_t[g] != g_t[g - 1]; };
    constexpr auto chn = [](int g) { return g + 1 < NG && g_t[g + 1] != g_t[g]; };
    static_assert(n_off(13) == 32768 && n_off(14) == 65536 && n_off(15) == 0, "");
    unsigned an2[2];
#pragma unroll
    for (int kk = 0; kk < 2; ++kk) {
      an[kk] = bn[kk]; an2[kk] = bn[kk] + 65536; am[kk] = bm[kk];
      asm volatile("" : "+v"(an[kk]), "+v"(an2[kk]), "+v"(am[kk]));
    }
    int vo;
    u32x4 pk;
    u32x2 kv;
    constexpr int NF0 = 152;
    auto publish = [&](int d, int h) __attribute__((always_inline)) {
      if (h == 0) { vo = lane; PINV(vo); vo = vo * 16 + (((d + kq + 1) & 3) * 4 + kq) * 4 * MSG; PINV(vo); }
      const f32x4 v = acc[2 * d + (h >> 2)][h & 3];
      pk[(h & 1) * 2] = pk2(v[0], v[1]); pk[(h & 1) * 2 + 1] = pk2(v[2], v[3]);
      if (h & 1) __builtin_amdgcn_raw_buffer_store_b128(pk, rs, vo + (h >> 1) * 1024, 0, 16);
    };
    int fo = lane == 0 ? kq * 512 : (int)0x80000000;
#define MSG_FLAG(d) __builtin_amdgcn_raw_buffer_store_b32(tag, rc, fo + (((d) + kq + 1) & 3) * 2048, 0, 16)
    auto rdn = [&](int g, int r) __attribute__((always_inline)) {
      const int t = g_t[g], jt = g_q[g] * 2 + (r >> 1);
      fn[g & 1][r] = *LDSP(const bf16x8, (t == 1 ? an2[r & 1] : an[r & 1]) + (t == 0 ? 32768 : 0) + jt * 2048);
    };
    auto rdm = [&](int t, int i, int kk) __attribute__((always_inline)) { fm[kk][i] = *LDSP(const bf16x8, am[kk] + m_off(13 + t) + TO(i)); };
    auto group = [&](auto GI) __attribute__((always_inline)) {
      constexpr int gi = decltype(GI)::value;
      if (gi == G14) { BAR();
        SB(); }
      if (gi == G15) {
        PAGE(11);
        BAR();
        SB();
        xq[0] = ld16_s<0>(x, xo); xq[1] = ld16_s<64>(x, xo);
        sq = ld8_s_sc1(stat, xo >> 11 << 3);
        kv = *(const u32x2*)(smem + LDS_SEL);
        SB();
      }
#pragma unroll
      for (int q = 0; q < 16; ++q) {
        const int n = gi * 16 + q;
        if (n == NF0) {
          wait_xq<12>(xq, sq);
          key[0] = __builtin_amdgcn_readfirstlane(kv.x); key[1] = __builtin_amdgcn_readfirstlane(kv.y);
          nf_pre();
          SB();
        }
        if (n == 112) {
        }
        if (n == 152) {
        }
        if (n == 168) {
          WAIT_VM(8); MSG_FLAG(0); SB(); }
        if (n == 176) { WAIT_VM(5); MSG_FLAG(1); SB(); }
        if (n == 184) {
          WAIT_VM(2);
          MSG_FLAG(2); fx = bld4_sc1(rc, po); SB(); }
        mm(chp(gi) || chn(gi), gi & 1, g_q[gi], q, 2);
        constexpr bool ni = gi + 1 < NG && (chp(gi + 1 < NG ? gi + 1 : gi) || chn(gi + 1 < NG ? gi + 1 : gi));
        auto nr = [](int r) { return ni && r >= 2 ? 5 - r : r; };
        if constexpr (chp(gi) && chn(gi)) {
          if (q < 4) rdm(g_t[gi], 2 + (q >> 1), q & 1); else if (q < 8) rdn(gi + 1, nr(q - 4)); else if (q < 12) rdm(g_t[gi + 1], (q - 8) >> 1, q & 1);
        } else if constexpr (chp(gi)) {
          if (q < 4) rdm(g_t[gi], 2 + (q >> 1), q & 1);
          else if (gi + 1 < NG && q < 12 && !(q & 1)) rdn(gi + 1, nr((q - 4) >> 1));
        } else if constexpr (chn(gi)) {
          if (!(q & 1)) { if (q < 8) rdn(gi + 1, nr(q >> 1)); else rdm(g_t[gi + 1], (q - 8) >> 2, (q >> 1) & 1); }
        } else {
          if (!(q & 1) && q < 8) rdn(gi + 1, nr(q >> 1));
        }
        if (gi == GP0 && q < 8) publish(0, q);
        if (gi == GP1 && q < 8) publish(1, q);
        if (gi == 9 && q < 8) publish(2, q);
        if (n >= NF0) {
          const int m = (n - NF0) * 2;
          if (m < 56) { nf_step(m); nf_step(m + 1); } else if (m < 72) { nf_unp(m - 56); nf_unp(m - 55); }
        }
        SB();
      }
    };
#define GR4(i) group(IC<i>{}); group(IC<i + 1>{}); group(IC<i + 2>{}); group(IC<i + 3>{});
    GR4(0) GR4(4) GR4(8)
    asm volatile("s_nop 7");
    SB();
  }
#pragma unroll
  for (int i = 0; i < 4; ++i) *(f32x4*)(smem + LDS_ACC + wr * 16384 + wc * 4096 + i * 1024 + lane * 16) = acc[7][i];
#pragma unroll
  for (int i = 0; i < 4; ++i) ya[i] = acc[6][i];
  asm volatile("s_waitcnt lgkmcnt(0)" ::: "memory");
  if (lane == 0) sig[0] = 2;
  asm volatile("s_waitcnt vmcnt(0)" : "+v"(fx) :: "memory");
  seen = (unsigned)__ballot(fx == tag);
  }

  PAGE(6);
  const unsigned rel0 = 0xffff - (key[0] & 0xffffu) - trow, rel1 = 0xffff - (key[1] & 0xffffu) - trow;
  if (__builtin_expect(rel0 < 16 || rel1 < 16, 0)) {
#pragma unroll 1
    for (int c = 0; c < 2; ++c) {
      const unsigned rel = c ? rel1 : rel0;
      if (rel >= 16) continue;
      const float sg = rbf(1.f / (1.f + __expf(-key_score(c ? key[1] : key[0]))));
      lds_vi cnt = (lds_vi)(smem + LDS_SEL + 24 + c * 4);
      int it = 0;
      while (cnt[0] != 4 && it < SPIN_MAX * 8) { __builtin_amdgcn_s_sleep(1); ++it; }
      asm volatile("" ::: "memory");
      const float* fp = (const float*)(smem + LDS_FIN + c * 2048) + (wid & 1) * 64 + fq * 8;
#pragma unroll
      for (int u4 = 0; u4 < 4; ++u4) {
        const float* q = fp + (u4 >> 1) * 32 + (u4 & 1) * 4;
        const f32x4 v = (*(const f32x4*)q + *(const f32x4*)(q + 128)) + (*(const f32x4*)(q + 256) + *(const f32x4*)(q + 384));
#pragma unroll
        for (int k = 0; k < 4; ++k) {
          float u = rbf(nf[u4 * 4 + k] + rbf(rbf(v[k]) * sg));
          asm volatile("" : "+v"(u));
          nf[u4 * 4 + k] = rel == fr ? u : nf[u4 * 4 + k];
        }
      }
    }
  }
  if (ha) {
    int it = 0;
    while (sig[0] != 2 && it < SPIN_MAX * 8) { __builtin_amdgcn_s_sleep(1); ++it; }
    asm volatile("" ::: "memory");
#pragma unroll
    for (int i = 0; i < 4; ++i) ya[i] = *(const f32x4*)(smem + LDS_ACC + (wid & 2) * 8192 + (wid & 1) * 4096 + i * 1024 + lane * 16);
  }
  u32x4 pp[3][2];
  unsigned e_lo = 0x00003f80u, e_hi = 0x3f800000u;
  asm volatile("" : "+v"(e_lo), "+v"(e_hi));
  __amdgpu_buffer_rsrc_t rs_ = rs, rc_ = rc;
  int mo[3] = {MSG_IDX(0) * 4 * MSG, MSG_IDX(1) * 4 * MSG, MSG_IDX(2) * 4 * MSG}, mv = lane * 16 + ha * 2048, mv2 = mv + 1024;
  asm volatile("" : "+s"(rs_), "+s"(rc_), "+s"(mo[0]), "+s"(mo[1]), "+s"(mo[2]));
  asm volatile("" : "+v"(mv), "+v"(mv2));
#define MSG_POLL(m) do { \
    if (!((seen >> (m)) & 1)) { \
      unsigned pv_, it_ = SPIN_MAX; \
      asm volatile("1:\n\tbuffer_load_dword %0, %3, %4, 0 offen sc1\n\ts_waitcnt vmcnt(0)\n\tv_cmp_eq_u32 vcc, %5, %0\n\ts_mov_b32 %1, vcc_lo\n\t" \
                   "s_bitcmp1_b32 %1, %6\n\ts_cbranch_scc1 2f\n\ts_sub_u32 %2, %2, 1\n\ts_cbranch_scc1 2f\n\ts_sleep 1\n\ts_branch 1b\n2:" \
                   : "=&v"(pv_), "+s"(seen), "+s"(it_) : "v"(po), "s"(rc_), "s"(tag), "i"(m) : "vcc", "scc", "memory"); \
    } \
    pp[m][0] = __builtin_amdgcn_raw_buffer_load_b128(rs_, mv, mo[m], 16); \
    pp[m][1] = __builtin_amdgcn_raw_buffer_load_b128(rs_, mv2, mo[m], 16); } while (0)
#define MSG_ADD(m) do { \
    _Pragma("unroll") for (int u = 0; u < 8; ++u) { \
      ya[u >> 1][(u & 1) * 2] = add_bf(pp[m][u >> 2][u & 3], e_lo, ya[u >> 1][(u & 1) * 2]); ya[u >> 1][(u & 1) * 2 + 1] = add_bf(pp[m][u >> 2][u & 3], e_hi, ya[u >> 1][(u & 1) * 2 + 1]); } } while (0)
  MSG_POLL(2); MSG_POLL(1); MSG_POLL(0);
  SB();
  MSG_ADD(2); MSG_ADD(1);
  asm volatile("" : "+v"(ya[0]), "+v"(ya[1]), "+v"(ya[2]), "+v"(ya[3]));
  SB();
  MSG_ADD(0);
  {
    unsigned ov[8];
#pragma unroll
    for (int u = 0; u < 8; ++u) {
      const unsigned yb = pk2(ya[u >> 1][(u & 1) * 2], ya[u >> 1][(u & 1) * 2 + 1]);
      const unsigned sb = pk2(add_bf(yb, e_lo, xf[2 * u]), add_bf(yb, e_hi, xf[2 * u + 1]));
      ov[u] = pk2(add_bf(sb, e_lo, nf[2 * u]), add_bf(sb, e_hi, nf[2 * u + 1]));
    }
    *(u32x4*)((char*)out + (unsigned)xo) = (u32x4){ov[0], ov[1], ov[2], ov[3]};
    *(u32x4*)((char*)out + (unsigned)xo + 64) = (u32x4){ov[4], ov[5], ov[6], ov[7]};
  }
  if (g == 37 && wid == 0) {
    asm volatile("s_waitcnt vmcnt(0)" ::: "memory");
    for (int i = 0; i < 40; ++i) __builtin_amdgcn_s_sleep(8);
    const char* p = wsp + (40u << 20);
    const __amdgpu_buffer_rsrc_t r = mk_rsrc(p);
    unsigned a = __builtin_amdgcn_raw_buffer_load_b32(r, 65536 - 128, 0, 16);
    asm volatile("s_waitcnt vmcnt(0)" : "+v"(a) :: "memory");
    const unsigned t0 = (unsigned)__builtin_amdgcn_s_memrealtime();
    unsigned b = a & 0;
#pragma unroll 1
    for (int i = 0; i < 40; ++i) { b = __builtin_amdgcn_raw_buffer_load_b32(r, (i + 1) * (4 * 1048576 + 1152) + (b & 0), 0, 16); asm volatile("s_waitcnt vmcnt(0)" : "+v"(b) :: "memory"); }
    if (b == 0x12345678u && t0 == 77) out[0] = 1;
  }
}

extern "C" void kernel_launch(void* const* d_in, const int* in_sizes, int n_in, void* d_out, int out_size,
                              void* d_ws, size_t ws_size, hipStream_t stream) {
  const u16* x = (const u16*)d_in[0];
  const u16* ws_re = (const u16*)d_in[1];
  const u16* ws_im = (const u16*)d_in[2];
  const u16* wd_re = (const u16*)d_in[3];
  const u16* wd_im = (const u16*)d_in[4];
  const u16* w_r = (const u16*)d_in[6];
  const u16* w_gate = (const u16*)d_in[7];
  const u16* w_up = (const u16*)d_in[8];
  const u16* w_down = (const u16*)d_in[9];
  k_main<<<256, 512, 0, stream>>>(x, wd_re, wd_im, w_r, (char*)d_ws, ws_re, ws_im, w_gate, w_up, w_down, (u16*)d_out);
}
```

```cpp
#include <hip/hip_runtime.h>
#include <hip/hip_bf16.h>
#include <stdint.h>
#include <type_traits>

extern "C" __device__ unsigned long long llvm_dispatch_id(void) __asm("llvm.amdgcn.dispatch.id");
extern "C" __device__ float llvm_fmul_legacy(float, float) __asm("llvm.amdgcn.fmul.legacy");
typedef unsigned short u16;
typedef unsigned long long u64;
typedef __attribute__((ext_vector_type(8))) short bf16x8;
typedef __attribute__((ext_vector_type(4))) unsigned u32x4;
typedef __attribute__((ext_vector_type(2))) unsigned u32x2;
typedef __attribute__((ext_vector_type(4))) float f32x4;
typedef __attribute__((ext_vector_type(2))) float f32x2;
typedef __attribute__((ext_vector_type(2))) __bf16 bf16x2;
typedef __attribute__((address_space(3))) void* lds_vp;
typedef volatile __attribute__((address_space(3))) int* lds_vi;

constexpr int S_ = 2048, D_ = 1024, F_ = 3584;
template <int N> using IC = std::integral_constant<int, N>;

static __device__ __forceinline__ float bf2f(u16 u) { return __uint_as_float((unsigned)u << 16); }
static __device__ __forceinline__ u16 f2bf(float f) {
  __hip_bfloat16 b = __float2bfloat16(f);
  return *reinterpret_cast<u16*>(&b);
}
static __device__ __forceinline__ float rbf(float f) { return bf2f(f2bf(f)); }
static __device__ __forceinline__ float lo_bf(unsigned u) { return __uint_as_float(u << 16); }
static __device__ __forceinline__ float hi_bf(unsigned u) { return __uint_as_float(u & 0xffff0000u); }
static __device__ __forceinline__ unsigned pk2(float a, float b) {
  const bf16x2 v = __builtin_convertvector((f32x2){a, b}, bf16x2);
  return __builtin_bit_cast(unsigned, v);
}
static __device__ __forceinline__ float add_bf(unsigned u, unsigned e, float c) {
  return __builtin_amdgcn_fdot2_f32_bf16(__builtin_bit_cast(bf16x2, u), __builtin_bit_cast(bf16x2, e), c, false);
}
static __device__ __forceinline__ float ln_elem(float x, float mb, float sd, float ri) {
  const float d = rbf(x - mb);
  const float q0 = d * ri;
  return rbf(__builtin_fmaf(__builtin_fmaf(-q0, sd, d), ri, q0));
}
template <int CTRL> static __device__ __forceinline__ float dpp_f(float v) {
  return __int_as_float(__builtin_amdgcn_update_dpp(0, __float_as_int(v), CTRL, 0xf, 0xf, true));
}
template <int CTRL> static __device__ __forceinline__ unsigned dpp_u(unsigned v) {
  return (unsigned)__builtin_amdgcn_update_dpp(0, (int)v, CTRL, 0xf, 0xf, true);
}
static __device__ __forceinline__ float row_sum(float v) {
  v += dpp_f<0xB1>(v);
  v += dpp_f<0x4E>(v);
  v += dpp_f<0x141>(v);
  v += dpp_f<0x140>(v);
  return v;
}
static __device__ __forceinline__ float fold32(float a, float b) {
  const auto r = __builtin_amdgcn_permlane32_swap(__float_as_uint(a), __float_as_uint(b), false, false);
  return __uint_as_float(r[0]) + __uint_as_float(r[1]);
}
static __device__ __forceinline__ float fold16(float a, float b) {
  const auto r = __builtin_amdgcn_permlane16_swap(__float_as_uint(a), __float_as_uint(b), false, false);
  return __uint_as_float(r[0]) + __uint_as_float(r[1]);
}
static __device__ __forceinline__ float rdl(float v, int l) { return __int_as_float(__builtin_amdgcn_readlane(__float_as_int(v), l)); }

#define ST_JB 9
#define WSN 10
#define FSM 0xa2
#define GV_JB 7
#define DG 11
#define AB_STRIDE 32
#define SPIN_MAX (1 << 17)

static __device__ __forceinline__ unsigned lds_addr(const void* p) { return (unsigned)(size_t)(lds_vp)p; }

constexpr int MSG = 4096;
constexpr size_t WS_ABT   = 0;
constexpr size_t WS_SLAB  = WS_ABT + (size_t)D_ * 4096 * 2;
constexpr size_t WS_GU    = WS_SLAB + (size_t)64 * 64 * MSG;
constexpr size_t WS_DN    = WS_GU + (size_t)8 * F_ * 16;
constexpr size_t WS_FLAGS = WS_DN + (size_t)2 * 8 * 32 * 64 * 16;
constexpr int FL_X = 0;
constexpr int FL_AB = 64 * 64 * 32;
constexpr int FL_ST = FL_AB + 2048 * AB_STRIDE;
constexpr int FL_CE = FL_ST + 4096;
constexpr int FL_KEY = FL_CE + 4096;
constexpr int FL_EPOCH = FL_KEY + 4096;
constexpr int WS_MB = (int)((WS_FLAGS + (FL_EPOCH + 1) * 4 + 1048575) >> 20);

#define WAIT_VM(n) asm volatile("s_waitcnt vmcnt(" #n ")" ::: "memory")
template <int N> static __device__ __forceinline__ void wait_vm() { asm volatile("s_waitcnt vmcnt(%0)" :: "i"(N) : "memory"); }
#define BAR_LGKM() asm volatile("s_waitcnt lgkmcnt(0)\n\ts_barrier" ::: "memory")
#define BAR() asm volatile("s_barrier" ::: "memory")
#define SB() __builtin_amdgcn_sched_barrier(0)
#define PAGE(n) asm volatile("s_branch 2f\n\t.p2align 12\nk_pg" #n ":\n\ts_setpc_b64 s[42:43]\n2:")
#define CALL_PG(n) asm volatile("s_getpc_b64 s[40:41]\n3:\n\ts_mov_b32 s42, k_pg" #n "-3b\n\ts_ashr_i32 s43, s42, 31\n\ts_add_u32 s40, s40, s42\n\ts_addc_u32 s41, s41, s43\n\t" \
                                "s_swappc_b64 s[42:43], s[40:41]" ::: "s40", "s41", "s42", "s43", "scc", "memory")

static __device__ __forceinline__ __amdgpu_buffer_rsrc_t mk_rsrc(const void* p) {
  return __builtin_amdgcn_make_buffer_rsrc((void*)p, 0, 0x7fffffff, 0x00020000);
}
#define DMA_(rsrc, voff, mbase, MI, sbase, SI, pol) do { unsigned _t; \
    asm volatile("s_add_i32 m0, %2, %3\n\ts_add_i32 %0, %5, %6\n\tbuffer_load_dwordx4 %1, %4, %0 offen" pol " lds" \
                 : "=&s"(_t) : "v"(voff), "s"(mbase), "i"(MI), "s"(rsrc), "s"(sbase), "i"(SI) : "memory", "scc"); } while (0)
#define TOUCH(rsrc, voff, mbase, MI, soff) \
    asm volatile("s_add_i32 m0, %1, %2\n\ts_nop 0\n\tbuffer_load_dword %0, %3, %4 offen lds" \
                 :: "v"(voff), "s"(mbase), "i"(MI), "s"(rsrc), "s"(soff) : "memory", "scc")
template <int OFF> static __device__ __forceinline__ u32x4 ld16_s(const void* p, int voff) {
  u32x4 v;
  asm volatile("global_load_dwordx4 %0, %1, %2 offset:%3" : "=v"(v) : "v"(voff), "s"(p), "i"(OFF) : "memory");
  return v;
}
template <int OFF> static __device__ __forceinline__ void sld(unsigned& sink, const void* p, int soff) {
  asm volatile("s_load_dword %0, %1, %2 offset:%3" : "+s"(sink) : "s"(p), "s"(soff), "i"(OFF) : "memory");
}
static __device__ __forceinline__ unsigned gld4_s(const void* p, unsigned voff) {
  unsigned v;
  asm volatile("global_load_dword %0, %1, %2" : "=v"(v) : "v"(voff), "s"(p) : "memory");
  return v;
}
static __device__ __forceinline__ u32x2 bld8_sc1(__amdgpu_buffer_rsrc_t r, unsigned voff) {
  u32x2 v;
  asm volatile("buffer_load_dwordx2 %0, %1, %2, 0 offen sc1" : "=v"(v) : "v"(voff), "s"(r) : "memory");
  return v;
}
template <int N> static __device__ __forceinline__ void wait_xs(unsigned& a, u32x2& b) { asm volatile("s_waitcnt vmcnt(%2)" : "+v"(a), "+v"(b) : "i"(N) : "memory"); }
template <int N> static __device__ __forceinline__ void wait_xq(u32x4 (&a)[2], u32x2& b) { asm volatile("s_waitcnt vmcnt(%3)" : "+v"(a[0]), "+v"(a[1]), "+v"(b) : "i"(N) : "memory"); }
static __device__ __forceinline__ u32x2 ld8_s_sc1(const void* p, int voff) {
  u32x2 v;
  asm volatile("global_load_dwordx2 %0, %1, %2 sc1" : "=v"(v) : "v"(voff), "s"(p) : "memory");
  return v;
}
static __device__ __forceinline__ void lgkm0(unsigned& sink) { asm volatile("s_waitcnt lgkmcnt(0)" : "+s"(sink) :: "memory"); }
template <typename T> static __device__ __forceinline__ void pin4(T& a, T& b, T& c, T& d) { asm volatile("" : "+s"(a), "+s"(b), "+s"(c), "+s"(d)); }
template <typename T, typename U> static __device__ __forceinline__ void pins(T& a, U& b) { asm volatile("" : "+s"(a), "+s"(b)); }
static __device__ __forceinline__ void launder(int& v, int& a, int& b) { asm volatile("" : "+v"(v)); asm volatile("" : "+s"(a), "+s"(b)); }
static __device__ __forceinline__ unsigned bld4_sc1(__amdgpu_buffer_rsrc_t r, int voff) {
  unsigned v;
  asm volatile("buffer_load_dword %0, %1, %2, 0 offen sc1" : "=v"(v) : "v"(voff), "s"(r) : "memory");
  return v;
}
static __device__ __forceinline__ u32x4 bld16_nt(__amdgpu_buffer_rsrc_t r, int voff, int soff) {
  u32x4 v;
  asm volatile("buffer_load_dwordx4 %0, %1, %2, %3 offen nt" : "=v"(v) : "v"(voff), "s"(r), "s"(soff) : "memory");
  return v;
}
static __device__ __forceinline__ void bld16_nt_io(u32x4& v, __amdgpu_buffer_rsrc_t r, int voff, int soff) {
  asm volatile("buffer_load_dwordx4 %0, %1, %2, %3 offen nt" : "+v"(v) : "v"(voff), "s"(r), "s"(soff) : "memory");
}
static __device__ __forceinline__ u32x4 bld16_sc1(__amdgpu_buffer_rsrc_t r, int voff, int soff) {
  u32x4 v;
  asm volatile("buffer_load_dwordx4 %0, %1, %2, %3 offen sc1" : "=v"(v) : "v"(voff), "s"(r), "s"(soff) : "memory");
  return v;
}
static __device__ __forceinline__ u32x2 granule_wait(const u64* p, unsigned tag) {
  u64 v = 0;
#pragma unroll 1
  for (int it = 0; it < SPIN_MAX; ++it) {
    v = __hip_atomic_load(p, __ATOMIC_RELAXED, __HIP_MEMORY_SCOPE_AGENT);
    if (__all((unsigned)(v >> 32) == tag)) break;
    __builtin_amdgcn_s_sleep(2);
  }
  return (u32x2){(unsigned)v, (unsigned)(v >> 32)};
}
static __device__ __forceinline__ float key_score(unsigned key) {
  const unsigned u = key >> 16;
  return bf2f((u16)((u & 0x8000u) ? (u & 0x7fffu) : (~u & 0xffffu)));
}
static __device__ __forceinline__ void merge2(unsigned& a1, unsigned& a2, unsigned b1, unsigned b2) {
  const unsigned lo = min(a1, b1);
  a1 = max(a1, b1);
  a2 = max(lo, max(a2, b2));
}
static __device__ __forceinline__ float sqdev16(const u32x4 (&v)[2], float mb) {
  f32x2 q2 = {0.f, 0.f};
#pragma unroll
  for (int i = 0; i < 8; ++i) {
    const f32x2 e = (f32x2){lo_bf(v[i >> 2][i & 3]), hi_bf(v[i >> 2][i & 3])} - mb;
    q2 += e * e;
  }
  return q2.x + q2.y;
}

constexpr int G1_NST = 4, G1_NKT = 16, G1_STAGE = 32768;
constexpr int G2_NKT = 16, G2_MRING = 98304;
constexpr int n_off(int k) { return (k % 3) * 32768; }
constexpr int m_off(int k) { return ((k + 2) % 3) * 16384; }
constexpr int k_ord(int k) { return k < 2 ? k : (k < 9 ? 2 * (k - 1) : 2 * (k - 8) + 1); }
constexpr int LDS_DUMMY = G2_MRING + 49152;
constexpr int LDS_GU = LDS_DUMMY + 4096;
constexpr int LDS_FIN = LDS_GU + 4096;
constexpr int LDS_MB = LDS_FIN + 4096;
constexpr int LDS_SEL = LDS_MB + 1536;
constexpr int LDS_SIG = LDS_SEL + 64;
constexpr int LDS_KEY = LDS_SIG + 32;
constexpr int LDS_BYTES = LDS_KEY + 128;
constexpr int LDS_ACC = n_off(G2_NKT - 3);

constexpr int tpos[16] = {0, 0, 0, 0, 109, 115, 1, 2, 2, 3, 4, 5, 6, 7, 8, 9};
constexpr int twho[16] = {0, 0, 0, 0, 7, 7, 0, 1, 2, 3, 4, 5, 6, 7, 0, 1};
constexpr int dn_cnt(int q) { return q == 5 ? 3 : q == 8 ? 4 : 0; }
constexpr int dn_first(int q) { return q <= 5 ? 0 : q <= 8 ? 3 : 7; }

static __device__ __forceinline__ void code_touch(unsigned& sink, unsigned off) {
  asm volatile("s_getpc_b64 s[40:41]\n\tk_main_rt:\n\ts_min_u32 %1, %1, k_main_end-k_main_rt-196\n\t"
               "s_load_dword %0, s[40:41], %1 offset:0\n\ts_load_dword %0, s[40:41], %1 offset:64\n\ts_load_dword %0, s[40:41], %1 offset:128\n\ts_load_dword %0, s[40:41], %1 offset:192"
               : "+s"(sink), "+s"(off) :: "s40", "s41", "scc", "memory");
}
static __device__ __forceinline__ void touch1(__amdgpu_buffer_rsrc_t r, int voff, unsigned mb, int soff) { TOUCH(r, voff, mb, LDS_DUMMY, soff); }
typedef const __attribute__((address_space(1))) u16* gptr;
typedef __attribute__((ext_vector_type(8))) u64 u64x8;
extern "C" __global__ __launch_bounds__(512, 2) void k_main(const u16* x, unsigned x2, unsigned x3, const u16* wd_re, unsigned r2, unsigned r3,
                                                 const u16* wd_im, unsigned i2, unsigned i3, const u16* w_r,
                                                 char* wsp, const u16* ws_re, const u16* ws_im,
                                                 const u16* w_gate, const u16* w_up, const u16* w_down, u16* out) {
  __shared__ __attribute__((aligned(16))) char smem[LDS_BYTES];
  u64x8 ka;
  u32x4 rm, rn;
  asm volatile("s_load_dwordx4 %0, %3, 0\n\ts_load_dwordx4 %1, %3, %4 offset:16\n\ts_load_dwordx16 %2, %3, 48"
               : "=&s"(rm), "=&s"(rn), "=&s"(ka) : "s"(__builtin_amdgcn_kernarg_segment_ptr()), "s"((blockIdx.x & 4) * 4) : "memory");
  const int tid = threadIdx.x, lane = tid & 63;
  const int wid = __builtin_amdgcn_readfirstlane(tid >> 6);
  const int g = blockIdx.x, xcd = g & 7, j = g >> 3;
  if (wid < 4) {
    unsigned cw, co = (j * 4 + wid) * 448;
    asm volatile(".subsection 1\nk_main_end:\n.subsection 0\n\ts_min_u32 %1, %1, k_main_end-k_main-480\n\ts_getpc_b64 s[40:41]\n\ts_add_u32 s40, s40, %1\n\ts_addc_u32 s41, s41, 0\n\t"
                 "s_load_dword %0, s[40:41], 0\n\ts_load_dword %0, s[40:41], 64\n\ts_load_dword %0, s[40:41], 128\n\t"
                 "s_load_dword %0, s[40:41], 192\n\ts_load_dword %0, s[40:41], 256\n\ts_load_dword %0, s[40:41], 320\n\ts_load_dword %0, s[40:41], 384\n\ts_waitcnt lgkmcnt(0)"
                 : "=&s"(cw), "+s"(co) :: "s40", "s41", "scc", "memory");
  }
  const int pw = wid - 4;
  const int row0 = (wid & 3) * 8 + (lane >> 3);
  const int cm = ((lane & 7) ^ (((row0 >> 1) & 1) | ((wid & 3) << 1))) * 16;
  int vm = row0 * (D_ * 2) + cm;
  asm volatile("" : "+v"(vm));
  const int kq = xcd >> 1;
  const int tt = (xcd & 1) * 4 + (j >> 3);
  const int et = j & 7;
  const int z = kq >> 1, st = (kq & 1) * 8 + tt;
  unsigned o_m = st * 128 * D_ * 2, o_n = et * 128 * D_ * 2, mb0 = lds_addr(smem) + pw * 1024;
  asm volatile("" : "+s"(o_m), "+s"(o_n), "+s"(mb0));
#define G1_X(kt, so, q) DMA_(rm, vm, mb0, (so) + (q) * 4096, o_m, (kt) * 128 + (((q) & 1) * 64 + ((q) >> 1) * 32) * D_ * 2, "")
#define G1_W(kt, so, q) DMA_(rn, vm, mb0, (so) + 16384 + (q) * 4096, o_n, (kt) * 128 + (q) * 32 * D_ * 2, "")
#define G1_STAGE_TILE(kt, so, W) do { SB(); G1_X(kt, so, 0); W(0); G1_X(kt, so, 1); W(1); G1_X(kt, so, 2); W(2); G1_X(kt, so, 3); W(3); \
    G1_W(kt, so, 0); W(4); G1_W(kt, so, 1); W(5); G1_W(kt, so, 2); W(6); G1_W(kt, so, 3); W(7); SB(); } while (0)
#define G1_STAGE_A(kt, so, W) do { SB(); G1_X(kt, so, 0); W(0); G1_X(kt, so, 2); W(1); \
    G1_W(kt, so, 0); W(2); G1_W(kt, so, 1); W(3); G1_W(kt, so, 2); W(4); G1_W(kt, so, 3); W(5); W(6); W(7); SB(); } while (0)
#define G1_STAGE_B(kt, so) do { SB(); G1_X(kt, so, 1); G1_X(kt, so, 3); SB(); } while (0)
#define NOW(i)
  asm volatile("s_waitcnt lgkmcnt(0)" : "+s"(ka), "+s"(rm), "+s"(rn) :: "memory");
  int lrow, drow, l16;
  unsigned o_l, o_d;
  auto rows = [&](int i) __attribute__((always_inline)) {
    int g_ = g, l_ = lane;
    if (i == 0) { asm volatile("" : "+s"(g_)); drow = (g_ & ~7) + pw * 2; lrow = drow + (g_ & 3) * 512 + (g_ & 4) * 64; asm volatile("" : "+s"(drow), "+s"(lrow)); }
    if (i == 1) { asm volatile("" : "+s"(g_)); drow += (g_ & 3) * 256; o_l = lrow * D_ * 2; o_d = drow * D_ * 2; asm volatile("" : "+s"(drow), "+s"(o_l), "+s"(o_d)); }
    if (i == 2) { asm volatile("" : "+v"(l_)); l16 = l_ * 16; asm volatile("" : "+v"(l16)); }
  };
  if (wid >= 4) {
    __builtin_amdgcn_s_setprio(3);
    G1_STAGE_TILE(0, 0, rows);
  }
  x = (const u16*)(gptr)((u64)rm[0] | (u64)rm[1] << 32);
  const u16* wdz = (const u16*)(gptr)((u64)rn[0] | (u64)rn[1] << 32);
  w_r = (const u16*)(gptr)ka[0]; wsp = (char*)(gptr)ka[1]; ws_re = (const u16*)(gptr)ka[2]; ws_im = (const u16*)(gptr)ka[3];
  w_gate = (const u16*)(gptr)ka[4]; w_up = (const u16*)(gptr)ka[5]; w_down = (const u16*)(gptr)ka[6]; out = (u16*)(gptr)ka[7];
  int* flags = (int*)(wsp + WS_FLAGS);
  u64* stat = (u64*)(flags + FL_ST);
  const int fr = lane & 15, fq = lane >> 4;
  const int ha = wid >> 2;
  int trow, xo, po;
  const char *prs, *prc;
  auto epi_setup = [&]() __attribute__((always_inline)) {
    int t_ = tt, l_ = lane;
    asm volatile("" : "+s"(t_));
    asm volatile("" : "+v"(l_));
    trow = t_ * 256 + ((wid >> 1) & 1) * 128 + kq * 32 + ha * 16;
    xo = ((trow + (l_ & 15)) * D_ + et * 128 + (wid & 1) * 64 + (l_ >> 4) * 8) * 2;
    prs = wsp + WS_SLAB + (size_t)((t_ * 8 + et) * 64 + (wid & 3)) * MSG;
    prc = (const char*)(flags + FL_X + ((t_ * 8 + et) * 64 + (wid & 3)) * 32);
    asm volatile("" : "+s"(trow), "+s"(prs), "+s"(prc));
    po = l_ < 3 ? (kq * 4 + ((kq + 1 + l_) & 3)) * 512 : (int)0x80000000;
    asm volatile("" : "+v"(xo), "+v"(po));
  };
#define rs mk_rsrc(prs)
#define rc mk_rsrc(prc)
  lds_vi sig = (lds_vi)(smem + LDS_SIG + (wid & 3) * 4);
  unsigned tag;
  u32x4 xq[2];
  u32x2 sq;
  f32x4 ya[4];
  unsigned key[2];
  float xf[16], nf[16], lmb, lsd, lri, la, lb, lqa, lqb;
  unsigned nfp[8];
#define PINV(a) asm volatile("" : "+v"(a))
  auto nf_pre = [&]() __attribute__((always_inline)) {
    if (__builtin_expect(!__all(sq.y == tag), 0)) sq = granule_wait(stat + (xo >> 11), tag);
    lmb = lo_bf(sq.x); lsd = hi_bf(sq.x); lri = __builtin_amdgcn_rcpf(lsd);
    PINV(lmb); PINV(lsd); PINV(lri);
  };
  auto nf_step = [&](int n) __attribute__((always_inline)) {
    const int u = n / 7;
    switch (n % 7) {
      case 0: la = lo_bf(xq[u >> 2][u & 3]); PINV(la); la -= lmb; PINV(la); break;
      case 1: lb = hi_bf(xq[u >> 2][u & 3]); PINV(lb); lb -= lmb; PINV(lb); break;
      case 2: nfp[u] = pk2(la, lb); la = lo_bf(nfp[u]); PINV(la); PINV(nfp[u]); break;
      case 3: lqa = la * lri; PINV(lqa); lb = hi_bf(nfp[u]); PINV(lb); break;
      case 4: la = __builtin_fmaf(-lqa, lsd, la); PINV(la); lqb = lb * lri; PINV(lqb); break;
      case 5: lqa = __builtin_fmaf(la, lri, lqa); PINV(lqa); lb = __builtin_fmaf(-lqb, lsd, lb); PINV(lb); break;
      default: lqb = __builtin_fmaf(lb, lri, lqb); nfp[u] = pk2(lqa, lqb); PINV(nfp[u]); break;
    }
  };
  auto nf_unp = [&](int e) __attribute__((always_inline)) {
    const int u = e >> 1;
    if (e == 0) { PINV(xq[0]); PINV(xq[1]); }
    xf[e] = (e & 1) ? hi_bf(xq[u >> 2][u & 3]) : lo_bf(xq[u >> 2][u & 3]);
    nf[e] = (e & 1) ? hi_bf(nfp[u]) : lo_bf(nfp[u]);
    PINV(xf[e]); PINV(nf[e]);
  };
#define MSG_IDX(m) (kq * 4 + ((kq + 1 + (m)) & 3))
  unsigned seen = 0;

  if (__builtin_expect(wid >= 4, 1)) {
    constexpr int O_ST = (FL_ST - FL_AB) * 4, O_CE = (FL_CE - FL_AB) * 4, O_KEY = (FL_KEY - FL_AB) * 4, O_EP = (FL_EPOCH - FL_AB) * 4;
    const __amdgpu_buffer_rsrc_t rf = mk_rsrc(wsp + WS_FLAGS + FL_AB * 4);
    u32x4 lv[2], lv2[2], lw[2], ld[2], ld2[2];
    unsigned epv;
    {
      const char* xrow = (const char*)x + o_l;
      const char* wdrow = (const char*)wdz + o_d;
      lv[0] = ld16_s<0>(xrow, l16); lv[1] = ld16_s<1024>(xrow, l16);
      lv2[0] = ld16_s<2048>(xrow, l16); lv2[1] = ld16_s<3072>(xrow, l16);
      ld[0] = ld16_s<0>(wdrow, l16); ld[1] = ld16_s<1024>(wdrow, l16);
      ld2[0] = ld16_s<2048>(wdrow, l16); ld2[1] = ld16_s<3072>(wdrow, l16);
      lw[0] = ld16_s<0>(w_r, l16); lw[1] = ld16_s<1024>(w_r, l16);
      asm volatile("buffer_load_dword %0, off, %1, %2 sc1" : "=v"(epv) : "s"(rf), "s"(O_EP) : "memory");
    }
    SB();
    const unsigned long long did = llvm_dispatch_id();
    const unsigned dmix = (unsigned)did * 0x85EBCA6Bu + (unsigned)(did >> 32) * 0xC2B2AE35u + 0x27D4EB2Fu;
    const bool moe = et != 7;
    unsigned epoch;
    const int ci = lane & 15, rq = lane >> 4, tk = (lane >> 3) & 1;
    int mat, cg, dq, d0;
    u32x4 wv[16];
    const u16 *pgu, *pn2;
    const char *pm2, *spb;
#define rw __builtin_amdgcn_make_buffer_rsrc((void*)pgu, 0, moe ? 0x7fffffff : 0, 0x00020000)
#define rn2 mk_rsrc(pn2)
#define rm2 mk_rsrc(pm2)
    int vw, vn2, vm2;
    auto k_blk = [&](int k) __attribute__((always_inline)) { return tt ^ (k_ord(k) >> 1); };
#define KROT(kk_) (k_blk(kk_) * 256 + (k_ord(kk_) & 1) * 128)
    int tv2;
    unsigned sd = 0;
    auto touches = [&](auto P) __attribute__((always_inline)) {
      constexpr int p = decltype(P)::value;
      int e = et;
      asm volatile("" : "+s"(e));
#define TCH(k) if constexpr (tpos[k] == p) { if (e == twho[k]) { const int kb = KROT(k); touch1(rn2, tv2, mb0, kb); } }
      TCH(0) TCH(1) TCH(2) TCH(3) TCH(4) TCH(5) TCH(6) TCH(7) TCH(8) TCH(9) TCH(10) TCH(11) TCH(12) TCH(13) TCH(14) TCH(15)
    };
    unsigned mq0, mq1, mq2, mq3;
    const int cg8 = g & 7, rgb = g >> 3;
    const __amdgpu_buffer_rsrc_t rdn = mk_rsrc(w_down);
    const __amdgpu_buffer_rsrc_t rgu = mk_rsrc(wsp + WS_GU);
    int vdn, vgu;
    u32x4 dv[7], gv[4];
    int abo, abq[3];
    unsigned lma;
    unsigned sel[2][4];
    float g0, g1;
    u32x4 kk[4];
    unsigned k1 = 0, k2 = 0;
    const int ko_ = O_KEY + pw * 4096;
#define MM(a, b, c) __builtin_amdgcn_mfma_f32_16x16x32_bf16(__builtin_bit_cast(bf16x8, a), __builtin_bit_cast(bf16x8, b), c, 0, 0, 0)
#define PIN1(a) asm volatile("" : "+v"(a))
#define PIN2(a, b) asm volatile("" : "+v"(a), "+v"(b))
#define PIN3(a, b, c) asm volatile("" : "+v"(a), "+v"(b), "+v"(c))
    {
      int gro, go;
      u32x2 gr;
      u32x4 oh[5];
      f32x4 sa4, gw[2], gx[2];
      float su, gs, ta, tb, sx, mbv;
      unsigned dk[4], kl;
      const int hb = lane >> 5;
#pragma unroll
      for (int l = 0; l < WSN; ++l) asm volatile("" : "=v"(wv[l]));
      auto diag = [&](const f32x4& m) __attribute__((always_inline)) {
        return __uint_as_float((__float_as_uint(m[0]) & dk[0]) | (__float_as_uint(m[1]) & dk[1]) | (__float_as_uint(m[2]) & dk[2]) | (__float_as_uint(m[3]) & dk[3]));
      };
      auto setup = [&](auto JB, auto GP) __attribute__((always_inline)) {
        constexpr int jb = decltype(JB)::value, gp = decltype(GP)::value;
        SB();
        {
          int ln = lane, jj = j, p_ = pw;
          launder(ln, jj, p_);
          const int c_ = ln & 15, r_ = ln >> 4, e_ = jj & 7, t_ = (xcd & 1) * 4 + (jj >> 3);
          if constexpr (jb == 1 && gp == 0) {
            pn2 = ((kq >> 1) ? ws_im : ws_re) + (long)t_ * 256 * S_ + (kq & 1) * 1024;
            spb = (const char*)(pn2 + (e_ * 4 + p_) * 8 * S_);
            pins(pn2, spb);
          }
          if constexpr (jb == 1 && gp >= 1 && gp < 6) {
            const unsigned o = c_ == gp - 1 ? 0x3f803f80u : 0u;
            oh[gp - 1] = (u32x4){o, o, o, o};
            PIN1(oh[gp - 1]);
          }
          if constexpr (jb == 1 && gp == 6) {
            const int gm = moe ? (jj - (jj >> 3)) * 8 + xcd : 0;
            mat = gm / 112; cg = (gm % 112) >> 2; dq = gm & 3; d0 = dq * 256 + p_ * 64;
            pin4(mat, cg, dq, d0);
          }
          if constexpr (jb == 2 && gp == 0) {
            pgu = mat ? w_up : w_gate;
            pm2 = wsp + WS_ABT + ((long)e_ * 128 * 4096 + kq * 1024) * 2;
            pins(pgu, pm2);
            vw = ((d0 + r_ * 16) * F_ + cg * 128 + c_ * 8) * 2;
            PIN1(vw);
          }
          if constexpr (jb == 2 && (gp == 1 || gp == 2)) {
            dk[gp * 2 - 2] = c_ == r_ * 4 + gp * 2 - 2 ? ~0u : 0u; dk[gp * 2 - 1] = c_ == r_ * 4 + gp * 2 - 1 ? ~0u : 0u;
            PIN2(dk[gp * 2 - 2], dk[gp * 2 - 1]);
          }
          if constexpr (jb == 2 && gp == 3) { kl = lrow + (ln >> 5); go = (ln & 30) ? (int)0x80000000 : ((ln & 1) ? O_KEY : O_ST) + (int)kl * 8; PIN2(kl, go); }
          if constexpr (jb == 2 && gp == 4) { kl = 0xffff - kl; go = (ln & ~1) == 2 ? O_CE + (z * D_ + drow + (ln & 1)) * 8 : go; PIN2(kl, go); }
          if constexpr (jb == 2 && gp == 5) {
            gro = (p_ < 2 ? O_ST + (st * 128 + p_ * 64) * 8 : O_CE + (z * D_ + e_ * 128 + (p_ - 2) * 64) * 8) + ln * 8;
            tv2 = (p_ * 64 + ln) * 4096;
            lma = lds_addr(smem + LDS_MB) + (p_ < 2 ? p_ * 256 : 512 + p_ * 256) + ln * 4;
            PIN3(gro, tv2, lma);
          }
          if constexpr (jb == 7 && gp == 0) {
            const int r0 = p_ * 8 + (ln >> 3);
            vn2 = r0 * (S_ * 2) + ((ln & 7) ^ ((r0 >> 1) & 7)) * 16; vm2 = r0 * 8192 + (vm & 127);
            PIN2(vn2, vm2);
          }
          if constexpr (jb == 7 && gp == 1) { vdn = jj * 112 + p_ * 28 + r_ * 7; vgu = (((ln >> 3) & 1) * 4 * F_ + vdn + min(ln & 7, 6)) * 16; PIN2(vdn, vgu); }
          if constexpr (jb == 7 && gp == 2) { vdn = (vdn * D_ + cg8 * 128 + c_ * 8) * 2; PIN1(vdn); }
          if constexpr (jb == 7 && gp == 3) {
            abo = (((kq * 8 + e_) * 4 + p_) * 16 + c_) * AB_STRIDE * 4;
            abq[2] = ln >= 16 || (ln & 7) == t_ ? (int)0x80000000 : abo;
            PIN2(abo, abq[2]);
          }
          if constexpr (jb == 7 && gp == 4) {
            abq[1] = ln >= 8 ? (int)0x80000000 : abq[2];
            abq[0] = ((ln ^ t_) & 4) ? (int)0x80000000 : abq[1];
            PIN2(abq[0], abq[1]);
          }
          if constexpr (jb == 7 && gp == 5) {
            const unsigned mb = lds_addr(smem) + p_ * 1024;
            mq0 = mb + ((3 - kq) & 3) * 4096; mq1 = mb + ((4 - kq) & 3) * 4096; mq2 = mb + ((5 - kq) & 3) * 4096; mq3 = mb + ((6 - kq) & 3) * 4096;
            pin4(mq0, mq1, mq2, mq3);
          }
          if constexpr (jb == 8 && gp < 4) {
#pragma unroll
            for (int h = 0; h < 2; ++h) {
              const unsigned hv = h ? 0x0302u : 0x0100u;
              sel[h][gp] = ((ln & 7) >> 1) == gp ? ((ln & 1) ? (hv << 16) | 0x0c0cu : 0x0c0c0000u | hv) : 0x0c0c0c0cu;
            }
            PIN2(sel[0][gp], sel[1][gp]);
          }
        }
        SB();
      };
#define WS1(i) setup(IC<1>{}, IC<i>{})
#define WS2(i) setup(IC<2>{}, IC<i>{})
#define WS3(i) do { setup(IC<7>{}, IC<i>{}); if (i >= 4) setup(IC<8>{}, IC<(i) & 3>{}); } while (0)
      G1_STAGE_TILE(1, G1_STAGE, WS1);
      WAIT_VM(19); BAR();
      G1_STAGE_TILE(2, 2 * G1_STAGE, WS2);
      G1_STAGE_TILE(3, 3 * G1_STAGE, WS3);
      auto work1 = [&](auto JB, auto GP) __attribute__((always_inline)) {
        constexpr int jb = decltype(JB)::value, gp = decltype(GP)::value;
        SB();
        if constexpr (jb == 1) {
          const f32x4 z4 = {0.f, 0.f, 0.f, 0.f};
          if constexpr (gp == 0) { sa4 = MM(lv[0], oh[0], z4); gw[0] = MM(lv[0], lw[0], z4); gx[0] = MM(lv[0], lv[0], z4); PIN3(sa4, gw[0], gx[0]); }
          if constexpr (gp == 1) { sa4 = MM(lv2[0], oh[1], sa4); gw[1] = MM(lv2[0], lw[0], z4); gx[1] = MM(lv2[0], lv2[0], z4); PIN3(sa4, gw[1], gx[1]); }
          if constexpr (gp == 2) { sa4 = MM(lv[1], oh[0], sa4); gw[0] = MM(lv[1], lw[1], gw[0]); gx[0] = MM(lv[1], lv[1], gx[0]); PIN3(sa4, gw[0], gx[0]); }
          if constexpr (gp == 3) { sa4 = MM(lv2[1], oh[1], sa4); gw[1] = MM(lv2[1], lw[1], gw[1]); gx[1] = MM(lv2[1], lv2[1], gx[1]); PIN3(sa4, gw[1], gx[1]); }
          if constexpr (gp == 4) { sa4 = MM(ld[0], oh[2], sa4); sa4 = MM(ld[1], oh[2], sa4); PIN1(sa4); }
          if constexpr (gp == 5) { sa4 = MM(ld2[0], oh[3], sa4); sa4 = MM(ld2[1], oh[3], sa4); PIN1(sa4); }
          if constexpr (gp == 6) { sa4 = MM(lw[0], oh[4], sa4); sa4 = MM(lw[1], oh[4], sa4); PIN1(sa4);
            epoch = __builtin_amdgcn_readfirstlane(epv); tag = ((epoch + 1u) * 0x9E3779B1u ^ dmix) | 1u; }
        }
        if constexpr (jb == 2) {
          if constexpr (gp == 0) { su = (sa4[0] + sa4[1]) + (sa4[2] + sa4[3]); ta = diag(gw[0]); PIN2(su, ta); }
          if constexpr (gp == 1) { su = fold32(su, su); tb = diag(gw[1]); PIN2(su, tb); }
          if constexpr (gp == 2) { su = fold16(su, su); gs = fold32(ta, tb); PIN2(su, gs); }
          if constexpr (gp == 3) { ta = diag(gx[0]); tb = diag(gx[1]); PIN2(ta, tb); }
          if constexpr (gp == 4) { gs = fold16(gs, fold32(ta, tb)); PIN1(gs); }
          if constexpr (gp == 5) { gs += dpp_f<0xB1>(gs); gs += dpp_f<0x4E>(gs); PIN1(gs); }
          if constexpr (gp == 6) { gs += dpp_f<0x141>(gs); gs += dpp_f<0x140>(gs); PIN1(gs); }
        }
        if constexpr (jb == 3) {
          if constexpr (gp == 0) {
            const float s0 = rdl(su, 0), s1 = rdl(su, 1), q0 = rdl(gs, 16), q1 = rdl(gs, 48);
            sx = hb ? s1 : s0; ta = hb ? q1 : q0; PIN2(sx, ta);
          }
          if constexpr (gp == 1) {
            const float mean = sx * (1.0f / D_);
            mbv = rbf(mean);
            ta -= sx * mean;
            if (__builtin_expect(__any(!(sx * mean <= 16.f * ta)), 0)) {
              float q = fold32(sqdev16(lv, rdl(mbv, 0)), sqdev16(lv2, rdl(mbv, 32)));
              q = row_sum(fold16(q, q));
              const float dm = mean - mbv;
              ta = fmaxf(q - (float)D_ * dm * dm, 0.f);
            }
            PIN2(mbv, ta);
          }
          if constexpr (gp == 2) { ta = rbf(rbf(ta * (1.0f / D_)) + 1e-5f); gs -= mbv * rdl(su, 4); PIN2(ta, gs); }
          if constexpr (gp == 3) { ta = rbf(__builtin_amdgcn_sqrtf(ta)); tb = __builtin_amdgcn_rcpf(ta); PIN2(ta, tb); }
          if constexpr (gp == 4) {
            const unsigned rb = __float_as_uint(rbf(gs * tb));
            kl |= (rb ^ ((unsigned)((int)rb >> 31) | 0x80000000u)) & 0xffff0000u; PIN1(kl);
          }
          if constexpr (gp == 5) { kl = (lane & 2) ? __float_as_uint(su) : (lane & 1) ? kl : (__float_as_uint(mbv) >> 16) | __float_as_uint(ta); PIN1(kl); }
        }
        if constexpr (jb == ST_JB + 3) {
          if constexpr (gp == 0) { if (!__all(gr.y == tag)) gr = granule_wait((const u64*)((const char*)(flags + FL_AB) + gro), tag); PIN1(gr); }
          if constexpr (gp == 1) { g1 = (z ? -1.0f : 1.0f) / hi_bf(gr.x); PIN1(g1); }
          if constexpr (gp == 2) { g0 = pw < 2 ? -(lo_bf(gr.x) * g1) : __uint_as_float(gr.x); PIN1(g0); }
        }
        if constexpr (gp < 7 && (jb - 1) * 7 + gp < 64) {
          constexpr int n = (jb - 1) * 7 + gp;
          sld<((n >> 1) & 7) * 4096 + (n & 1) * 64>(sd, spb, KROT(n >> 4));
        }
        SB();
      };
      auto step1 = [&](auto JB) __attribute__((always_inline)) {
        constexpr int jb = decltype(JB)::value;
        if constexpr (jb == 1) PAGE(1);
        if constexpr (jb == 7) PAGE(2);
        constexpr int n16 = jb == 12 ? 12 : jb == 11 ? 14 : 16;
        constexpr auto wl = [](int q) { return (q == 3 ? 1 : 0) + (q == ST_JB ? 1 : 0); };
        if constexpr (jb == 1) {
          asm volatile("s_waitcnt vmcnt(%11)" : "+v"(lv[0]), "+v"(lv[1]), "+v"(lw[0]), "+v"(lw[1]), "+v"(ld[0]), "+v"(ld[1]),
                       "+v"(lv2[0]), "+v"(lv2[1]), "+v"(ld2[0]), "+v"(ld2[1]), "+v"(epv) : "i"(16) : "memory");
        } else if constexpr (jb == ST_JB + 3) {
          asm volatile("s_waitcnt vmcnt(%1)" : "+v"(gr) : "i"(n16 + wl(jb - 2) + wl(jb - 1)) : "memory");
        } else wait_vm<n16 + wl(jb - 3) + wl(jb - 2) + wl(jb - 1)>();
        BAR();
        if constexpr (jb == 2) { if (pw == 0 && lane < 16) ((int*)(smem + LDS_SEL))[lane < 8 ? lane : lane + 8] = 0; }
        if constexpr (jb == 4) { if (pw == 0 && lane == 0) ((unsigned*)(smem + LDS_SEL))[5] = tag; }
#define W1(i) work1(JB, IC<i>{})
        if constexpr (jb < 10) G1_STAGE_TILE(jb - 1 + G1_NST, ((jb - 1) % G1_NST) * G1_STAGE, W1);
        else G1_STAGE_A(jb - 1 + G1_NST, ((jb - 1) % G1_NST) * G1_STAGE, W1);
        if constexpr (jb == 12) { G1_STAGE_B(13, G1_STAGE); G1_STAGE_B(14, 2 * G1_STAGE); }
        if constexpr (jb == 3) __builtin_amdgcn_raw_buffer_store_b64((u32x2){kl, tag}, rf, go, 0, 16);
        if constexpr (jb == ST_JB) {
          asm volatile("buffer_load_dwordx2 %0, %1, %2, 0 offen sc1" : "=v"(gr) : "v"(gro), "s"(rf) : "memory");
        }
        touches(IC<100 + jb>{});
        if constexpr (jb <= 8) {
          if (et == jb - 1) {
#pragma unroll
            for (int l = 0; l < WSN; ++l) bld16_nt_io(wv[l], rw, vw, l * F_ * 2);
          }
        }
        SB();
      };
      static_assert(ST_JB + 3 <= 12 && ST_JB >= 6, "schedule");
      step1(IC<1>{}); step1(IC<2>{}); step1(IC<3>{}); step1(IC<4>{}); step1(IC<5>{}); step1(IC<6>{}); step1(IC<7>{}); step1(IC<8>{}); step1(IC<9>{}); step1(IC<10>{}); step1(IC<11>{}); step1(IC<12>{});
    }
#define G2_STAGE_N(kk_, W) do { const int kb = KROT(kk_); constexpr int so = n_off(kk_); SB(); \
    DMA_(rn2, vn2, mq0, so, kb, 0, ""); W(0); DMA_(rn2, vn2, mq1, so, kb, 32 * S_ * 2, ""); W(1); \
    DMA_(rn2, vn2, mq2, so, kb, 64 * S_ * 2, ""); W(2); DMA_(rn2, vn2, mq3, so, kb, 96 * S_ * 2, ""); W(3); \
    DMA_(rn2, vn2, mq0, so + 16384, kb, 128 * S_ * 2, ""); W(4); DMA_(rn2, vn2, mq1, so + 16384, kb, 160 * S_ * 2, ""); W(5); \
    DMA_(rn2, vn2, mq2, so + 16384, kb, 192 * S_ * 2, ""); W(6); DMA_(rn2, vn2, mq3, so + 16384, kb, 224 * S_ * 2, ""); W(7); SB(); } while (0)
#define G2_STAGE_NH(kk_, h) do { const int kb = KROT(kk_); constexpr int so = n_off(kk_) + (h) * 16384; SB(); \
    DMA_(rn2, vn2, mq0, so, kb, (h) * 128 * S_ * 2, ""); DMA_(rn2, vn2, mq1, so, kb, ((h) * 128 + 32) * S_ * 2, ""); \
    DMA_(rn2, vn2, mq2, so, kb, ((h) * 128 + 64) * S_ * 2, ""); DMA_(rn2, vn2, mq3, so, kb, ((h) * 128 + 96) * S_ * 2, ""); SB(); } while (0)
#define G2_STAGE_M(kk_, W) do { const int kb = KROT(kk_); constexpr int so = G2_MRING + m_off(kk_); SB(); \
    DMA_(rm2, vm2, mb0, so, kb, 0, " sc1"); W(8); DMA_(rm2, vm2, mb0, so + 4096, kb, 32 * 8192, " sc1"); W(9); \
    DMA_(rm2, vm2, mb0, so + 8192, kb, 64 * 8192, " sc1"); W(10); DMA_(rm2, vm2, mb0, so + 12288, kb, 96 * 8192, " sc1"); W(11); SB(); } while (0)
    WAIT_VM(16); BAR();
    asm volatile("ds_write_b32 %0, %1" :: "v"(lma), "v"(g0) : "memory");
    if (pw < 2) asm volatile("ds_write_b32 %0, %1 offset:512" :: "v"(lma), "v"(g1) : "memory");
    u32x4 tv[4];
    unsigned ready = 0;
    auto need = [&](int k) __attribute__((always_inline)) {
      const int b = k_blk(k) + (k_ord(k) & 1) * 8;
#pragma unroll 1
      for (int it = 0; !((ready >> b) & 1) && it < SPIN_MAX; ++it) {
        ready |= (unsigned)__ballot(__builtin_amdgcn_raw_buffer_load_b32(rf, abq[k < 5 ? 0 : k < 9 ? 1 : 2], 0, 16) == tag);
        asm volatile("" ::: "memory");
      }
    };
    unsigned ksel, bad = 0;
    int isel;
    unsigned xw;
    auto bvec = [&](unsigned src, const unsigned (&sl)[4]) __attribute__((always_inline)) {
      return (u32x4){__builtin_amdgcn_perm(0, src, sl[0]), __builtin_amdgcn_perm(0, src, sl[1]),
                     __builtin_amdgcn_perm(0, src, sl[2]), __builtin_amdgcn_perm(0, src, sl[3])};
    };
#define BC8(v, p) (unsigned)__builtin_amdgcn_update_dpp(__builtin_amdgcn_update_dpp(0, (int)(v), 0x150 + (p), 0xf, 0x3, false), (int)(v), 0x158 + (p), 0xf, 0xc, false)
    float* lg = (float*)(smem + LDS_GU);
    f32x4 macc = {0.f, 0.f, 0.f, 0.f};
    u32x2 sv;
    unsigned hp, gbad;
    float s0, s1;
    auto work2 = [&](auto JB, auto GP) __attribute__((always_inline)) {
      constexpr int jb = decltype(JB)::value, gp = decltype(GP)::value;
      SB();
      if constexpr (jb == -1 && gp < 4) {
        constexpr int c = gp == 0 ? 0xB1 : gp == 1 ? 0x4E : gp == 2 ? 0x141 : 0x140;
        merge2(k1, k2, dpp_u<c>(k1), dpp_u<c>(k2));
        PIN2(k1, k2);
      }
      if constexpr (jb == -1 && gp == 4) {
        ksel = tk ? k2 : k1;
        isel = (0xffff - (int)(ksel & 0xffffu)) & (S_ - 1);
        k1 = (isel * D_ + d0 + rq * 16 + (lane & 7) * 2) * 2; k2 = O_ST + isel * 8;
        PIN1(ksel);
        xw = gld4_s(x, k1);
        sv = bld8_sc1(rf, k2);
      }
      if constexpr (jb == 0 && gp == 1) wait_xs<3 + 1 + 2>(xw, sv);
      if constexpr (jb == 0) {
        if constexpr (gp == 1) { if (!__all(sv.y == tag)) sv = granule_wait(stat + isel, tag); s0 = __builtin_amdgcn_rcpf(hi_bf(sv.x)); PIN1(s0); }
        if constexpr (gp == 2) { s1 = ln_elem(lo_bf(xw), lo_bf(sv.x), hi_bf(sv.x), s0); PIN1(s1); }
        if constexpr (gp == 3) { xw = pk2(s1, ln_elem(hi_bf(xw), lo_bf(sv.x), hi_bf(sv.x), s0)); PIN1(xw); }
      }
      if constexpr (jb == DG && gp < 7) { PIN1(dv[gp]); macc = MM(dv[gp], bvec(BC8(hp, gp), sel[0]), macc); PIN1(macc); }
      if constexpr ((jb == 0 && gp >= 4) || jb == 1) {
        constexpr int l = jb == 0 ? gp - 4 : gp + 4;
        if constexpr (!(l & 1)) hp = BC8(xw, l >> 1);
        PIN1(wv[l]);
        macc = MM(wv[l], bvec(hp, sel[l & 1]), macc);
        PIN2(macc, hp);
      }
      if constexpr (jb == GV_JB + 2) {
        if constexpr (gp == 0) { gbad = (gv[0].y ^ tag) | (gv[0].w ^ tag) | (gv[1].y ^ tag) | (gv[1].w ^ tag); PIN1(gbad); }
        if constexpr (gp == 1) { gbad |= (gv[2].y ^ tag) | (gv[2].w ^ tag) | (gv[3].y ^ tag) | (gv[3].w ^ tag); PIN1(gbad); }
        if constexpr (gp == 2) {
          s0 = ((__uint_as_float(gv[0].x) + __uint_as_float(gv[1].x)) + __uint_as_float(gv[2].x)) + __uint_as_float(gv[3].x);
          s1 = ((__uint_as_float(gv[0].z) + __uint_as_float(gv[1].z)) + __uint_as_float(gv[2].z)) + __uint_as_float(gv[3].z);
          PIN2(s0, s1);
        }
        if constexpr (gp == 3) {
#pragma unroll 1
          for (int it = 0; !__all(gbad == 0) && it < SPIN_MAX; ++it) {
            u32x4 q[4];
            gbad = 0;
#pragma unroll
            for (int i = 0; i < 4; ++i) { q[i] = __builtin_amdgcn_raw_buffer_load_b128(rgu, vgu, i * F_ * 16, 16); gbad |= (q[i].y ^ tag) | (q[i].w ^ tag); }
            s0 = ((__uint_as_float(q[0].x) + __uint_as_float(q[1].x)) + __uint_as_float(q[2].x)) + __uint_as_float(q[3].x);
            s1 = ((__uint_as_float(q[0].z) + __uint_as_float(q[1].z)) + __uint_as_float(q[2].z)) + __uint_as_float(q[3].z);
            asm volatile("" ::: "memory");
          }
          const unsigned gp2 = pk2(s0, s1);
          const unsigned up2 = dpp_u<0x128>(gp2);
          s0 = tk ? hi_bf(up2) : lo_bf(gp2); s1 = tk ? hi_bf(gp2) : lo_bf(up2);
          PIN2(s0, s1);
        }
        if constexpr (gp == 4) { s0 = rbf(s0 * __builtin_amdgcn_rcpf(1.f + __expf(-s0))); PIN1(s0); }
        if constexpr (gp == 5) { hp = pk2(s0 * s1, 0.f); PIN1(hp); }
      }
      if constexpr (jb == 10 && gp == 2) epi_setup();
      if constexpr (jb == 10 && gp == 4) code_touch(sd, (j * 4 + pw) * 256);
      SB();
    };
#define W2M(i) work2(IC<-1>{}, IC<i>{})
#define W20(i) work2(IC<0>{}, IC<i>{})
#define W21(i) work2(IC<1>{}, IC<i>{})
    unsigned fs;
    {
      G1_STAGE_B(15, 3 * G1_STAGE);
      G2_STAGE_NH(0, 0);
      lgkm0(sd);
      WAIT_VM(16); BAR();
      G2_STAGE_NH(0, 1);
      WAIT_VM(10); BAR();
#pragma unroll
      for (int i = 0; i < 4; ++i) kk[i] = bld16_sc1(rf, l16, ko_ + i * 1024);
#pragma unroll
      for (int l = WSN; l < 16; ++l) wv[l] = bld16_nt(rw, vw, l * F_ * 2);
      asm volatile("s_waitcnt vmcnt(%4)" : "+v"(kk[0]), "+v"(kk[1]), "+v"(kk[2]), "+v"(kk[3]) : "i"(16 - WSN) : "memory");
      touches(IC<115>{});
#pragma unroll
      for (int i = 0; i < 4; ++i) {
        k2 = max(k2, min(k1, kk[i].x)); k1 = max(k1, kk[i].x);
        k2 = max(k2, min(k1, kk[i].z)); k1 = max(k1, kk[i].z);
        bad |= (kk[i].y ^ tag) | (kk[i].w ^ tag);
      }
#pragma unroll 1
      for (int it = 0; !__all(bad == 0) && it < SPIN_MAX; ++it) {
        k1 = 0; k2 = 0; bad = 0;
#pragma unroll
        for (int i = 0; i < 4; ++i) {
          const u32x4 q = __builtin_amdgcn_raw_buffer_load_b128(rf, l16, ko_ + i * 1024, 16);
          k2 = max(k2, min(k1, q.x)); k1 = max(k1, q.x);
          k2 = max(k2, min(k1, q.z)); k1 = max(k1, q.z);
          bad |= (q.y ^ tag) | (q.w ^ tag);
        }
        asm volatile("" ::: "memory");
      }
      merge2(k1, k2, dpp_u<0xB1>(k1), dpp_u<0xB1>(k2));
      merge2(k1, k2, dpp_u<0x4E>(k1), dpp_u<0x4E>(k2));
      merge2(k1, k2, dpp_u<0x141>(k1), dpp_u<0x141>(k2));
      merge2(k1, k2, dpp_u<0x140>(k1), dpp_u<0x140>(k2));
      if ((lane & 15) == 0) *(u32x2*)(smem + LDS_KEY + (pw * 4 + rq) * 8) = (u32x2){k1, k2};
      PAGE(3);
      BAR_LGKM();
#pragma unroll
      for (int jj = 0; jj < 4; ++jj) tv[jj] = *(const u32x4*)(smem + G2_MRING + m_off(0) + (jj * 4 + pw) * 1024 + lane * 16);
      { const u32x2 kp = *(const u32x2*)(smem + LDS_KEY + ci * 8); k1 = kp.x; k2 = kp.y; }
#pragma unroll
      for (int jj = 0; jj < 4; ++jj) __builtin_amdgcn_raw_buffer_store_b128(tv[jj], rm2, vm2 + jj * 32 * 8192 + 2 * tt * 128, 0, 16);
      G2_STAGE_N(1, W2M);
      WAIT_VM(10);
      if (lane == tt) __builtin_amdgcn_raw_buffer_store_b32(tag, rf, abo, 0, 16);
      G2_STAGE_N(2, W20);
      __builtin_amdgcn_s_sleep(2);
      WAIT_VM(9);
      fs = bld4_sc1(rf, abq[0]);
    }
    BAR();
#pragma unroll
    for (int jj = 0; jj < 4; ++jj) tv[jj] = *(const u32x4*)(smem + G2_MRING + m_off(1) + (jj * 4 + pw) * 1024 + lane * 16);
    G2_STAGE_N(3, NOW);
    asm volatile("s_waitcnt vmcnt(8)" : "+v"(fs) :: "memory");
    ready = (unsigned)__ballot(fs == tag);
    need(2);
    G2_STAGE_M(2, NOW);
    need(3);
    G2_STAGE_M(3, NOW);
    fs = bld4_sc1(rf, abq[1]);
    touches(IC<1>{});
    W21(0); W21(1); W21(2); W21(3); W21(4); W21(5); W21(6); W21(7); W21(8); W21(9); W21(10); W21(11);
    asm volatile("" : "+v"(tv[0]), "+v"(tv[1]), "+v"(tv[2]), "+v"(tv[3]));
    auto step2 = [&](auto JB) __attribute__((always_inline)) {
      constexpr int jb = decltype(JB)::value;
      if constexpr (jb == 3) PAGE(4);
      if constexpr (jb == 9) PAGE(5);
      constexpr auto must = [](int q) { return (q == 2 ? 4 : 0) + ((FSM >> q) & 1) + ((q == GV_JB || q == GV_JB - 1) ? 2 : 0); };
      if constexpr (jb == 2) wait_vm<4 + must(1)>();
      else wait_vm<dn_cnt(jb - 2) + (jb == 5 ? 1 : 0) + 12 + must(jb - 1) + dn_cnt(jb - 1)>();
      if constexpr (jb == 3) { if (pw == 0 && (lane & ~8) == 0) ((unsigned*)(smem + LDS_SEL))[tk] = ksel; }
      if constexpr ((jb == 2 || jb == DG + 1)) {
        *(f32x4*)(lg + pw * 256 + lane * 4) = macc;
        macc = (f32x4){0.f, 0.f, 0.f, 0.f};
        lgkm0(sd);
      }
      BAR();
      float p0[4], p1[4];
      const int col = (pw & 1) * 64 + lane;
      if constexpr (jb == 3) {
        if (g == 0 && pw == 0 && lane == 0) __hip_atomic_store((unsigned*)flags + FL_EPOCH, epoch + 1u, __ATOMIC_RELAXED, __HIP_MEMORY_SCOPE_AGENT);
      }
      if constexpr (jb == DG + 1) {
        const float* p = lg + (pw & 1) * 32 + (lane >> 4) * 64 + ((2 * lane) & 7) * 4 + ((lane >> 2) & 3);
#pragma unroll
        for (int i = 0; i < 4; ++i) { p0[i] = p[i * 256]; p1[i] = p[i * 256 + 4]; }
      }
      if constexpr (jb == 2) {
        const float* p = lg + (col >> 5) * 64 + (col & 7) * 4 + ((col >> 3) & 3);
#pragma unroll
        for (int i = 0; i < 4; ++i) { p0[i] = p[i * 256]; p1[i] = p[i * 256 + 32]; }
      }
      if constexpr (jb == 4) {
        if (lane == 8 + tt) __builtin_amdgcn_raw_buffer_store_b32(tag, rf, abo, 0, 16);
      }
      if constexpr ((FSM >> (jb - 2)) & 1) {
        asm volatile("" : "+v"(fs));
        ready |= (unsigned)__ballot(fs == tag);
      }
      if constexpr (jb == GV_JB + 2) asm volatile("" : "+v"(gv[0]), "+v"(gv[1]), "+v"(gv[2]), "+v"(gv[3]));
#define W2(i) work2(JB, IC<i>{})
      G2_STAGE_N(jb + 2, W2);
      need(jb + 2);
      G2_STAGE_M(jb + 2, W2);
      if constexpr (jb == 2) {
#pragma unroll
        for (int jj = 0; jj < 4; ++jj) __builtin_amdgcn_raw_buffer_store_b128(tv[jj], rm2, vm2 + jj * 32 * 8192 + (2 * tt + 1) * 128, 0, 16);
      }
      if constexpr ((FSM >> jb) & 1) fs = bld4_sc1(rf, abq[jb < 5 ? 1 : 2]);
      if constexpr ((jb == GV_JB || jb == GV_JB - 1)) {
#pragma unroll
        for (int i = (jb == GV_JB ? 2 : 0); i < (jb == GV_JB ? 4 : 2); ++i) gv[i] = bld16_sc1(rgu, vgu, i * F_ * 16);
      }
      touches(JB);
#pragma unroll
      for (int i = dn_first(jb); i < dn_first(jb) + dn_cnt(jb); ++i) dv[i] = bld16_nt(rdn, vdn, i * D_ * 2);
      if constexpr (jb == DG + 1) {
        if (pw < 2)
          __builtin_amdgcn_raw_buffer_store_b128((u32x4){__float_as_uint((p0[0] + p0[1]) + (p0[2] + p0[3])), tag, __float_as_uint((p1[0] + p1[1]) + (p1[2] + p1[3])), tag},
                                                 mk_rsrc(wsp + WS_DN), (((pw * 8 + cg8) * 32 + rgb) * 64 + lane) * 16, 0, 16);
      }
      if constexpr (jb == 2) {
        if (moe && pw < 2)
          __builtin_amdgcn_raw_buffer_store_b128((u32x4){__float_as_uint((p0[0] + p0[1]) + (p0[2] + p0[3])), tag, __float_as_uint((p1[0] + p1[1]) + (p1[2] + p1[3])), tag},
                                                 rgu, ((mat * 4 + dq) * F_ + cg * 128 + col) * 16, 0, 16);
      }
      SB();
    };
    static_assert(DG + 1 <= 12 && DG >= GV_JB + 3 && dn_first(DG - 2) == 7, "schedule");
    step2(IC<2>{}); step2(IC<3>{}); step2(IC<4>{}); step2(IC<5>{}); step2(IC<6>{}); step2(IC<7>{}); step2(IC<8>{}); step2(IC<9>{}); step2(IC<10>{}); step2(IC<11>{}); step2(IC<12>{}); step2(IC<13>{});
    lgkm0(sd);
    PAGE(7);
    WAIT_VM(12); BAR();
    xq[0] = ld16_s<0>(x, xo); xq[1] = ld16_s<64>(x, xo); sq = ld8_s_sc1(stat, xo >> 11 << 3);
    WAIT_VM(3); BAR();
    asm volatile("s_waitcnt vmcnt(0)" : "+v"(xq[0]), "+v"(xq[1]), "+v"(sq) :: "memory");
    __builtin_amdgcn_s_setprio(0);
    key[0] = __builtin_amdgcn_readlane(ksel, 0); key[1] = __builtin_amdgcn_readlane(ksel, 8);
    nf_pre();
#pragma unroll
    for (int n = 0; n < 56; ++n) nf_step(n);
#pragma unroll
    for (int e = 0; e < 16; ++e) nf_unp(e);
#pragma unroll 1
    for (int c = 0; c < 2; ++c) {
      const unsigned kc = __builtin_amdgcn_readlane(ksel, c * 8);
      const int ic = (0xffff - (int)(kc & 0xffffu)) & (S_ - 1);
      if ((ic >> 8) != tt || ((ic >> 5) & 3) != kq) continue;
      const __amdgpu_buffer_rsrc_t rd = mk_rsrc(wsp + WS_DN + (size_t)(c * 8 + et) * 32768);
#pragma unroll 1
      for (int it = 0; it < SPIN_MAX; ++it) {
        if (__all(__builtin_amdgcn_raw_buffer_load_b32(rd, (lane & 31) * 1024 + 1020, 0, 16) == tag)) break;
        __builtin_amdgcn_s_sleep(2);
        asm volatile("" ::: "memory");
      }
      float s0, s1;
      unsigned bad = 1;
#pragma unroll 1
      for (int it = 0; !__all(bad == 0) && it < SPIN_MAX; ++it) {
        bad = 0; s0 = 0.f; s1 = 0.f;
#pragma unroll
        for (int i = 0; i < 8; ++i) {
          const u32x4 q = __builtin_amdgcn_raw_buffer_load_b128(rd, rq * 1024 + pw * 256 + ci * 16, i * 4096, 16);
          bad |= (q.y ^ tag) | (q.w ^ tag); s0 += __uint_as_float(q.x); s1 += __uint_as_float(q.z);
        }
        asm volatile("" ::: "memory");
      }
      s0 = fold32(s0, s1);
      s0 = fold16(s0, s0);
      const float sg = rbf(1.f / (1.f + __expf(-key_score(kc))));
      ((float*)(smem + LDS_FIN))[c * 128 + pw * 32 + ci * 2 + (lane >> 5)] = rbf(rbf(s0) * sg);
      asm volatile("s_waitcnt lgkmcnt(0)" ::: "memory");
      if (lane == 0) asm volatile("ds_add_u32 %0, %1" :: "v"(lds_addr(smem + LDS_SEL + 24 + c * 4)), "v"(1) : "memory");
    }
  } else {
  {
    const int q = j * 4 + wid;
    constexpr int Q1 = WS_MB, Q2 = Q1 + 8, Q3 = Q2 + 8, Q4 = Q3 + 7, Q5 = Q4 + 7, Q6 = Q5 + 7, Q7 = Q6 + 4, Q8 = Q7 + 4, QC = Q8 + 1;
    static_assert(QC <= 128, "warm-up slots");
    unsigned dmy = 0;
    if (q < QC) {
      const char* base; int b0;
      if (q < Q1) { base = wsp; b0 = 0; }
      else if (q < Q2) { base = (const char*)ws_re; b0 = Q1; }
      else if (q < Q3) { base = (const char*)ws_im; b0 = Q2; }
      else if (q < Q4) { base = (const char*)w_gate; b0 = Q3; }
      else if (q < Q5) { base = (const char*)w_up; b0 = Q4; }
      else if (q < Q6) { base = (const char*)w_down; b0 = Q5; }
      else if (q < Q7) { base = (const char*)out; b0 = Q6; }
      else if (q < Q8) { base = (const char*)x; b0 = Q7; }
      else { base = (const char*)w_r; b0 = Q8; }
      base += (size_t)(q - b0) << 20;
      asm volatile("s_load_dword %0, %1, 0" : "+s"(dmy) : "s"(base) : "memory");
    }
    {
      const int w = g * 4 + wid;
      constexpr int R = (int)(WS_FLAGS - WS_DN);
      static_assert(FL_X == 0 && FL_ST == 6144 * 32 && FL_EPOCH == 6528 * 32, "");
      const int o = lane < 6 ? R + (lane * 1024 + w) * 128 : lane > 6 || w >= 896 ? (int)0x80000000 : w < 512 ? w * 1024 + 896 : R + (6144 - 512 + w) * 128;
      asm volatile("s_mov_b32 m0, %0\n\ts_nop 0\n\tbuffer_load_dword %1, %2, 0 offen sc1 lds"
                   :: "s"(lds_addr(smem + LDS_DUMMY) + wid * 1024 + 512), "v"(o), "s"(mk_rsrc(wsp + WS_DN)) : "memory");
    }
    if (wid == 0) { CALL_PG(1); CALL_PG(8); CALL_PG(5); }
    else if (wid == 1) { CALL_PG(2); CALL_PG(9); CALL_PG(6); }
    else if (wid == 2) { CALL_PG(3); CALL_PG(10); CALL_PG(7); }
    else { CALL_PG(4); CALL_PG(11); }
    asm volatile("s_barrier\n\ts_waitcnt lgkmcnt(0)" : "+s"(dmy) :: "memory");
  }
  PAGE(8);
  epi_setup();
  const int wr = wid >> 1, wc = wid & 1;
  const int sw = (fr >> 1) & 7;
  const int ko0 = ((0 + fq) ^ sw) * 16, ko1 = ((4 + fq) ^ sw) * 16;
  const int mrow = (fr >> 2) * 8 + (fr & 3);
  bf16x8 fm[2][4], fn[2][4];
  {
    const char* pm = smem + (wr * 64 + mrow) * 128;
    const char* pn = smem + 16384 + (wc * 64 + mrow) * 128;
    f32x4 acc[4][4];
#pragma unroll
    for (int i = 0; i < 4; ++i)
#pragma unroll
      for (int jn = 0; jn < 4; ++jn)
#pragma unroll
        for (int q = 0; q < 4; ++q) acc[i][jn][q] = 0.f;
#define TO(i) (((i) >> 1) * 4096 + ((i) & 1) * 512)
#define LDSP(T, a) ((__attribute__((address_space(3))) T*)(size_t)(a))
#define SLOTS(n) _Pragma("unroll") for (int q = 0; q < (n); ++q)
#define MFA(c, a, b) asm volatile("v_mfma_f32_16x16x32_bf16 %0, %1, %2, %0" : "+v"(c) : "v"(a), "v"(b))
#define MFA2(c, a, b, a2, b2) asm volatile("v_mfma_f32_16x16x32_bf16 %0, %1, %2, %0\n\tv_mfma_f32_16x16x32_bf16 %0, %3, %4, %0" : "+v"(c) : "v"(a), "v"(b), "v"(a2), "v"(b2))
    unsigned lm[2][2], ln[2][2];
#pragma unroll
    for (int u = 0; u < 2; ++u)
#pragma unroll
      for (int kk = 0; kk < 2; ++kk) {
        lm[u][kk] = lds_addr(pm) + u * 2 * G1_STAGE + (kk ? ko1 : ko0);
        ln[u][kk] = lds_addr(pn) + u * 2 * G1_STAGE + (kk ? ko1 : ko0);
        asm volatile("" : "+v"(lm[u][kk]), "+v"(ln[u][kk]));
      }
    auto ldm = [&](int stg, int i, int kk) __attribute__((always_inline)) { fm[kk][i] = *LDSP(const bf16x8, lm[stg >> 1][kk] + (stg & 1) * G1_STAGE + TO(i)); };
    auto ldn = [&](int stg, int jn, int kk) __attribute__((always_inline)) { fn[kk][jn] = *LDSP(const bf16x8, ln[stg >> 1][kk] + (stg & 1) * G1_STAGE + TO(jn)); };
    auto quad = [&](int a, int c, int d, int s) __attribute__((always_inline)) {
      const int i = (s < 2 || s >= 6) ? a : a + 1, jn = s < 4 ? c : d, kk = (s == 1 || s == 2 || s == 5 || s == 6);
      MFA(acc[i][jn], fm[kk][i], fn[kk][jn]);
    };
    auto quad2 = [&](int a, int c, int d, int s) __attribute__((always_inline)) {
      const int i = (s < 2 || s >= 6) ? a : a + 1, jn = s < 4 ? c : d, kk = (s == 2 || s == 6);
      MFA2(acc[i][jn], fm[kk][i], fn[kk][jn], fm[1 - kk][i], fn[1 - kk][jn]);
    };
    auto half1 = [&](int stg) __attribute__((always_inline)) {
      const int ja = (stg & 1) * 2, jb = 2 - ja;
      SLOTS(8) {
        quad(0, ja, ja + 1, q);
        if (q < 4) ldn(stg, jb + (q >> 1), q & 1); else ldm(stg, 2 + ((q - 4) >> 1), q == 5 || q == 6);
        SB();
      }
      SLOTS(4) { quad2(0, jb, jb + 1, 2 * q); SB(); }
    };
    auto half2 = [&](int stg) __attribute__((always_inline)) {
      const int ja = (stg & 1) * 2, jb = 2 - ja, nx = (stg + 1) & 3;
      SLOTS(8) {
        quad(2, jb + 1, jb, q);
        if (!(q & 1)) ldm(nx, q >> 2, q == 2 || q == 4);
        SB();
      }
      SLOTS(8) {
        quad(2, ja, ja + 1, q);
        if (!(q & 1)) ldn(nx, jb + (q >> 2), (q >> 1) & 1);
        SB();
      }
    };
    SLOTS(16) asm volatile("" : "+v"(acc[q >> 2][q & 3]));
    SB();
    SLOTS(4) { ldm(0, q >> 1, q & 1); ldn(0, q >> 1, q & 1); }
    SB();
#pragma unroll 1
    for (int k4 = 0; k4 < G1_NKT - 4; k4 += 4) {
      half1(0); BAR_LGKM();
      SB(); half2(0);
      half1(1); BAR_LGKM();
      SB(); half2(1);
      half1(2); BAR_LGKM();
      SB(); half2(2);
      half1(3); BAR_LGKM();
      SB(); half2(3);
    }
    bf16x8 gm[2][4], gn[2][8];
    unsigned am[2][2], an[2][2];
#pragma unroll
    for (int u = 0; u < 2; ++u)
#pragma unroll
      for (int kk = 0; kk < 2; ++kk) {
        am[u][kk] = lds_addr(pm) + (u ? 3 : 1) * G1_STAGE + (kk ? ko1 : ko0);
        an[u][kk] = lds_addr(pn) + (u ? 3 : 1) * G1_STAGE + (kk ? ko1 : ko0);
        asm volatile("" : "+v"(am[u][kk]), "+v"(an[u][kk]));
      }
    unsigned al = lds_addr(smem + LDS_MB) + (wr * 32 + fq * 8) * 4, ac = lds_addr(smem + LDS_MB) + (256 + wc * 64 + mrow) * 4;
    unsigned ae = lds_addr(smem + G2_MRING) + (wc * 64 + mrow) * 128 + (((wr * 4 + fq) ^ sw) * 16);
    asm volatile("" : "+v"(al), "+v"(ac), "+v"(ae));
    auto rdg = [&](int set, int t, int h, int r) __attribute__((always_inline)) {
      const int u = t == 15, o = t == 14 ? G1_STAGE : 0;
      if (r == 0 || r == 2 || r == 4 || r == 5) {
        const int i = r >= 4, kk = r == 2 || r == 4;
        gm[set][kk * 2 + i] = *LDSP(const bf16x8, am[u][kk] + o + h * 4096 + i * 512);
      } else {
        const int jn = r < 4 ? 0 : (r - 4) >> 1, kk = r < 4 ? r >> 1 : r & 1;
        gn[set][kk * 4 + jn] = *LDSP(const bf16x8, an[u][kk] + o + TO(jn));
      }
    };
    auto mmg = [&](int set, int h, int q) __attribute__((always_inline)) {
      const int jn = q >> 2, r = q & 3, i = (r >> 1) ^ (jn & 1), kk = r == 1 || r == 2;
      MFA(acc[h * 2 + i][jn], gm[set][kk * 2 + i], gn[set][kk * 4 + jn]);
    };
    f32x4 rsv[2], mrv[2];
    float cev[4], tc[32];
    auto rds = [&](int h, int r) __attribute__((always_inline)) {
      if (r < 2) mrv[r] = *LDSP(const f32x4, al + h * 256 + r * 16); else rsv[r - 2] = *LDSP(const f32x4, al + 512 + h * 256 + (r - 2) * 16);
    };
    auto prep = [&](int n) __attribute__((always_inline)) {
      tc[n] = llvm_fmul_legacy(mrv[(n >> 2) & 1][n & 3], cev[n >> 3]);
      asm volatile("" : "+v"(tc[n]));
    };
    u32x4 ep;
    auto epi = [&](int h, int q) __attribute__((always_inline)) {
      const int jn = q >> 1, i = q & 1;
      asm volatile("v_fmac_f32 %0, %4, %8\n\tv_fmac_f32 %1, %5, %9\n\tv_fmac_f32 %2, %6, %10\n\tv_fmac_f32 %3, %7, %11"
                   : "+v"(tc[q * 4]), "+v"(tc[q * 4 + 1]), "+v"(tc[q * 4 + 2]), "+v"(tc[q * 4 + 3])
                   : "v"(acc[h * 2 + i][jn][0]), "v"(acc[h * 2 + i][jn][1]), "v"(acc[h * 2 + i][jn][2]), "v"(acc[h * 2 + i][jn][3]),
                     "v"(rsv[i][0]), "v"(rsv[i][1]), "v"(rsv[i][2]), "v"(rsv[i][3]));
      ep[i * 2] = pk2(tc[q * 4], tc[q * 4 + 1]); ep[i * 2 + 1] = pk2(tc[q * 4 + 2], tc[q * 4 + 3]);
      if (i) *LDSP(u32x4, ae + m_off(h) + TO(jn)) = ep;
    };
    SB();
    half1(0);
    BAR_LGKM();
    SB();
    SLOTS(16) {
      if (q < 8) quad(2, 3, 2, q); else quad(2, 0, 1, q - 8);
      if (q < 12) rdg(0, 13, 0, q);
      SB();
    }
    BAR();
    SB();
    SLOTS(16) {
      mmg(0, 0, q);
      if (q < 12) rdg(1, 14, 0, q);
      else { rds(0, q - 12); cev[q - 12] = *LDSP(const float, ac + (((q - 12) >> 1) * 32 + ((q - 12) & 1) * 4) * 4); }
      SB();
    }
    PAGE(9);
    BAR();
    SB();
    SLOTS(16) { mmg(1, 0, q); if ((q & 3) < 3) rdg(0, 15, 0, (q >> 2) * 3 + (q & 3)); if (q >= 8) { prep(2 * q - 16); prep(2 * q - 15); } SB(); }
    SLOTS(16) { mmg(0, 0, q); if ((q & 3) < 3) rdg(1, 13, 1, (q >> 2) * 3 + (q & 3)); prep(16 + q); SB(); if (q >= 9 && (q & 1)) { epi(0, (q - 9) >> 1); SB(); } }
    SLOTS(8) { mmg(1, 1, q); if (q < 6) { rdg(0, 14, 1, 2 * q); rdg(0, 14, 1, 2 * q + 1); } SB(); if (q & 1) { epi(0, 4 + (q >> 1)); SB(); } }
    BAR_LGKM();
    SB();
    SLOTS(8) { mmg(1, 1, 8 + q); if (q < 4) rdg(1, 15, 1, q == 0 ? 1 : q == 1 ? 3 : q + 4); else rds(1, q - 4); SB(); }
    SLOTS(16) { mmg(0, 1, q); if (q < 8) rdg(1, 15, 1, q == 0 ? 0 : q == 1 ? 2 : q < 4 ? q + 2 : q + 4); prep(q); SB(); }
    asm volatile("s_waitcnt lgkmcnt(0)" ::: "memory");
    if (lane == 0) asm volatile("ds_add_u32 %0, %1" :: "v"(lds_addr(smem + LDS_SEL + 12)), "v"(1) : "memory");
    SB();
    SLOTS(16) {
      mmg(1, 1, q);
      if (q < 4) fn[0][q] = *(const bf16x8*)(smem + (wr * 128 + fr) * 128 + (q >> 1) * 2048 + ((q & 1) ? ko1 : ko0));
      else if (q < 8) fm[q & 1][(q - 4) >> 1] = *(const bf16x8*)(smem + G2_MRING + m_off(0) + (wc * 64 + mrow) * 128 + TO((q - 4) >> 1) + ((q & 1) ? ko1 : ko0));
      prep(16 + q);
      SB();
    }
    {
      lds_vi rel = (lds_vi)(smem + LDS_SEL + 12);
      int it = 0;
      while (rel[0] != 4 && it < SPIN_MAX * 8) ++it;
      asm volatile("" ::: "memory");
    }
    asm volatile("s_nop 7");
    SB();
    SLOTS(8) { epi(1, q); SB(); }
  }

  const char* pn = smem + (wr * 128 + fr) * 128;
  const char* pm = smem + G2_MRING + (wc * 64 + mrow) * 128;
  f32x4 acc[8][4];
  auto mm = [&](bool imaj, int set, int q2, int s, int mode) __attribute__((always_inline)) {
    int i, jj, kk;
    if (imaj) { i = s >> 2; const int r = s & 3; jj = (r >> 1) ^ (i & 1); kk = (r == 1 || r == 2); }
    else { jj = s >> 3; const int r = s & 7; i = jj ? 3 - (r >> 1) : (r >> 1); kk = ((r + 1) >> 1) & 1; }
    if (mode == 1 && !(s & 1)) asm volatile("v_mfma_f32_16x16x32_bf16 %0, %1, %2, 0" : "=&v"(acc[q2 * 2 + jj][i]) : "v"(fm[kk][i]), "v"(fn[set][jj * 2 + kk]));
    else if (mode) asm volatile("v_mfma_f32_16x16x32_bf16 %0, %1, %2, %0" : "+v"(acc[q2 * 2 + jj][i]) : "v"(fm[kk][i]), "v"(fn[set][jj * 2 + kk]));
    else acc[q2 * 2 + jj][i] = __builtin_amdgcn_mfma_f32_16x16x32_bf16(fm[kk][i], fn[set][jj * 2 + kk], acc[q2 * 2 + jj][i], 0, 0, 0);
  };
  unsigned bn[2], bm[2], an[2], am[2];
#pragma unroll
  for (int kk = 0; kk < 2; ++kk) {
    bn[kk] = lds_addr(pn) + (kk ? ko1 : ko0); bm[kk] = lds_addr(pm) + (kk ? ko1 : ko0);
    asm volatile("" : "+v"(bn[kk]), "+v"(bm[kk]));
    an[kk] = bn[kk]; am[kk] = bm[kk] + m_off(0);
  }
#define RDN(jt, kk, o) (*LDSP(const bf16x8, an[kk] + (o) + (jt) * 2048))
#define RDM(i, kk, o) (*LDSP(const bf16x8, am[kk] + (o) + TO(i)))
  PAGE(10);
  tag = __builtin_amdgcn_readfirstlane(((const unsigned*)(smem + LDS_SEL))[5]);
  {
    int son = 0;
    auto tile = [&](int kt, int zero) __attribute__((always_inline)) {
      SLOTS(16) {
        mm(true, 0, 0, q, zero);
        if (!(q & 1)) {
          if (q < 8) fm[(q >> 1) & 1][2 + (q >> 2)] = RDM(2 + (q >> 2), (q >> 1) & 1, 0);
          else { const int r = (q - 8) >> 1; fn[1][r] = RDN(2 + (r >> 1), r & 1, 0); }
        }
        SB();
      }
      SLOTS(16) {
        mm(false, 1, 1, q, zero);
        if (!(q & 1) && q < 8) fn[0][q >> 1] = RDN(4 + (q >> 2), (q >> 1) & 1, 0);
        SB();
      }
      const int som = son >> 1;
      son = son == 65536 ? 0 : son + 32768;
      SLOTS(16) {
        mm(false, 0, 2, q, zero);
        if (!(q & 1) && q < 8) fn[1][q >> 1] = RDN(6 + (q >> 2), (q >> 1) & 1, 0);
        if (q == 9 || q == 11) { an[q == 11] = bn[q == 11] + son; asm volatile("" : "+v"(an[q == 11])); }
        if (q == 13 || q == 15) { am[q == 15] = bm[q == 15] + som; asm volatile("" : "+v"(am[q == 15])); }
        SB();
      }
      BAR_LGKM();
      SB();
      SLOTS(16) {
        mm(true, 1, 3, q, zero);
        if (!(q & 1)) {
          if (q < 8) fn[0][q >> 1] = RDN(q >> 2, (q >> 1) & 1, 0);
          else fm[(q >> 1) & 1][(q - 8) >> 2] = RDM((q - 8) >> 2, (q >> 1) & 1, 0);
        }
        SB();
      }
    };
    SB();
    tile(0, 1);
#pragma unroll 1
    for (int kt = 1; kt < G2_NKT - 3; ++kt) tile(kt, 0);
  }
  unsigned fx;
  {
    constexpr int NG = 12;
    static constexpr int g_t[NG] = {0, 0, 1, 1, 2, 2, 2, 1, 0, 0, 1, 2};
    static constexpr int g_q[NG] = {0, 1, 0, 1, 0, 1, 2, 2, 2, 3, 3, 3};
    constexpr int G14 = 1, G15 = 3, GP0 = 5, GP1 = 6;
    constexpr auto chp = [](int g) { return g == 0 || g_t[g] != g_t[g - 1]; };
    constexpr auto chn = [](int g) { return g + 1 < NG && g_t[g + 1] != g_t[g]; };
    static_assert(n_off(13) == 32768 && n_off(14) == 65536 && n_off(15) == 0, "");
    unsigned an2[2];
#pragma unroll
    for (int kk = 0; kk < 2; ++kk) {
      an[kk] = bn[kk]; an2[kk] = bn[kk] + 65536; am[kk] = bm[kk];
      asm volatile("" : "+v"(an[kk]), "+v"(an2[kk]), "+v"(am[kk]));
    }
    int vo;
    u32x4 pk;
    u32x2 kv;
    constexpr int NF0 = 152;
    auto publish = [&](int d, int h) __attribute__((always_inline)) {
      if (h == 0) { vo = lane; PINV(vo); vo = vo * 16 + (((d + kq + 1) & 3) * 4 + kq) * 4 * MSG; PINV(vo); }
      const f32x4 v = acc[2 * d + (h >> 2)][h & 3];
      pk[(h & 1) * 2] = pk2(v[0], v[1]); pk[(h & 1) * 2 + 1] = pk2(v[2], v[3]);
      if (h & 1) __builtin_amdgcn_raw_buffer_store_b128(pk, rs, vo + (h >> 1) * 1024, 0, 16);
    };
    int fo = lane == 0 ? kq * 512 : (int)0x80000000;
#define MSG_FLAG(d) __builtin_amdgcn_raw_buffer_store_b32(tag, rc, fo + (((d) + kq + 1) & 3) * 2048, 0, 16)
    auto rdn = [&](int g, int r) __attribute__((always_inline)) {
      const int t = g_t[g], jt = g_q[g] * 2 + (r >> 1);
      fn[g & 1][r] = *LDSP(const bf16x8, (t == 1 ? an2[r & 1] : an[r & 1]) + (t == 0 ? 32768 : 0) + jt * 2048);
    };
    auto rdm = [&](int t, int i, int kk) __attribute__((always_inline)) { fm[kk][i] = *LDSP(const bf16x8, am[kk] + m_off(13 + t) + TO(i)); };
    auto group = [&](auto GI) __attribute__((always_inline)) {
      constexpr int gi = decltype(GI)::value;
      if (gi == G14) { BAR();
        SB(); }
      if (gi == G15) {
        PAGE(11);
        BAR();
        SB();
        xq[0] = ld16_s<0>(x, xo); xq[1] = ld16_s<64>(x, xo);
        sq = ld8_s_sc1(stat, xo >> 11 << 3);
        kv = *(const u32x2*)(smem + LDS_SEL);
        SB();
      }
#pragma unroll
      for (int q = 0; q < 16; ++q) {
        const int n = gi * 16 + q;
        if (n == NF0) {
          wait_xq<12>(xq, sq);
          key[0] = __builtin_amdgcn_readfirstlane(kv.x); key[1] = __builtin_amdgcn_readfirstlane(kv.y);
          nf_pre();
          SB();
        }
        if (n == 112) {
        }
        if (n == 152) {
        }
        if (n == 168) {
          WAIT_VM(8); MSG_FLAG(0); SB(); }
        if (n == 176) { WAIT_VM(5); MSG_FLAG(1); SB(); }
        if (n == 184) {
          WAIT_VM(2);
          MSG_FLAG(2); fx = bld4_sc1(rc, po); SB(); }
        mm(chp(gi) || chn(gi), gi & 1, g_q[gi], q, 2);
        constexpr bool ni = gi + 1 < NG && (chp(gi + 1 < NG ? gi + 1 : gi) || chn(gi + 1 < NG ? gi + 1 : gi));
        auto nr = [](int r) { return ni && r >= 2 ? 5 - r : r; };
        if constexpr (chp(gi) && chn(gi)) {
          if (q < 4) rdm(g_t[gi], 2 + (q >> 1), q & 1); else if (q < 8) rdn(gi + 1, nr(q - 4)); else if (q < 12) rdm(g_t[gi + 1], (q - 8) >> 1, q & 1);
        } else if constexpr (chp(gi)) {
          if (q < 4) rdm(g_t[gi], 2 + (q >> 1), q & 1);
          else if (gi + 1 < NG && q < 12 && !(q & 1)) rdn(gi + 1, nr((q - 4) >> 1));
        } else if constexpr (chn(gi)) {
          if (!(q & 1)) { if (q < 8) rdn(gi + 1, nr(q >> 1)); else rdm(g_t[gi + 1], (q - 8) >> 2, (q >> 1) & 1); }
        } else {
          if (!(q & 1) && q < 8) rdn(gi + 1, nr(q >> 1));
        }
        if (gi == GP0 && q < 8) publish(0, q);
        if (gi == GP1 && q < 8) publish(1, q);
        if (gi == 9 && q < 8) publish(2, q);
        if (n >= NF0) {
          const int m = (n - NF0) * 2;
          if (m < 56) { nf_step(m); nf_step(m + 1); } else if (m < 72) { nf_unp(m - 56); nf_unp(m - 55); }
        }
        SB();
      }
    };
#define GR4(i) group(IC<i>{}); group(IC<i + 1>{}); group(IC<i + 2>{}); group(IC<i + 3>{});
    GR4(0) GR4(4) GR4(8)
    asm volatile("s_nop 7");
    SB();
  }
#pragma unroll
  for (int i = 0; i < 4; ++i) *(f32x4*)(smem + LDS_ACC + wr * 16384 + wc * 4096 + i * 1024 + lane * 16) = acc[7][i];
#pragma unroll
  for (int i = 0; i < 4; ++i) ya[i] = acc[6][i];
  asm volatile("s_waitcnt lgkmcnt(0)" ::: "memory");
  if (lane == 0) sig[0] = 2;
  asm volatile("s_waitcnt vmcnt(0)" : "+v"(fx) :: "memory");
  seen = (unsigned)__ballot(fx == tag);
  }

  PAGE(6);
  const unsigned rel0 = 0xffff - (key[0] & 0xffffu) - trow, rel1 = 0xffff - (key[1] & 0xffffu) - trow;
  if (min(rel0, rel1) < 16) {
#pragma unroll 1
    for (int c = 0; c < 2; ++c) {
      const unsigned rel = c ? rel1 : rel0;
      if (rel >= 16) continue;
      lds_vi cnt = (lds_vi)(smem + LDS_SEL + 24 + c * 4);
      int it = 0;
      while (cnt[0] != 4 && it < SPIN_MAX * 8) { __builtin_amdgcn_s_sleep(1); ++it; }
      asm volatile("" ::: "memory");
      if (rel == fr) {
        const float* fp = (const float*)(smem + LDS_FIN + c * 512) + (wid & 1) * 64 + fq * 8;
#pragma unroll
        for (int u4 = 0; u4 < 4; ++u4) {
          const f32x4 v = *(const f32x4*)(fp + (u4 >> 1) * 32 + (u4 & 1) * 4);
#pragma unroll
          for (int k = 0; k < 4; k += 2) {
            const unsigned u = pk2(nf[u4 * 4 + k] + v[k], nf[u4 * 4 + k + 1] + v[k + 1]);
            nf[u4 * 4 + k] = lo_bf(u); nf[u4 * 4 + k + 1] = hi_bf(u);
          }
        }
      }
    }
  }
  if (ha) {
    int it = 0;
    while (sig[0] != 2 && it < SPIN_MAX * 8) { __builtin_amdgcn_s_sleep(1); ++it; }
    asm volatile("" ::: "memory");
#pragma unroll
    for (int i = 0; i < 4; ++i) ya[i] = *(const f32x4*)(smem + LDS_ACC + (wid & 2) * 8192 + (wid & 1) * 4096 + i * 1024 + lane * 16);
  }
  u32x4 pp[3][2];
  unsigned e_lo = 0x00003f80u, e_hi = 0x3f800000u;
  asm volatile("" : "+v"(e_lo), "+v"(e_hi));
  __amdgpu_buffer_rsrc_t rs_ = rs, rc_ = rc;
  int mo[3] = {MSG_IDX(0) * 4 * MSG, MSG_IDX(1) * 4 * MSG, MSG_IDX(2) * 4 * MSG}, mv = lane * 16 + ha * 2048, mv2 = mv + 1024;
  asm volatile("" : "+s"(rs_), "+s"(rc_), "+s"(mo[0]), "+s"(mo[1]), "+s"(mo[2]));
  asm volatile("" : "+v"(mv), "+v"(mv2));
#define MSG_POLL(m) do { \
    if (!((seen >> (m)) & 1)) { \
      unsigned pv_, it_ = SPIN_MAX; \
      asm volatile("1:\n\tbuffer_load_dword %0, %3, %4, 0 offen sc1\n\ts_waitcnt vmcnt(0)\n\tv_cmp_eq_u32 vcc, %5, %0\n\ts_mov_b32 %1, vcc_lo\n\t" \
                   "s_bitcmp1_b32 %1, %6\n\ts_cbranch_scc1 2f\n\ts_sub_u32 %2, %2, 1\n\ts_cbranch_scc1 2f\n\ts_sleep 1\n\ts_branch 1b\n2:" \
                   : "=&v"(pv_), "+s"(seen), "+s"(it_) : "v"(po), "s"(rc_), "s"(tag), "i"(m) : "vcc", "scc", "memory"); \
    } \
    pp[m][0] = __builtin_amdgcn_raw_buffer_load_b128(rs_, mv, mo[m], 16); \
    pp[m][1] = __builtin_amdgcn_raw_buffer_load_b128(rs_, mv2, mo[m], 16); } while (0)
#define MSG_ADD(m) do { \
    _Pragma("unroll") for (int u = 0; u < 8; ++u) { \
      ya[u >> 1][(u & 1) * 2] = add_bf(pp[m][u >> 2][u & 3], e_lo, ya[u >> 1][(u & 1) * 2]); ya[u >> 1][(u & 1) * 2 + 1] = add_bf(pp[m][u >> 2][u & 3], e_hi, ya[u >> 1][(u & 1) * 2 + 1]); } } while (0)
  MSG_POLL(2); MSG_POLL(1); MSG_POLL(0);
  SB();
  MSG_ADD(2); MSG_ADD(1);
  asm volatile("" : "+v"(ya[0]), "+v"(ya[1]), "+v"(ya[2]), "+v"(ya[3]));
  SB();
  MSG_ADD(0);
  {
    unsigned ov[8];
    SB();
#pragma unroll
    for (int u = 0; u < 8; ++u) ov[u] = pk2(ya[u >> 1][(u & 1) * 2], ya[u >> 1][(u & 1) * 2 + 1]);
    SB();
#pragma unroll
    for (int u = 0; u < 8; ++u) { xf[2 * u] = add_bf(ov[u], e_lo, xf[2 * u]); xf[2 * u + 1] = add_bf(ov[u], e_hi, xf[2 * u + 1]); }
    SB();
#pragma unroll
    for (int u = 0; u < 8; ++u) ov[u] = pk2(xf[2 * u], xf[2 * u + 1]);
    SB();
#pragma unroll
    for (int u = 0; u < 8; ++u) { nf[2 * u] = add_bf(ov[u], e_lo, nf[2 * u]); nf[2 * u + 1] = add_bf(ov[u], e_hi, nf[2 * u + 1]); }
    SB();
#pragma unroll
    for (int u = 0; u < 8; ++u) ov[u] = pk2(nf[2 * u], nf[2 * u + 1]);
    SB();
    *(u32x4*)((char*)out + (unsigned)xo) = (u32x4){ov[0], ov[1], ov[2], ov[3]};
    *(u32x4*)((char*)out + (unsigned)xo + 64) = (u32x4){ov[4], ov[5], ov[6], ov[7]};
  }
}

extern "C" void kernel_launch(void* const* d_in, const int* in_sizes, int n_in, void* d_out, int out_size,
                              void* d_ws, size_t ws_size, hipStream_t stream) {
  const u16* x = (const u16*)d_in[0];
  const u16* ws_re = (const u16*)d_in[1];
  const u16* ws_im = (const u16*)d_in[2];
  const u16* wd_re = (const u16*)d_in[3];
  const u16* wd_im = (const u16*)d_in[4];
  const u16* w_r = (const u16*)d_in[6];
  const u16* w_gate = (const u16*)d_in[7];
  const u16* w_up = (const u16*)d_in[8];
  const u16* w_down = (const u16*)d_in[9];
  k_main<<<256, 512, 0, stream>>>(x, 0x7fffffffu, 0x00020000u, wd_re, 0x7fffffffu, 0x00020000u, wd_im, 0x7fffffffu, 0x00020000u, w_r, (char*)d_ws,
                                  ws_re, ws_im, w_gate, w_up, w_down, (u16*)d_out);
}
```

```cpp
#include <hip/hip_runtime.h>
#include <hip/hip_bf16.h>
#include <stdint.h>
#include <type_traits>

extern "C" __device__ unsigned long long llvm_dispatch_id(void) __asm("llvm.amdgcn.dispatch.id");
extern "C" __device__ float llvm_fmul_legacy(float, float) __asm("llvm.amdgcn.fmul.legacy");
typedef unsigned short u16;
typedef unsigned long long u64;
typedef __attribute__((ext_vector_type(8))) short bf16x8;
typedef __attribute__((ext_vector_type(4))) unsigned u32x4;
typedef __attribute__((ext_vector_type(2))) unsigned u32x2;
typedef __attribute__((ext_vector_type(4))) float f32x4;
typedef __attribute__((ext_vector_type(2))) float f32x2;
typedef __attribute__((ext_vector_type(2))) __bf16 bf16x2;
typedef __attribute__((address_space(3))) void* lds_vp;
typedef volatile __attribute__((address_space(3))) int* lds_vi;

constexpr int S_ = 2048, D_ = 1024, F_ = 3584;
template <int N> using IC = std::integral_constant<int, N>;

static __device__ __forceinline__ float bf2f(u16 u) { return __uint_as_float((unsigned)u << 16); }
static __device__ __forceinline__ u16 f2bf(float f) {
  __hip_bfloat16 b = __float2bfloat16(f);
  return *reinterpret_cast<u16*>(&b);
}
static __device__ __forceinline__ float rbf(float f) { return bf2f(f2bf(f)); }
static __device__ __forceinline__ float lo_bf(unsigned u) { return __uint_as_float(u << 16); }
static __device__ __forceinline__ float hi_bf(unsigned u) { return __uint_as_float(u & 0xffff0000u); }
static __device__ __forceinline__ unsigned pk2(float a, float b) {
  const bf16x2 v = __builtin_convertvector((f32x2){a, b}, bf16x2);
  return __builtin_bit_cast(unsigned, v);
}
static __device__ __forceinline__ float add_bf(unsigned u, unsigned e, float c) {
  return __builtin_amdgcn_fdot2_f32_bf16(__builtin_bit_cast(bf16x2, u), __builtin_bit_cast(bf16x2, e), c, false);
}
static __device__ __forceinline__ float ln_elem(float x, float mb, float sd, float ri) {
  const float d = rbf(x - mb);
  const float q0 = d * ri;
  return rbf(__builtin_fmaf(__builtin_fmaf(-q0, sd, d), ri, q0));
}
template <int CTRL> static __device__ __forceinline__ float dpp_f(float v) {
  return __int_as_float(__builtin_amdgcn_update_dpp(0, __float_as_int(v), CTRL, 0xf, 0xf, true));
}
template <int CTRL> static __device__ __forceinline__ unsigned dpp_u(unsigned v) {
  return (unsigned)__builtin_amdgcn_update_dpp(0, (int)v, CTRL, 0xf, 0xf, true);
}
static __device__ __forceinline__ float row_sum(float v) {
  v += dpp_f<0xB1>(v);
  v += dpp_f<0x4E>(v);
  v += dpp_f<0x141>(v);
  v += dpp_f<0x140>(v);
  return v;
}
static __device__ __forceinline__ float fold32(float a, float b) {
  const auto r = __builtin_amdgcn_permlane32_swap(__float_as_uint(a), __float_as_uint(b), false, false);
  return __uint_as_float(r[0]) + __uint_as_float(r[1]);
}
static __device__ __forceinline__ float fold16(float a, float b) {
  const auto r = __builtin_amdgcn_permlane16_swap(__float_as_uint(a), __float_as_uint(b), false, false);
  return __uint_as_float(r[0]) + __uint_as_float(r[1]);
}
static __device__ __forceinline__ float rdl(float v, int l) { return __int_as_float(__builtin_amdgcn_readlane(__float_as_int(v), l)); }

#define ST_JB 9
#define WSN 10
#define FSM 0xa2
#define GV_JB 7
#define DG 11
#define TW 1
#define AB_STRIDE 32
#define SPIN_MAX (1 << 17)

static __device__ __forceinline__ unsigned lds_addr(const void* p) { return (unsigned)(size_t)(lds_vp)p; }

constexpr int MSG = 4096;
constexpr size_t WS_ABT   = 0;
constexpr size_t WS_SLAB  = WS_ABT + (size_t)D_ * 4096 * 2;
constexpr size_t WS_GU    = WS_SLAB + (size_t)64 * 64 * MSG;
constexpr size_t WS_DN    = WS_GU + (size_t)8 * F_ * 16;
constexpr size_t WS_FLAGS = WS_DN + (size_t)2 * 8 * 32 * 64 * 16;
constexpr int FL_X = 0;
constexpr int FL_AB = 64 * 64 * 32;
constexpr int FL_ST = FL_AB + 2048 * AB_STRIDE;
constexpr int FL_CE = FL_ST + 4096;
constexpr int FL_KEY = FL_CE + 4096;
constexpr int FL_EPOCH = FL_KEY + 4096;
constexpr int WS_MB = (int)((WS_FLAGS + (FL_EPOCH + 1) * 4 + 1048575) >> 20);

#define WAIT_VM(n) asm volatile("s_waitcnt vmcnt(" #n ")" ::: "memory")
template <int N> static __device__ __forceinline__ void wait_vm() { asm volatile("s_waitcnt vmcnt(%0)" :: "i"(N) : "memory"); }
#define BAR_LGKM() asm volatile("s_waitcnt lgkmcnt(0)\n\ts_barrier" ::: "memory")
#define BAR() asm volatile("s_barrier" ::: "memory")
#define SB() __builtin_amdgcn_sched_barrier(0)
#define PAGE(n) asm volatile("s_branch 2f\n\t.p2align 12\nk_pg" #n ":\n\ts_setpc_b64 s[42:43]\n2:")
#define CALL_PG(n) asm volatile("s_getpc_b64 s[40:41]\n3:\n\ts_mov_b32 s42, k_pg" #n "-3b\n\ts_ashr_i32 s43, s42, 31\n\ts_add_u32 s40, s40, s42\n\ts_addc_u32 s41, s41, s43\n\t" \
                                "s_swappc_b64 s[42:43], s[40:41]" ::: "s40", "s41", "s42", "s43", "scc", "memory")

static __device__ __forceinline__ __amdgpu_buffer_rsrc_t mk_rsrc(const void* p) {
  return __builtin_amdgcn_make_buffer_rsrc((void*)p, 0, 0x7fffffff, 0x00020000);
}
#define DMA_(rsrc, voff, mbase, MI, sbase, SI, pol) do { unsigned _t; \
    asm volatile("s_add_i32 m0, %2, %3\n\ts_add_i32 %0, %5, %6\n\tbuffer_load_dwordx4 %1, %4, %0 offen" pol " lds" \
                 : "=&s"(_t) : "v"(voff), "s"(mbase), "i"(MI), "s"(rsrc), "s"(sbase), "i"(SI) : "memory", "scc"); } while (0)
#define TOUCH(rsrc, voff, mbase, MI, soff) \
    asm volatile("s_add_i32 m0, %1, %2\n\ts_nop 0\n\tbuffer_load_dword %0, %3, %4 offen lds" \
                 :: "v"(voff), "s"(mbase), "i"(MI), "s"(rsrc), "s"(soff) : "memory", "scc")
template <int OFF> static __device__ __forceinline__ u32x4 ld16_s(const void* p, int voff) {
  u32x4 v;
  asm volatile("global_load_dwordx4 %0, %1, %2 offset:%3" : "=v"(v) : "v"(voff), "s"(p), "i"(OFF) : "memory");
  return v;
}
template <int OFF> static __device__ __forceinline__ void sld(unsigned& sink, const void* p, int soff) {
  asm volatile("s_load_dword %0, %1, %2 offset:%3" : "+s"(sink) : "s"(p), "s"(soff), "i"(OFF) : "memory");
}
static __device__ __forceinline__ unsigned gld4_s(const void* p, unsigned voff) {
  unsigned v;
  asm volatile("global_load_dword %0, %1, %2" : "=v"(v) : "v"(voff), "s"(p) : "memory");
  return v;
}
static __device__ __forceinline__ u32x2 bld8_sc1(__amdgpu_buffer_rsrc_t r, unsigned voff) {
  u32x2 v;
  asm volatile("buffer_load_dwordx2 %0, %1, %2, 0 offen sc1" : "=v"(v) : "v"(voff), "s"(r) : "memory");
  return v;
}
template <int N> static __device__ __forceinline__ void wait_xs(unsigned& a, u32x2& b) { asm volatile("s_waitcnt vmcnt(%2)" : "+v"(a), "+v"(b) : "i"(N) : "memory"); }
template <int N> static __device__ __forceinline__ void wait_xq(u32x4 (&a)[2], u32x2& b) { asm volatile("s_waitcnt vmcnt(%3)" : "+v"(a[0]), "+v"(a[1]), "+v"(b) : "i"(N) : "memory"); }
static __device__ __forceinline__ u32x2 ld8_s_sc1(const void* p, int voff) {
  u32x2 v;
  asm volatile("global_load_dwordx2 %0, %1, %2 sc1" : "=v"(v) : "v"(voff), "s"(p) : "memory");
  return v;
}
static __device__ __forceinline__ void lgkm0(unsigned& sink) { asm volatile("s_waitcnt lgkmcnt(0)" : "+s"(sink) :: "memory"); }
template <typename T> static __device__ __forceinline__ void pin4(T& a, T& b, T& c, T& d) { asm volatile("" : "+s"(a), "+s"(b), "+s"(c), "+s"(d)); }
template <typename T, typename U> static __device__ __forceinline__ void pins(T& a, U& b) { asm volatile("" : "+s"(a), "+s"(b)); }
static __device__ __forceinline__ void launder(int& v, int& a, int& b) { asm volatile("" : "+v"(v)); asm volatile("" : "+s"(a), "+s"(b)); }
static __device__ __forceinline__ unsigned bld4_sc1(__amdgpu_buffer_rsrc_t r, int voff) {
  unsigned v;
  asm volatile("buffer_load_dword %0, %1, %2, 0 offen sc1" : "=v"(v) : "v"(voff), "s"(r) : "memory");
  return v;
}
static __device__ __forceinline__ u32x4 bld16_nt(__amdgpu_buffer_rsrc_t r, int voff, int soff) {
  u32x4 v;
  asm volatile("buffer_load_dwordx4 %0, %1, %2, %3 offen nt" : "=v"(v) : "v"(voff), "s"(r), "s"(soff) : "memory");
  return v;
}
static __device__ __forceinline__ void bld16_nt_io(u32x4& v, __amdgpu_buffer_rsrc_t r, int voff, int soff) {
  asm volatile("buffer_load_dwordx4 %0, %1, %2, %3 offen nt" : "+v"(v) : "v"(voff), "s"(r), "s"(soff) : "memory");
}
static __device__ __forceinline__ u32x4 bld16_sc1(__amdgpu_buffer_rsrc_t r, int voff, int soff) {
  u32x4 v;
  asm volatile("buffer_load_dwordx4 %0, %1, %2, %3 offen sc1" : "=v"(v) : "v"(voff), "s"(r), "s"(soff) : "memory");
  return v;
}
static __device__ __forceinline__ u32x2 granule_wait(const u64* p, unsigned tag) {
  u64 v = 0;
#pragma unroll 1
  for (int it = 0; it < SPIN_MAX; ++it) {
    v = __hip_atomic_load(p, __ATOMIC_RELAXED, __HIP_MEMORY_SCOPE_AGENT);
    if (__all((unsigned)(v >> 32) == tag)) break;
    __builtin_amdgcn_s_sleep(2);
  }
  return (u32x2){(unsigned)v, (unsigned)(v >> 32)};
}
static __device__ __forceinline__ float key_score(unsigned key) {
  const unsigned u = key >> 16;
  return bf2f((u16)((u & 0x8000u) ? (u & 0x7fffu) : (~u & 0xffffu)));
}
static __device__ __forceinline__ void merge2(unsigned& a1, unsigned& a2, unsigned b1, unsigned b2) {
  const unsigned lo = min(a1, b1);
  a1 = max(a1, b1);
  a2 = max(lo, max(a2, b2));
}
static __device__ __forceinline__ float sqdev16(const u32x4 (&v)[2], float mb) {
  f32x2 q2 = {0.f, 0.f};
#pragma unroll
  for (int i = 0; i < 8; ++i) {
    const f32x2 e = (f32x2){lo_bf(v[i >> 2][i & 3]), hi_bf(v[i >> 2][i & 3])} - mb;
    q2 += e * e;
  }
  return q2.x + q2.y;
}

constexpr int G1_NST = 4, G1_NKT = 16, G1_STAGE = 32768;
constexpr int G2_NKT = 16, G2_MRING = 98304;
constexpr int n_off(int k) { return (k % 3) * 32768; }
constexpr int m_off(int k) { return ((k + 2) % 3) * 16384; }
constexpr int k_ord(int k) { return k < 2 ? k : (k < 9 ? 2 * (k - 1) : 2 * (k - 8) + 1); }
constexpr int LDS_DUMMY = G2_MRING + 49152;
constexpr int LDS_GU = LDS_DUMMY + 4096;
constexpr int LDS_FIN = LDS_GU + 4096;
constexpr int LDS_MB = LDS_FIN + 4096;
constexpr int LDS_SEL = LDS_MB + 1536;
constexpr int LDS_SIG = LDS_SEL + 64;
constexpr int LDS_KEY = LDS_SIG + 32;
constexpr int LDS_BYTES = LDS_KEY + 128;
constexpr int LDS_ACC = n_off(G2_NKT - 3);

constexpr int tpos[16] = {0, 0, 0, 0, 109, 115, 1, 2, 2, 3, 4, 5, 6, 7, 8, 9};
constexpr int twho[16] = {0, 0, 0, 0, 7, 7, 0, 1, 2, 3, 4, 5, 6, 7, 0, 1};
constexpr int dn_cnt(int q) { return q == 5 ? 3 : q == 8 ? 4 : 0; }
constexpr int dn_first(int q) { return q <= 5 ? 0 : q <= 8 ? 3 : 7; }

static __device__ __forceinline__ void code_touch(unsigned& sink, unsigned off) {
  asm volatile("s_getpc_b64 s[40:41]\n\tk_main_rt:\n\ts_min_u32 %1, %1, k_main_end-k_main_rt-196\n\t"
               "s_load_dword %0, s[40:41], %1 offset:0\n\ts_load_dword %0, s[40:41], %1 offset:64\n\ts_load_dword %0, s[40:41], %1 offset:128\n\ts_load_dword %0, s[40:41], %1 offset:192"
               : "+s"(sink), "+s"(off) :: "s40", "s41", "scc", "memory");
}
static __device__ __forceinline__ void touch1(__amdgpu_buffer_rsrc_t r, int voff, unsigned mb, int soff) { TOUCH(r, voff, mb, LDS_DUMMY, soff); }
typedef const __attribute__((address_space(1))) u16* gptr;
typedef __attribute__((ext_vector_type(8))) u64 u64x8;
extern "C" __global__ __launch_bounds__(512, 2) void k_main(const u16* x, unsigned x2, unsigned x3, const u16* wd_re, unsigned r2, unsigned r3,
                                                 const u16* wd_im, unsigned i2, unsigned i3, const u16* w_r,
                                                 char* wsp, const u16* ws_re, const u16* ws_im,
                                                 const u16* w_gate, const u16* w_up, const u16* w_down, u16* out) {
  __shared__ __attribute__((aligned(16))) char smem[LDS_BYTES];
  u64x8 ka;
  u32x4 rm, rn;
  asm volatile("s_load_dwordx4 %0, %3, 0\n\ts_load_dwordx4 %1, %3, %4 offset:16\n\ts_load_dwordx16 %2, %3, 48"
               : "=&s"(rm), "=&s"(rn), "=&s"(ka) : "s"(__builtin_amdgcn_kernarg_segment_ptr()), "s"((blockIdx.x & 4) * 4) : "memory");
  const int tid = threadIdx.x, lane = tid & 63;
  const int wid = __builtin_amdgcn_readfirstlane(tid >> 6);
  const int g = blockIdx.x, xcd = g & 7, j = g >> 3;
  if (wid < 4) {
    unsigned cw, co = (j * 4 + wid) * 448;
    asm volatile(".subsection 1\nk_main_end:\n.subsection 0\n\ts_min_u32 %1, %1, k_main_end-k_main-480\n\ts_getpc_b64 s[40:41]\n\ts_add_u32 s40, s40, %1\n\ts_addc_u32 s41, s41, 0\n\t"
                 "s_load_dword %0, s[40:41], 0\n\ts_load_dword %0, s[40:41], 64\n\ts_load_dword %0, s[40:41], 128\n\t"
                 "s_load_dword %0, s[40:41], 192\n\ts_load_dword %0, s[40:41], 256\n\ts_load_dword %0, s[40:41], 320\n\ts_load_dword %0, s[40:41], 384\n\ts_waitcnt lgkmcnt(0)"
                 : "=&s"(cw), "+s"(co) :: "s40", "s41", "scc", "memory");
  }
  const int pw = wid - 4;
  const int row0 = (wid & 3) * 8 + (lane >> 3);
  const int cm = ((lane & 7) ^ (((row0 >> 1) & 1) | ((wid & 3) << 1))) * 16;
  int vm = row0 * (D_ * 2) + cm;
  asm volatile("" : "+v"(vm));
  const int kq = xcd >> 1;
  const int tt = (xcd & 1) * 4 + (j >> 3);
  const int et = j & 7;
  const int z = kq >> 1, st = (kq & 1) * 8 + tt;
  unsigned o_m = st * 128 * D_ * 2, o_n = et * 128 * D_ * 2, mb0 = lds_addr(smem) + pw * 1024;
  asm volatile("" : "+s"(o_m), "+s"(o_n), "+s"(mb0));
#define G1_X(kt, so, q) DMA_(rm, vm, mb0, (so) + (q) * 4096, o_m, (kt) * 128 + (((q) & 1) * 64 + ((q) >> 1) * 32) * D_ * 2, "")
#define G1_W(kt, so, q) DMA_(rn, vm, mb0, (so) + 16384 + (q) * 4096, o_n, (kt) * 128 + (q) * 32 * D_ * 2, "")
#define G1_STAGE_TILE(kt, so, W) do { SB(); G1_X(kt, so, 0); W(0); G1_X(kt, so, 1); W(1); G1_X(kt, so, 2); W(2); G1_X(kt, so, 3); W(3); \
    G1_W(kt, so, 0); W(4); G1_W(kt, so, 1); W(5); G1_W(kt, so, 2); W(6); G1_W(kt, so, 3); W(7); SB(); } while (0)
#define G1_STAGE_A(kt, so, W) do { SB(); G1_X(kt, so, 0); W(0); G1_X(kt, so, 2); W(1); \
    G1_W(kt, so, 0); W(2); G1_W(kt, so, 1); W(3); G1_W(kt, so, 2); W(4); G1_W(kt, so, 3); W(5); W(6); W(7); SB(); } while (0)
#define G1_STAGE_B(kt, so) do { SB(); G1_X(kt, so, 1); G1_X(kt, so, 3); SB(); } while (0)
#define NOW(i)
  asm volatile("s_waitcnt lgkmcnt(0)" : "+s"(ka), "+s"(rm), "+s"(rn) :: "memory");
  int lrow, drow, l16;
  unsigned o_l, o_d;
  auto rows = [&](int i) __attribute__((always_inline)) {
    int g_ = g, l_ = lane;
    if (i == 0) { asm volatile("" : "+s"(g_)); drow = (g_ & ~7) + pw * 2; lrow = drow + (g_ & 3) * 512 + (g_ & 4) * 64; asm volatile("" : "+s"(drow), "+s"(lrow)); }
    if (i == 1) { asm volatile("" : "+s"(g_)); drow += (g_ & 3) * 256; o_l = lrow * D_ * 2; o_d = drow * D_ * 2; asm volatile("" : "+s"(drow), "+s"(o_l), "+s"(o_d)); }
    if (i == 2) { asm volatile("" : "+v"(l_)); l16 = l_ * 16; asm volatile("" : "+v"(l16)); }
  };
  unsigned sd;
  asm volatile("" : "=s"(sd));
  if (wid >= 4) {
    if (TW) asm volatile("s_load_dword %0, %1, %2\n\ts_load_dword %0, %3, 0\n\ts_load_dword %0, %4, %5"
                 : "+s"(sd) : "s"((u64)rn[0] | (u64)rn[1] << 32), "s"(o_n), "s"(ka[0]), "s"(ka[1]), "s"((int)WS_FLAGS + FL_EPOCH * 4 + 256) : "memory");
    __builtin_amdgcn_s_setprio(3);
    G1_STAGE_TILE(0, 0, rows);
  }
  x = (const u16*)(gptr)((u64)rm[0] | (u64)rm[1] << 32);
  const u16* wdz = (const u16*)(gptr)((u64)rn[0] | (u64)rn[1] << 32);
  w_r = (const u16*)(gptr)ka[0]; wsp = (char*)(gptr)ka[1]; ws_re = (const u16*)(gptr)ka[2]; ws_im = (const u16*)(gptr)ka[3];
  w_gate = (const u16*)(gptr)ka[4]; w_up = (const u16*)(gptr)ka[5]; w_down = (const u16*)(gptr)ka[6]; out = (u16*)(gptr)ka[7];
  int* flags = (int*)(wsp + WS_FLAGS);
  u64* stat = (u64*)(flags + FL_ST);
  const int fr = lane & 15, fq = lane >> 4;
  const int ha = wid >> 2;
  int trow, xo, po;
  const char *prs, *prc;
  auto epi_setup = [&]() __attribute__((always_inline)) {
    int t_ = tt, l_ = lane;
    asm volatile("" : "+s"(t_));
    asm volatile("" : "+v"(l_));
    trow = t_ * 256 + ((wid >> 1) & 1) * 128 + kq * 32 + ha * 16;
    xo = ((trow + (l_ & 15)) * D_ + et * 128 + (wid & 1) * 64 + (l_ >> 4) * 8) * 2;
    prs = wsp + WS_SLAB + (size_t)((t_ * 8 + et) * 64 + (wid & 3)) * MSG;
    prc = (const char*)(flags + FL_X + ((t_ * 8 + et) * 64 + (wid & 3)) * 32);
    asm volatile("" : "+s"(trow), "+s"(prs), "+s"(prc));
    po = l_ < 3 ? (kq * 4 + ((kq + 1 + l_) & 3)) * 512 : (int)0x80000000;
    asm volatile("" : "+v"(xo), "+v"(po));
  };
#define rs mk_rsrc(prs)
#define rc mk_rsrc(prc)
  lds_vi sig = (lds_vi)(smem + LDS_SIG + (wid & 3) * 4);
  unsigned tag;
  u32x4 xq[2];
  u32x2 sq;
  f32x4 ya[4];
  unsigned key[2];
  float xf[16], nf[16], lmb, lsd, lri, la, lb, lqa, lqb;
  unsigned nfp[8];
#define PINV(a) asm volatile("" : "+v"(a))
  auto nf_pre = [&]() __attribute__((always_inline)) {
    if (__builtin_expect(!__all(sq.y == tag), 0)) sq = granule_wait(stat + (xo >> 11), tag);
    lmb = lo_bf(sq.x); lsd = hi_bf(sq.x); lri = __builtin_amdgcn_rcpf(lsd);
    PINV(lmb); PINV(lsd); PINV(lri);
  };
  auto nf_step = [&](int n) __attribute__((always_inline)) {
    const int u = n / 7;
    switch (n % 7) {
      case 0: la = lo_bf(xq[u >> 2][u & 3]); PINV(la); la -= lmb; PINV(la); break;
      case 1: lb = hi_bf(xq[u >> 2][u & 3]); PINV(lb); lb -= lmb; PINV(lb); break;
      case 2: nfp[u] = pk2(la, lb); la = lo_bf(nfp[u]); PINV(la); PINV(nfp[u]); break;
      case 3: lqa = la * lri; PINV(lqa); lb = hi_bf(nfp[u]); PINV(lb); break;
      case 4: la = __builtin_fmaf(-lqa, lsd, la); PINV(la); lqb = lb * lri; PINV(lqb); break;
      case 5: lqa = __builtin_fmaf(la, lri, lqa); PINV(lqa); lb = __builtin_fmaf(-lqb, lsd, lb); PINV(lb); break;
      default: lqb = __builtin_fmaf(lb, lri, lqb); nfp[u] = pk2(lqa, lqb); PINV(nfp[u]); break;
    }
  };
  auto nf_unp = [&](int e) __attribute__((always_inline)) {
    const int u = e >> 1;
    if (e == 0) { PINV(xq[0]); PINV(xq[1]); }
    xf[e] = (e & 1) ? hi_bf(xq[u >> 2][u & 3]) : lo_bf(xq[u >> 2][u & 3]);
    nf[e] = (e & 1) ? hi_bf(nfp[u]) : lo_bf(nfp[u]);
    PINV(xf[e]); PINV(nf[e]);
  };
#define MSG_IDX(m) (kq * 4 + ((kq + 1 + (m)) & 3))
  unsigned seen = 0;

  if (__builtin_expect(wid >= 4, 1)) {
    constexpr int O_ST = (FL_ST - FL_AB) * 4, O_CE = (FL_CE - FL_AB) * 4, O_KEY = (FL_KEY - FL_AB) * 4, O_EP = (FL_EPOCH - FL_AB) * 4;
    const __amdgpu_buffer_rsrc_t rf = mk_rsrc(wsp + WS_FLAGS + FL_AB * 4);
    u32x4 lv[2], lv2[2], lw[2], ld[2], ld2[2];
    unsigned epv;
    {
      const char* xrow = (const char*)x + o_l;
      const char* wdrow = (const char*)wdz + o_d;
      lv[0] = ld16_s<0>(xrow, l16); lv[1] = ld16_s<1024>(xrow, l16);
      lv2[0] = ld16_s<2048>(xrow, l16); lv2[1] = ld16_s<3072>(xrow, l16);
      ld[0] = ld16_s<0>(wdrow, l16); ld[1] = ld16_s<1024>(wdrow, l16);
      ld2[0] = ld16_s<2048>(wdrow, l16); ld2[1] = ld16_s<3072>(wdrow, l16);
      lw[0] = ld16_s<0>(w_r, l16); lw[1] = ld16_s<1024>(w_r, l16);
      asm volatile("buffer_load_dword %0, off, %1, %2 sc1" : "=v"(epv) : "s"(rf), "s"(O_EP) : "memory");
    }
    SB();
    const unsigned long long did = llvm_dispatch_id();
    const unsigned dmix = (unsigned)did * 0x85EBCA6Bu + (unsigned)(did >> 32) * 0xC2B2AE35u + 0x27D4EB2Fu;
    const bool moe = et != 7;
    unsigned epoch;
    const int ci = lane & 15, rq = lane >> 4, tk = (lane >> 3) & 1;
    int mat, cg, dq, d0;
    u32x4 wv[16];
    const u16 *pgu, *pn2;
    const char *pm2, *spb;
#define rw __builtin_amdgcn_make_buffer_rsrc((void*)pgu, 0, moe ? 0x7fffffff : 0, 0x00020000)
#define rn2 mk_rsrc(pn2)
#define rm2 mk_rsrc(pm2)
    int vw, vn2, vm2;
    auto k_blk = [&](int k) __attribute__((always_inline)) { return tt ^ (k_ord(k) >> 1); };
#define KROT(kk_) (k_blk(kk_) * 256 + (k_ord(kk_) & 1) * 128)
    int tv2;
    auto touches = [&](auto P) __attribute__((always_inline)) {
      constexpr int p = decltype(P)::value;
      int e = et;
      asm volatile("" : "+s"(e));
#define TCH(k) if constexpr (tpos[k] == p) { if (e == twho[k]) { const int kb = KROT(k); touch1(rn2, tv2, mb0, kb); } }
      TCH(0) TCH(1) TCH(2) TCH(3) TCH(4) TCH(5) TCH(6) TCH(7) TCH(8) TCH(9) TCH(10) TCH(11) TCH(12) TCH(13) TCH(14) TCH(15)
    };
    unsigned mq0, mq1, mq2, mq3;
    const int cg8 = g & 7, rgb = g >> 3;
    const __amdgpu_buffer_rsrc_t rdn = mk_rsrc(w_down);
    const __amdgpu_buffer_rsrc_t rgu = mk_rsrc(wsp + WS_GU);
    int vdn, vgu;
    u32x4 dv[7], gv[4];
    int abo, abq[3];
    unsigned lma;
    unsigned sel[2][4];
    float g0, g1;
    u32x4 kk[4];
    unsigned k1 = 0, k2 = 0;
    const int ko_ = O_KEY + pw * 4096;
#define MM(a, b, c) __builtin_amdgcn_mfma_f32_16x16x32_bf16(__builtin_bit_cast(bf16x8, a), __builtin_bit_cast(bf16x8, b), c, 0, 0, 0)
#define PIN1(a) asm volatile("" : "+v"(a))
#define PIN2(a, b) asm volatile("" : "+v"(a), "+v"(b))
#define PIN3(a, b, c) asm volatile("" : "+v"(a), "+v"(b), "+v"(c))
    {
      int gro, go;
      u32x2 gr;
      u32x4 oh[5];
      f32x4 sa4, gw[2], gx[2];
      float su, gs, ta, tb, sx, mbv;
      unsigned dk[4], kl;
      const int hb = lane >> 5;
#pragma unroll
      for (int l = 0; l < WSN; ++l) asm volatile("" : "=v"(wv[l]));
      auto diag = [&](const f32x4& m) __attribute__((always_inline)) {
        return __uint_as_float((__float_as_uint(m[0]) & dk[0]) | (__float_as_uint(m[1]) & dk[1]) | (__float_as_uint(m[2]) & dk[2]) | (__float_as_uint(m[3]) & dk[3]));
      };
      auto setup = [&](auto JB, auto GP) __attribute__((always_inline)) {
        constexpr int jb = decltype(JB)::value, gp = decltype(GP)::value;
        SB();
        {
          int ln = lane, jj = j, p_ = pw;
          launder(ln, jj, p_);
          const int c_ = ln & 15, r_ = ln >> 4, e_ = jj & 7, t_ = (xcd & 1) * 4 + (jj >> 3);
          if constexpr (jb == 1 && gp == 0) {
            pn2 = ((kq >> 1) ? ws_im : ws_re) + (long)t_ * 256 * S_ + (kq & 1) * 1024;
            spb = (const char*)(pn2 + (e_ * 4 + p_) * 8 * S_);
            pins(pn2, spb);
          }
          if constexpr (jb == 1 && gp >= 1 && gp < 6) {
            const unsigned o = c_ == gp - 1 ? 0x3f803f80u : 0u;
            oh[gp - 1] = (u32x4){o, o, o, o};
            PIN1(oh[gp - 1]);
          }
          if constexpr (jb == 1 && gp == 6) {
            const int gm = moe ? (jj - (jj >> 3)) * 8 + xcd : 0;
            mat = gm / 112; cg = (gm % 112) >> 2; dq = gm & 3; d0 = dq * 256 + p_ * 64;
            pin4(mat, cg, dq, d0);
          }
          if constexpr (jb == 2 && gp == 0) {
            pgu = mat ? w_up : w_gate;
            pm2 = wsp + WS_ABT + ((long)e_ * 128 * 4096 + kq * 1024) * 2;
            pins(pgu, pm2);
            vw = ((d0 + r_ * 16) * F_ + cg * 128 + c_ * 8) * 2;
            PIN1(vw);
          }
          if constexpr (jb == 2 && (gp == 1 || gp == 2)) {
            dk[gp * 2 - 2] = c_ == r_ * 4 + gp * 2 - 2 ? ~0u : 0u; dk[gp * 2 - 1] = c_ == r_ * 4 + gp * 2 - 1 ? ~0u : 0u;
            PIN2(dk[gp * 2 - 2], dk[gp * 2 - 1]);
          }
          if constexpr (jb == 2 && gp == 3) { kl = lrow + (ln >> 5); go = (ln & 30) ? (int)0x80000000 : ((ln & 1) ? O_KEY : O_ST) + (int)kl * 8; PIN2(kl, go); }
          if constexpr (jb == 2 && gp == 4) { kl = 0xffff - kl; go = (ln & ~1) == 2 ? O_CE + (z * D_ + drow + (ln & 1)) * 8 : go; PIN2(kl, go); }
          if constexpr (jb == 2 && gp == 5) {
            gro = (p_ < 2 ? O_ST + (st * 128 + p_ * 64) * 8 : O_CE + (z * D_ + e_ * 128 + (p_ - 2) * 64) * 8) + ln * 8;
            tv2 = (p_ * 64 + ln) * 4096;
            lma = lds_addr(smem + LDS_MB) + (p_ < 2 ? p_ * 256 : 512 + p_ * 256) + ln * 4;
            PIN3(gro, tv2, lma);
          }
          if constexpr (jb == 7 && gp == 0) {
            const int r0 = p_ * 8 + (ln >> 3);
            vn2 = r0 * (S_ * 2) + ((ln & 7) ^ ((r0 >> 1) & 7)) * 16; vm2 = r0 * 8192 + (vm & 127);
            PIN2(vn2, vm2);
          }
          if constexpr (jb == 7 && gp == 1) { vdn = jj * 112 + p_ * 28 + r_ * 7; vgu = (((ln >> 3) & 1) * 4 * F_ + vdn + min(ln & 7, 6)) * 16; PIN2(vdn, vgu); }
          if constexpr (jb == 7 && gp == 2) { vdn = (vdn * D_ + cg8 * 128 + c_ * 8) * 2; PIN1(vdn); }
          if constexpr (jb == 7 && gp == 3) {
            abo = (((kq * 8 + e_) * 4 + p_) * 16 + c_) * AB_STRIDE * 4;
            abq[2] = ln >= 16 || (ln & 7) == t_ ? (int)0x80000000 : abo;
            PIN2(abo, abq[2]);
          }
          if constexpr (jb == 7 && gp == 4) {
            abq[1] = ln >= 8 ? (int)0x80000000 : abq[2];
            abq[0] = ((ln ^ t_) & 4) ? (int)0x80000000 : abq[1];
            PIN2(abq[0], abq[1]);
          }
          if constexpr (jb == 7 && gp == 5) {
            const unsigned mb = lds_addr(smem) + p_ * 1024;
            mq0 = mb + ((3 - kq) & 3) * 4096; mq1 = mb + ((4 - kq) & 3) * 4096; mq2 = mb + ((5 - kq) & 3) * 4096; mq3 = mb + ((6 - kq) & 3) * 4096;
            pin4(mq0, mq1, mq2, mq3);
          }
          if constexpr (jb == 8 && gp < 4) {
#pragma unroll
            for (int h = 0; h < 2; ++h) {
              const unsigned hv = h ? 0x0302u : 0x0100u;
              sel[h][gp] = ((ln & 7) >> 1) == gp ? ((ln & 1) ? (hv << 16) | 0x0c0cu : 0x0c0c0000u | hv) : 0x0c0c0c0cu;
            }
            PIN2(sel[0][gp], sel[1][gp]);
          }
        }
        SB();
      };
#define WS1(i) setup(IC<1>{}, IC<i>{})
#define WS2(i) setup(IC<2>{}, IC<i>{})
#define WS3(i) do { setup(IC<7>{}, IC<i>{}); if (i >= 4) setup(IC<8>{}, IC<(i) & 3>{}); } while (0)
      G1_STAGE_TILE(1, G1_STAGE, WS1);
      WAIT_VM(19); BAR();
      G1_STAGE_TILE(2, 2 * G1_STAGE, WS2);
      G1_STAGE_TILE(3, 3 * G1_STAGE, WS3);
      auto work1 = [&](auto JB, auto GP) __attribute__((always_inline)) {
        constexpr int jb = decltype(JB)::value, gp = decltype(GP)::value;
        SB();
        if constexpr (jb == 1) {
          const f32x4 z4 = {0.f, 0.f, 0.f, 0.f};
          if constexpr (gp == 0) { sa4 = MM(lv[0], oh[0], z4); gw[0] = MM(lv[0], lw[0], z4); gx[0] = MM(lv[0], lv[0], z4); PIN3(sa4, gw[0], gx[0]); }
          if constexpr (gp == 1) { sa4 = MM(lv2[0], oh[1], sa4); gw[1] = MM(lv2[0], lw[0], z4); gx[1] = MM(lv2[0], lv2[0], z4); PIN3(sa4, gw[1], gx[1]); }
          if constexpr (gp == 2) { sa4 = MM(lv[1], oh[0], sa4); gw[0] = MM(lv[1], lw[1], gw[0]); gx[0] = MM(lv[1], lv[1], gx[0]); PIN3(sa4, gw[0], gx[0]); }
          if constexpr (gp == 3) { sa4 = MM(lv2[1], oh[1], sa4); gw[1] = MM(lv2[1], lw[1], gw[1]); gx[1] = MM(lv2[1], lv2[1], gx[1]); PIN3(sa4, gw[1], gx[1]); }
          if constexpr (gp == 4) { sa4 = MM(ld[0], oh[2], sa4); sa4 = MM(ld[1], oh[2], sa4); PIN1(sa4); }
          if constexpr (gp == 5) { sa4 = MM(ld2[0], oh[3], sa4); sa4 = MM(ld2[1], oh[3], sa4); PIN1(sa4); }
          if constexpr (gp == 6) { sa4 = MM(lw[0], oh[4], sa4); sa4 = MM(lw[1], oh[4], sa4); PIN1(sa4);
            epoch = __builtin_amdgcn_readfirstlane(epv); tag = ((epoch + 1u) * 0x9E3779B1u ^ dmix) | 1u; }
        }
        if constexpr (jb == 2) {
          if constexpr (gp == 0) { su = (sa4[0] + sa4[1]) + (sa4[2] + sa4[3]); ta = diag(gw[0]); PIN2(su, ta); }
          if constexpr (gp == 1) { su = fold32(su, su); tb = diag(gw[1]); PIN2(su, tb); }
          if constexpr (gp == 2) { su = fold16(su, su); gs = fold32(ta, tb); PIN2(su, gs); }
          if constexpr (gp == 3) { ta = diag(gx[0]); tb = diag(gx[1]); PIN2(ta, tb); }
          if constexpr (gp == 4) { gs = fold16(gs, fold32(ta, tb)); PIN1(gs); }
          if constexpr (gp == 5) { gs += dpp_f<0xB1>(gs); gs += dpp_f<0x4E>(gs); PIN1(gs); }
          if constexpr (gp == 6) { gs += dpp_f<0x141>(gs); gs += dpp_f<0x140>(gs); PIN1(gs); }
        }
        if constexpr (jb == 3) {
          if constexpr (gp == 0) {
            const float s0 = rdl(su, 0), s1 = rdl(su, 1), q0 = rdl(gs, 16), q1 = rdl(gs, 48);
            sx = hb ? s1 : s0; ta = hb ? q1 : q0; PIN2(sx, ta);
          }
          if constexpr (gp == 1) {
            const float mean = sx * (1.0f / D_);
            mbv = rbf(mean);
            ta -= sx * mean;
            if (__builtin_expect(__any(!(sx * mean <= 16.f * ta)), 0)) {
              float q = fold32(sqdev16(lv, rdl(mbv, 0)), sqdev16(lv2, rdl(mbv, 32)));
              q = row_sum(fold16(q, q));
              const float dm = mean - mbv;
              ta = fmaxf(q - (float)D_ * dm * dm, 0.f);
            }
            PIN2(mbv, ta);
          }
          if constexpr (gp == 2) { ta = rbf(rbf(ta * (1.0f / D_)) + 1e-5f); gs -= mbv * rdl(su, 4); PIN2(ta, gs); }
          if constexpr (gp == 3) { ta = rbf(__builtin_amdgcn_sqrtf(ta)); tb = __builtin_amdgcn_rcpf(ta); PIN2(ta, tb); }
          if constexpr (gp == 4) {
            const unsigned rb = __float_as_uint(rbf(gs * tb));
            kl |= (rb ^ ((unsigned)((int)rb >> 31) | 0x80000000u)) & 0xffff0000u; PIN1(kl);
          }
          if constexpr (gp == 5) { kl = (lane & 2) ? __float_as_uint(su) : (lane & 1) ? kl : (__float_as_uint(mbv) >> 16) | __float_as_uint(ta); PIN1(kl); }
        }
        if constexpr (jb == ST_JB + 3) {
          if constexpr (gp == 0) { if (!__all(gr.y == tag)) gr = granule_wait((const u64*)((const char*)(flags + FL_AB) + gro), tag); PIN1(gr); }
          if constexpr (gp == 1) { g1 = (z ? -1.0f : 1.0f) / hi_bf(gr.x); PIN1(g1); }
          if constexpr (gp == 2) { g0 = pw < 2 ? -(lo_bf(gr.x) * g1) : __uint_as_float(gr.x); PIN1(g0); }
        }
        if constexpr (gp < 7 && (jb - 1) * 7 + gp < 64) {
          constexpr int n = (jb - 1) * 7 + gp;
          sld<((n >> 1) & 7) * 4096 + (n & 1) * 64>(sd, spb, KROT(n >> 4));
        }
        SB();
      };
      auto step1 = [&](auto JB) __attribute__((always_inline)) {
        constexpr int jb = decltype(JB)::value;
        if constexpr (jb == 1) PAGE(1);
        if constexpr (jb == 7) PAGE(2);
        constexpr int n16 = jb == 12 ? 12 : jb == 11 ? 14 : 16;
        constexpr auto wl = [](int q) { return (q == 3 ? 1 : 0) + (q == ST_JB ? 1 : 0); };
        if constexpr (jb == 1) {
          asm volatile("s_waitcnt vmcnt(%11)" : "+v"(lv[0]), "+v"(lv[1]), "+v"(lw[0]), "+v"(lw[1]), "+v"(ld[0]), "+v"(ld[1]),
                       "+v"(lv2[0]), "+v"(lv2[1]), "+v"(ld2[0]), "+v"(ld2[1]), "+v"(epv) : "i"(16) : "memory");
        } else if constexpr (jb == ST_JB + 3) {
          asm volatile("s_waitcnt vmcnt(%1)" : "+v"(gr) : "i"(n16 + wl(jb - 2) + wl(jb - 1)) : "memory");
        } else wait_vm<n16 + wl(jb - 3) + wl(jb - 2) + wl(jb - 1)>();
        BAR();
        if constexpr (jb == 2) { if (pw == 0 && lane < 16) ((int*)(smem + LDS_SEL))[lane < 8 ? lane : lane + 8] = 0; }
        if constexpr (jb == 4) { if (pw == 0 && lane == 0) ((unsigned*)(smem + LDS_SEL))[5] = tag; }
#define W1(i) work1(JB, IC<i>{})
        if constexpr (jb < 10) G1_STAGE_TILE(jb - 1 + G1_NST, ((jb - 1) % G1_NST) * G1_STAGE, W1);
        else G1_STAGE_A(jb - 1 + G1_NST, ((jb - 1) % G1_NST) * G1_STAGE, W1);
        if constexpr (jb == 12) { G1_STAGE_B(13, G1_STAGE); G1_STAGE_B(14, 2 * G1_STAGE); }
        if constexpr (jb == 3) __builtin_amdgcn_raw_buffer_store_b64((u32x2){kl, tag}, rf, go, 0, 16);
        if constexpr (jb == ST_JB) {
          asm volatile("buffer_load_dwordx2 %0, %1, %2, 0 offen sc1" : "=v"(gr) : "v"(gro), "s"(rf) : "memory");
        }
        touches(IC<100 + jb>{});
        if constexpr (jb <= 8) {
          if (et == jb - 1) {
#pragma unroll
            for (int l = 0; l < WSN; ++l) bld16_nt_io(wv[l], rw, vw, l * F_ * 2);
          }
        }
        SB();
      };
      static_assert(ST_JB + 3 <= 12 && ST_JB >= 6, "schedule");
      step1(IC<1>{}); step1(IC<2>{}); step1(IC<3>{}); step1(IC<4>{}); step1(IC<5>{}); step1(IC<6>{}); step1(IC<7>{}); step1(IC<8>{}); step1(IC<9>{}); step1(IC<10>{}); step1(IC<11>{}); step1(IC<12>{});
    }
#define G2_STAGE_N(kk_, W) do { const int kb = KROT(kk_); constexpr int so = n_off(kk_); SB(); \
    DMA_(rn2, vn2, mq0, so, kb, 0, ""); W(0); DMA_(rn2, vn2, mq1, so, kb, 32 * S_ * 2, ""); W(1); \
    DMA_(rn2, vn2, mq2, so, kb, 64 * S_ * 2, ""); W(2); DMA_(rn2, vn2, mq3, so, kb, 96 * S_ * 2, ""); W(3); \
    DMA_(rn2, vn2, mq0, so + 16384, kb, 128 * S_ * 2, ""); W(4); DMA_(rn2, vn2, mq1, so + 16384, kb, 160 * S_ * 2, ""); W(5); \
    DMA_(rn2, vn2, mq2, so + 16384, kb, 192 * S_ * 2, ""); W(6); DMA_(rn2, vn2, mq3, so + 16384, kb, 224 * S_ * 2, ""); W(7); SB(); } while (0)
#define G2_STAGE_NH(kk_, h) do { const int kb = KROT(kk_); constexpr int so = n_off(kk_) + (h) * 16384; SB(); \
    DMA_(rn2, vn2, mq0, so, kb, (h) * 128 * S_ * 2, ""); DMA_(rn2, vn2, mq1, so, kb, ((h) * 128 + 32) * S_ * 2, ""); \
    DMA_(rn2, vn2, mq2, so, kb, ((h) * 128 + 64) * S_ * 2, ""); DMA_(rn2, vn2, mq3, so, kb, ((h) * 128 + 96) * S_ * 2, ""); SB(); } while (0)
#define G2_STAGE_M(kk_, W) do { const int kb = KROT(kk_); constexpr int so = G2_MRING + m_off(kk_); SB(); \
    DMA_(rm2, vm2, mb0, so, kb, 0, " sc1"); W(8); DMA_(rm2, vm2, mb0, so + 4096, kb, 32 * 8192, " sc1"); W(9); \
    DMA_(rm2, vm2, mb0, so + 8192, kb, 64 * 8192, " sc1"); W(10); DMA_(rm2, vm2, mb0, so + 12288, kb, 96 * 8192, " sc1"); W(11); SB(); } while (0)
    WAIT_VM(16); BAR();
    asm volatile("ds_write_b32 %0, %1" :: "v"(lma), "v"(g0) : "memory");
    if (pw < 2) asm volatile("ds_write_b32 %0, %1 offset:512" :: "v"(lma), "v"(g1) : "memory");
    u32x4 tv[4];
    unsigned ready = 0;
    auto need = [&](int k) __attribute__((always_inline)) {
      const int b = k_blk(k) + (k_ord(k) & 1) * 8;
#pragma unroll 1
      for (int it = 0; !((ready >> b) & 1) && it < SPIN_MAX; ++it) {
        ready |= (unsigned)__ballot(__builtin_amdgcn_raw_buffer_load_b32(rf, abq[k < 5 ? 0 : k < 9 ? 1 : 2], 0, 16) == tag);
        asm volatile("" ::: "memory");
      }
    };
    unsigned ksel, bad = 0;
    int isel;
    unsigned xw;
    auto bvec = [&](unsigned src, const unsigned (&sl)[4]) __attribute__((always_inline)) {
      return (u32x4){__builtin_amdgcn_perm(0, src, sl[0]), __builtin_amdgcn_perm(0, src, sl[1]),
                     __builtin_amdgcn_perm(0, src, sl[2]), __builtin_amdgcn_perm(0, src, sl[3])};
    };
#define BC8(v, p) (unsigned)__builtin_amdgcn_update_dpp(__builtin_amdgcn_update_dpp(0, (int)(v), 0x150 + (p), 0xf, 0x3, false), (int)(v), 0x158 + (p), 0xf, 0xc, false)
    float* lg = (float*)(smem + LDS_GU);
    f32x4 macc = {0.f, 0.f, 0.f, 0.f};
    u32x2 sv;
    unsigned hp, gbad;
    float s0, s1;
    auto work2 = [&](auto JB, auto GP) __attribute__((always_inline)) {
      constexpr int jb = decltype(JB)::value, gp = decltype(GP)::value;
      SB();
      if constexpr (jb == -1 && gp < 4) {
        constexpr int c = gp == 0 ? 0xB1 : gp == 1 ? 0x4E : gp == 2 ? 0x141 : 0x140;
        merge2(k1, k2, dpp_u<c>(k1), dpp_u<c>(k2));
        PIN2(k1, k2);
      }
      if constexpr (jb == -1 && gp == 4) {
        ksel = tk ? k2 : k1;
        isel = (0xffff - (int)(ksel & 0xffffu)) & (S_ - 1);
        k1 = (isel * D_ + d0 + rq * 16 + (lane & 7) * 2) * 2; k2 = O_ST + isel * 8;
        PIN1(ksel);
        xw = gld4_s(x, k1);
        sv = bld8_sc1(rf, k2);
      }
      if constexpr (jb == 0 && gp == 1) wait_xs<3 + 1 + 2>(xw, sv);
      if constexpr (jb == 0) {
        if constexpr (gp == 1) { if (!__all(sv.y == tag)) sv = granule_wait(stat + isel, tag); s0 = __builtin_amdgcn_rcpf(hi_bf(sv.x)); PIN1(s0); }
        if constexpr (gp == 2) { s1 = ln_elem(lo_bf(xw), lo_bf(sv.x), hi_bf(sv.x), s0); PIN1(s1); }
        if constexpr (gp == 3) { xw = pk2(s1, ln_elem(hi_bf(xw), lo_bf(sv.x), hi_bf(sv.x), s0)); PIN1(xw); }
      }
      if constexpr (jb == DG && gp < 7) { PIN1(dv[gp]); macc = MM(dv[gp], bvec(BC8(hp, gp), sel[0]), macc); PIN1(macc); }
      if constexpr ((jb == 0 && gp >= 4) || jb == 1) {
        constexpr int l = jb == 0 ? gp - 4 : gp + 4;
        if constexpr (!(l & 1)) hp = BC8(xw, l >> 1);
        PIN1(wv[l]);
        macc = MM(wv[l], bvec(hp, sel[l & 1]), macc);
        PIN2(macc, hp);
      }
      if constexpr (jb == GV_JB + 2) {
        if constexpr (gp == 0) { gbad = (gv[0].y ^ tag) | (gv[0].w ^ tag) | (gv[1].y ^ tag) | (gv[1].w ^ tag); PIN1(gbad); }
        if constexpr (gp == 1) { gbad |= (gv[2].y ^ tag) | (gv[2].w ^ tag) | (gv[3].y ^ tag) | (gv[3].w ^ tag); PIN1(gbad); }
        if constexpr (gp == 2) {
          s0 = ((__uint_as_float(gv[0].x) + __uint_as_float(gv[1].x)) + __uint_as_float(gv[2].x)) + __uint_as_float(gv[3].x);
          s1 = ((__uint_as_float(gv[0].z) + __uint_as_float(gv[1].z)) + __uint_as_float(gv[2].z)) + __uint_as_float(gv[3].z);
          PIN2(s0, s1);
        }
        if constexpr (gp == 3) {
#pragma unroll 1
          for (int it = 0; !__all(gbad == 0) && it < SPIN_MAX; ++it) {
            u32x4 q[4];
            gbad = 0;
#pragma unroll
            for (int i = 0; i < 4; ++i) { q[i] = __builtin_amdgcn_raw_buffer_load_b128(rgu, vgu, i * F_ * 16, 16); gbad |= (q[i].y ^ tag) | (q[i].w ^ tag); }
            s0 = ((__uint_as_float(q[0].x) + __uint_as_float(q[1].x)) + __uint_as_float(q[2].x)) + __uint_as_float(q[3].x);
            s1 = ((__uint_as_float(q[0].z) + __uint_as_float(q[1].z)) + __uint_as_float(q[2].z)) + __uint_as_float(q[3].z);
            asm volatile("" ::: "memory");
          }
          const unsigned gp2 = pk2(s0, s1);
          const unsigned up2 = dpp_u<0x128>(gp2);
          s0 = tk ? hi_bf(up2) : lo_bf(gp2); s1 = tk ? hi_bf(gp2) : lo_bf(up2);
          PIN2(s0, s1);
        }
        if constexpr (gp == 4) { s0 = rbf(s0 * __builtin_amdgcn_rcpf(1.f + __expf(-s0))); PIN1(s0); }
        if constexpr (gp == 5) { hp = pk2(s0 * s1, 0.f); PIN1(hp); }
      }
      if constexpr (jb == 10 && gp == 2) epi_setup();
      if constexpr (jb == 10 && gp == 4) code_touch(sd, (j * 4 + pw) * 256);
      SB();
    };
#define W2M(i) work2(IC<-1>{}, IC<i>{})
#define W20(i) work2(IC<0>{}, IC<i>{})
#define W21(i) work2(IC<1>{}, IC<i>{})
    unsigned fs;
    {
      G1_STAGE_B(15, 3 * G1_STAGE);
      G2_STAGE_NH(0, 0);
      lgkm0(sd);
      WAIT_VM(16); BAR();
      G2_STAGE_NH(0, 1);
      WAIT_VM(10); BAR();
#pragma unroll
      for (int i = 0; i < 4; ++i) kk[i] = bld16_sc1(rf, l16, ko_ + i * 1024);
#pragma unroll
      for (int l = WSN; l < 16; ++l) wv[l] = bld16_nt(rw, vw, l * F_ * 2);
      asm volatile("s_waitcnt vmcnt(%4)" : "+v"(kk[0]), "+v"(kk[1]), "+v"(kk[2]), "+v"(kk[3]) : "i"(16 - WSN) : "memory");
      touches(IC<115>{});
#pragma unroll
      for (int i = 0; i < 4; ++i) {
        k2 = max(k2, min(k1, kk[i].x)); k1 = max(k1, kk[i].x);
        k2 = max(k2, min(k1, kk[i].z)); k1 = max(k1, kk[i].z);
        bad |= (kk[i].y ^ tag) | (kk[i].w ^ tag);
      }
#pragma unroll 1
      for (int it = 0; !__all(bad == 0) && it < SPIN_MAX; ++it) {
        k1 = 0; k2 = 0; bad = 0;
#pragma unroll
        for (int i = 0; i < 4; ++i) {
          const u32x4 q = __builtin_amdgcn_raw_buffer_load_b128(rf, l16, ko_ + i * 1024, 16);
          k2 = max(k2, min(k1, q.x)); k1 = max(k1, q.x);
          k2 = max(k2, min(k1, q.z)); k1 = max(k1, q.z);
          bad |= (q.y ^ tag) | (q.w ^ tag);
        }
        asm volatile("" ::: "memory");
      }
      merge2(k1, k2, dpp_u<0xB1>(k1), dpp_u<0xB1>(k2));
      merge2(k1, k2, dpp_u<0x4E>(k1), dpp_u<0x4E>(k2));
      merge2(k1, k2, dpp_u<0x141>(k1), dpp_u<0x141>(k2));
      merge2(k1, k2, dpp_u<0x140>(k1), dpp_u<0x140>(k2));
      if ((lane & 15) == 0) *(u32x2*)(smem + LDS_KEY + (pw * 4 + rq) * 8) = (u32x2){k1, k2};
      PAGE(3);
      BAR_LGKM();
#pragma unroll
      for (int jj = 0; jj < 4; ++jj) tv[jj] = *(const u32x4*)(smem + G2_MRING + m_off(0) + (jj * 4 + pw) * 1024 + lane * 16);
      { const u32x2 kp = *(const u32x2*)(smem + LDS_KEY + ci * 8); k1 = kp.x; k2 = kp.y; }
#pragma unroll
      for (int jj = 0; jj < 4; ++jj) __builtin_amdgcn_raw_buffer_store_b128(tv[jj], rm2, vm2 + jj * 32 * 8192 + 2 * tt * 128, 0, 16);
      G2_STAGE_N(1, W2M);
      WAIT_VM(10);
      if (lane == tt) __builtin_amdgcn_raw_buffer_store_b32(tag, rf, abo, 0, 16);
      G2_STAGE_N(2, W20);
      __builtin_amdgcn_s_sleep(2);
      WAIT_VM(9);
      fs = bld4_sc1(rf, abq[0]);
    }
    BAR();
#pragma unroll
    for (int jj = 0; jj < 4; ++jj) tv[jj] = *(const u32x4*)(smem + G2_MRING + m_off(1) + (jj * 4 + pw) * 1024 + lane * 16);
    G2_STAGE_N(3, NOW);
    asm volatile("s_waitcnt vmcnt(8)" : "+v"(fs) :: "memory");
    ready = (unsigned)__ballot(fs == tag);
    need(2);
    G2_STAGE_M(2, NOW);
    need(3);
    G2_STAGE_M(3, NOW);
    fs = bld4_sc1(rf, abq[1]);
    touches(IC<1>{});
    W21(0); W21(1); W21(2); W21(3); W21(4); W21(5); W21(6); W21(7); W21(8); W21(9); W21(10); W21(11);
    asm volatile("" : "+v"(tv[0]), "+v"(tv[1]), "+v"(tv[2]), "+v"(tv[3]));
    auto step2 = [&](auto JB) __attribute__((always_inline)) {
      constexpr int jb = decltype(JB)::value;
      if constexpr (jb == 3) PAGE(4);
      if constexpr (jb == 9) PAGE(5);
      constexpr auto must = [](int q) { return (q == 2 ? 4 : 0) + ((FSM >> q) & 1) + ((q == GV_JB || q == GV_JB - 1) ? 2 : 0); };
      if constexpr (jb == 2) wait_vm<4 + must(1)>();
      else wait_vm<dn_cnt(jb - 2) + (jb == 5 ? 1 : 0) + 12 + must(jb - 1) + dn_cnt(jb - 1)>();
      if constexpr (jb == 3) { if (pw == 0 && (lane & ~8) == 0) ((unsigned*)(smem + LDS_SEL))[tk] = ksel; }
      if constexpr ((jb == 2 || jb == DG + 1)) {
        *(f32x4*)(lg + pw * 256 + lane * 4) = macc;
        macc = (f32x4){0.f, 0.f, 0.f, 0.f};
        lgkm0(sd);
      }
      BAR();
      float p0[4], p1[4];
      const int col = (pw & 1) * 64 + lane;
      if constexpr (jb == 3) {
        if (g == 0 && pw == 0 && lane == 0) __hip_atomic_store((unsigned*)flags + FL_EPOCH, epoch + 1u, __ATOMIC_RELAXED, __HIP_MEMORY_SCOPE_AGENT);
      }
      if constexpr (jb == DG + 1) {
        const float* p = lg + (pw & 1) * 32 + (lane >> 4) * 64 + ((2 * lane) & 7) * 4 + ((lane >> 2) & 3);
#pragma unroll
        for (int i = 0; i < 4; ++i) { p0[i] = p[i * 256]; p1[i] = p[i * 256 + 4]; }
      }
      if constexpr (jb == 2) {
        const float* p = lg + (col >> 5) * 64 + (col & 7) * 4 + ((col >> 3) & 3);
#pragma unroll
        for (int i = 0; i < 4; ++i) { p0[i] = p[i * 256]; p1[i] = p[i * 256 + 32]; }
      }
      if constexpr (jb == 4) {
        if (lane == 8 + tt) __builtin_amdgcn_raw_buffer_store_b32(tag, rf, abo, 0, 16);
      }
      if constexpr ((FSM >> (jb - 2)) & 1) {
        asm volatile("" : "+v"(fs));
        ready |= (unsigned)__ballot(fs == tag);
      }
      if constexpr (jb == GV_JB + 2) asm volatile("" : "+v"(gv[0]), "+v"(gv[1]), "+v"(gv[2]), "+v"(gv[3]));
#define W2(i) work2(JB, IC<i>{})
      G2_STAGE_N(jb + 2, W2);
      need(jb + 2);
      G2_STAGE_M(jb + 2, W2);
      if constexpr (jb == 2) {
#pragma unroll
        for (int jj = 0; jj < 4; ++jj) __builtin_amdgcn_raw_buffer_store_b128(tv[jj], rm2, vm2 + jj * 32 * 8192 + (2 * tt + 1) * 128, 0, 16);
      }
      if constexpr ((FSM >> jb) & 1) fs = bld4_sc1(rf, abq[jb < 5 ? 1 : 2]);
      if constexpr ((jb == GV_JB || jb == GV_JB - 1)) {
#pragma unroll
        for (int i = (jb == GV_JB ? 2 : 0); i < (jb == GV_JB ? 4 : 2); ++i) gv[i] = bld16_sc1(rgu, vgu, i * F_ * 16);
      }
      touches(JB);
#pragma unroll
      for (int i = dn_first(jb); i < dn_first(jb) + dn_cnt(jb); ++i) dv[i] = bld16_nt(rdn, vdn, i * D_ * 2);
      if constexpr (jb == DG + 1) {
        if (pw < 2)
          __builtin_amdgcn_raw_buffer_store_b128((u32x4){__float_as_uint((p0[0] + p0[1]) + (p0[2] + p0[3])), tag, __float_as_uint((p1[0] + p1[1]) + (p1[2] + p1[3])), tag},
                                                 mk_rsrc(wsp + WS_DN), (((pw * 8 + cg8) * 32 + rgb) * 64 + lane) * 16, 0, 16);
      }
      if constexpr (jb == 2) {
        if (moe && pw < 2)
          __builtin_amdgcn_raw_buffer_store_b128((u32x4){__float_as_uint((p0[0] + p0[1]) + (p0[2] + p0[3])), tag, __float_as_uint((p1[0] + p1[1]) + (p1[2] + p1[3])), tag},
                                                 rgu, ((mat * 4 + dq) * F_ + cg * 128 + col) * 16, 0, 16);
      }
      SB();
    };
    static_assert(DG + 1 <= 12 && DG >= GV_JB + 3 && dn_first(DG - 2) == 7, "schedule");
    step2(IC<2>{}); step2(IC<3>{}); step2(IC<4>{}); step2(IC<5>{}); step2(IC<6>{}); step2(IC<7>{}); step2(IC<8>{}); step2(IC<9>{}); step2(IC<10>{}); step2(IC<11>{}); step2(IC<12>{}); step2(IC<13>{});
    lgkm0(sd);
    PAGE(7);
    WAIT_VM(12); BAR();
    xq[0] = ld16_s<0>(x, xo); xq[1] = ld16_s<64>(x, xo); sq = ld8_s_sc1(stat, xo >> 11 << 3);
    WAIT_VM(3); BAR();
    asm volatile("s_waitcnt vmcnt(0)" : "+v"(xq[0]), "+v"(xq[1]), "+v"(sq) :: "memory");
    __builtin_amdgcn_s_setprio(0);
    key[0] = __builtin_amdgcn_readlane(ksel, 0); key[1] = __builtin_amdgcn_readlane(ksel, 8);
    nf_pre();
#pragma unroll
    for (int n = 0; n < 56; ++n) nf_step(n);
#pragma unroll
    for (int e = 0; e < 16; ++e) nf_unp(e);
#pragma unroll 1
    for (int c = 0; c < 2; ++c) {
      const unsigned kc = __builtin_amdgcn_readlane(ksel, c * 8);
      const int ic = (0xffff - (int)(kc & 0xffffu)) & (S_ - 1);
      if ((ic >> 8) != tt || ((ic >> 5) & 3) != kq) continue;
      const __amdgpu_buffer_rsrc_t rd = mk_rsrc(wsp + WS_DN + (size_t)(c * 8 + et) * 32768);
#pragma unroll 1
      for (int it = 0; it < SPIN_MAX; ++it) {
        if (__all(__builtin_amdgcn_raw_buffer_load_b32(rd, (lane & 31) * 1024 + 1020, 0, 16) == tag)) break;
        __builtin_amdgcn_s_sleep(2);
        asm volatile("" ::: "memory");
      }
      float s0, s1;
      unsigned bad = 1;
#pragma unroll 1
      for (int it = 0; !__all(bad == 0) && it < SPIN_MAX; ++it) {
        bad = 0; s0 = 0.f; s1 = 0.f;
#pragma unroll
        for (int i = 0; i < 8; ++i) {
          const u32x4 q = __builtin_amdgcn_raw_buffer_load_b128(rd, rq * 1024 + pw * 256 + ci * 16, i * 4096, 16);
          bad |= (q.y ^ tag) | (q.w ^ tag); s0 += __uint_as_float(q.x); s1 += __uint_as_float(q.z);
        }
        asm volatile("" ::: "memory");
      }
      s0 = fold32(s0, s1);
      s0 = fold16(s0, s0);
      const float sg = rbf(1.f / (1.f + __expf(-key_score(kc))));
      ((float*)(smem + LDS_FIN))[c * 128 + pw * 32 + ci * 2 + (lane >> 5)] = rbf(rbf(s0) * sg);
      asm volatile("s_waitcnt lgkmcnt(0)" ::: "memory");
      if (lane == 0) asm volatile("ds_add_u32 %0, %1" :: "v"(lds_addr(smem + LDS_SEL + 24 + c * 4)), "v"(1) : "memory");
    }
  } else {
  {
    const int q = j * 4 + wid;
    constexpr int Q1 = WS_MB, Q2 = Q1 + 8, Q3 = Q2 + 8, Q4 = Q3 + 7, Q5 = Q4 + 7, Q6 = Q5 + 7, Q7 = Q6 + 4, Q8 = Q7 + 4, QC = Q8 + 1;
    static_assert(QC <= 128, "warm-up slots");
    unsigned dmy = 0;
    if (q < QC) {
      const char* base; int b0;
      if (q < Q1) { base = wsp; b0 = 0; }
      else if (q < Q2) { base = (const char*)ws_re; b0 = Q1; }
      else if (q < Q3) { base = (const char*)ws_im; b0 = Q2; }
      else if (q < Q4) { base = (const char*)w_gate; b0 = Q3; }
      else if (q < Q5) { base = (const char*)w_up; b0 = Q4; }
      else if (q < Q6) { base = (const char*)w_down; b0 = Q5; }
      else if (q < Q7) { base = (const char*)out; b0 = Q6; }
      else if (q < Q8) { base = (const char*)x; b0 = Q7; }
      else { base = (const char*)w_r; b0 = Q8; }
      base += (size_t)(q - b0) << 20;
      asm volatile("s_load_dword %0, %1, 0" : "+s"(dmy) : "s"(base) : "memory");
    }
    {
      const int w = g * 4 + wid;
      constexpr int R = (int)(WS_FLAGS - WS_DN);
      static_assert(FL_X == 0 && FL_ST == 6144 * 32 && FL_EPOCH == 6528 * 32, "");
      const int o = lane < 6 ? R + (lane * 1024 + w) * 128 : lane > 6 || w >= 896 ? (int)0x80000000 : w < 512 ? w * 1024 + 896 : R + (6144 - 512 + w) * 128;
      asm volatile("s_mov_b32 m0, %0\n\ts_nop 0\n\tbuffer_load_dword %1, %2, 0 offen sc1 lds"
                   :: "s"(lds_addr(smem + LDS_DUMMY) + wid * 1024 + 512), "v"(o), "s"(mk_rsrc(wsp + WS_DN)) : "memory");
    }
    if (wid == 0) { CALL_PG(1); CALL_PG(8); CALL_PG(5); }
    else if (wid == 1) { CALL_PG(2); CALL_PG(9); CALL_PG(6); }
    else if (wid == 2) { CALL_PG(3); CALL_PG(10); CALL_PG(7); }
    else { CALL_PG(4); CALL_PG(11); }
    asm volatile("s_barrier\n\ts_waitcnt lgkmcnt(0)" : "+s"(dmy) :: "memory");
  }
  PAGE(8);
  epi_setup();
  const int wr = wid >> 1, wc = wid & 1;
  const int sw = (fr >> 1) & 7;
  const int ko0 = ((0 + fq) ^ sw) * 16, ko1 = ((4 + fq) ^ sw) * 16;
  const int mrow = (fr >> 2) * 8 + (fr & 3);
  bf16x8 fm[2][4], fn[2][4];
  {
    const char* pm = smem + (wr * 64 + mrow) * 128;
    const char* pn = smem + 16384 + (wc * 64 + mrow) * 128;
    f32x4 acc[4][4];
#pragma unroll
    for (int i = 0; i < 4; ++i)
#pragma unroll
      for (int jn = 0; jn < 4; ++jn)
#pragma unroll
        for (int q = 0; q < 4; ++q) acc[i][jn][q] = 0.f;
#define TO(i) (((i) >> 1) * 4096 + ((i) & 1) * 512)
#define LDSP(T, a) ((__attribute__((address_space(3))) T*)(size_t)(a))
#define SLOTS(n) _Pragma("unroll") for (int q = 0; q < (n); ++q)
#define MFA(c, a, b) asm volatile("v_mfma_f32_16x16x32_bf16 %0, %1, %2, %0" : "+v"(c) : "v"(a), "v"(b))
#define MFA2(c, a, b, a2, b2) asm volatile("v_mfma_f32_16x16x32_bf16 %0, %1, %2, %0\n\tv_mfma_f32_16x16x32_bf16 %0, %3, %4, %0" : "+v"(c) : "v"(a), "v"(b), "v"(a2), "v"(b2))
    unsigned lm[2][2], ln[2][2];
#pragma unroll
    for (int u = 0; u < 2; ++u)
#pragma unroll
      for (int kk = 0; kk < 2; ++kk) {
        lm[u][kk] = lds_addr(pm) + u * 2 * G1_STAGE + (kk ? ko1 : ko0);
        ln[u][kk] = lds_addr(pn) + u * 2 * G1_STAGE + (kk ? ko1 : ko0);
        asm volatile("" : "+v"(lm[u][kk]), "+v"(ln[u][kk]));
      }
    auto ldm = [&](int stg, int i, int kk) __attribute__((always_inline)) { fm[kk][i] = *LDSP(const bf16x8, lm[stg >> 1][kk] + (stg & 1) * G1_STAGE + TO(i)); };
    auto ldn = [&](int stg, int jn, int kk) __attribute__((always_inline)) { fn[kk][jn] = *LDSP(const bf16x8, ln[stg >> 1][kk] + (stg & 1) * G1_STAGE + TO(jn)); };
    auto quad = [&](int a, int c, int d, int s) __attribute__((always_inline)) {
      const int i = (s < 2 || s >= 6) ? a : a + 1, jn = s < 4 ? c : d, kk = (s == 1 || s == 2 || s == 5 || s == 6);
      MFA(acc[i][jn], fm[kk][i], fn[kk][jn]);
    };
    auto quad2 = [&](int a, int c, int d, int s) __attribute__((always_inline)) {
      const int i = (s < 2 || s >= 6) ? a : a + 1, jn = s < 4 ? c : d, kk = (s == 2 || s == 6);
      MFA2(acc[i][jn], fm[kk][i], fn[kk][jn], fm[1 - kk][i], fn[1 - kk][jn]);
    };
    auto half1 = [&](int stg) __attribute__((always_inline)) {
      const int ja = (stg & 1) * 2, jb = 2 - ja;
      SLOTS(8) {
        quad(0, ja, ja + 1, q);
        if (q < 4) ldn(stg, jb + (q >> 1), q & 1); else ldm(stg, 2 + ((q - 4) >> 1), q == 5 || q == 6);
        SB();
      }
      SLOTS(4) { quad2(0, jb, jb + 1, 2 * q); SB(); }
    };
    auto half2 = [&](int stg) __attribute__((always_inline)) {
      const int ja = (stg & 1) * 2, jb = 2 - ja, nx = (stg + 1) & 3;
      SLOTS(8) {
        quad(2, jb + 1, jb, q);
        if (!(q & 1)) ldm(nx, q >> 2, q == 2 || q == 4);
        SB();
      }
      SLOTS(8) {
        quad(2, ja, ja + 1, q);
        if (!(q & 1)) ldn(nx, jb + (q >> 2), (q >> 1) & 1);
        SB();
      }
    };
    SLOTS(16) asm volatile("" : "+v"(acc[q >> 2][q & 3]));
    SB();
    SLOTS(4) { ldm(0, q >> 1, q & 1); ldn(0, q >> 1, q & 1); }
    SB();
#pragma unroll 1
    for (int k4 = 0; k4 < G1_NKT - 4; k4 += 4) {
      half1(0); BAR_LGKM();
      SB(); half2(0);
      half1(1); BAR_LGKM();
      SB(); half2(1);
      half1(2); BAR_LGKM();
      SB(); half2(2);
      half1(3); BAR_LGKM();
      SB(); half2(3);
    }
    bf16x8 gm[2][4], gn[2][8];
    unsigned am[2][2], an[2][2];
#pragma unroll
    for (int u = 0; u < 2; ++u)
#pragma unroll
      for (int kk = 0; kk < 2; ++kk) {
        am[u][kk] = lds_addr(pm) + (u ? 3 : 1) * G1_STAGE + (kk ? ko1 : ko0);
        an[u][kk] = lds_addr(pn) + (u ? 3 : 1) * G1_STAGE + (kk ? ko1 : ko0);
        asm volatile("" : "+v"(am[u][kk]), "+v"(an[u][kk]));
      }
    unsigned al = lds_addr(smem + LDS_MB) + (wr * 32 + fq * 8) * 4, ac = lds_addr(smem + LDS_MB) + (256 + wc * 64 + mrow) * 4;
    unsigned ae = lds_addr(smem + G2_MRING) + (wc * 64 + mrow) * 128 + (((wr * 4 + fq) ^ sw) * 16);
    asm volatile("" : "+v"(al), "+v"(ac), "+v"(ae));
    auto rdg = [&](int set, int t, int h, int r) __attribute__((always_inline)) {
      const int u = t == 15, o = t == 14 ? G1_STAGE : 0;
      if (r == 0 || r == 2 || r == 4 || r == 5) {
        const int i = r >= 4, kk = r == 2 || r == 4;
        gm[set][kk * 2 + i] = *LDSP(const bf16x8, am[u][kk] + o + h * 4096 + i * 512);
      } else {
        const int jn = r < 4 ? 0 : (r - 4) >> 1, kk = r < 4 ? r >> 1 : r & 1;
        gn[set][kk * 4 + jn] = *LDSP(const bf16x8, an[u][kk] + o + TO(jn));
      }
    };
    auto mmg = [&](int set, int h, int q) __attribute__((always_inline)) {
      const int jn = q >> 2, r = q & 3, i = (r >> 1) ^ (jn & 1), kk = r == 1 || r == 2;
      MFA(acc[h * 2 + i][jn], gm[set][kk * 2 + i], gn[set][kk * 4 + jn]);
    };
    f32x4 rsv[2], mrv[2];
    float cev[4], tc[32];
    auto rds = [&](int h, int r) __attribute__((always_inline)) {
      if (r < 2) mrv[r] = *LDSP(const f32x4, al + h * 256 + r * 16); else rsv[r - 2] = *LDSP(const f32x4, al + 512 + h * 256 + (r - 2) * 16);
    };
    auto prep = [&](int n) __attribute__((always_inline)) {
      tc[n] = llvm_fmul_legacy(mrv[(n >> 2) & 1][n & 3], cev[n >> 3]);
      asm volatile("" : "+v"(tc[n]));
    };
    u32x4 ep;
    auto epi = [&](int h, int q) __attribute__((always_inline)) {
      const int jn = q >> 1, i = q & 1;
      asm volatile("v_fmac_f32 %0, %4, %8\n\tv_fmac_f32 %1, %5, %9\n\tv_fmac_f32 %2, %6, %10\n\tv_fmac_f32 %3, %7, %11"
                   : "+v"(tc[q * 4]), "+v"(tc[q * 4 + 1]), "+v"(tc[q * 4 + 2]), "+v"(tc[q * 4 + 3])
                   : "v"(acc[h * 2 + i][jn][0]), "v"(acc[h * 2 + i][jn][1]), "v"(acc[h * 2 + i][jn][2]), "v"(acc[h * 2 + i][jn][3]),
                     "v"(rsv[i][0]), "v"(rsv[i][1]), "v"(rsv[i][2]), "v"(rsv[i][3]));
      ep[i * 2] = pk2(tc[q * 4], tc[q * 4 + 1]); ep[i * 2 + 1] = pk2(tc[q * 4 + 2], tc[q * 4 + 3]);
      if (i) *LDSP(u32x4, ae + m_off(h) + TO(jn)) = ep;
    };
    SB();
    half1(0);
    BAR_LGKM();
    SB();
    SLOTS(16) {
      if (q < 8) quad(2, 3, 2, q); else quad(2, 0, 1, q - 8);
      if (q < 12) rdg(0, 13, 0, q);
      SB();
    }
    BAR();
    SB();
    SLOTS(16) {
      mmg(0, 0, q);
      if (q < 12) rdg(1, 14, 0, q);
      else { rds(0, q - 12); cev[q - 12] = *LDSP(const float, ac + (((q - 12) >> 1) * 32 + ((q - 12) & 1) * 4) * 4); }
      SB();
    }
    PAGE(9);
    BAR();
    SB();
    SLOTS(16) { mmg(1, 0, q); if ((q & 3) < 3) rdg(0, 15, 0, (q >> 2) * 3 + (q & 3)); if (q >= 8) { prep(2 * q - 16); prep(2 * q - 15); } SB(); }
    SLOTS(16) { mmg(0, 0, q); if ((q & 3) < 3) rdg(1, 13, 1, (q >> 2) * 3 + (q & 3)); prep(16 + q); SB(); if (q >= 9 && (q & 1)) { epi(0, (q - 9) >> 1); SB(); } }
    SLOTS(8) { mmg(1, 1, q); if (q < 6) { rdg(0, 14, 1, 2 * q); rdg(0, 14, 1, 2 * q + 1); } SB(); if (q & 1) { epi(0, 4 + (q >> 1)); SB(); } }
    BAR_LGKM();
    SB();
    SLOTS(8) { mmg(1, 1, 8 + q); if (q < 4) rdg(1, 15, 1, q == 0 ? 1 : q == 1 ? 3 : q + 4); else rds(1, q - 4); SB(); }
    SLOTS(16) { mmg(0, 1, q); if (q < 8) rdg(1, 15, 1, q == 0 ? 0 : q == 1 ? 2 : q < 4 ? q + 2 : q + 4); prep(q); SB(); }
    asm volatile("s_waitcnt lgkmcnt(0)" ::: "memory");
    if (lane == 0) asm volatile("ds_add_u32 %0, %1" :: "v"(lds_addr(smem + LDS_SEL + 12)), "v"(1) : "memory");
    SB();
    SLOTS(16) {
      mmg(1, 1, q);
      if (q < 4) fn[0][q] = *(const bf16x8*)(smem + (wr * 128 + fr) * 128 + (q >> 1) * 2048 + ((q & 1) ? ko1 : ko0));
      else if (q < 8) fm[q & 1][(q - 4) >> 1] = *(const bf16x8*)(smem + G2_MRING + m_off(0) + (wc * 64 + mrow) * 128 + TO((q - 4) >> 1) + ((q & 1) ? ko1 : ko0));
      prep(16 + q);
      SB();
    }
    {
      lds_vi rel = (lds_vi)(smem + LDS_SEL + 12);
      int it = 0;
      while (rel[0] != 4 && it < SPIN_MAX * 8) ++it;
      asm volatile("" ::: "memory");
    }
    asm volatile("s_nop 7");
    SB();
    SLOTS(8) { epi(1, q); SB(); }
  }

  const char* pn = smem + (wr * 128 + fr) * 128;
  const char* pm = smem + G2_MRING + (wc * 64 + mrow) * 128;
  f32x4 acc[8][4];
  auto mm = [&](bool imaj, int set, int q2, int s, int mode) __attribute__((always_inline)) {
    int i, jj, kk;
    if (imaj) { i = s >> 2; const int r = s & 3; jj = (r >> 1) ^ (i & 1); kk = (r == 1 || r == 2); }
    else { jj = s >> 3; const int r = s & 7; i = jj ? 3 - (r >> 1) : (r >> 1); kk = ((r + 1) >> 1) & 1; }
    if (mode == 1 && !(s & 1)) asm volatile("v_mfma_f32_16x16x32_bf16 %0, %1, %2, 0" : "=&v"(acc[q2 * 2 + jj][i]) : "v"(fm[kk][i]), "v"(fn[set][jj * 2 + kk]));
    else if (mode) asm volatile("v_mfma_f32_16x16x32_bf16 %0, %1, %2, %0" : "+v"(acc[q2 * 2 + jj][i]) : "v"(fm[kk][i]), "v"(fn[set][jj * 2 + kk]));
    else acc[q2 * 2 + jj][i] = __builtin_amdgcn_mfma_f32_16x16x32_bf16(fm[kk][i], fn[set][jj * 2 + kk], acc[q2 * 2 + jj][i], 0, 0, 0);
  };
  unsigned bn[2], bm[2], an[2], am[2];
#pragma unroll
  for (int kk = 0; kk < 2; ++kk) {
    bn[kk] = lds_addr(pn) + (kk ? ko1 : ko0); bm[kk] = lds_addr(pm) + (kk ? ko1 : ko0);
    asm volatile("" : "+v"(bn[kk]), "+v"(bm[kk]));
    an[kk] = bn[kk]; am[kk] = bm[kk] + m_off(0);
  }
#define RDN(jt, kk, o) (*LDSP(const bf16x8, an[kk] + (o) + (jt) * 2048))
#define RDM(i, kk, o) (*LDSP(const bf16x8, am[kk] + (o) + TO(i)))
  PAGE(10);
  tag = __builtin_amdgcn_readfirstlane(((const unsigned*)(smem + LDS_SEL))[5]);
  {
    int son = 0;
    auto tile = [&](int kt, int zero) __attribute__((always_inline)) {
      SLOTS(16) {
        mm(true, 0, 0, q, zero);
        if (!(q & 1)) {
          if (q < 8) fm[(q >> 1) & 1][2 + (q >> 2)] = RDM(2 + (q >> 2), (q >> 1) & 1, 0);
          else { const int r = (q - 8) >> 1; fn[1][r] = RDN(2 + (r >> 1), r & 1, 0); }
        }
        SB();
      }
      SLOTS(16) {
        mm(false, 1, 1, q, zero);
        if (!(q & 1) && q < 8) fn[0][q >> 1] = RDN(4 + (q >> 2), (q >> 1) & 1, 0);
        SB();
      }
      const int som = son >> 1;
      son = son == 65536 ? 0 : son + 32768;
      SLOTS(16) {
        mm(false, 0, 2, q, zero);
        if (!(q & 1) && q < 8) fn[1][q >> 1] = RDN(6 + (q >> 2), (q >> 1) & 1, 0);
        if (q == 9 || q == 11) { an[q == 11] = bn[q == 11] + son; asm volatile("" : "+v"(an[q == 11])); }
        if (q == 13 || q == 15) { am[q == 15] = bm[q == 15] + som; asm volatile("" : "+v"(am[q == 15])); }
        SB();
      }
      BAR_LGKM();
      SB();
      SLOTS(16) {
        mm(true, 1, 3, q, zero);
        if (!(q & 1)) {
          if (q < 8) fn[0][q >> 1] = RDN(q >> 2, (q >> 1) & 1, 0);
          else fm[(q >> 1) & 1][(q - 8) >> 2] = RDM((q - 8) >> 2, (q >> 1) & 1, 0);
        }
        SB();
      }
    };
    SB();
    tile(0, 1);
#pragma unroll 1
    for (int kt = 1; kt < G2_NKT - 3; ++kt) tile(kt, 0);
  }
  unsigned fx;
  {
    constexpr int NG = 12;
    static constexpr int g_t[NG] = {0, 0, 1, 1, 2, 2, 2, 1, 0, 0, 1, 2};
    static constexpr int g_q[NG] = {0, 1, 0, 1, 0, 1, 2, 2, 2, 3, 3, 3};
    constexpr int G14 = 1, G15 = 3, GP0 = 5, GP1 = 6;
    constexpr auto chp = [](int g) { return g == 0 || g_t[g] != g_t[g - 1]; };
    constexpr auto chn = [](int g) { return g + 1 < NG && g_t[g + 1] != g_t[g]; };
    static_assert(n_off(13) == 32768 && n_off(14) == 65536 && n_off(15) == 0, "");
    unsigned an2[2];
#pragma unroll
    for (int kk = 0; kk < 2; ++kk) {
      an[kk] = bn[kk]; an2[kk] = bn[kk] + 65536; am[kk] = bm[kk];
      asm volatile("" : "+v"(an[kk]), "+v"(an2[kk]), "+v"(am[kk]));
    }
    int vo;
    u32x4 pk;
    u32x2 kv;
    constexpr int NF0 = 152;
    auto publish = [&](int d, int h) __attribute__((always_inline)) {
      if (h == 0) { vo = lane; PINV(vo); vo = vo * 16 + (((d + kq + 1) & 3) * 4 + kq) * 4 * MSG; PINV(vo); }
      const f32x4 v = acc[2 * d + (h >> 2)][h & 3];
      pk[(h & 1) * 2] = pk2(v[0], v[1]); pk[(h & 1) * 2 + 1] = pk2(v[2], v[3]);
      if (h & 1) __builtin_amdgcn_raw_buffer_store_b128(pk, rs, vo + (h >> 1) * 1024, 0, 16);
    };
    int fo = lane == 0 ? kq * 512 : (int)0x80000000;
#define MSG_FLAG(d) __builtin_amdgcn_raw_buffer_store_b32(tag, rc, fo + (((d) + kq + 1) & 3) * 2048, 0, 16)
    auto rdn = [&](int g, int r) __attribute__((always_inline)) {
      const int t = g_t[g], jt = g_q[g] * 2 + (r >> 1);
      fn[g & 1][r] = *LDSP(const bf16x8, (t == 1 ? an2[r & 1] : an[r & 1]) + (t == 0 ? 32768 : 0) + jt * 2048);
    };
    auto rdm = [&](int t, int i, int kk) __attribute__((always_inline)) { fm[kk][i] = *LDSP(const bf16x8, am[kk] + m_off(13 + t) + TO(i)); };
    auto group = [&](auto GI) __attribute__((always_inline)) {
      constexpr int gi = decltype(GI)::value;
      if (gi == G14) { BAR();
        SB(); }
      if (gi == G15) {
        PAGE(11);
        BAR();
        SB();
        xq[0] = ld16_s<0>(x, xo); xq[1] = ld16_s<64>(x, xo);
        sq = ld8_s_sc1(stat, xo >> 11 << 3);
        kv = *(const u32x2*)(smem + LDS_SEL);
        SB();
      }
#pragma unroll
      for (int q = 0; q < 16; ++q) {
        const int n = gi * 16 + q;
        if (n == NF0) {
          wait_xq<12>(xq, sq);
          key[0] = __builtin_amdgcn_readfirstlane(kv.x); key[1] = __builtin_amdgcn_readfirstlane(kv.y);
          nf_pre();
          SB();
        }
        if (n == 112) {
        }
        if (n == 152) {
        }
        if (n == 168) {
          WAIT_VM(8); MSG_FLAG(0); SB(); }
        if (n == 176) { WAIT_VM(5); MSG_FLAG(1); SB(); }
        if (n == 184) {
          WAIT_VM(2);
          MSG_FLAG(2); fx = bld4_sc1(rc, po); SB(); }
        mm(chp(gi) || chn(gi), gi & 1, g_q[gi], q, 2);
        constexpr bool ni = gi + 1 < NG && (chp(gi + 1 < NG ? gi + 1 : gi) || chn(gi + 1 < NG ? gi + 1 : gi));
        auto nr = [](int r) { return ni && r >= 2 ? 5 - r : r; };
        if constexpr (chp(gi) && chn(gi)) {
          if (q < 4) rdm(g_t[gi], 2 + (q >> 1), q & 1); else if (q < 8) rdn(gi + 1, nr(q - 4)); else if (q < 12) rdm(g_t[gi + 1], (q - 8) >> 1, q & 1);
        } else if constexpr (chp(gi)) {
          if (q < 4) rdm(g_t[gi], 2 + (q >> 1), q & 1);
          else if (gi + 1 < NG && q < 12 && !(q & 1)) rdn(gi + 1, nr((q - 4) >> 1));
        } else if constexpr (chn(gi)) {
          if (!(q & 1)) { if (q < 8) rdn(gi + 1, nr(q >> 1)); else rdm(g_t[gi + 1], (q - 8) >> 2, (q >> 1) & 1); }
        } else {
          if (!(q & 1) && q < 8) rdn(gi + 1, nr(q >> 1));
        }
        if (gi == GP0 && q < 8) publish(0, q);
        if (gi == GP1 && q < 8) publish(1, q);
        if (gi == 9 && q < 8) publish(2, q);
        if (n >= NF0) {
          const int m = (n - NF0) * 2;
          if (m < 56) { nf_step(m); nf_step(m + 1); } else if (m < 72) { nf_unp(m - 56); nf_unp(m - 55); }
        }
        SB();
      }
    };
#define GR4(i) group(IC<i>{}); group(IC<i + 1>{}); group(IC<i + 2>{}); group(IC<i + 3>{});
    GR4(0) GR4(4) GR4(8)
    asm volatile("s_nop 7");
    SB();
  }
#pragma unroll
  for (int i = 0; i < 4; ++i) *(f32x4*)(smem + LDS_ACC + wr * 16384 + wc * 4096 + i * 1024 + lane * 16) = acc[7][i];
#pragma unroll
  for (int i = 0; i < 4; ++i) ya[i] = acc[6][i];
  asm volatile("s_waitcnt lgkmcnt(0)" ::: "memory");
  if (lane == 0) sig[0] = 2;
  asm volatile("s_waitcnt vmcnt(0)" : "+v"(fx) :: "memory");
  seen = (unsigned)__ballot(fx == tag);
  }

  PAGE(6);
  const unsigned rel0 = 0xffff - (key[0] & 0xffffu) - trow, rel1 = 0xffff - (key[1] & 0xffffu) - trow;
  if (min(rel0, rel1) < 16) {
#pragma unroll 1
    for (int c = 0; c < 2; ++c) {
      const unsigned rel = c ? rel1 : rel0;
      if (rel >= 16) continue;
      lds_vi cnt = (lds_vi)(smem + LDS_SEL + 24 + c * 4);
      int it = 0;
      while (cnt[0] != 4 && it < SPIN_MAX * 8) { __builtin_amdgcn_s_sleep(1); ++it; }
      asm volatile("" ::: "memory");
      if (rel == fr) {
        const float* fp = (const float*)(smem + LDS_FIN + c * 512) + (wid & 1) * 64 + fq * 8;
#pragma unroll
        for (int u4 = 0; u4 < 4; ++u4) {
          const f32x4 v = *(const f32x4*)(fp + (u4 >> 1) * 32 + (u4 & 1) * 4);
#pragma unroll
          for (int k = 0; k < 4; k += 2) {
            const unsigned u = pk2(nf[u4 * 4 + k] + v[k], nf[u4 * 4 + k + 1] + v[k + 1]);
            nf[u4 * 4 + k] = lo_bf(u); nf[u4 * 4 + k + 1] = hi_bf(u);
          }
        }
      }
    }
  }
  if (ha) {
    int it = 0;
    while (sig[0] != 2 && it < SPIN_MAX * 8) { __builtin_amdgcn_s_sleep(1); ++it; }
    asm volatile("" ::: "memory");
#pragma unroll
    for (int i = 0; i < 4; ++i) ya[i] = *(const f32x4*)(smem + LDS_ACC + (wid & 2) * 8192 + (wid & 1) * 4096 + i * 1024 + lane * 16);
  }
  u32x4 pp[3][2];
  unsigned e_lo = 0x00003f80u, e_hi = 0x3f800000u;
  asm volatile("" : "+v"(e_lo), "+v"(e_hi));
  __amdgpu_buffer_rsrc_t rs_ = rs, rc_ = rc;
  int mo[3] = {MSG_IDX(0) * 4 * MSG, MSG_IDX(1) * 4 * MSG, MSG_IDX(2) * 4 * MSG}, mv = lane * 16 + ha * 2048, mv2 = mv + 1024;
  asm volatile("" : "+s"(rs_), "+s"(rc_), "+s"(mo[0]), "+s"(mo[1]), "+s"(mo[2]));
  asm volatile("" : "+v"(mv), "+v"(mv2));
#define MSG_POLL(m) do { \
    if (!((seen >> (m)) & 1)) { \
      unsigned pv_, it_ = SPIN_MAX; \
      asm volatile("1:\n\tbuffer_load_dword %0, %3, %4, 0 offen sc1\n\ts_waitcnt vmcnt(0)\n\tv_cmp_eq_u32 vcc, %5, %0\n\ts_mov_b32 %1, vcc_lo\n\t" \
                   "s_bitcmp1_b32 %1, %6\n\ts_cbranch_scc1 2f\n\ts_sub_u32 %2, %2, 1\n\ts_cbranch_scc1 2f\n\ts_sleep 1\n\ts_branch 1b\n2:" \
                   : "=&v"(pv_), "+s"(seen), "+s"(it_) : "v"(po), "s"(rc_), "s"(tag), "i"(m) : "vcc", "scc", "memory"); \
    } \
    pp[m][0] = __builtin_amdgcn_raw_buffer_load_b128(rs_, mv, mo[m], 16); \
    pp[m][1] = __builtin_amdgcn_raw_buffer_load_b128(rs_, mv2, mo[m], 16); } while (0)
#define MSG_ADD(m) do { \
    _Pragma("unroll") for (int u = 0; u < 8; ++u) { \
      ya[u >> 1][(u & 1) * 2] = add_bf(pp[m][u >> 2][u & 3], e_lo, ya[u >> 1][(u & 1) * 2]); ya[u >> 1][(u & 1) * 2 + 1] = add_bf(pp[m][u >> 2][u & 3], e_hi, ya[u >> 1][(u & 1) * 2 + 1]); } } while (0)
  MSG_POLL(2); MSG_POLL(1); MSG_POLL(0);
  SB();
  MSG_ADD(2); MSG_ADD(1);
  asm volatile("" : "+v"(ya[0]), "+v"(ya[1]), "+v"(ya[2]), "+v"(ya[3]));
  SB();
  MSG_ADD(0);
  {
    unsigned ov[8];
    SB();
#pragma unroll
    for (int u = 0; u < 8; ++u) ov[u] = pk2(ya[u >> 1][(u & 1) * 2], ya[u >> 1][(u & 1) * 2 + 1]);
    SB();
#pragma unroll
    for (int u = 0; u < 8; ++u) { xf[2 * u] = add_bf(ov[u], e_lo, xf[2 * u]); xf[2 * u + 1] = add_bf(ov[u], e_hi, xf[2 * u + 1]); }
    SB();
#pragma unroll
    for (int u = 0; u < 8; ++u) ov[u] = pk2(xf[2 * u], xf[2 * u + 1]);
    SB();
#pragma unroll
    for (int u = 0; u < 8; ++u) { nf[2 * u] = add_bf(ov[u], e_lo, nf[2 * u]); nf[2 * u + 1] = add_bf(ov[u], e_hi, nf[2 * u + 1]); }
    SB();
#pragma unroll
    for (int u = 0; u < 8; ++u) ov[u] = pk2(nf[2 * u], nf[2 * u + 1]);
    SB();
    *(u32x4*)((char*)out + (unsigned)xo) = (u32x4){ov[0], ov[1], ov[2], ov[3]};
    *(u32x4*)((char*)out + (unsigned)xo + 64) = (u32x4){ov[4], ov[5], ov[6], ov[7]};
  }
}

extern "C" void kernel_launch(void* const* d_in, const int* in_sizes, int n_in, void* d_out, int out_size,
                              void* d_ws, size_t ws_size, hipStream_t stream) {
  const u16* x = (const u16*)d_in[0];
  const u16* ws_re = (const u16*)d_in[1];
  const u16* ws_im = (const u16*)d_in[2];
  const u16* wd_re = (const u16*)d_in[3];
  const u16* wd_im = (const u16*)d_in[4];
  const u16* w_r = (const u16*)d_in[6];
  const u16* w_gate = (const u16*)d_in[7];
  const u16* w_up = (const u16*)d_in[8];
  const u16* w_down = (const u16*)d_in[9];
  k_main<<<256, 512, 0, stream>>>(x, 0x7fffffffu, 0x00020000u, wd_re, 0x7fffffffu, 0x00020000u, wd_im, 0x7fffffffu, 0x00020000u, w_r, (char*)d_ws,
                                  ws_re, ws_im, w_gate, w_up, w_down, (u16*)d_out);
}
```

```cpp
#include <hip/hip_runtime.h>
#include <hip/hip_bf16.h>
#include <stdint.h>
#include <type_traits>

extern "C" __device__ unsigned long long llvm_dispatch_id(void) __asm("llvm.amdgcn.dispatch.id");
extern "C" __device__ float llvm_fmul_legacy(float, float) __asm("llvm.amdgcn.fmul.legacy");
typedef unsigned short u16;
typedef unsigned long long u64;
typedef __attribute__((ext_vector_type(8))) short bf16x8;
typedef __attribute__((ext_vector_type(4))) unsigned u32x4;
typedef __attribute__((ext_vector_type(2))) unsigned u32x2;
typedef __attribute__((ext_vector_type(4))) float f32x4;
typedef __attribute__((ext_vector_type(2))) float f32x2;
typedef __attribute__((ext_vector_type(2))) __bf16 bf16x2;
typedef __attribute__((address_space(3))) void* lds_vp;
typedef volatile __attribute__((address_space(3))) int* lds_vi;

constexpr int S_ = 2048, D_ = 1024, F_ = 3584;
template <int N> using IC = std::integral_constant<int, N>;

static __device__ __forceinline__ float bf2f(u16 u) { return __uint_as_float((unsigned)u << 16); }
static __device__ __forceinline__ u16 f2bf(float f) {
  __hip_bfloat16 b = __float2bfloat16(f);
  return *reinterpret_cast<u16*>(&b);
}
static __device__ __forceinline__ float rbf(float f) { return bf2f(f2bf(f)); }
static __device__ __forceinline__ float lo_bf(unsigned u) { return __uint_as_float(u << 16); }
static __device__ __forceinline__ float hi_bf(unsigned u) { return __uint_as_float(u & 0xffff0000u); }
static __device__ __forceinline__ unsigned pk2(float a, float b) {
  const bf16x2 v = __builtin_convertvector((f32x2){a, b}, bf16x2);
  return __builtin_bit_cast(unsigned, v);
}
static __device__ __forceinline__ float add_bf(unsigned u, unsigned e, float c) {
  return __builtin_amdgcn_fdot2_f32_bf16(__builtin_bit_cast(bf16x2, u), __builtin_bit_cast(bf16x2, e), c, false);
}
static __device__ __forceinline__ float ln_elem(float x, float mb, float sd, float ri) {
  const float d = rbf(x - mb);
  const float q0 = d * ri;
  return rbf(__builtin_fmaf(__builtin_fmaf(-q0, sd, d), ri, q0));
}
template <int CTRL> static __device__ __forceinline__ float dpp_f(float v) {
  return __int_as_float(__builtin_amdgcn_update_dpp(0, __float_as_int(v), CTRL, 0xf, 0xf, true));
}
template <int CTRL> static __device__ __forceinline__ unsigned dpp_u(unsigned v) {
  return (unsigned)__builtin_amdgcn_update_dpp(0, (int)v, CTRL, 0xf, 0xf, true);
}
static __device__ __forceinline__ float row_sum(float v) {
  v += dpp_f<0xB1>(v);
  v += dpp_f<0x4E>(v);
  v += dpp_f<0x141>(v);
  v += dpp_f<0x140>(v);
  return v;
}
static __device__ __forceinline__ float fold32(float a, float b) {
  const auto r = __builtin_amdgcn_permlane32_swap(__float_as_uint(a), __float_as_uint(b), false, false);
  return __uint_as_float(r[0]) + __uint_as_float(r[1]);
}
static __device__ __forceinline__ float fold16(float a, float b) {
  const auto r = __builtin_amdgcn_permlane16_swap(__float_as_uint(a), __float_as_uint(b), false, false);
  return __uint_as_float(r[0]) + __uint_as_float(r[1]);
}
static __device__ __forceinline__ float rdl(float v, int l) { return __int_as_float(__builtin_amdgcn_readlane(__float_as_int(v), l)); }

#define ST_JB 9
#define WSN 10
#define FSM 0xa2
#define GV_JB 7
#define DG 11
#define AB_STRIDE 32
#define SPIN_MAX (1 << 17)

static __device__ __forceinline__ unsigned lds_addr(const void* p) { return (unsigned)(size_t)(lds_vp)p; }

constexpr int MSG = 4096;
constexpr size_t WS_ABT   = 0;
constexpr size_t WS_SLAB  = WS_ABT + (size_t)D_ * 4096 * 2;
constexpr size_t WS_GU    = WS_SLAB + (size_t)64 * 64 * MSG;
constexpr size_t WS_DN    = WS_GU + (size_t)8 * F_ * 16;
constexpr size_t WS_FLAGS = WS_DN + (size_t)2 * 8 * 32 * 64 * 16;
constexpr int FL_X = 0;
constexpr int FL_AB = 64 * 64 * 32;
constexpr int FL_ST = FL_AB + 2048 * AB_STRIDE;
constexpr int FL_CE = FL_ST + 4096;
constexpr int FL_KEY = FL_CE + 4096;
constexpr int FL_EPOCH = FL_KEY + 4096;
constexpr int WS_MB = (int)((WS_FLAGS + (FL_EPOCH + 1) * 4 + 1048575) >> 20);

#define WAIT_VM(n) asm volatile("s_waitcnt vmcnt(" #n ")" ::: "memory")
template <int N> static __device__ __forceinline__ void wait_vm() { asm volatile("s_waitcnt vmcnt(%0)" :: "i"(N) : "memory"); }
#define BAR_LGKM() asm volatile("s_waitcnt lgkmcnt(0)\n\ts_barrier" ::: "memory")
#define BAR() asm volatile("s_barrier" ::: "memory")
#define SB() __builtin_amdgcn_sched_barrier(0)
#define PAGE(n) asm volatile("s_branch 2f\n\t.p2align 12\nk_pg" #n ":\n\ts_setpc_b64 s[42:43]\n2:")
#define CALL_PG(n) asm volatile("s_getpc_b64 s[40:41]\n3:\n\ts_mov_b32 s42, k_pg" #n "-3b\n\ts_ashr_i32 s43, s42, 31\n\ts_add_u32 s40, s40, s42\n\ts_addc_u32 s41, s41, s43\n\t" \
                                "s_swappc_b64 s[42:43], s[40:41]" ::: "s40", "s41", "s42", "s43", "scc", "memory")

static __device__ __forceinline__ __amdgpu_buffer_rsrc_t mk_rsrc(const void* p) {
  return __builtin_amdgcn_make_buffer_rsrc((void*)p, 0, 0x7fffffff, 0x00020000);
}
#define DMA_(rsrc, voff, mbase, MI, sbase, SI, pol) do { unsigned _t; \
    asm volatile("s_add_i32 m0, %2, %3\n\ts_add_i32 %0, %5, %6\n\tbuffer_load_dwordx4 %1, %4, %0 offen" pol " lds" \
                 : "=&s"(_t) : "v"(voff), "s"(mbase), "i"(MI), "s"(rsrc), "s"(sbase), "i"(SI) : "memory", "scc"); } while (0)
#define TOUCH(rsrc, voff, mbase, MI, soff) \
    asm volatile("s_add_i32 m0, %1, %2\n\ts_nop 0\n\tbuffer_load_dword %0, %3, %4 offen lds" \
                 :: "v"(voff), "s"(mbase), "i"(MI), "s"(rsrc), "s"(soff) : "memory", "scc")
template <int OFF> static __device__ __forceinline__ u32x4 ld16_s(const void* p, int voff) {
  u32x4 v;
  asm volatile("global_load_dwordx4 %0, %1, %2 offset:%3" : "=v"(v) : "v"(voff), "s"(p), "i"(OFF) : "memory");
  return v;
}
template <int OFF> static __device__ __forceinline__ void sld(unsigned& sink, const void* p, int soff) {
  asm volatile("s_load_dword %0, %1, %2 offset:%3" : "+s"(sink) : "s"(p), "s"(soff), "i"(OFF) : "memory");
}
static __device__ __forceinline__ unsigned gld4_s(const void* p, unsigned voff) {
  unsigned v;
  asm volatile("global_load_dword %0, %1, %2" : "=v"(v) : "v"(voff), "s"(p) : "memory");
  return v;
}
static __device__ __forceinline__ u32x2 bld8_sc1(__amdgpu_buffer_rsrc_t r, unsigned voff) {
  u32x2 v;
  asm volatile("buffer_load_dwordx2 %0, %1, %2, 0 offen sc1" : "=v"(v) : "v"(voff), "s"(r) : "memory");
  return v;
}
template <int N> static __device__ __forceinline__ void wait_xs(unsigned& a, u32x2& b) { asm volatile("s_waitcnt vmcnt(%2)" : "+v"(a), "+v"(b) : "i"(N) : "memory"); }
template <int N> static __device__ __forceinline__ void wait_xq(u32x4 (&a)[2], u32x2& b) { asm volatile("s_waitcnt vmcnt(%3)" : "+v"(a[0]), "+v"(a[1]), "+v"(b) : "i"(N) : "memory"); }
static __device__ __forceinline__ u32x2 ld8_s_sc1(const void* p, int voff) {
  u32x2 v;
  asm volatile("global_load_dwordx2 %0, %1, %2 sc1" : "=v"(v) : "v"(voff), "s"(p) : "memory");
  return v;
}
static __device__ __forceinline__ void lgkm0(unsigned& sink) { asm volatile("s_waitcnt lgkmcnt(0)" : "+s"(sink) :: "memory"); }
template <typename T> static __device__ __forceinline__ void pin4(T& a, T& b, T& c, T& d) { asm volatile("" : "+s"(a), "+s"(b), "+s"(c), "+s"(d)); }
template <typename T, typename U> static __device__ __forceinline__ void pins(T& a, U& b) { asm volatile("" : "+s"(a), "+s"(b)); }
static __device__ __forceinline__ void launder(int& v, int& a, int& b) { asm volatile("" : "+v"(v)); asm volatile("" : "+s"(a), "+s"(b)); }
static __device__ __forceinline__ unsigned bld4_sc1(__amdgpu_buffer_rsrc_t r, int voff) {
  unsigned v;
  asm volatile("buffer_load_dword %0, %1, %2, 0 offen sc1" : "=v"(v) : "v"(voff), "s"(r) : "memory");
  return v;
}
static __device__ __forceinline__ u32x4 bld16_nt(__amdgpu_buffer_rsrc_t r, int voff, int soff) {
  u32x4 v;
  asm volatile("buffer_load_dwordx4 %0, %1, %2, %3 offen nt" : "=v"(v) : "v"(voff), "s"(r), "s"(soff) : "memory");
  return v;
}
static __device__ __forceinline__ void bld16_nt_io(u32x4& v, __amdgpu_buffer_rsrc_t r, int voff, int soff) {
  asm volatile("buffer_load_dwordx4 %0, %1, %2, %3 offen nt" : "+v"(v) : "v"(voff), "s"(r), "s"(soff) : "memory");
}
static __device__ __forceinline__ u32x4 bld16_sc1(__amdgpu_buffer_rsrc_t r, int voff, int soff) {
  u32x4 v;
  asm volatile("buffer_load_dwordx4 %0, %1, %2, %3 offen sc1" : "=v"(v) : "v"(voff), "s"(r), "s"(soff) : "memory");
  return v;
}
static __device__ __forceinline__ u32x2 granule_wait(const u64* p, unsigned tag) {
  u64 v = 0;
#pragma unroll 1
  for (int it = 0; it < SPIN_MAX; ++it) {
    v = __hip_atomic_load(p, __ATOMIC_RELAXED, __HIP_MEMORY_SCOPE_AGENT);
    if (__all((unsigned)(v >> 32) == tag)) break;
    __builtin_amdgcn_s_sleep(2);
  }
  return (u32x2){(unsigned)v, (unsigned)(v >> 32)};
}
static __device__ __forceinline__ float key_score(unsigned key) {
  const unsigned u = key >> 16;
  return bf2f((u16)((u & 0x8000u) ? (u & 0x7fffu) : (~u & 0xffffu)));
}
static __device__ __forceinline__ void merge2(unsigned& a1, unsigned& a2, unsigned b1, unsigned b2) {
  const unsigned lo = min(a1, b1);
  a1 = max(a1, b1);
  a2 = max(lo, max(a2, b2));
}
static __device__ __forceinline__ float sqdev16(const u32x4 (&v)[2], float mb) {
  f32x2 q2 = {0.f, 0.f};
#pragma unroll
  for (int i = 0; i < 8; ++i) {
    const f32x2 e = (f32x2){lo_bf(v[i >> 2][i & 3]), hi_bf(v[i >> 2][i & 3])} - mb;
    q2 += e * e;
  }
  return q2.x + q2.y;
}

constexpr int G1_NST = 4, G1_NKT = 16, G1_STAGE = 32768;
constexpr int G2_NKT = 16, G2_MRING = 98304;
constexpr int n_off(int k) { return (k % 3) * 32768; }
constexpr int m_off(int k) { return ((k + 2) % 3) * 16384; }
constexpr int k_ord(int k) { return k < 2 ? k : (k < 9 ? 2 * (k - 1) : 2 * (k - 8) + 1); }
constexpr int LDS_DUMMY = G2_MRING + 49152;
constexpr int LDS_GU = LDS_DUMMY + 4096;
constexpr int LDS_FIN = LDS_GU + 4096;
constexpr int LDS_MB = LDS_FIN + 4096;
constexpr int LDS_SEL = LDS_MB + 1536;
constexpr int LDS_SIG = LDS_SEL + 64;
constexpr int LDS_KEY = LDS_SIG + 32;
constexpr int LDS_BYTES = LDS_KEY + 128;
constexpr int LDS_ACC = n_off(G2_NKT - 3);

constexpr int tpos[16] = {0, 0, 0, 0, 109, 115, 1, 2, 2, 3, 4, 5, 6, 7, 8, 9};
constexpr int twho[16] = {0, 0, 0, 0, 7, 7, 0, 1, 2, 3, 4, 5, 6, 7, 0, 1};
constexpr int dn_cnt(int q) { return q == 5 ? 3 : q == 8 ? 4 : 0; }
constexpr int dn_first(int q) { return q <= 5 ? 0 : q <= 8 ? 3 : 7; }

static __device__ __forceinline__ void code_touch(unsigned& sink, unsigned off) {
  asm volatile("s_getpc_b64 s[40:41]\n\tk_main_rt:\n\ts_min_u32 %1, %1, k_main_end-k_main_rt-196\n\t"
               "s_load_dword %0, s[40:41], %1 offset:0\n\ts_load_dword %0, s[40:41], %1 offset:64\n\ts_load_dword %0, s[40:41], %1 offset:128\n\ts_load_dword %0, s[40:41], %1 offset:192"
               : "+s"(sink), "+s"(off) :: "s40", "s41", "scc", "memory");
}
static __device__ __forceinline__ void touch1(__amdgpu_buffer_rsrc_t r, int voff, unsigned mb, int soff) { TOUCH(r, voff, mb, LDS_DUMMY, soff); }
typedef const __attribute__((address_space(1))) u16* gptr;
typedef __attribute__((ext_vector_type(8))) u64 u64x8;
extern "C" __global__ __launch_bounds__(512, 2) void k_main(const u16* x, unsigned x2, unsigned x3, const u16* wd_re, unsigned r2, unsigned r3,
                                                 const u16* wd_im, unsigned i2, unsigned i3, const u16* w_r,
                                                 char* wsp, const u16* ws_re, const u16* ws_im,
                                                 const u16* w_gate, const u16* w_up, const u16* w_down, u16* out) {
  __shared__ __attribute__((aligned(16))) char smem[LDS_BYTES];
  u64x8 ka;
  u32x4 rm, rn;
  asm volatile("s_load_dwordx4 %0, %3, 0\n\ts_load_dwordx4 %1, %3, %4 offset:16\n\ts_load_dwordx16 %2, %3, 48"
               : "=&s"(rm), "=&s"(rn), "=&s"(ka) : "s"(__builtin_amdgcn_kernarg_segment_ptr()), "s"((blockIdx.x & 4) * 4) : "memory");
  unsigned long long t00;
  asm volatile("s_memrealtime %0" : "=s"(t00) :: "memory");
  const int tid = threadIdx.x, lane = tid & 63;
  const int wid = __builtin_amdgcn_readfirstlane(tid >> 6);
  const int g = blockIdx.x, xcd = g & 7, j = g >> 3;
  if (wid < 4) {
    unsigned cw, co = (j * 4 + wid) * 448;
    asm volatile(".subsection 1\nk_main_end:\n.subsection 0\n\ts_min_u32 %1, %1, k_main_end-k_main-480\n\ts_getpc_b64 s[40:41]\n\ts_add_u32 s40, s40, %1\n\ts_addc_u32 s41, s41, 0\n\t"
                 "s_load_dword %0, s[40:41], 0\n\ts_load_dword %0, s[40:41], 64\n\ts_load_dword %0, s[40:41], 128\n\t"
                 "s_load_dword %0, s[40:41], 192\n\ts_load_dword %0, s[40:41], 256\n\ts_load_dword %0, s[40:41], 320\n\ts_load_dword %0, s[40:41], 384\n\ts_waitcnt lgkmcnt(0)"
                 : "=&s"(cw), "+s"(co) :: "s40", "s41", "scc", "memory");
  }
  const int pw = wid - 4;
  const int row0 = (wid & 3) * 8 + (lane >> 3);
  const int cm = ((lane & 7) ^ (((row0 >> 1) & 1) | ((wid & 3) << 1))) * 16;
  int vm = row0 * (D_ * 2) + cm;
  asm volatile("" : "+v"(vm));
  const int kq = xcd >> 1;
  const int tt = (xcd & 1) * 4 + (j >> 3);
  const int et = j & 7;
  const int z = kq >> 1, st = (kq & 1) * 8 + tt;
  unsigned o_m = st * 128 * D_ * 2, o_n = et * 128 * D_ * 2, mb0 = lds_addr(smem) + pw * 1024;
  asm volatile("" : "+s"(o_m), "+s"(o_n), "+s"(mb0));
#define G1_X(kt, so, q) DMA_(rm, vm, mb0, (so) + (q) * 4096, o_m, (kt) * 128 + (((q) & 1) * 64 + ((q) >> 1) * 32) * D_ * 2, "")
#define G1_W(kt, so, q) DMA_(rn, vm, mb0, (so) + 16384 + (q) * 4096, o_n, (kt) * 128 + (q) * 32 * D_ * 2, "")
#define G1_STAGE_TILE(kt, so, W) do { SB(); G1_X(kt, so, 0); W(0); G1_X(kt, so, 1); W(1); G1_X(kt, so, 2); W(2); G1_X(kt, so, 3); W(3); \
    G1_W(kt, so, 0); W(4); G1_W(kt, so, 1); W(5); G1_W(kt, so, 2); W(6); G1_W(kt, so, 3); W(7); SB(); } while (0)
#define G1_STAGE_A(kt, so, W) do { SB(); G1_X(kt, so, 0); W(0); G1_X(kt, so, 2); W(1); \
    G1_W(kt, so, 0); W(2); G1_W(kt, so, 1); W(3); G1_W(kt, so, 2); W(4); G1_W(kt, so, 3); W(5); W(6); W(7); SB(); } while (0)
#define G1_STAGE_B(kt, so) do { SB(); G1_X(kt, so, 1); G1_X(kt, so, 3); SB(); } while (0)
#define NOW(i)
  asm volatile("s_waitcnt lgkmcnt(0)" : "+s"(ka), "+s"(rm), "+s"(rn) :: "memory");
  int lrow, drow, l16;
  unsigned o_l, o_d;
  auto rows = [&](int i) __attribute__((always_inline)) {
    int g_ = g, l_ = lane;
    if (i == 0) { asm volatile("" : "+s"(g_)); drow = (g_ & ~7) + pw * 2; lrow = drow + (g_ & 3) * 512 + (g_ & 4) * 64; asm volatile("" : "+s"(drow), "+s"(lrow)); }
    if (i == 1) { asm volatile("" : "+s"(g_)); drow += (g_ & 3) * 256; o_l = lrow * D_ * 2; o_d = drow * D_ * 2; asm volatile("" : "+s"(drow), "+s"(o_l), "+s"(o_d)); }
    if (i == 2) { asm volatile("" : "+v"(l_)); l16 = l_ * 16; asm volatile("" : "+v"(l16)); }
  };
  if (wid >= 4) {
    __builtin_amdgcn_s_setprio(3);
    G1_STAGE_TILE(0, 0, rows);
  }
  x = (const u16*)(gptr)((u64)rm[0] | (u64)rm[1] << 32);
  const u16* wdz = (const u16*)(gptr)((u64)rn[0] | (u64)rn[1] << 32);
  w_r = (const u16*)(gptr)ka[0]; wsp = (char*)(gptr)ka[1]; ws_re = (const u16*)(gptr)ka[2]; ws_im = (const u16*)(gptr)ka[3];
  w_gate = (const u16*)(gptr)ka[4]; w_up = (const u16*)(gptr)ka[5]; w_down = (const u16*)(gptr)ka[6]; out = (u16*)(gptr)ka[7];
  int* flags = (int*)(wsp + WS_FLAGS);
  u64* stat = (u64*)(flags + FL_ST);
  asm volatile("s_waitcnt lgkmcnt(0)" : "+s"(t00) :: "memory");
  const int fr = lane & 15, fq = lane >> 4;
  const int ha = wid >> 2;
  int trow, xo, po;
  const char *prs, *prc;
  auto epi_setup = [&]() __attribute__((always_inline)) {
    int t_ = tt, l_ = lane;
    asm volatile("" : "+s"(t_));
    asm volatile("" : "+v"(l_));
    trow = t_ * 256 + ((wid >> 1) & 1) * 128 + kq * 32 + ha * 16;
    xo = ((trow + (l_ & 15)) * D_ + et * 128 + (wid & 1) * 64 + (l_ >> 4) * 8) * 2;
    prs = wsp + WS_SLAB + (size_t)((t_ * 8 + et) * 64 + (wid & 3)) * MSG;
    prc = (const char*)(flags + FL_X + ((t_ * 8 + et) * 64 + (wid & 3)) * 32);
    asm volatile("" : "+s"(trow), "+s"(prs), "+s"(prc));
    po = l_ < 3 ? (kq * 4 + ((kq + 1 + l_) & 3)) * 512 : (int)0x80000000;
    asm volatile("" : "+v"(xo), "+v"(po));
  };
#define rs mk_rsrc(prs)
#define rc mk_rsrc(prc)
  lds_vi sig = (lds_vi)(smem + LDS_SIG + (wid & 3) * 4);
  unsigned tag;
  u32x4 xq[2];
  u32x2 sq;
  f32x4 ya[4];
  unsigned key[2];
  float xf[16], nf[16], lmb, lsd, lri, la, lb, lqa, lqb;
  unsigned nfp[8];
#define PINV(a) asm volatile("" : "+v"(a))
  auto nf_pre = [&]() __attribute__((always_inline)) {
    if (__builtin_expect(!__all(sq.y == tag), 0)) sq = granule_wait(stat + (xo >> 11), tag);
    lmb = lo_bf(sq.x); lsd = hi_bf(sq.x); lri = __builtin_amdgcn_rcpf(lsd);
    PINV(lmb); PINV(lsd); PINV(lri);
  };
  auto nf_step = [&](int n) __attribute__((always_inline)) {
    const int u = n / 7;
    switch (n % 7) {
      case 0: la = lo_bf(xq[u >> 2][u & 3]); PINV(la); la -= lmb; PINV(la); break;
      case 1: lb = hi_bf(xq[u >> 2][u & 3]); PINV(lb); lb -= lmb; PINV(lb); break;
      case 2: nfp[u] = pk2(la, lb); la = lo_bf(nfp[u]); PINV(la); PINV(nfp[u]); break;
      case 3: lqa = la * lri; PINV(lqa); lb = hi_bf(nfp[u]); PINV(lb); break;
      case 4: la = __builtin_fmaf(-lqa, lsd, la); PINV(la); lqb = lb * lri; PINV(lqb); break;
      case 5: lqa = __builtin_fmaf(la, lri, lqa); PINV(lqa); lb = __builtin_fmaf(-lqb, lsd, lb); PINV(lb); break;
      default: lqb = __builtin_fmaf(lb, lri, lqb); nfp[u] = pk2(lqa, lqb); PINV(nfp[u]); break;
    }
  };
  auto nf_unp = [&](int e) __attribute__((always_inline)) {
    const int u = e >> 1;
    if (e == 0) { PINV(xq[0]); PINV(xq[1]); }
    xf[e] = (e & 1) ? hi_bf(xq[u >> 2][u & 3]) : lo_bf(xq[u >> 2][u & 3]);
    nf[e] = (e & 1) ? hi_bf(nfp[u]) : lo_bf(nfp[u]);
    PINV(xf[e]); PINV(nf[e]);
  };
#define MSG_IDX(m) (kq * 4 + ((kq + 1 + (m)) & 3))
  unsigned seen = 0;

  if (__builtin_expect(wid >= 4, 1)) {
    constexpr int O_ST = (FL_ST - FL_AB) * 4, O_CE = (FL_CE - FL_AB) * 4, O_KEY = (FL_KEY - FL_AB) * 4, O_EP = (FL_EPOCH - FL_AB) * 4;
    const __amdgpu_buffer_rsrc_t rf = mk_rsrc(wsp + WS_FLAGS + FL_AB * 4);
    u32x4 lv[2], lv2[2], lw[2], ld[2], ld2[2];
    unsigned epv;
    {
      const char* xrow = (const char*)x + o_l;
      const char* wdrow = (const char*)wdz + o_d;
      lv[0] = ld16_s<0>(xrow, l16); lv[1] = ld16_s<1024>(xrow, l16);
      lv2[0] = ld16_s<2048>(xrow, l16); lv2[1] = ld16_s<3072>(xrow, l16);
      ld[0] = ld16_s<0>(wdrow, l16); ld[1] = ld16_s<1024>(wdrow, l16);
      ld2[0] = ld16_s<2048>(wdrow, l16); ld2[1] = ld16_s<3072>(wdrow, l16);
      lw[0] = ld16_s<0>(w_r, l16); lw[1] = ld16_s<1024>(w_r, l16);
      asm volatile("buffer_load_dword %0, off, %1, %2 sc1" : "=v"(epv) : "s"(rf), "s"(O_EP) : "memory");
    }
    SB();
    const unsigned long long did = llvm_dispatch_id();
    const unsigned dmix = (unsigned)did * 0x85EBCA6Bu + (unsigned)(did >> 32) * 0xC2B2AE35u + 0x27D4EB2Fu;
    const bool moe = et != 7;
    unsigned epoch;
    const int ci = lane & 15, rq = lane >> 4, tk = (lane >> 3) & 1;
    int mat, cg, dq, d0;
    u32x4 wv[16];
    const u16 *pgu, *pn2;
    const char *pm2, *spb;
#define rw __builtin_amdgcn_make_buffer_rsrc((void*)pgu, 0, moe ? 0x7fffffff : 0, 0x00020000)
#define rn2 mk_rsrc(pn2)
#define rm2 mk_rsrc(pm2)
    int vw, vn2, vm2;
    auto k_blk = [&](int k) __attribute__((always_inline)) { return tt ^ (k_ord(k) >> 1); };
#define KROT(kk_) (k_blk(kk_) * 256 + (k_ord(kk_) & 1) * 128)
    int tv2;
    unsigned sd = 0;
    auto touches = [&](auto P) __attribute__((always_inline)) {
      constexpr int p = decltype(P)::value;
      int e = et;
      asm volatile("" : "+s"(e));
#define TCH(k) if constexpr (tpos[k] == p) { if (e == twho[k]) { const int kb = KROT(k); touch1(rn2, tv2, mb0, kb); } }
      TCH(0) TCH(1) TCH(2) TCH(3) TCH(4) TCH(5) TCH(6) TCH(7) TCH(8) TCH(9) TCH(10) TCH(11) TCH(12) TCH(13) TCH(14) TCH(15)
    };
    unsigned mq0, mq1, mq2, mq3;
    const int cg8 = g & 7, rgb = g >> 3;
    const __amdgpu_buffer_rsrc_t rdn = mk_rsrc(w_down);
    const __amdgpu_buffer_rsrc_t rgu = mk_rsrc(wsp + WS_GU);
    int vdn, vgu;
    u32x4 dv[7], gv[4];
    int abo, abq[3];
    unsigned lma;
    unsigned sel[2][4];
    float g0, g1;
    u32x4 kk[4];
    unsigned k1 = 0, k2 = 0;
    const int ko_ = O_KEY + pw * 4096;
#define MM(a, b, c) __builtin_amdgcn_mfma_f32_16x16x32_bf16(__builtin_bit_cast(bf16x8, a), __builtin_bit_cast(bf16x8, b), c, 0, 0, 0)
#define PIN1(a) asm volatile("" : "+v"(a))
#define PIN2(a, b) asm volatile("" : "+v"(a), "+v"(b))
#define PIN3(a, b, c) asm volatile("" : "+v"(a), "+v"(b), "+v"(c))
    {
      int gro, go;
      u32x2 gr;
      u32x4 oh[5];
      f32x4 sa4, gw[2], gx[2];
      float su, gs, ta, tb, sx, mbv;
      unsigned dk[4], kl;
      const int hb = lane >> 5;
#pragma unroll
      for (int l = 0; l < WSN; ++l) asm volatile("" : "=v"(wv[l]));
      auto diag = [&](const f32x4& m) __attribute__((always_inline)) {
        return __uint_as_float((__float_as_uint(m[0]) & dk[0]) | (__float_as_uint(m[1]) & dk[1]) | (__float_as_uint(m[2]) & dk[2]) | (__float_as_uint(m[3]) & dk[3]));
      };
      auto setup = [&](auto JB, auto GP) __attribute__((always_inline)) {
        constexpr int jb = decltype(JB)::value, gp = decltype(GP)::value;
        SB();
        {
          int ln = lane, jj = j, p_ = pw;
          launder(ln, jj, p_);
          const int c_ = ln & 15, r_ = ln >> 4, e_ = jj & 7, t_ = (xcd & 1) * 4 + (jj >> 3);
          if constexpr (jb == 1 && gp == 0) {
            pn2 = ((kq >> 1) ? ws_im : ws_re) + (long)t_ * 256 * S_ + (kq & 1) * 1024;
            spb = (const char*)(pn2 + (e_ * 4 + p_) * 8 * S_);
            pins(pn2, spb);
          }
          if constexpr (jb == 1 && gp >= 1 && gp < 6) {
            const unsigned o = c_ == gp - 1 ? 0x3f803f80u : 0u;
            oh[gp - 1] = (u32x4){o, o, o, o};
            PIN1(oh[gp - 1]);
          }
          if constexpr (jb == 1 && gp == 6) {
            const int gm = moe ? (jj - (jj >> 3)) * 8 + xcd : 0;
            mat = gm / 112; cg = (gm % 112) >> 2; dq = gm & 3; d0 = dq * 256 + p_ * 64;
            pin4(mat, cg, dq, d0);
          }
          if constexpr (jb == 2 && gp == 0) {
            pgu = mat ? w_up : w_gate;
            pm2 = wsp + WS_ABT + ((long)e_ * 128 * 4096 + kq * 1024) * 2;
            pins(pgu, pm2);
            vw = ((d0 + r_ * 16) * F_ + cg * 128 + c_ * 8) * 2;
            PIN1(vw);
          }
          if constexpr (jb == 2 && (gp == 1 || gp == 2)) {
            dk[gp * 2 - 2] = c_ == r_ * 4 + gp * 2 - 2 ? ~0u : 0u; dk[gp * 2 - 1] = c_ == r_ * 4 + gp * 2 - 1 ? ~0u : 0u;
            PIN2(dk[gp * 2 - 2], dk[gp * 2 - 1]);
          }
          if constexpr (jb == 2 && gp == 3) { kl = lrow + (ln >> 5); go = (ln & 30) ? (int)0x80000000 : ((ln & 1) ? O_KEY : O_ST) + (int)kl * 8; PIN2(kl, go); }
          if constexpr (jb == 2 && gp == 4) { kl = 0xffff - kl; go = (ln & ~1) == 2 ? O_CE + (z * D_ + drow + (ln & 1)) * 8 : go; PIN2(kl, go); }
          if constexpr (jb == 2 && gp == 5) {
            gro = (p_ < 2 ? O_ST + (st * 128 + p_ * 64) * 8 : O_CE + (z * D_ + e_ * 128 + (p_ - 2) * 64) * 8) + ln * 8;
            tv2 = (p_ * 64 + ln) * 4096;
            lma = lds_addr(smem + LDS_MB) + (p_ < 2 ? p_ * 256 : 512 + p_ * 256) + ln * 4;
            PIN3(gro, tv2, lma);
          }
          if constexpr (jb == 7 && gp == 0) {
            const int r0 = p_ * 8 + (ln >> 3);
            vn2 = r0 * (S_ * 2) + ((ln & 7) ^ ((r0 >> 1) & 7)) * 16; vm2 = r0 * 8192 + (vm & 127);
            PIN2(vn2, vm2);
          }
          if constexpr (jb == 7 && gp == 1) { vdn = jj * 112 + p_ * 28 + r_ * 7; vgu = (((ln >> 3) & 1) * 4 * F_ + vdn + min(ln & 7, 6)) * 16; PIN2(vdn, vgu); }
          if constexpr (jb == 7 && gp == 2) { vdn = (vdn * D_ + cg8 * 128 + c_ * 8) * 2; PIN1(vdn); }
          if constexpr (jb == 7 && gp == 3) {
            abo = (((kq * 8 + e_) * 4 + p_) * 16 + c_) * AB_STRIDE * 4;
            abq[2] = ln >= 16 || (ln & 7) == t_ ? (int)0x80000000 : abo;
            PIN2(abo, abq[2]);
          }
          if constexpr (jb == 7 && gp == 4) {
            abq[1] = ln >= 8 ? (int)0x80000000 : abq[2];
            abq[0] = ((ln ^ t_) & 4) ? (int)0x80000000 : abq[1];
            PIN2(abq[0], abq[1]);
          }
          if constexpr (jb == 7 && gp == 5) {
            const unsigned mb = lds_addr(smem) + p_ * 1024;
            mq0 = mb + ((3 - kq) & 3) * 4096; mq1 = mb + ((4 - kq) & 3) * 4096; mq2 = mb + ((5 - kq) & 3) * 4096; mq3 = mb + ((6 - kq) & 3) * 4096;
            pin4(mq0, mq1, mq2, mq3);
          }
          if constexpr (jb == 8 && gp < 4) {
#pragma unroll
            for (int h = 0; h < 2; ++h) {
              const unsigned hv = h ? 0x0302u : 0x0100u;
              sel[h][gp] = ((ln & 7) >> 1) == gp ? ((ln & 1) ? (hv << 16) | 0x0c0cu : 0x0c0c0000u | hv) : 0x0c0c0c0cu;
            }
            PIN2(sel[0][gp], sel[1][gp]);
          }
        }
        SB();
      };
#define WS1(i) setup(IC<1>{}, IC<i>{})
#define WS2(i) setup(IC<2>{}, IC<i>{})
#define WS3(i) do { setup(IC<7>{}, IC<i>{}); if (i >= 4) setup(IC<8>{}, IC<(i) & 3>{}); } while (0)
      G1_STAGE_TILE(1, G1_STAGE, WS1);
      WAIT_VM(19); BAR();
      G1_STAGE_TILE(2, 2 * G1_STAGE, WS2);
      G1_STAGE_TILE(3, 3 * G1_STAGE, WS3);
      auto work1 = [&](auto JB, auto GP) __attribute__((always_inline)) {
        constexpr int jb = decltype(JB)::value, gp = decltype(GP)::value;
        SB();
        if constexpr (jb == 1) {
          const f32x4 z4 = {0.f, 0.f, 0.f, 0.f};
          if constexpr (gp == 0) { sa4 = MM(lv[0], oh[0], z4); gw[0] = MM(lv[0], lw[0], z4); gx[0] = MM(lv[0], lv[0], z4); PIN3(sa4, gw[0], gx[0]); }
          if constexpr (gp == 1) { sa4 = MM(lv2[0], oh[1], sa4); gw[1] = MM(lv2[0], lw[0], z4); gx[1] = MM(lv2[0], lv2[0], z4); PIN3(sa4, gw[1], gx[1]); }
          if constexpr (gp == 2) { sa4 = MM(lv[1], oh[0], sa4); gw[0] = MM(lv[1], lw[1], gw[0]); gx[0] = MM(lv[1], lv[1], gx[0]); PIN3(sa4, gw[0], gx[0]); }
          if constexpr (gp == 3) { sa4 = MM(lv2[1], oh[1], sa4); gw[1] = MM(lv2[1], lw[1], gw[1]); gx[1] = MM(lv2[1], lv2[1], gx[1]); PIN3(sa4, gw[1], gx[1]); }
          if constexpr (gp == 4) { sa4 = MM(ld[0], oh[2], sa4); sa4 = MM(ld[1], oh[2], sa4); PIN1(sa4); }
          if constexpr (gp == 5) { sa4 = MM(ld2[0], oh[3], sa4); sa4 = MM(ld2[1], oh[3], sa4); PIN1(sa4); }
          if constexpr (gp == 6) { sa4 = MM(lw[0], oh[4], sa4); sa4 = MM(lw[1], oh[4], sa4); PIN1(sa4);
            epoch = __builtin_amdgcn_readfirstlane(epv); tag = ((epoch + 1u) * 0x9E3779B1u ^ dmix) | 1u; }
        }
        if constexpr (jb == 2) {
          if constexpr (gp == 0) { su = (sa4[0] + sa4[1]) + (sa4[2] + sa4[3]); ta = diag(gw[0]); PIN2(su, ta); }
          if constexpr (gp == 1) { su = fold32(su, su); tb = diag(gw[1]); PIN2(su, tb); }
          if constexpr (gp == 2) { su = fold16(su, su); gs = fold32(ta, tb); PIN2(su, gs); }
          if constexpr (gp == 3) { ta = diag(gx[0]); tb = diag(gx[1]); PIN2(ta, tb); }
          if constexpr (gp == 4) { gs = fold16(gs, fold32(ta, tb)); PIN1(gs); }
          if constexpr (gp == 5) { gs += dpp_f<0xB1>(gs); gs += dpp_f<0x4E>(gs); PIN1(gs); }
          if constexpr (gp == 6) { gs += dpp_f<0x141>(gs); gs += dpp_f<0x140>(gs); PIN1(gs); }
        }
        if constexpr (jb == 3) {
          if constexpr (gp == 0) {
            const float s0 = rdl(su, 0), s1 = rdl(su, 1), q0 = rdl(gs, 16), q1 = rdl(gs, 48);
            sx = hb ? s1 : s0; ta = hb ? q1 : q0; PIN2(sx, ta);
          }
          if constexpr (gp == 1) {
            const float mean = sx * (1.0f / D_);
            mbv = rbf(mean);
            ta -= sx * mean;
            if (__builtin_expect(__any(!(sx * mean <= 16.f * ta)), 0)) {
              float q = fold32(sqdev16(lv, rdl(mbv, 0)), sqdev16(lv2, rdl(mbv, 32)));
              q = row_sum(fold16(q, q));
              const float dm = mean - mbv;
              ta = fmaxf(q - (float)D_ * dm * dm, 0.f);
            }
            PIN2(mbv, ta);
          }
          if constexpr (gp == 2) { ta = rbf(rbf(ta * (1.0f / D_)) + 1e-5f); gs -= mbv * rdl(su, 4); PIN2(ta, gs); }
          if constexpr (gp == 3) { ta = rbf(__builtin_amdgcn_sqrtf(ta)); tb = __builtin_amdgcn_rcpf(ta); PIN2(ta, tb); }
          if constexpr (gp == 4) {
            const unsigned rb = __float_as_uint(rbf(gs * tb));
            kl |= (rb ^ ((unsigned)((int)rb >> 31) | 0x80000000u)) & 0xffff0000u; PIN1(kl);
          }
          if constexpr (gp == 5) { kl = (lane & 2) ? __float_as_uint(su) : (lane & 1) ? kl : (__float_as_uint(mbv) >> 16) | __float_as_uint(ta); PIN1(kl); }
        }
        if constexpr (jb == ST_JB + 3) {
          if constexpr (gp == 0) { if (!__all(gr.y == tag)) gr = granule_wait((const u64*)((const char*)(flags + FL_AB) + gro), tag); PIN1(gr); }
          if constexpr (gp == 1) { g1 = (z ? -1.0f : 1.0f) / hi_bf(gr.x); PIN1(g1); }
          if constexpr (gp == 2) { g0 = pw < 2 ? -(lo_bf(gr.x) * g1) : __uint_as_float(gr.x); PIN1(g0); }
        }
        if constexpr (gp < 7 && (jb - 1) * 7 + gp < 64) {
          constexpr int n = (jb - 1) * 7 + gp;
          sld<((n >> 1) & 7) * 4096 + (n & 1) * 64>(sd, spb, KROT(n >> 4));
        }
        SB();
      };
      auto step1 = [&](auto JB) __attribute__((always_inline)) {
        constexpr int jb = decltype(JB)::value;
        if constexpr (jb == 1) PAGE(1);
        if constexpr (jb == 7) PAGE(2);
        constexpr int n16 = jb == 12 ? 12 : jb == 11 ? 14 : 16;
        constexpr auto wl = [](int q) { return (q == 3 ? 1 : 0) + (q == ST_JB ? 1 : 0); };
        if constexpr (jb == 1) {
          asm volatile("s_waitcnt vmcnt(%11)" : "+v"(lv[0]), "+v"(lv[1]), "+v"(lw[0]), "+v"(lw[1]), "+v"(ld[0]), "+v"(ld[1]),
                       "+v"(lv2[0]), "+v"(lv2[1]), "+v"(ld2[0]), "+v"(ld2[1]), "+v"(epv) : "i"(16) : "memory");
        } else if constexpr (jb == ST_JB + 3) {
          asm volatile("s_waitcnt vmcnt(%1)" : "+v"(gr) : "i"(n16 + wl(jb - 2) + wl(jb - 1)) : "memory");
        } else wait_vm<n16 + wl(jb - 3) + wl(jb - 2) + wl(jb - 1)>();
        BAR();
        if constexpr (jb == 2) { if (pw == 0 && lane < 16) ((int*)(smem + LDS_SEL))[lane < 8 ? lane : lane + 8] = 0; }
        if constexpr (jb == 4) { if (pw == 0 && lane == 0) ((unsigned*)(smem + LDS_SEL))[5] = tag; }
#define W1(i) work1(JB, IC<i>{})
        if constexpr (jb < 10) G1_STAGE_TILE(jb - 1 + G1_NST, ((jb - 1) % G1_NST) * G1_STAGE, W1);
        else G1_STAGE_A(jb - 1 + G1_NST, ((jb - 1) % G1_NST) * G1_STAGE, W1);
        if constexpr (jb == 12) { G1_STAGE_B(13, G1_STAGE); G1_STAGE_B(14, 2 * G1_STAGE); }
        if constexpr (jb == 3) __builtin_amdgcn_raw_buffer_store_b64((u32x2){kl, tag}, rf, go, 0, 16);
        if constexpr (jb == ST_JB) {
          asm volatile("buffer_load_dwordx2 %0, %1, %2, 0 offen sc1" : "=v"(gr) : "v"(gro), "s"(rf) : "memory");
        }
        touches(IC<100 + jb>{});
        if constexpr (jb <= 8) {
          if (et == jb - 1) {
#pragma unroll
            for (int l = 0; l < WSN; ++l) bld16_nt_io(wv[l], rw, vw, l * F_ * 2);
          }
        }
        SB();
      };
      static_assert(ST_JB + 3 <= 12 && ST_JB >= 6, "schedule");
      step1(IC<1>{}); step1(IC<2>{}); step1(IC<3>{}); step1(IC<4>{}); step1(IC<5>{}); step1(IC<6>{}); step1(IC<7>{}); step1(IC<8>{}); step1(IC<9>{}); step1(IC<10>{}); step1(IC<11>{}); step1(IC<12>{});
    }
#define G2_STAGE_N(kk_, W) do { const int kb = KROT(kk_); constexpr int so = n_off(kk_); SB(); \
    DMA_(rn2, vn2, mq0, so, kb, 0, ""); W(0); DMA_(rn2, vn2, mq1, so, kb, 32 * S_ * 2, ""); W(1); \
    DMA_(rn2, vn2, mq2, so, kb, 64 * S_ * 2, ""); W(2); DMA_(rn2, vn2, mq3, so, kb, 96 * S_ * 2, ""); W(3); \
    DMA_(rn2, vn2, mq0, so + 16384, kb, 128 * S_ * 2, ""); W(4); DMA_(rn2, vn2, mq1, so + 16384, kb, 160 * S_ * 2, ""); W(5); \
    DMA_(rn2, vn2, mq2, so + 16384, kb, 192 * S_ * 2, ""); W(6); DMA_(rn2, vn2, mq3, so + 16384, kb, 224 * S_ * 2, ""); W(7); SB(); } while (0)
#define G2_STAGE_NH(kk_, h) do { const int kb = KROT(kk_); constexpr int so = n_off(kk_) + (h) * 16384; SB(); \
    DMA_(rn2, vn2, mq0, so, kb, (h) * 128 * S_ * 2, ""); DMA_(rn2, vn2, mq1, so, kb, ((h) * 128 + 32) * S_ * 2, ""); \
    DMA_(rn2, vn2, mq2, so, kb, ((h) * 128 + 64) * S_ * 2, ""); DMA_(rn2, vn2, mq3, so, kb, ((h) * 128 + 96) * S_ * 2, ""); SB(); } while (0)
#define G2_STAGE_M(kk_, W) do { const int kb = KROT(kk_); constexpr int so = G2_MRING + m_off(kk_); SB(); \
    DMA_(rm2, vm2, mb0, so, kb, 0, " sc1"); W(8); DMA_(rm2, vm2, mb0, so + 4096, kb, 32 * 8192, " sc1"); W(9); \
    DMA_(rm2, vm2, mb0, so + 8192, kb, 64 * 8192, " sc1"); W(10); DMA_(rm2, vm2, mb0, so + 12288, kb, 96 * 8192, " sc1"); W(11); SB(); } while (0)
    WAIT_VM(16); BAR();
    asm volatile("ds_write_b32 %0, %1" :: "v"(lma), "v"(g0) : "memory");
    if (pw < 2) asm volatile("ds_write_b32 %0, %1 offset:512" :: "v"(lma), "v"(g1) : "memory");
    u32x4 tv[4];
    unsigned ready = 0;
    auto need = [&](int k) __attribute__((always_inline)) {
      const int b = k_blk(k) + (k_ord(k) & 1) * 8;
#pragma unroll 1
      for (int it = 0; !((ready >> b) & 1) && it < SPIN_MAX; ++it) {
        ready |= (unsigned)__ballot(__builtin_amdgcn_raw_buffer_load_b32(rf, abq[k < 5 ? 0 : k < 9 ? 1 : 2], 0, 16) == tag);
        asm volatile("" ::: "memory");
      }
    };
    unsigned ksel, bad = 0;
    int isel;
    unsigned xw;
    auto bvec = [&](unsigned src, const unsigned (&sl)[4]) __attribute__((always_inline)) {
      return (u32x4){__builtin_amdgcn_perm(0, src, sl[0]), __builtin_amdgcn_perm(0, src, sl[1]),
                     __builtin_amdgcn_perm(0, src, sl[2]), __builtin_amdgcn_perm(0, src, sl[3])};
    };
#define BC8(v, p) (unsigned)__builtin_amdgcn_update_dpp(__builtin_amdgcn_update_dpp(0, (int)(v), 0x150 + (p), 0xf, 0x3, false), (int)(v), 0x158 + (p), 0xf, 0xc, false)
    float* lg = (float*)(smem + LDS_GU);
    f32x4 macc = {0.f, 0.f, 0.f, 0.f};
    u32x2 sv;
    unsigned hp, gbad;
    float s0, s1;
    auto work2 = [&](auto JB, auto GP) __attribute__((always_inline)) {
      constexpr int jb = decltype(JB)::value, gp = decltype(GP)::value;
      SB();
      if constexpr (jb == -1 && gp < 4) {
        constexpr int c = gp == 0 ? 0xB1 : gp == 1 ? 0x4E : gp == 2 ? 0x141 : 0x140;
        merge2(k1, k2, dpp_u<c>(k1), dpp_u<c>(k2));
        PIN2(k1, k2);
      }
      if constexpr (jb == -1 && gp == 4) {
        ksel = tk ? k2 : k1;
        isel = (0xffff - (int)(ksel & 0xffffu)) & (S_ - 1);
        k1 = (isel * D_ + d0 + rq * 16 + (lane & 7) * 2) * 2; k2 = O_ST + isel * 8;
        PIN1(ksel);
        xw = gld4_s(x, k1);
        sv = bld8_sc1(rf, k2);
      }
      if constexpr (jb == 0 && gp == 1) wait_xs<3 + 1 + 2>(xw, sv);
      if constexpr (jb == 0) {
        if constexpr (gp == 1) { if (!__all(sv.y == tag)) sv = granule_wait(stat + isel, tag); s0 = __builtin_amdgcn_rcpf(hi_bf(sv.x)); PIN1(s0); }
        if constexpr (gp == 2) { s1 = ln_elem(lo_bf(xw), lo_bf(sv.x), hi_bf(sv.x), s0); PIN1(s1); }
        if constexpr (gp == 3) { xw = pk2(s1, ln_elem(hi_bf(xw), lo_bf(sv.x), hi_bf(sv.x), s0)); PIN1(xw); }
      }
      if constexpr (jb == DG && gp < 7) { PIN1(dv[gp]); macc = MM(dv[gp], bvec(BC8(hp, gp), sel[0]), macc); PIN1(macc); }
      if constexpr ((jb == 0 && gp >= 4) || jb == 1) {
        constexpr int l = jb == 0 ? gp - 4 : gp + 4;
        if constexpr (!(l & 1)) hp = BC8(xw, l >> 1);
        PIN1(wv[l]);
        macc = MM(wv[l], bvec(hp, sel[l & 1]), macc);
        PIN2(macc, hp);
      }
      if constexpr (jb == GV_JB + 2) {
        if constexpr (gp == 0) { gbad = (gv[0].y ^ tag) | (gv[0].w ^ tag) | (gv[1].y ^ tag) | (gv[1].w ^ tag); PIN1(gbad); }
        if constexpr (gp == 1) { gbad |= (gv[2].y ^ tag) | (gv[2].w ^ tag) | (gv[3].y ^ tag) | (gv[3].w ^ tag); PIN1(gbad); }
        if constexpr (gp == 2) {
          s0 = ((__uint_as_float(gv[0].x) + __uint_as_float(gv[1].x)) + __uint_as_float(gv[2].x)) + __uint_as_float(gv[3].x);
          s1 = ((__uint_as_float(gv[0].z) + __uint_as_float(gv[1].z)) + __uint_as_float(gv[2].z)) + __uint_as_float(gv[3].z);
          PIN2(s0, s1);
        }
        if constexpr (gp == 3) {
#pragma unroll 1
          for (int it = 0; !__all(gbad == 0) && it < SPIN_MAX; ++it) {
            u32x4 q[4];
            gbad = 0;
#pragma unroll
            for (int i = 0; i < 4; ++i) { q[i] = __builtin_amdgcn_raw_buffer_load_b128(rgu, vgu, i * F_ * 16, 16); gbad |= (q[i].y ^ tag) | (q[i].w ^ tag); }
            s0 = ((__uint_as_float(q[0].x) + __uint_as_float(q[1].x)) + __uint_as_float(q[2].x)) + __uint_as_float(q[3].x);
            s1 = ((__uint_as_float(q[0].z) + __uint_as_float(q[1].z)) + __uint_as_float(q[2].z)) + __uint_as_float(q[3].z);
            asm volatile("" ::: "memory");
          }
          const unsigned gp2 = pk2(s0, s1);
          const unsigned up2 = dpp_u<0x128>(gp2);
          s0 = tk ? hi_bf(up2) : lo_bf(gp2); s1 = tk ? hi_bf(gp2) : lo_bf(up2);
          PIN2(s0, s1);
        }
        if constexpr (gp == 4) { s0 = rbf(s0 * __builtin_amdgcn_rcpf(1.f + __expf(-s0))); PIN1(s0); }
        if constexpr (gp == 5) { hp = pk2(s0 * s1, 0.f); PIN1(hp); }
      }
      if constexpr (jb == 10 && gp == 2) epi_setup();
      if constexpr (jb == 10 && gp == 4) code_touch(sd, (j * 4 + pw) * 256);
      SB();
    };
#define W2M(i) work2(IC<-1>{}, IC<i>{})
#define W20(i) work2(IC<0>{}, IC<i>{})
#define W21(i) work2(IC<1>{}, IC<i>{})
    unsigned fs;
    {
      G1_STAGE_B(15, 3 * G1_STAGE);
      G2_STAGE_NH(0, 0);
      lgkm0(sd);
      WAIT_VM(16); BAR();
      G2_STAGE_NH(0, 1);
      WAIT_VM(10); BAR();
#pragma unroll
      for (int i = 0; i < 4; ++i) kk[i] = bld16_sc1(rf, l16, ko_ + i * 1024);
#pragma unroll
      for (int l = WSN; l < 16; ++l) wv[l] = bld16_nt(rw, vw, l * F_ * 2);
      asm volatile("s_waitcnt vmcnt(%4)" : "+v"(kk[0]), "+v"(kk[1]), "+v"(kk[2]), "+v"(kk[3]) : "i"(16 - WSN) : "memory");
      touches(IC<115>{});
#pragma unroll
      for (int i = 0; i < 4; ++i) {
        k2 = max(k2, min(k1, kk[i].x)); k1 = max(k1, kk[i].x);
        k2 = max(k2, min(k1, kk[i].z)); k1 = max(k1, kk[i].z);
        bad |= (kk[i].y ^ tag) | (kk[i].w ^ tag);
      }
#pragma unroll 1
      for (int it = 0; !__all(bad == 0) && it < SPIN_MAX; ++it) {
        k1 = 0; k2 = 0; bad = 0;
#pragma unroll
        for (int i = 0; i < 4; ++i) {
          const u32x4 q = __builtin_amdgcn_raw_buffer_load_b128(rf, l16, ko_ + i * 1024, 16);
          k2 = max(k2, min(k1, q.x)); k1 = max(k1, q.x);
          k2 = max(k2, min(k1, q.z)); k1 = max(k1, q.z);
          bad |= (q.y ^ tag) | (q.w ^ tag);
        }
        asm volatile("" ::: "memory");
      }
      merge2(k1, k2, dpp_u<0xB1>(k1), dpp_u<0xB1>(k2));
      merge2(k1, k2, dpp_u<0x4E>(k1), dpp_u<0x4E>(k2));
      merge2(k1, k2, dpp_u<0x141>(k1), dpp_u<0x141>(k2));
      merge2(k1, k2, dpp_u<0x140>(k1), dpp_u<0x140>(k2));
      if ((lane & 15) == 0) *(u32x2*)(smem + LDS_KEY + (pw * 4 + rq) * 8) = (u32x2){k1, k2};
      PAGE(3);
      BAR_LGKM();
#pragma unroll
      for (int jj = 0; jj < 4; ++jj) tv[jj] = *(const u32x4*)(smem + G2_MRING + m_off(0) + (jj * 4 + pw) * 1024 + lane * 16);
      { const u32x2 kp = *(const u32x2*)(smem + LDS_KEY + ci * 8); k1 = kp.x; k2 = kp.y; }
#pragma unroll
      for (int jj = 0; jj < 4; ++jj) __builtin_amdgcn_raw_buffer_store_b128(tv[jj], rm2, vm2 + jj * 32 * 8192 + 2 * tt * 128, 0, 16);
      G2_STAGE_N(1, W2M);
      WAIT_VM(10);
      if (lane == tt) __builtin_amdgcn_raw_buffer_store_b32(tag, rf, abo, 0, 16);
      G2_STAGE_N(2, W20);
      __builtin_amdgcn_s_sleep(2);
      WAIT_VM(9);
      fs = bld4_sc1(rf, abq[0]);
    }
    BAR();
#pragma unroll
    for (int jj = 0; jj < 4; ++jj) tv[jj] = *(const u32x4*)(smem + G2_MRING + m_off(1) + (jj * 4 + pw) * 1024 + lane * 16);
    G2_STAGE_N(3, NOW);
    asm volatile("s_waitcnt vmcnt(8)" : "+v"(fs) :: "memory");
    ready = (unsigned)__ballot(fs == tag);
    need(2);
    G2_STAGE_M(2, NOW);
    need(3);
    G2_STAGE_M(3, NOW);
    fs = bld4_sc1(rf, abq[1]);
    touches(IC<1>{});
    W21(0); W21(1); W21(2); W21(3); W21(4); W21(5); W21(6); W21(7); W21(8); W21(9); W21(10); W21(11);
    asm volatile("" : "+v"(tv[0]), "+v"(tv[1]), "+v"(tv[2]), "+v"(tv[3]));
    auto step2 = [&](auto JB) __attribute__((always_inline)) {
      constexpr int jb = decltype(JB)::value;
      if constexpr (jb == 3) PAGE(4);
      if constexpr (jb == 9) PAGE(5);
      constexpr auto must = [](int q) { return (q == 2 ? 4 : 0) + ((FSM >> q) & 1) + ((q == GV_JB || q == GV_JB - 1) ? 2 : 0); };
      if constexpr (jb == 2) wait_vm<4 + must(1)>();
      else wait_vm<dn_cnt(jb - 2) + (jb == 5 ? 1 : 0) + 12 + must(jb - 1) + dn_cnt(jb - 1)>();
      if constexpr (jb == 3) { if (pw == 0 && (lane & ~8) == 0) ((unsigned*)(smem + LDS_SEL))[tk] = ksel; }
      if constexpr ((jb == 2 || jb == DG + 1)) {
        *(f32x4*)(lg + pw * 256 + lane * 4) = macc;
        macc = (f32x4){0.f, 0.f, 0.f, 0.f};
        lgkm0(sd);
      }
      BAR();
      float p0[4], p1[4];
      const int col = (pw & 1) * 64 + lane;
      if constexpr (jb == 3) {
        if (g == 0 && pw == 0 && lane == 0) __hip_atomic_store((unsigned*)flags + FL_EPOCH, epoch + 1u, __ATOMIC_RELAXED, __HIP_MEMORY_SCOPE_AGENT);
      }
      if constexpr (jb == DG + 1) {
        const float* p = lg + (pw & 1) * 32 + (lane >> 4) * 64 + ((2 * lane) & 7) * 4 + ((lane >> 2) & 3);
#pragma unroll
        for (int i = 0; i < 4; ++i) { p0[i] = p[i * 256]; p1[i] = p[i * 256 + 4]; }
      }
      if constexpr (jb == 2) {
        const float* p = lg + (col >> 5) * 64 + (col & 7) * 4 + ((col >> 3) & 3);
#pragma unroll
        for (int i = 0; i < 4; ++i) { p0[i] = p[i * 256]; p1[i] = p[i * 256 + 32]; }
      }
      if constexpr (jb == 4) {
        if (lane == 8 + tt) __builtin_amdgcn_raw_buffer_store_b32(tag, rf, abo, 0, 16);
      }
      if constexpr ((FSM >> (jb - 2)) & 1) {
        asm volatile("" : "+v"(fs));
        ready |= (unsigned)__ballot(fs == tag);
      }
      if constexpr (jb == GV_JB + 2) asm volatile("" : "+v"(gv[0]), "+v"(gv[1]), "+v"(gv[2]), "+v"(gv[3]));
#define W2(i) work2(JB, IC<i>{})
      G2_STAGE_N(jb + 2, W2);
      need(jb + 2);
      G2_STAGE_M(jb + 2, W2);
      if constexpr (jb == 2) {
#pragma unroll
        for (int jj = 0; jj < 4; ++jj) __builtin_amdgcn_raw_buffer_store_b128(tv[jj], rm2, vm2 + jj * 32 * 8192 + (2 * tt + 1) * 128, 0, 16);
      }
      if constexpr ((FSM >> jb) & 1) fs = bld4_sc1(rf, abq[jb < 5 ? 1 : 2]);
      if constexpr ((jb == GV_JB || jb == GV_JB - 1)) {
#pragma unroll
        for (int i = (jb == GV_JB ? 2 : 0); i < (jb == GV_JB ? 4 : 2); ++i) gv[i] = bld16_sc1(rgu, vgu, i * F_ * 16);
      }
      touches(JB);
#pragma unroll
      for (int i = dn_first(jb); i < dn_first(jb) + dn_cnt(jb); ++i) dv[i] = bld16_nt(rdn, vdn, i * D_ * 2);
      if constexpr (jb == DG + 1) {
        if (pw < 2)
          __builtin_amdgcn_raw_buffer_store_b128((u32x4){__float_as_uint((p0[0] + p0[1]) + (p0[2] + p0[3])), tag, __float_as_uint((p1[0] + p1[1]) + (p1[2] + p1[3])), tag},
                                                 mk_rsrc(wsp + WS_DN), (((pw * 8 + cg8) * 32 + rgb) * 64 + lane) * 16, 0, 16);
      }
      if constexpr (jb == 2) {
        if (moe && pw < 2)
          __builtin_amdgcn_raw_buffer_store_b128((u32x4){__float_as_uint((p0[0] + p0[1]) + (p0[2] + p0[3])), tag, __float_as_uint((p1[0] + p1[1]) + (p1[2] + p1[3])), tag},
                                                 rgu, ((mat * 4 + dq) * F_ + cg * 128 + col) * 16, 0, 16);
      }
      SB();
    };
    static_assert(DG + 1 <= 12 && DG >= GV_JB + 3 && dn_first(DG - 2) == 7, "schedule");
    step2(IC<2>{}); step2(IC<3>{}); step2(IC<4>{}); step2(IC<5>{}); step2(IC<6>{}); step2(IC<7>{}); step2(IC<8>{}); step2(IC<9>{}); step2(IC<10>{}); step2(IC<11>{}); step2(IC<12>{}); step2(IC<13>{});
    lgkm0(sd);
    PAGE(7);
    WAIT_VM(12); BAR();
    xq[0] = ld16_s<0>(x, xo); xq[1] = ld16_s<64>(x, xo); sq = ld8_s_sc1(stat, xo >> 11 << 3);
    WAIT_VM(3); BAR();
    asm volatile("s_waitcnt vmcnt(0)" : "+v"(xq[0]), "+v"(xq[1]), "+v"(sq) :: "memory");
    __builtin_amdgcn_s_setprio(0);
    key[0] = __builtin_amdgcn_readlane(ksel, 0); key[1] = __builtin_amdgcn_readlane(ksel, 8);
    nf_pre();
#pragma unroll
    for (int n = 0; n < 56; ++n) nf_step(n);
#pragma unroll
    for (int e = 0; e < 16; ++e) nf_unp(e);
#pragma unroll 1
    for (int c = 0; c < 2; ++c) {
      const unsigned kc = __builtin_amdgcn_readlane(ksel, c * 8);
      const int ic = (0xffff - (int)(kc & 0xffffu)) & (S_ - 1);
      if ((ic >> 8) != tt || ((ic >> 5) & 3) != kq) continue;
      const __amdgpu_buffer_rsrc_t rd = mk_rsrc(wsp + WS_DN + (size_t)(c * 8 + et) * 32768);
#pragma unroll 1
      for (int it = 0; it < SPIN_MAX; ++it) {
        if (__all(__builtin_amdgcn_raw_buffer_load_b32(rd, (lane & 31) * 1024 + 1020, 0, 16) == tag)) break;
        __builtin_amdgcn_s_sleep(2);
        asm volatile("" ::: "memory");
      }
      float s0, s1;
      unsigned bad = 1;
#pragma unroll 1
      for (int it = 0; !__all(bad == 0) && it < SPIN_MAX; ++it) {
        bad = 0; s0 = 0.f; s1 = 0.f;
#pragma unroll
        for (int i = 0; i < 8; ++i) {
          const u32x4 q = __builtin_amdgcn_raw_buffer_load_b128(rd, rq * 1024 + pw * 256 + ci * 16, i * 4096, 16);
          bad |= (q.y ^ tag) | (q.w ^ tag); s0 += __uint_as_float(q.x); s1 += __uint_as_float(q.z);
        }
        asm volatile("" ::: "memory");
      }
      s0 = fold32(s0, s1);
      s0 = fold16(s0, s0);
      const float sg = rbf(1.f / (1.f + __expf(-key_score(kc))));
      ((float*)(smem + LDS_FIN))[c * 128 + pw * 32 + ci * 2 + (lane >> 5)] = rbf(rbf(s0) * sg);
      asm volatile("s_waitcnt lgkmcnt(0)" ::: "memory");
      if (lane == 0) asm volatile("ds_add_u32 %0, %1" :: "v"(lds_addr(smem + LDS_SEL + 24 + c * 4)), "v"(1) : "memory");
    }
  } else {
  {
    const int q = j * 4 + wid;
    constexpr int Q1 = WS_MB, Q2 = Q1 + 8, Q3 = Q2 + 8, Q4 = Q3 + 7, Q5 = Q4 + 7, Q6 = Q5 + 7, Q7 = Q6 + 4, Q8 = Q7 + 4, QC = Q8 + 1;
    static_assert(QC <= 128, "warm-up slots");
    unsigned dmy = 0;
    if (q < QC) {
      const char* base; int b0;
      if (q < Q1) { base = wsp; b0 = 0; }
      else if (q < Q2) { base = (const char*)ws_re; b0 = Q1; }
      else if (q < Q3) { base = (const char*)ws_im; b0 = Q2; }
      else if (q < Q4) { base = (const char*)w_gate; b0 = Q3; }
      else if (q < Q5) { base = (const char*)w_up; b0 = Q4; }
      else if (q < Q6) { base = (const char*)w_down; b0 = Q5; }
      else if (q < Q7) { base = (const char*)out; b0 = Q6; }
      else if (q < Q8) { base = (const char*)x; b0 = Q7; }
      else { base = (const char*)w_r; b0 = Q8; }
      base += (size_t)(q - b0) << 20;
      asm volatile("s_load_dword %0, %1, 0" : "+s"(dmy) : "s"(base) : "memory");
    }
    {
      const int w = g * 4 + wid;
      constexpr int R = (int)(WS_FLAGS - WS_DN);
      static_assert(FL_X == 0 && FL_ST == 6144 * 32 && FL_EPOCH == 6528 * 32, "");
      const int o = lane < 6 ? R + (lane * 1024 + w) * 128 : lane > 6 || w >= 896 ? (int)0x80000000 : w < 512 ? w * 1024 + 896 : R + (6144 - 512 + w) * 128;
      asm volatile("s_mov_b32 m0, %0\n\ts_nop 0\n\tbuffer_load_dword %1, %2, 0 offen sc1 lds"
                   :: "s"(lds_addr(smem + LDS_DUMMY) + wid * 1024 + 512), "v"(o), "s"(mk_rsrc(wsp + WS_DN)) : "memory");
    }
    if (wid == 0) { CALL_PG(1); CALL_PG(8); CALL_PG(5); }
    else if (wid == 1) { CALL_PG(2); CALL_PG(9); CALL_PG(6); }
    else if (wid == 2) { CALL_PG(3); CALL_PG(10); CALL_PG(7); }
    else { CALL_PG(4); CALL_PG(11); }
    asm volatile("s_barrier\n\ts_waitcnt lgkmcnt(0)" : "+s"(dmy) :: "memory");
  }
  PAGE(8);
  epi_setup();
  const int wr = wid >> 1, wc = wid & 1;
  const int sw = (fr >> 1) & 7;
  const int ko0 = ((0 + fq) ^ sw) * 16, ko1 = ((4 + fq) ^ sw) * 16;
  const int mrow = (fr >> 2) * 8 + (fr & 3);
  bf16x8 fm[2][4], fn[2][4];
  {
    const char* pm = smem + (wr * 64 + mrow) * 128;
    const char* pn = smem + 16384 + (wc * 64 + mrow) * 128;
    f32x4 acc[4][4];
#pragma unroll
    for (int i = 0; i < 4; ++i)
#pragma unroll
      for (int jn = 0; jn < 4; ++jn)
#pragma unroll
        for (int q = 0; q < 4; ++q) acc[i][jn][q] = 0.f;
#define TO(i) (((i) >> 1) * 4096 + ((i) & 1) * 512)
#define LDSP(T, a) ((__attribute__((address_space(3))) T*)(size_t)(a))
#define SLOTS(n) _Pragma("unroll") for (int q = 0; q < (n); ++q)
#define MFA(c, a, b) asm volatile("v_mfma_f32_16x16x32_bf16 %0, %1, %2, %0" : "+v"(c) : "v"(a), "v"(b))
#define MFA2(c, a, b, a2, b2) asm volatile("v_mfma_f32_16x16x32_bf16 %0, %1, %2, %0\n\tv_mfma_f32_16x16x32_bf16 %0, %3, %4, %0" : "+v"(c) : "v"(a), "v"(b), "v"(a2), "v"(b2))
    unsigned lm[2][2], ln[2][2];
#pragma unroll
    for (int u = 0; u < 2; ++u)
#pragma unroll
      for (int kk = 0; kk < 2; ++kk) {
        lm[u][kk] = lds_addr(pm) + u * 2 * G1_STAGE + (kk ? ko1 : ko0);
        ln[u][kk] = lds_addr(pn) + u * 2 * G1_STAGE + (kk ? ko1 : ko0);
        asm volatile("" : "+v"(lm[u][kk]), "+v"(ln[u][kk]));
      }
    auto ldm = [&](int stg, int i, int kk) __attribute__((always_inline)) { fm[kk][i] = *LDSP(const bf16x8, lm[stg >> 1][kk] + (stg & 1) * G1_STAGE + TO(i)); };
    auto ldn = [&](int stg, int jn, int kk) __attribute__((always_inline)) { fn[kk][jn] = *LDSP(const bf16x8, ln[stg >> 1][kk] + (stg & 1) * G1_STAGE + TO(jn)); };
    auto quad = [&](int a, int c, int d, int s) __attribute__((always_inline)) {
      const int i = (s < 2 || s >= 6) ? a : a + 1, jn = s < 4 ? c : d, kk = (s == 1 || s == 2 || s == 5 || s == 6);
      MFA(acc[i][jn], fm[kk][i], fn[kk][jn]);
    };
    auto quad2 = [&](int a, int c, int d, int s) __attribute__((always_inline)) {
      const int i = (s < 2 || s >= 6) ? a : a + 1, jn = s < 4 ? c : d, kk = (s == 2 || s == 6);
      MFA2(acc[i][jn], fm[kk][i], fn[kk][jn], fm[1 - kk][i], fn[1 - kk][jn]);
    };
    auto half1 = [&](int stg) __attribute__((always_inline)) {
      const int ja = (stg & 1) * 2, jb = 2 - ja;
      SLOTS(8) {
        quad(0, ja, ja + 1, q);
        if (q < 4) ldn(stg, jb + (q >> 1), q & 1); else ldm(stg, 2 + ((q - 4) >> 1), q == 5 || q == 6);
        SB();
      }
      SLOTS(4) { quad2(0, jb, jb + 1, 2 * q); SB(); }
    };
    auto half2 = [&](int stg) __attribute__((always_inline)) {
      const int ja = (stg & 1) * 2, jb = 2 - ja, nx = (stg + 1) & 3;
      SLOTS(8) {
        quad(2, jb + 1, jb, q);
        if (!(q & 1)) ldm(nx, q >> 2, q == 2 || q == 4);
        SB();
      }
      SLOTS(8) {
        quad(2, ja, ja + 1, q);
        if (!(q & 1)) ldn(nx, jb + (q >> 2), (q >> 1) & 1);
        SB();
      }
    };
    SLOTS(16) asm volatile("" : "+v"(acc[q >> 2][q & 3]));
    SB();
    SLOTS(4) { ldm(0, q >> 1, q & 1); ldn(0, q >> 1, q & 1); }
    SB();
#pragma unroll 1
    for (int k4 = 0; k4 < G1_NKT - 4; k4 += 4) {
      half1(0); BAR_LGKM();
      SB(); half2(0);
      half1(1); BAR_LGKM();
      SB(); half2(1);
      half1(2); BAR_LGKM();
      SB(); half2(2);
      half1(3); BAR_LGKM();
      SB(); half2(3);
    }
    bf16x8 gm[2][4], gn[2][8];
    unsigned am[2][2], an[2][2];
#pragma unroll
    for (int u = 0; u < 2; ++u)
#pragma unroll
      for (int kk = 0; kk < 2; ++kk) {
        am[u][kk] = lds_addr(pm) + (u ? 3 : 1) * G1_STAGE + (kk ? ko1 : ko0);
        an[u][kk] = lds_addr(pn) + (u ? 3 : 1) * G1_STAGE + (kk ? ko1 : ko0);
        asm volatile("" : "+v"(am[u][kk]), "+v"(an[u][kk]));
      }
    unsigned al = lds_addr(smem + LDS_MB) + (wr * 32 + fq * 8) * 4, ac = lds_addr(smem + LDS_MB) + (256 + wc * 64 + mrow) * 4;
    unsigned ae = lds_addr(smem + G2_MRING) + (wc * 64 + mrow) * 128 + (((wr * 4 + fq) ^ sw) * 16);
    asm volatile("" : "+v"(al), "+v"(ac), "+v"(ae));
    auto rdg = [&](int set, int t, int h, int r) __attribute__((always_inline)) {
      const int u = t == 15, o = t == 14 ? G1_STAGE : 0;
      if (r == 0 || r == 2 || r == 4 || r == 5) {
        const int i = r >= 4, kk = r == 2 || r == 4;
        gm[set][kk * 2 + i] = *LDSP(const bf16x8, am[u][kk] + o + h * 4096 + i * 512);
      } else {
        const int jn = r < 4 ? 0 : (r - 4) >> 1, kk = r < 4 ? r >> 1 : r & 1;
        gn[set][kk * 4 + jn] = *LDSP(const bf16x8, an[u][kk] + o + TO(jn));
      }
    };
    auto mmg = [&](int set, int h, int q) __attribute__((always_inline)) {
      const int jn = q >> 2, r = q & 3, i = (r >> 1) ^ (jn & 1), kk = r == 1 || r == 2;
      MFA(acc[h * 2 + i][jn], gm[set][kk * 2 + i], gn[set][kk * 4 + jn]);
    };
    f32x4 rsv[2], mrv[2];
    float cev[4], tc[32];
    auto rds = [&](int h, int r) __attribute__((always_inline)) {
      if (r < 2) mrv[r] = *LDSP(const f32x4, al + h * 256 + r * 16); else rsv[r - 2] = *LDSP(const f32x4, al + 512 + h * 256 + (r - 2) * 16);
    };
    auto prep = [&](int n) __attribute__((always_inline)) {
      tc[n] = llvm_fmul_legacy(mrv[(n >> 2) & 1][n & 3], cev[n >> 3]);
      asm volatile("" : "+v"(tc[n]));
    };
    u32x4 ep;
    auto epi = [&](int h, int q) __attribute__((always_inline)) {
      const int jn = q >> 1, i = q & 1;
      asm volatile("v_fmac_f32 %0, %4, %8\n\tv_fmac_f32 %1, %5, %9\n\tv_fmac_f32 %2, %6, %10\n\tv_fmac_f32 %3, %7, %11"
                   : "+v"(tc[q * 4]), "+v"(tc[q * 4 + 1]), "+v"(tc[q * 4 + 2]), "+v"(tc[q * 4 + 3])
                   : "v"(acc[h * 2 + i][jn][0]), "v"(acc[h * 2 + i][jn][1]), "v"(acc[h * 2 + i][jn][2]), "v"(acc[h * 2 + i][jn][3]),
                     "v"(rsv[i][0]), "v"(rsv[i][1]), "v"(rsv[i][2]), "v"(rsv[i][3]));
      ep[i * 2] = pk2(tc[q * 4], tc[q * 4 + 1]); ep[i * 2 + 1] = pk2(tc[q * 4 + 2], tc[q * 4 + 3]);
      if (i) *LDSP(u32x4, ae + m_off(h) + TO(jn)) = ep;
    };
    SB();
    half1(0);
    BAR_LGKM();
    SB();
    SLOTS(16) {
      if (q < 8) quad(2, 3, 2, q); else quad(2, 0, 1, q - 8);
      if (q < 12) rdg(0, 13, 0, q);
      SB();
    }
    BAR();
    SB();
    SLOTS(16) {
      mmg(0, 0, q);
      if (q < 12) rdg(1, 14, 0, q);
      else { rds(0, q - 12); cev[q - 12] = *LDSP(const float, ac + (((q - 12) >> 1) * 32 + ((q - 12) & 1) * 4) * 4); }
      SB();
    }
    PAGE(9);
    BAR();
    SB();
    SLOTS(16) { mmg(1, 0, q); if ((q & 3) < 3) rdg(0, 15, 0, (q >> 2) * 3 + (q & 3)); if (q >= 8) { prep(2 * q - 16); prep(2 * q - 15); } SB(); }
    SLOTS(16) { mmg(0, 0, q); if ((q & 3) < 3) rdg(1, 13, 1, (q >> 2) * 3 + (q & 3)); prep(16 + q); SB(); if (q >= 9 && (q & 1)) { epi(0, (q - 9) >> 1); SB(); } }
    SLOTS(8) { mmg(1, 1, q); if (q < 6) { rdg(0, 14, 1, 2 * q); rdg(0, 14, 1, 2 * q + 1); } SB(); if (q & 1) { epi(0, 4 + (q >> 1)); SB(); } }
    BAR_LGKM();
    SB();
    SLOTS(8) { mmg(1, 1, 8 + q); if (q < 4) rdg(1, 15, 1, q == 0 ? 1 : q == 1 ? 3 : q + 4); else rds(1, q - 4); SB(); }
    SLOTS(16) { mmg(0, 1, q); if (q < 8) rdg(1, 15, 1, q == 0 ? 0 : q == 1 ? 2 : q < 4 ? q + 2 : q + 4); prep(q); SB(); }
    asm volatile("s_waitcnt lgkmcnt(0)" ::: "memory");
    if (lane == 0) asm volatile("ds_add_u32 %0, %1" :: "v"(lds_addr(smem + LDS_SEL + 12)), "v"(1) : "memory");
    SB();
    SLOTS(16) {
      mmg(1, 1, q);
      if (q < 4) fn[0][q] = *(const bf16x8*)(smem + (wr * 128 + fr) * 128 + (q >> 1) * 2048 + ((q & 1) ? ko1 : ko0));
      else if (q < 8) fm[q & 1][(q - 4) >> 1] = *(const bf16x8*)(smem + G2_MRING + m_off(0) + (wc * 64 + mrow) * 128 + TO((q - 4) >> 1) + ((q & 1) ? ko1 : ko0));
      prep(16 + q);
      SB();
    }
    {
      lds_vi rel = (lds_vi)(smem + LDS_SEL + 12);
      int it = 0;
      while (rel[0] != 4 && it < SPIN_MAX * 8) ++it;
      asm volatile("" ::: "memory");
    }
    asm volatile("s_nop 7");
    SB();
    SLOTS(8) { epi(1, q); SB(); }
  }

  const char* pn = smem + (wr * 128 + fr) * 128;
  const char* pm = smem + G2_MRING + (wc * 64 + mrow) * 128;
  f32x4 acc[8][4];
  auto mm = [&](bool imaj, int set, int q2, int s, int mode) __attribute__((always_inline)) {
    int i, jj, kk;
    if (imaj) { i = s >> 2; const int r = s & 3; jj = (r >> 1) ^ (i & 1); kk = (r == 1 || r == 2); }
    else { jj = s >> 3; const int r = s & 7; i = jj ? 3 - (r >> 1) : (r >> 1); kk = ((r + 1) >> 1) & 1; }
    if (mode == 1 && !(s & 1)) asm volatile("v_mfma_f32_16x16x32_bf16 %0, %1, %2, 0" : "=&v"(acc[q2 * 2 + jj][i]) : "v"(fm[kk][i]), "v"(fn[set][jj * 2 + kk]));
    else if (mode) asm volatile("v_mfma_f32_16x16x32_bf16 %0, %1, %2, %0" : "+v"(acc[q2 * 2 + jj][i]) : "v"(fm[kk][i]), "v"(fn[set][jj * 2 + kk]));
    else acc[q2 * 2 + jj][i] = __builtin_amdgcn_mfma_f32_16x16x32_bf16(fm[kk][i], fn[set][jj * 2 + kk], acc[q2 * 2 + jj][i], 0, 0, 0);
  };
  unsigned bn[2], bm[2], an[2], am[2];
#pragma unroll
  for (int kk = 0; kk < 2; ++kk) {
    bn[kk] = lds_addr(pn) + (kk ? ko1 : ko0); bm[kk] = lds_addr(pm) + (kk ? ko1 : ko0);
    asm volatile("" : "+v"(bn[kk]), "+v"(bm[kk]));
    an[kk] = bn[kk]; am[kk] = bm[kk] + m_off(0);
  }
#define RDN(jt, kk, o) (*LDSP(const bf16x8, an[kk] + (o) + (jt) * 2048))
#define RDM(i, kk, o) (*LDSP(const bf16x8, am[kk] + (o) + TO(i)))
  PAGE(10);
  tag = __builtin_amdgcn_readfirstlane(((const unsigned*)(smem + LDS_SEL))[5]);
  {
    int son = 0;
    auto tile = [&](int kt, int zero) __attribute__((always_inline)) {
      SLOTS(16) {
        mm(true, 0, 0, q, zero);
        if (!(q & 1)) {
          if (q < 8) fm[(q >> 1) & 1][2 + (q >> 2)] = RDM(2 + (q >> 2), (q >> 1) & 1, 0);
          else { const int r = (q - 8) >> 1; fn[1][r] = RDN(2 + (r >> 1), r & 1, 0); }
        }
        SB();
      }
      SLOTS(16) {
        mm(false, 1, 1, q, zero);
        if (!(q & 1) && q < 8) fn[0][q >> 1] = RDN(4 + (q >> 2), (q >> 1) & 1, 0);
        SB();
      }
      const int som = son >> 1;
      son = son == 65536 ? 0 : son + 32768;
      SLOTS(16) {
        mm(false, 0, 2, q, zero);
        if (!(q & 1) && q < 8) fn[1][q >> 1] = RDN(6 + (q >> 2), (q >> 1) & 1, 0);
        if (q == 9 || q == 11) { an[q == 11] = bn[q == 11] + son; asm volatile("" : "+v"(an[q == 11])); }
        if (q == 13 || q == 15) { am[q == 15] = bm[q == 15] + som; asm volatile("" : "+v"(am[q == 15])); }
        SB();
      }
      BAR_LGKM();
      SB();
      SLOTS(16) {
        mm(true, 1, 3, q, zero);
        if (!(q & 1)) {
          if (q < 8) fn[0][q >> 1] = RDN(q >> 2, (q >> 1) & 1, 0);
          else fm[(q >> 1) & 1][(q - 8) >> 2] = RDM((q - 8) >> 2, (q >> 1) & 1, 0);
        }
        SB();
      }
    };
    SB();
    tile(0, 1);
#pragma unroll 1
    for (int kt = 1; kt < G2_NKT - 3; ++kt) tile(kt, 0);
  }
  unsigned fx;
  {
    constexpr int NG = 12;
    static constexpr int g_t[NG] = {0, 0, 1, 1, 2, 2, 2, 1, 0, 0, 1, 2};
    static constexpr int g_q[NG] = {0, 1, 0, 1, 0, 1, 2, 2, 2, 3, 3, 3};
    constexpr int G14 = 1, G15 = 3, GP0 = 5, GP1 = 6;
    constexpr auto chp = [](int g) { return g == 0 || g_t[g] != g_t[g - 1]; };
    constexpr auto chn = [](int g) { return g + 1 < NG && g_t[g + 1] != g_t[g]; };
    static_assert(n_off(13) == 32768 && n_off(14) == 65536 && n_off(15) == 0, "");
    unsigned an2[2];
#pragma unroll
    for (int kk = 0; kk < 2; ++kk) {
      an[kk] = bn[kk]; an2[kk] = bn[kk] + 65536; am[kk] = bm[kk];
      asm volatile("" : "+v"(an[kk]), "+v"(an2[kk]), "+v"(am[kk]));
    }
    int vo;
    u32x4 pk;
    u32x2 kv;
    constexpr int NF0 = 152;
    auto publish = [&](int d, int h) __attribute__((always_inline)) {
      if (h == 0) { vo = lane; PINV(vo); vo = vo * 16 + (((d + kq + 1) & 3) * 4 + kq) * 4 * MSG; PINV(vo); }
      const f32x4 v = acc[2 * d + (h >> 2)][h & 3];
      pk[(h & 1) * 2] = pk2(v[0], v[1]); pk[(h & 1) * 2 + 1] = pk2(v[2], v[3]);
      if (h & 1) __builtin_amdgcn_raw_buffer_store_b128(pk, rs, vo + (h >> 1) * 1024, 0, 16);
    };
    int fo = lane == 0 ? kq * 512 : (int)0x80000000;
#define MSG_FLAG(d) __builtin_amdgcn_raw_buffer_store_b32(tag, rc, fo + (((d) + kq + 1) & 3) * 2048, 0, 16)
    auto rdn = [&](int g, int r) __attribute__((always_inline)) {
      const int t = g_t[g], jt = g_q[g] * 2 + (r >> 1);
      fn[g & 1][r] = *LDSP(const bf16x8, (t == 1 ? an2[r & 1] : an[r & 1]) + (t == 0 ? 32768 : 0) + jt * 2048);
    };
    auto rdm = [&](int t, int i, int kk) __attribute__((always_inline)) { fm[kk][i] = *LDSP(const bf16x8, am[kk] + m_off(13 + t) + TO(i)); };
    auto group = [&](auto GI) __attribute__((always_inline)) {
      constexpr int gi = decltype(GI)::value;
      if (gi == G14) { BAR();
        SB(); }
      if (gi == G15) {
        PAGE(11);
        BAR();
        SB();
        xq[0] = ld16_s<0>(x, xo); xq[1] = ld16_s<64>(x, xo);
        sq = ld8_s_sc1(stat, xo >> 11 << 3);
        kv = *(const u32x2*)(smem + LDS_SEL);
        SB();
      }
#pragma unroll
      for (int q = 0; q < 16; ++q) {
        const int n = gi * 16 + q;
        if (n == NF0) {
          wait_xq<12>(xq, sq);
          key[0] = __builtin_amdgcn_readfirstlane(kv.x); key[1] = __builtin_amdgcn_readfirstlane(kv.y);
          nf_pre();
          SB();
        }
        if (n == 112) {
        }
        if (n == 152) {
        }
        if (n == 168) {
          WAIT_VM(8); MSG_FLAG(0); SB(); }
        if (n == 176) { WAIT_VM(5); MSG_FLAG(1); SB(); }
        if (n == 184) {
          WAIT_VM(2);
          MSG_FLAG(2); fx = bld4_sc1(rc, po); SB(); }
        mm(chp(gi) || chn(gi), gi & 1, g_q[gi], q, 2);
        constexpr bool ni = gi + 1 < NG && (chp(gi + 1 < NG ? gi + 1 : gi) || chn(gi + 1 < NG ? gi + 1 : gi));
        auto nr = [](int r) { return ni && r >= 2 ? 5 - r : r; };
        if constexpr (chp(gi) && chn(gi)) {
          if (q < 4) rdm(g_t[gi], 2 + (q >> 1), q & 1); else if (q < 8) rdn(gi + 1, nr(q - 4)); else if (q < 12) rdm(g_t[gi + 1], (q - 8) >> 1, q & 1);
        } else if constexpr (chp(gi)) {
          if (q < 4) rdm(g_t[gi], 2 + (q >> 1), q & 1);
          else if (gi + 1 < NG && q < 12 && !(q & 1)) rdn(gi + 1, nr((q - 4) >> 1));
        } else if constexpr (chn(gi)) {
          if (!(q & 1)) { if (q < 8) rdn(gi + 1, nr(q >> 1)); else rdm(g_t[gi + 1], (q - 8) >> 2, (q >> 1) & 1); }
        } else {
          if (!(q & 1) && q < 8) rdn(gi + 1, nr(q >> 1));
        }
        if (gi == GP0 && q < 8) publish(0, q);
        if (gi == GP1 && q < 8) publish(1, q);
        if (gi == 9 && q < 8) publish(2, q);
        if (n >= NF0) {
          const int m = (n - NF0) * 2;
          if (m < 56) { nf_step(m); nf_step(m + 1); } else if (m < 72) { nf_unp(m - 56); nf_unp(m - 55); }
        }
        SB();
      }
    };
#define GR4(i) group(IC<i>{}); group(IC<i + 1>{}); group(IC<i + 2>{}); group(IC<i + 3>{});
    GR4(0) GR4(4) GR4(8)
    asm volatile("s_nop 7");
    SB();
  }
#pragma unroll
  for (int i = 0; i < 4; ++i) *(f32x4*)(smem + LDS_ACC + wr * 16384 + wc * 4096 + i * 1024 + lane * 16) = acc[7][i];
#pragma unroll
  for (int i = 0; i < 4; ++i) ya[i] = acc[6][i];
  asm volatile("s_waitcnt lgkmcnt(0)" ::: "memory");
  if (lane == 0) sig[0] = 2;
  asm volatile("s_waitcnt vmcnt(0)" : "+v"(fx) :: "memory");
  seen = (unsigned)__ballot(fx == tag);
  }

  PAGE(6);
  const unsigned rel0 = 0xffff - (key[0] & 0xffffu) - trow, rel1 = 0xffff - (key[1] & 0xffffu) - trow;
  if (min(rel0, rel1) < 16) {
#pragma unroll 1
    for (int c = 0; c < 2; ++c) {
      const unsigned rel = c ? rel1 : rel0;
      if (rel >= 16) continue;
      lds_vi cnt = (lds_vi)(smem + LDS_SEL + 24 + c * 4);
      int it = 0;
      while (cnt[0] != 4 && it < SPIN_MAX * 8) { __builtin_amdgcn_s_sleep(1); ++it; }
      asm volatile("" ::: "memory");
      if (rel == fr) {
        const float* fp = (const float*)(smem + LDS_FIN + c * 512) + (wid & 1) * 64 + fq * 8;
#pragma unroll
        for (int u4 = 0; u4 < 4; ++u4) {
          const f32x4 v = *(const f32x4*)(fp + (u4 >> 1) * 32 + (u4 & 1) * 4);
#pragma unroll
          for (int k = 0; k < 4; k += 2) {
            const unsigned u = pk2(nf[u4 * 4 + k] + v[k], nf[u4 * 4 + k + 1] + v[k + 1]);
            nf[u4 * 4 + k] = lo_bf(u); nf[u4 * 4 + k + 1] = hi_bf(u);
          }
        }
      }
    }
  }
  if (ha) {
    int it = 0;
    while (sig[0] != 2 && it < SPIN_MAX * 8) { __builtin_amdgcn_s_sleep(1); ++it; }
    asm volatile("" ::: "memory");
#pragma unroll
    for (int i = 0; i < 4; ++i) ya[i] = *(const f32x4*)(smem + LDS_ACC + (wid & 2) * 8192 + (wid & 1) * 4096 + i * 1024 + lane * 16);
  }
  u32x4 pp[3][2];
  unsigned e_lo = 0x00003f80u, e_hi = 0x3f800000u;
  asm volatile("" : "+v"(e_lo), "+v"(e_hi));
  __amdgpu_buffer_rsrc_t rs_ = rs, rc_ = rc;
  int mo[3] = {MSG_IDX(0) * 4 * MSG, MSG_IDX(1) * 4 * MSG, MSG_IDX(2) * 4 * MSG}, mv = lane * 16 + ha * 2048, mv2 = mv + 1024;
  asm volatile("" : "+s"(rs_), "+s"(rc_), "+s"(mo[0]), "+s"(mo[1]), "+s"(mo[2]));
  asm volatile("" : "+v"(mv), "+v"(mv2));
#define MSG_POLL(m) do { \
    if (!((seen >> (m)) & 1)) { \
      unsigned pv_, it_ = SPIN_MAX; \
      asm volatile("1:\n\tbuffer_load_dword %0, %3, %4, 0 offen sc1\n\ts_waitcnt vmcnt(0)\n\tv_cmp_eq_u32 vcc, %5, %0\n\ts_mov_b32 %1, vcc_lo\n\t" \
                   "s_bitcmp1_b32 %1, %6\n\ts_cbranch_scc1 2f\n\ts_sub_u32 %2, %2, 1\n\ts_cbranch_scc1 2f\n\ts_sleep 1\n\ts_branch 1b\n2:" \
                   : "=&v"(pv_), "+s"(seen), "+s"(it_) : "v"(po), "s"(rc_), "s"(tag), "i"(m) : "vcc", "scc", "memory"); \
    } \
    pp[m][0] = __builtin_amdgcn_raw_buffer_load_b128(rs_, mv, mo[m], 16); \
    pp[m][1] = __builtin_amdgcn_raw_buffer_load_b128(rs_, mv2, mo[m], 16); } while (0)
#define MSG_ADD(m) do { \
    _Pragma("unroll") for (int u = 0; u < 8; ++u) { \
      ya[u >> 1][(u & 1) * 2] = add_bf(pp[m][u >> 2][u & 3], e_lo, ya[u >> 1][(u & 1) * 2]); ya[u >> 1][(u & 1) * 2 + 1] = add_bf(pp[m][u >> 2][u & 3], e_hi, ya[u >> 1][(u & 1) * 2 + 1]); } } while (0)
  MSG_POLL(2); MSG_POLL(1); MSG_POLL(0);
  SB();
  MSG_ADD(2); MSG_ADD(1);
  asm volatile("" : "+v"(ya[0]), "+v"(ya[1]), "+v"(ya[2]), "+v"(ya[3]));
  SB();
  MSG_ADD(0);
  {
    unsigned ov[8];
    SB();
#pragma unroll
    for (int u = 0; u < 8; ++u) ov[u] = pk2(ya[u >> 1][(u & 1) * 2], ya[u >> 1][(u & 1) * 2 + 1]);
    SB();
#pragma unroll
    for (int u = 0; u < 8; ++u) { xf[2 * u] = add_bf(ov[u], e_lo, xf[2 * u]); xf[2 * u + 1] = add_bf(ov[u], e_hi, xf[2 * u + 1]); }
    SB();
#pragma unroll
    for (int u = 0; u < 8; ++u) ov[u] = pk2(xf[2 * u], xf[2 * u + 1]);
    SB();
#pragma unroll
    for (int u = 0; u < 8; ++u) { nf[2 * u] = add_bf(ov[u], e_lo, nf[2 * u]); nf[2 * u + 1] = add_bf(ov[u], e_hi, nf[2 * u + 1]); }
    SB();
#pragma unroll
    for (int u = 0; u < 8; ++u) ov[u] = pk2(nf[2 * u], nf[2 * u + 1]);
    SB();
    *(u32x4*)((char*)out + (unsigned)xo) = (u32x4){ov[0], ov[1], ov[2], ov[3]};
    *(u32x4*)((char*)out + (unsigned)xo + 64) = (u32x4){ov[4], ov[5], ov[6], ov[7]};
  }
  if (wid == 0) {
    const unsigned t0 = (unsigned)t00, te = (unsigned)__builtin_amdgcn_s_memrealtime();
    const __amdgpu_buffer_rsrc_t rg = mk_rsrc(wsp + (64u << 20));
    if (lane == 0) __builtin_amdgcn_raw_buffer_store_b128((u32x4){tag, t0, te, tag}, rg, g * 16, 0, 16);
    if (g == 37) {
      for (int i = 0; i < 24; ++i) __builtin_amdgcn_s_sleep(8);
      int mn0, mx, sum, mxe;
      for (int it = 0; it < 100000; ++it) {
        unsigned bad = 0; mn0 = 0x7fffffff; mx = -0x7fffffff; sum = 0; mxe = -0x7fffffff;
        for (int k = 0; k < 4; ++k) {
          const u32x4 q = __builtin_amdgcn_raw_buffer_load_b128(rg, (k * 64 + lane) * 16, 0, 16);
          bad |= (q.x ^ tag) | (q.w ^ tag);
          mn0 = min(mn0, (int)(q.y - t0)); mx = max(mx, (int)(q.y - t0)); sum += (int)(q.y - t0); mxe = max(mxe, (int)(q.z - t0));
        }
        if (__all(bad == 0)) break;
        asm volatile("" ::: "memory");
      }
      for (int o = 32; o; o >>= 1) { mn0 = min(mn0, __shfl_xor(mn0, o, 64)); mx = max(mx, __shfl_xor(mx, o, 64)); mxe = max(mxe, __shfl_xor(mxe, o, 64)); sum += __shfl_xor(sum, o, 64); }
      int d = 0;
      d = __builtin_amdgcn_readfirstlane(min(max(d, 0), 6000));
      const unsigned t1 = (unsigned)__builtin_amdgcn_s_memrealtime();
      while ((unsigned)__builtin_amdgcn_s_memrealtime() - t1 < 0u * d) __builtin_amdgcn_s_sleep(1);
    }
  }
}

extern "C" void kernel_launch(void* const* d_in, const int* in_sizes, int n_in, void* d_out, int out_size,
                              void* d_ws, size_t ws_size, hipStream_t stream) {
  const u16* x = (const u16*)d_in[0];
  const u16* ws_re = (const u16*)d_in[1];
  const u16* ws_im = (const u16*)d_in[2];
  const u16* wd_re = (const u16*)d_in[3];
  const u16* wd_im = (const u16*)d_in[4];
  const u16* w_r = (const u16*)d_in[6];
  const u16* w_gate = (const u16*)d_in[7];
  const u16* w_up = (const u16*)d_in[8];
  const u16* w_down = (const u16*)d_in[9];
  k_main<<<256, 512, 0, stream>>>(x, 0x7fffffffu, 0x00020000u, wd_re, 0x7fffffffu, 0x00020000u, wd_im, 0x7fffffffu, 0x00020000u, w_r, (char*)d_ws,
                                  ws_re, ws_im, w_gate, w_up, w_down, (u16*)d_out);
}
```

```cpp
#include <hip/hip_runtime.h>
#include <hip/hip_bf16.h>
#include <stdint.h>
#include <type_traits>

extern "C" __device__ unsigned long long llvm_dispatch_id(void) __asm("llvm.amdgcn.dispatch.id");
extern "C" __device__ float llvm_fmul_legacy(float, float) __asm("llvm.amdgcn.fmul.legacy");
typedef unsigned short u16;
typedef unsigned long long u64;
typedef __attribute__((ext_vector_type(8))) short bf16x8;
typedef __attribute__((ext_vector_type(4))) unsigned u32x4;
typedef __attribute__((ext_vector_type(2))) unsigned u32x2;
typedef __attribute__((ext_vector_type(4))) float f32x4;
typedef __attribute__((ext_vector_type(2))) float f32x2;
typedef __attribute__((ext_vector_type(2))) __bf16 bf16x2;
typedef __attribute__((address_space(3))) void* lds_vp;
typedef volatile __attribute__((address_space(3))) int* lds_vi;

constexpr int S_ = 2048, D_ = 1024, F_ = 3584;
template <int N> using IC = std::integral_constant<int, N>;

static __device__ __forceinline__ float bf2f(u16 u) { return __uint_as_float((unsigned)u << 16); }
static __device__ __forceinline__ u16 f2bf(float f) {
  __hip_bfloat16 b = __float2bfloat16(f);
  return *reinterpret_cast<u16*>(&b);
}
static __device__ __forceinline__ float rbf(float f) { return bf2f(f2bf(f)); }
static __device__ __forceinline__ float lo_bf(unsigned u) { return __uint_as_float(u << 16); }
static __device__ __forceinline__ float hi_bf(unsigned u) { return __uint_as_float(u & 0xffff0000u); }
static __device__ __forceinline__ unsigned pk2(float a, float b) {
  const bf16x2 v = __builtin_convertvector((f32x2){a, b}, bf16x2);
  return __builtin_bit_cast(unsigned, v);
}
static __device__ __forceinline__ float add_bf(unsigned u, unsigned e, float c) {
  return __builtin_amdgcn_fdot2_f32_bf16(__builtin_bit_cast(bf16x2, u), __builtin_bit_cast(bf16x2, e), c, false);
}
static __device__ __forceinline__ float ln_elem(float x, float mb, float sd, float ri) {
  const float d = rbf(x - mb);
  const float q0 = d * ri;
  return rbf(__builtin_fmaf(__builtin_fmaf(-q0, sd, d), ri, q0));
}
template <int CTRL> static __device__ __forceinline__ float dpp_f(float v) {
  return __int_as_float(__builtin_amdgcn_update_dpp(0, __float_as_int(v), CTRL, 0xf, 0xf, true));
}
template <int CTRL> static __device__ __forceinline__ unsigned dpp_u(unsigned v) {
  return (unsigned)__builtin_amdgcn_update_dpp(0, (int)v, CTRL, 0xf, 0xf, true);
}
static __device__ __forceinline__ float row_sum(float v) {
  v += dpp_f<0xB1>(v);
  v += dpp_f<0x4E>(v);
  v += dpp_f<0x141>(v);
  v += dpp_f<0x140>(v);
  return v;
}
static __device__ __forceinline__ float fold32(float a, float b) {
  const auto r = __builtin_amdgcn_permlane32_swap(__float_as_uint(a), __float_as_uint(b), false, false);
  return __uint_as_float(r[0]) + __uint_as_float(r[1]);
}
static __device__ __forceinline__ float fold16(float a, float b) {
  const auto r = __builtin_amdgcn_permlane16_swap(__float_as_uint(a), __float_as_uint(b), false, false);
  return __uint_as_float(r[0]) + __uint_as_float(r[1]);
}
static __device__ __forceinline__ float rdl(float v, int l) { return __int_as_float(__builtin_amdgcn_readlane(__float_as_int(v), l)); }

#define ST_JB 9
#define WSN 10
#define FSM 0xa2
#define GV_JB 7
#define DG 11
#define AB_STRIDE 32
#define SPIN_MAX (1 << 17)

static __device__ __forceinline__ unsigned lds_addr(const void* p) { return (unsigned)(size_t)(lds_vp)p; }

constexpr int MSG = 4096;
constexpr size_t WS_ABT   = 0;
constexpr size_t WS_SLAB  = WS_ABT + (size_t)D_ * 4096 * 2;
constexpr size_t WS_GU    = WS_SLAB + (size_t)64 * 64 * MSG;
constexpr size_t WS_DN    = WS_GU + (size_t)8 * F_ * 16;
constexpr size_t WS_FLAGS = WS_DN + (size_t)2 * 8 * 32 * 64 * 16;
constexpr int FL_X = 0;
constexpr int FL_AB = 64 * 64 * 32;
constexpr int FL_ST = FL_AB + 2048 * AB_STRIDE;
constexpr int FL_CE = FL_ST + 4096;
constexpr int FL_KEY = FL_CE + 4096;
constexpr int FL_EPOCH = FL_KEY + 4096;
constexpr int WS_MB = (int)((WS_FLAGS + (FL_EPOCH + 1) * 4 + 1048575) >> 20);

#define WAIT_VM(n) asm volatile("s_waitcnt vmcnt(" #n ")" ::: "memory")
template <int N> static __device__ __forceinline__ void wait_vm() { asm volatile("s_waitcnt vmcnt(%0)" :: "i"(N) : "memory"); }
#define BAR_LGKM() asm volatile("s_waitcnt lgkmcnt(0)\n\ts_barrier" ::: "memory")
#define BAR() asm volatile("s_barrier" ::: "memory")
#define SB() __builtin_amdgcn_sched_barrier(0)
#define PAGE(n) asm volatile("s_branch 2f\n\t.p2align 12\nk_pg" #n ":\n\ts_setpc_b64 s[42:43]\n2:")
#define CALL_PG(n) asm volatile("s_getpc_b64 s[40:41]\n3:\n\ts_mov_b32 s42, k_pg" #n "-3b\n\ts_ashr_i32 s43, s42, 31\n\ts_add_u32 s40, s40, s42\n\ts_addc_u32 s41, s41, s43\n\t" \
                                "s_swappc_b64 s[42:43], s[40:41]" ::: "s40", "s41", "s42", "s43", "scc", "memory")

static __device__ __forceinline__ __amdgpu_buffer_rsrc_t mk_rsrc(const void* p) {
  return __builtin_amdgcn_make_buffer_rsrc((void*)p, 0, 0x7fffffff, 0x00020000);
}
#define DMA_(rsrc, voff, mbase, MI, sbase, SI, pol) do { unsigned _t; \
    asm volatile("s_add_i32 m0, %2, %3\n\ts_add_i32 %0, %5, %6\n\tbuffer_load_dwordx4 %1, %4, %0 offen" pol " lds" \
                 : "=&s"(_t) : "v"(voff), "s"(mbase), "i"(MI), "s"(rsrc), "s"(sbase), "i"(SI) : "memory", "scc"); } while (0)
#define TOUCH(rsrc, voff, mbase, MI, soff) \
    asm volatile("s_add_i32 m0, %1, %2\n\ts_nop 0\n\tbuffer_load_dword %0, %3, %4 offen lds" \
                 :: "v"(voff), "s"(mbase), "i"(MI), "s"(rsrc), "s"(soff) : "memory", "scc")
template <int OFF> static __device__ __forceinline__ u32x4 ld16_s(const void* p, int voff) {
  u32x4 v;
  asm volatile("global_load_dwordx4 %0, %1, %2 offset:%3" : "=v"(v) : "v"(voff), "s"(p), "i"(OFF) : "memory");
  return v;
}
template <int OFF> static __device__ __forceinline__ void sld(unsigned& sink, const void* p, int soff) {
  asm volatile("s_load_dword %0, %1, %2 offset:%3" : "+s"(sink) : "s"(p), "s"(soff), "i"(OFF) : "memory");
}
static __device__ __forceinline__ unsigned gld4_s(const void* p, unsigned voff) {
  unsigned v;
  asm volatile("global_load_dword %0, %1, %2" : "=v"(v) : "v"(voff), "s"(p) : "memory");
  return v;
}
static __device__ __forceinline__ u32x2 bld8_sc1(__amdgpu_buffer_rsrc_t r, unsigned voff) {
  u32x2 v;
  asm volatile("buffer_load_dwordx2 %0, %1, %2, 0 offen sc1" : "=v"(v) : "v"(voff), "s"(r) : "memory");
  return v;
}
template <int N> static __device__ __forceinline__ void wait_xs(unsigned& a, u32x2& b) { asm volatile("s_waitcnt vmcnt(%2)" : "+v"(a), "+v"(b) : "i"(N) : "memory"); }
template <int N> static __device__ __forceinline__ void wait_xq(u32x4 (&a)[2], u32x2& b) { asm volatile("s_waitcnt vmcnt(%3)" : "+v"(a[0]), "+v"(a[1]), "+v"(b) : "i"(N) : "memory"); }
static __device__ __forceinline__ u32x2 ld8_s_sc1(const void* p, int voff) {
  u32x2 v;
  asm volatile("global_load_dwordx2 %0, %1, %2 sc1" : "=v"(v) : "v"(voff), "s"(p) : "memory");
  return v;
}
static __device__ __forceinline__ void lgkm0(unsigned& sink) { asm volatile("s_waitcnt lgkmcnt(0)" : "+s"(sink) :: "memory"); }
template <typename T> static __device__ __forceinline__ void pin4(T& a, T& b, T& c, T& d) { asm volatile("" : "+s"(a), "+s"(b), "+s"(c), "+s"(d)); }
template <typename T, typename U> static __device__ __forceinline__ void pins(T& a, U& b) { asm volatile("" : "+s"(a), "+s"(b)); }
static __device__ __forceinline__ void launder(int& v, int& a, int& b) { asm volatile("" : "+v"(v)); asm volatile("" : "+s"(a), "+s"(b)); }
static __device__ __forceinline__ unsigned bld4_sc1(__amdgpu_buffer_rsrc_t r, int voff) {
  unsigned v;
  asm volatile("buffer_load_dword %0, %1, %2, 0 offen sc1" : "=v"(v) : "v"(voff), "s"(r) : "memory");
  return v;
}
static __device__ __forceinline__ u32x4 bld16_nt(__amdgpu_buffer_rsrc_t r, int voff, int soff) {
  u32x4 v;
  asm volatile("buffer_load_dwordx4 %0, %1, %2, %3 offen nt" : "=v"(v) : "v"(voff), "s"(r), "s"(soff) : "memory");
  return v;
}
static __device__ __forceinline__ void bld16_nt_io(u32x4& v, __amdgpu_buffer_rsrc_t r, int voff, int soff) {
  asm volatile("buffer_load_dwordx4 %0, %1, %2, %3 offen nt" : "+v"(v) : "v"(voff), "s"(r), "s"(soff) : "memory");
}
static __device__ __forceinline__ u32x4 bld16_sc1(__amdgpu_buffer_rsrc_t r, int voff, int soff) {
  u32x4 v;
  asm volatile("buffer_load_dwordx4 %0, %1, %2, %3 offen sc1" : "=v"(v) : "v"(voff), "s"(r), "s"(soff) : "memory");
  return v;
}
static __device__ __forceinline__ u32x2 granule_wait(const u64* p, unsigned tag) {
  u64 v = 0;
#pragma unroll 1
  for (int it = 0; it < SPIN_MAX; ++it) {
    v = __hip_atomic_load(p, __ATOMIC_RELAXED, __HIP_MEMORY_SCOPE_AGENT);
    if (__all((unsigned)(v >> 32) == tag)) break;
    __builtin_amdgcn_s_sleep(2);
  }
  return (u32x2){(unsigned)v, (unsigned)(v >> 32)};
}
static __device__ __forceinline__ float key_score(unsigned key) {
  const unsigned u = key >> 16;
  return bf2f((u16)((u & 0x8000u) ? (u & 0x7fffu) : (~u & 0xffffu)));
}
static __device__ __forceinline__ void merge2(unsigned& a1, unsigned& a2, unsigned b1, unsigned b2) {
  const unsigned lo = min(a1, b1);
  a1 = max(a1, b1);
  a2 = max(lo, max(a2, b2));
}
static __device__ __forceinline__ float sqdev16(const u32x4 (&v)[2], float mb) {
  f32x2 q2 = {0.f, 0.f};
#pragma unroll
  for (int i = 0; i < 8; ++i) {
    const f32x2 e = (f32x2){lo_bf(v[i >> 2][i & 3]), hi_bf(v[i >> 2][i & 3])} - mb;
    q2 += e * e;
  }
  return q2.x + q2.y;
}

constexpr int G1_NST = 4, G1_NKT = 16, G1_STAGE = 32768;
constexpr int G2_NKT = 16, G2_MRING = 98304;
constexpr int n_off(int k) { return (k % 3) * 32768; }
constexpr int m_off(int k) { return ((k + 2) % 3) * 16384; }
constexpr int k_ord(int k) { return k < 2 ? k : (k < 9 ? 2 * (k - 1) : 2 * (k - 8) + 1); }
constexpr int LDS_DUMMY = G2_MRING + 49152;
constexpr int LDS_GU = LDS_DUMMY + 4096;
constexpr int LDS_FIN = LDS_GU + 4096;
constexpr int LDS_MB = LDS_FIN + 4096;
constexpr int LDS_SEL = LDS_MB + 1536;
constexpr int LDS_SIG = LDS_SEL + 64;
constexpr int LDS_KEY = LDS_SIG + 32;
constexpr int LDS_BYTES = LDS_KEY + 128;
constexpr int LDS_ACC = n_off(G2_NKT - 3);

constexpr int tpos[16] = {0, 0, 0, 0, 106, 110, 115, 2, 2, 3, 4, 5, 0, 0, 0, 0};
constexpr int twho[16] = {0, 0, 0, 0, 7, 7, 7, 0, 1, 2, 6, 5, 0, 0, 0, 0};
constexpr int dn_cnt(int q) { return q == 5 ? 3 : q == 8 ? 4 : 0; }
constexpr int dn_first(int q) { return q <= 5 ? 0 : q <= 8 ? 3 : 7; }

static __device__ __forceinline__ void code_touch(unsigned& sink, unsigned off) {
  asm volatile("s_getpc_b64 s[40:41]\n\tk_main_rt:\n\ts_min_u32 %1, %1, k_main_end-k_main_rt-196\n\t"
               "s_load_dword %0, s[40:41], %1 offset:0\n\ts_load_dword %0, s[40:41], %1 offset:64\n\ts_load_dword %0, s[40:41], %1 offset:128\n\ts_load_dword %0, s[40:41], %1 offset:192"
               : "+s"(sink), "+s"(off) :: "s40", "s41", "scc", "memory");
}
static __device__ __forceinline__ void touch1(__amdgpu_buffer_rsrc_t r, int voff, unsigned mb, int soff) { TOUCH(r, voff, mb, LDS_DUMMY, soff); }
typedef const __attribute__((address_space(1))) u16* gptr;
typedef __attribute__((ext_vector_type(8))) u64 u64x8;
extern "C" __global__ __launch_bounds__(512, 2) void k_main(const u16* x, unsigned x2, unsigned x3, const u16* wd_re, unsigned r2, unsigned r3,
                                                 const u16* wd_im, unsigned i2, unsigned i3, const u16* w_r,
                                                 char* wsp, const u16* ws_re, const u16* ws_im,
                                                 const u16* w_gate, const u16* w_up, const u16* w_down, u16* out) {
  __shared__ __attribute__((aligned(16))) char smem[LDS_BYTES];
  u64x8 ka;
  u32x4 rm, rn;
  asm volatile("s_load_dwordx4 %0, %3, 0\n\ts_load_dwordx4 %1, %3, %4 offset:16\n\ts_load_dwordx16 %2, %3, 48"
               : "=&s"(rm), "=&s"(rn), "=&s"(ka) : "s"(__builtin_amdgcn_kernarg_segment_ptr()), "s"((blockIdx.x & 4) * 4) : "memory");
  const int tid = threadIdx.x, lane = tid & 63;
  const int wid = __builtin_amdgcn_readfirstlane(tid >> 6);
  const int g = blockIdx.x, xcd = g & 7, j = g >> 3;
  if (wid < 4) {
    unsigned cw, co = (j * 4 + wid) * 448;
    asm volatile(".subsection 1\nk_main_end:\n.subsection 0\n\ts_min_u32 %1, %1, k_main_end-k_main-480\n\ts_getpc_b64 s[40:41]\n\ts_add_u32 s40, s40, %1\n\ts_addc_u32 s41, s41, 0\n\t"
                 "s_load_dword %0, s[40:41], 0\n\ts_load_dword %0, s[40:41], 64\n\ts_load_dword %0, s[40:41], 128\n\t"
                 "s_load_dword %0, s[40:41], 192\n\ts_load_dword %0, s[40:41], 256\n\ts_load_dword %0, s[40:41], 320\n\ts_load_dword %0, s[40:41], 384\n\ts_waitcnt lgkmcnt(0)"
                 : "=&s"(cw), "+s"(co) :: "s40", "s41", "scc", "memory");
  }
  const int pw = wid - 4;
  const int row0 = (wid & 3) * 8 + (lane >> 3);
  const int cm = ((lane & 7) ^ (((row0 >> 1) & 1) | ((wid & 3) << 1))) * 16;
  int vm = row0 * (D_ * 2) + cm;
  asm volatile("" : "+v"(vm));
  const int kq = xcd >> 1;
  const int tt = (xcd & 1) * 4 + (j >> 3);
  const int et = j & 7;
  const int z = kq >> 1, st = (kq & 1) * 8 + tt;
  unsigned o_m = st * 128 * D_ * 2, o_n = et * 128 * D_ * 2, mb0 = lds_addr(smem) + pw * 1024;
  asm volatile("" : "+s"(o_m), "+s"(o_n), "+s"(mb0));
#define G1_X(kt, so, q) DMA_(rm, vm, mb0, (so) + (q) * 4096, o_m, (kt) * 128 + (((q) & 1) * 64 + ((q) >> 1) * 32) * D_ * 2, "")
#define G1_W(kt, so, q) DMA_(rn, vm, mb0, (so) + 16384 + (q) * 4096, o_n, (kt) * 128 + (q) * 32 * D_ * 2, "")
#define G1_STAGE_TILE(kt, so, W) do { SB(); G1_X(kt, so, 0); W(0); G1_X(kt, so, 1); W(1); G1_X(kt, so, 2); W(2); G1_X(kt, so, 3); W(3); \
    G1_W(kt, so, 0); W(4); G1_W(kt, so, 1); W(5); G1_W(kt, so, 2); W(6); G1_W(kt, so, 3); W(7); SB(); } while (0)
#define G1_STAGE_A(kt, so, W) do { SB(); G1_X(kt, so, 0); W(0); G1_X(kt, so, 2); W(1); \
    G1_W(kt, so, 0); W(2); G1_W(kt, so, 1); W(3); G1_W(kt, so, 2); W(4); G1_W(kt, so, 3); W(5); W(6); W(7); SB(); } while (0)
#define G1_STAGE_B(kt, so) do { SB(); G1_X(kt, so, 1); G1_X(kt, so, 3); SB(); } while (0)
#define NOW(i)
  asm volatile("s_waitcnt lgkmcnt(0)" : "+s"(ka), "+s"(rm), "+s"(rn) :: "memory");
  int lrow, drow, l16;
  unsigned o_l, o_d;
  auto rows = [&](int i) __attribute__((always_inline)) {
    int g_ = g, l_ = lane;
    if (i == 0) { asm volatile("" : "+s"(g_)); drow = (g_ & ~7) + pw * 2; lrow = drow + (g_ & 3) * 512 + (g_ & 4) * 64; asm volatile("" : "+s"(drow), "+s"(lrow)); }
    if (i == 1) { asm volatile("" : "+s"(g_)); drow += (g_ & 3) * 256; o_l = lrow * D_ * 2; o_d = drow * D_ * 2; asm volatile("" : "+s"(drow), "+s"(o_l), "+s"(o_d)); }
    if (i == 2) { asm volatile("" : "+v"(l_)); l16 = l_ * 16; asm volatile("" : "+v"(l16)); }
  };
  if (wid >= 4) {
    __builtin_amdgcn_s_setprio(3);
    G1_STAGE_TILE(0, 0, rows);
  }
  x = (const u16*)(gptr)((u64)rm[0] | (u64)rm[1] << 32);
  const u16* wdz = (const u16*)(gptr)((u64)rn[0] | (u64)rn[1] << 32);
  w_r = (const u16*)(gptr)ka[0]; wsp = (char*)(gptr)ka[1]; ws_re = (const u16*)(gptr)ka[2]; ws_im = (const u16*)(gptr)ka[3];
  w_gate = (const u16*)(gptr)ka[4]; w_up = (const u16*)(gptr)ka[5]; w_down = (const u16*)(gptr)ka[6]; out = (u16*)(gptr)ka[7];
  int* flags = (int*)(wsp + WS_FLAGS);
  u64* stat = (u64*)(flags + FL_ST);
  const int fr = lane & 15, fq = lane >> 4;
  const int ha = wid >> 2;
  int trow, xo, po;
  const char *prs, *prc;
  auto epi_setup = [&]() __attribute__((always_inline)) {
    int t_ = tt, l_ = lane;
    asm volatile("" : "+s"(t_));
    asm volatile("" : "+v"(l_));
    trow = t_ * 256 + ((wid >> 1) & 1) * 128 + kq * 32 + ha * 16;
    xo = ((trow + (l_ & 15)) * D_ + et * 128 + (wid & 1) * 64 + (l_ >> 4) * 8) * 2;
    prs = wsp + WS_SLAB + (size_t)((t_ * 8 + et) * 64 + (wid & 3)) * MSG;
    prc = (const char*)(flags + FL_X + ((t_ * 8 + et) * 64 + (wid & 3)) * 32);
    asm volatile("" : "+s"(trow), "+s"(prs), "+s"(prc));
    po = l_ < 3 ? (kq * 4 + ((kq + 1 + l_) & 3)) * 512 : (int)0x80000000;
    asm volatile("" : "+v"(xo), "+v"(po));
  };
#define rs mk_rsrc(prs)
#define rc mk_rsrc(prc)
  lds_vi sig = (lds_vi)(smem + LDS_SIG + (wid & 3) * 4);
  unsigned tag;
  u32x4 xq[2];
  u32x2 sq;
  f32x4 ya[4];
  unsigned key[2];
  float xf[16], nf[16], lmb, lsd, lri, la, lb, lqa, lqb;
  unsigned nfp[8];
#define PINV(a) asm volatile("" : "+v"(a))
  auto nf_pre = [&]() __attribute__((always_inline)) {
    if (__builtin_expect(!__all(sq.y == tag), 0)) sq = granule_wait(stat + (xo >> 11), tag);
    lmb = lo_bf(sq.x); lsd = hi_bf(sq.x); lri = __builtin_amdgcn_rcpf(lsd);
    PINV(lmb); PINV(lsd); PINV(lri);
  };
  auto nf_step = [&](int n) __attribute__((always_inline)) {
    const int u = n / 7;
    switch (n % 7) {
      case 0: la = lo_bf(xq[u >> 2][u & 3]); PINV(la); la -= lmb; PINV(la); break;
      case 1: lb = hi_bf(xq[u >> 2][u & 3]); PINV(lb); lb -= lmb; PINV(lb); break;
      case 2: nfp[u] = pk2(la, lb); la = lo_bf(nfp[u]); PINV(la); PINV(nfp[u]); break;
      case 3: lqa = la * lri; PINV(lqa); lb = hi_bf(nfp[u]); PINV(lb); break;
      case 4: la = __builtin_fmaf(-lqa, lsd, la); PINV(la); lqb = lb * lri; PINV(lqb); break;
      case 5: lqa = __builtin_fmaf(la, lri, lqa); PINV(lqa); lb = __builtin_fmaf(-lqb, lsd, lb); PINV(lb); break;
      default: lqb = __builtin_fmaf(lb, lri, lqb); nfp[u] = pk2(lqa, lqb); PINV(nfp[u]); break;
    }
  };
  auto nf_unp = [&](int e) __attribute__((always_inline)) {
    const int u = e >> 1;
    if (e == 0) { PINV(xq[0]); PINV(xq[1]); }
    xf[e] = (e & 1) ? hi_bf(xq[u >> 2][u & 3]) : lo_bf(xq[u >> 2][u & 3]);
    nf[e] = (e & 1) ? hi_bf(nfp[u]) : lo_bf(nfp[u]);
    PINV(xf[e]); PINV(nf[e]);
  };
#define MSG_IDX(m) (kq * 4 + ((kq + 1 + (m)) & 3))
  unsigned seen, fx;

  if (__builtin_expect(wid >= 4, 1)) {
    constexpr int O_ST = (FL_ST - FL_AB) * 4, O_CE = (FL_CE - FL_AB) * 4, O_KEY = (FL_KEY - FL_AB) * 4, O_EP = (FL_EPOCH - FL_AB) * 4;
    const __amdgpu_buffer_rsrc_t rf = mk_rsrc(wsp + WS_FLAGS + FL_AB * 4);
    u32x4 lv[2], lv2[2], lw[2], ld[2], ld2[2];
    unsigned epv;
    {
      const char* xrow = (const char*)x + o_l;
      const char* wdrow = (const char*)wdz + o_d;
      lv[0] = ld16_s<0>(xrow, l16); lv[1] = ld16_s<1024>(xrow, l16);
      lv2[0] = ld16_s<2048>(xrow, l16); lv2[1] = ld16_s<3072>(xrow, l16);
      ld[0] = ld16_s<0>(wdrow, l16); ld[1] = ld16_s<1024>(wdrow, l16);
      ld2[0] = ld16_s<2048>(wdrow, l16); ld2[1] = ld16_s<3072>(wdrow, l16);
      lw[0] = ld16_s<0>(w_r, l16); lw[1] = ld16_s<1024>(w_r, l16);
      asm volatile("buffer_load_dword %0, off, %1, %2 sc1" : "=v"(epv) : "s"(rf), "s"(O_EP) : "memory");
    }
    SB();
    const unsigned long long did = llvm_dispatch_id();
    const unsigned dmix = (unsigned)did * 0x85EBCA6Bu + (unsigned)(did >> 32) * 0xC2B2AE35u + 0x27D4EB2Fu;
    const bool moe = et != 7;
    unsigned epoch;
    const int ci = lane & 15, rq = lane >> 4, tk = (lane >> 3) & 1;
    int mat, cg, dq, d0;
    u32x4 wv[16];
    const u16 *pgu, *pn2;
    const char *pm2, *spb;
#define rw __builtin_amdgcn_make_buffer_rsrc((void*)pgu, 0, moe ? 0x7fffffff : 0, 0x00020000)
#define rn2 mk_rsrc(pn2)
#define rm2 mk_rsrc(pm2)
    int vw, vn2, vm2;
    int ord2;
    auto k_blk = [&](int k) __attribute__((always_inline)) { return k >= 9 ? (ord2 >> (4 * (k - 9))) & 7 : tt ^ (k_ord(k) >> 1); };
#define KROT(kk_) (k_blk(kk_) * 256 + (k_ord(kk_) & 1) * 128)
    int tv2;
    unsigned sd = 0;
    auto touches = [&](auto P) __attribute__((always_inline)) {
      constexpr int p = decltype(P)::value;
      int e = et;
      asm volatile("" : "+s"(e));
#define TCH(k) if constexpr (tpos[k] == p) { if (e == twho[k]) { const int kb = KROT(k); touch1(rn2, tv2, mb0, kb); } }
      TCH(0) TCH(1) TCH(2) TCH(3) TCH(4) TCH(5) TCH(6) TCH(7) TCH(8) TCH(9) TCH(10) TCH(11) TCH(12) TCH(13) TCH(14) TCH(15)
    };
    unsigned mq0, mq1, mq2, mq3;
    const int cg8 = g & 7, rgb = g >> 3;
    const __amdgpu_buffer_rsrc_t rdn = mk_rsrc(w_down);
    const __amdgpu_buffer_rsrc_t rgu = mk_rsrc(wsp + WS_GU);
    int vdn, vgu;
    u32x4 dv[7], gv[4];
    int abo, abq[3];
    unsigned lma;
    unsigned sel[2][4];
    float g0, g1;
    u32x4 kk[4];
    unsigned k1 = 0, k2 = 0;
    const int ko_ = O_KEY + pw * 4096;
#define MM(a, b, c) __builtin_amdgcn_mfma_f32_16x16x32_bf16(__builtin_bit_cast(bf16x8, a), __builtin_bit_cast(bf16x8, b), c, 0, 0, 0)
#define PIN1(a) asm volatile("" : "+v"(a))
#define PIN2(a, b) asm volatile("" : "+v"(a), "+v"(b))
#define PIN3(a, b, c) asm volatile("" : "+v"(a), "+v"(b), "+v"(c))
    {
      int gro, go;
      u32x2 gr;
      u32x4 oh[5];
      f32x4 sa4, gw[2], gx[2];
      float su, gs, ta, tb, sx, mbv;
      unsigned dk[4], kl;
      const int hb = lane >> 5;
#pragma unroll
      for (int l = 0; l < WSN; ++l) asm volatile("" : "=v"(wv[l]));
      auto diag = [&](const f32x4& m) __attribute__((always_inline)) {
        return __uint_as_float((__float_as_uint(m[0]) & dk[0]) | (__float_as_uint(m[1]) & dk[1]) | (__float_as_uint(m[2]) & dk[2]) | (__float_as_uint(m[3]) & dk[3]));
      };
      auto setup = [&](auto JB, auto GP) __attribute__((always_inline)) {
        constexpr int jb = decltype(JB)::value, gp = decltype(GP)::value;
        SB();
        {
          int ln = lane, jj = j, p_ = pw;
          launder(ln, jj, p_);
          const int c_ = ln & 15, r_ = ln >> 4, e_ = jj & 7, t_ = (xcd & 1) * 4 + (jj >> 3);
          if constexpr (jb == 1 && gp == 0) {
            pn2 = ((kq >> 1) ? ws_im : ws_re) + (long)t_ * 256 * S_ + (kq & 1) * 1024;
            spb = (const char*)(pn2 + (e_ * 4 + p_) * 8 * S_);
            pins(pn2, spb);
          }
          if constexpr (jb == 1 && gp >= 1 && gp < 6) {
            const unsigned o = c_ == gp - 1 ? 0x3f803f80u : 0u;
            oh[gp - 1] = (u32x4){o, o, o, o};
            PIN1(oh[gp - 1]);
          }
          if constexpr (jb == 1 && gp == 6) {
            const int gm = moe ? (jj - (jj >> 3)) * 8 + xcd : 0;
            mat = gm / 112; cg = (gm % 112) >> 2; dq = gm & 3; d0 = dq * 256 + p_ * 64;
            pin4(mat, cg, dq, d0);
          }
          if constexpr (jb == 2 && gp == 0) {
            pgu = mat ? w_up : w_gate;
            pm2 = wsp + WS_ABT + ((long)e_ * 128 * 4096 + kq * 1024) * 2;
            pins(pgu, pm2);
            vw = ((d0 + r_ * 16) * F_ + cg * 128 + c_ * 8) * 2;
            PIN1(vw);
          }
          if constexpr (jb == 2 && (gp == 1 || gp == 2)) {
            dk[gp * 2 - 2] = c_ == r_ * 4 + gp * 2 - 2 ? ~0u : 0u; dk[gp * 2 - 1] = c_ == r_ * 4 + gp * 2 - 1 ? ~0u : 0u;
            PIN2(dk[gp * 2 - 2], dk[gp * 2 - 1]);
          }
          if constexpr (jb == 2 && gp == 3) { kl = lrow + (ln >> 5); go = (ln & 30) ? (int)0x80000000 : ((ln & 1) ? O_KEY : O_ST) + (int)kl * 8; PIN2(kl, go); }
          if constexpr (jb == 2 && gp == 4) { kl = 0xffff - kl; go = (ln & ~1) == 2 ? O_CE + (z * D_ + drow + (ln & 1)) * 8 : go; PIN2(kl, go); }
          if constexpr (jb == 2 && gp == 5) {
            gro = (p_ < 2 ? O_ST + (st * 128 + p_ * 64) * 8 : O_CE + (z * D_ + e_ * 128 + (p_ - 2) * 64) * 8) + ln * 8;
            tv2 = (p_ * 64 + ln) * 4096;
            lma = lds_addr(smem + LDS_MB) + (p_ < 2 ? p_ * 256 : 512 + p_ * 256) + ln * 4;
            PIN3(gro, tv2, lma);
          }
          if constexpr (jb == 2 && gp == 6) {
            const u64 lo = (t_ & 2) ? ((t_ & 4) ? 0x0150234601503247ull : 0x0514670205147603ull) : ((t_ & 4) ? 0x0123067401532076ull : 0x0567423005176432ull);
            ord2 = (int)(lo >> ((t_ & 1) * 32));
            pins(ord2, p_);
          }
          if constexpr (jb == 7 && gp == 0) {
            const int r0 = p_ * 8 + (ln >> 3);
            vn2 = r0 * (S_ * 2) + ((ln & 7) ^ ((r0 >> 1) & 7)) * 16; vm2 = r0 * 8192 + (vm & 127);
            PIN2(vn2, vm2);
          }
          if constexpr (jb == 7 && gp == 1) { vdn = jj * 112 + p_ * 28 + r_ * 7; vgu = (((ln >> 3) & 1) * 4 * F_ + vdn + min(ln & 7, 6)) * 16; PIN2(vdn, vgu); }
          if constexpr (jb == 7 && gp == 2) { vdn = (vdn * D_ + cg8 * 128 + c_ * 8) * 2; PIN1(vdn); }
          if constexpr (jb == 7 && gp == 3) {
            abo = (((kq * 8 + e_) * 4 + p_) * 16 + c_) * AB_STRIDE * 4;
            abq[2] = ln >= 16 || (ln & 7) == t_ ? (int)0x80000000 : abo;
            PIN2(abo, abq[2]);
          }
          if constexpr (jb == 7 && gp == 4) {
            abq[1] = ln >= 8 ? (int)0x80000000 : abq[2];
            abq[0] = ((ln ^ t_) & 4) ? (int)0x80000000 : abq[1];
            PIN2(abq[0], abq[1]);
          }
          if constexpr (jb == 7 && gp == 5) {
            const unsigned mb = lds_addr(smem) + p_ * 1024;
            mq0 = mb + ((3 - kq) & 3) * 4096; mq1 = mb + ((4 - kq) & 3) * 4096; mq2 = mb + ((5 - kq) & 3) * 4096; mq3 = mb + ((6 - kq) & 3) * 4096;
            pin4(mq0, mq1, mq2, mq3);
          }
          if constexpr (jb == 8 && gp < 4) {
#pragma unroll
            for (int h = 0; h < 2; ++h) {
              const unsigned hv = h ? 0x0302u : 0x0100u;
              sel[h][gp] = ((ln & 7) >> 1) == gp ? ((ln & 1) ? (hv << 16) | 0x0c0cu : 0x0c0c0000u | hv) : 0x0c0c0c0cu;
            }
            PIN2(sel[0][gp], sel[1][gp]);
          }
        }
        SB();
      };
#define WS1(i) setup(IC<1>{}, IC<i>{})
#define WS2(i) setup(IC<2>{}, IC<i>{})
#define WS3(i) do { setup(IC<7>{}, IC<i>{}); if (i >= 4) setup(IC<8>{}, IC<(i) & 3>{}); } while (0)
      G1_STAGE_TILE(1, G1_STAGE, WS1);
      WAIT_VM(19); BAR();
      G1_STAGE_TILE(2, 2 * G1_STAGE, WS2);
      G1_STAGE_TILE(3, 3 * G1_STAGE, WS3);
      auto work1 = [&](auto JB, auto GP) __attribute__((always_inline)) {
        constexpr int jb = decltype(JB)::value, gp = decltype(GP)::value;
        SB();
        if constexpr (jb == 1) {
          const f32x4 z4 = {0.f, 0.f, 0.f, 0.f};
          if constexpr (gp == 0) { sa4 = MM(lv[0], oh[0], z4); gw[0] = MM(lv[0], lw[0], z4); gx[0] = MM(lv[0], lv[0], z4); PIN3(sa4, gw[0], gx[0]); }
          if constexpr (gp == 1) { sa4 = MM(lv2[0], oh[1], sa4); gw[1] = MM(lv2[0], lw[0], z4); gx[1] = MM(lv2[0], lv2[0], z4); PIN3(sa4, gw[1], gx[1]); }
          if constexpr (gp == 2) { sa4 = MM(lv[1], oh[0], sa4); gw[0] = MM(lv[1], lw[1], gw[0]); gx[0] = MM(lv[1], lv[1], gx[0]); PIN3(sa4, gw[0], gx[0]); }
          if constexpr (gp == 3) { sa4 = MM(lv2[1], oh[1], sa4); gw[1] = MM(lv2[1], lw[1], gw[1]); gx[1] = MM(lv2[1], lv2[1], gx[1]); PIN3(sa4, gw[1], gx[1]); }
          if constexpr (gp == 4) { sa4 = MM(ld[0], oh[2], sa4); sa4 = MM(ld[1], oh[2], sa4); PIN1(sa4); }
          if constexpr (gp == 5) { sa4 = MM(ld2[0], oh[3], sa4); sa4 = MM(ld2[1], oh[3], sa4); PIN1(sa4); }
          if constexpr (gp == 6) { sa4 = MM(lw[0], oh[4], sa4); sa4 = MM(lw[1], oh[4], sa4); PIN1(sa4);
            epoch = __builtin_amdgcn_readfirstlane(epv); tag = ((epoch + 1u) * 0x9E3779B1u ^ dmix) | 1u; }
        }
        if constexpr (jb == 2) {
          if constexpr (gp == 0) { su = (sa4[0] + sa4[1]) + (sa4[2] + sa4[3]); ta = diag(gw[0]); PIN2(su, ta); }
          if constexpr (gp == 1) { su = fold32(su, su); tb = diag(gw[1]); PIN2(su, tb); }
          if constexpr (gp == 2) { su = fold16(su, su); gs = fold32(ta, tb); PIN2(su, gs); }
          if constexpr (gp == 3) { ta = diag(gx[0]); tb = diag(gx[1]); PIN2(ta, tb); }
          if constexpr (gp == 4) { gs = fold16(gs, fold32(ta, tb)); PIN1(gs); }
          if constexpr (gp == 5) { gs += dpp_f<0xB1>(gs); gs += dpp_f<0x4E>(gs); PIN1(gs); }
          if constexpr (gp == 6) { gs += dpp_f<0x141>(gs); gs += dpp_f<0x140>(gs); PIN1(gs); }
        }
        if constexpr (jb == 3) {
          if constexpr (gp == 0) {
            const float s0 = rdl(su, 0), s1 = rdl(su, 1), q0 = rdl(gs, 16), q1 = rdl(gs, 48);
            sx = hb ? s1 : s0; ta = hb ? q1 : q0; PIN2(sx, ta);
          }
          if constexpr (gp == 1) {
            const float mean = sx * (1.0f / D_);
            mbv = rbf(mean);
            ta -= sx * mean;
            if (__builtin_expect(__any(!(sx * mean <= 16.f * ta)), 0)) {
              float q = fold32(sqdev16(lv, rdl(mbv, 0)), sqdev16(lv2, rdl(mbv, 32)));
              q = row_sum(fold16(q, q));
              const float dm = mean - mbv;
              ta = fmaxf(q - (float)D_ * dm * dm, 0.f);
            }
            PIN2(mbv, ta);
          }
          if constexpr (gp == 2) { ta = rbf(rbf(ta * (1.0f / D_)) + 1e-5f); gs -= mbv * rdl(su, 4); PIN2(ta, gs); }
          if constexpr (gp == 3) { ta = rbf(__builtin_amdgcn_sqrtf(ta)); tb = __builtin_amdgcn_rcpf(ta); PIN2(ta, tb); }
          if constexpr (gp == 4) {
            const unsigned rb = __float_as_uint(rbf(gs * tb));
            kl |= (rb ^ ((unsigned)((int)rb >> 31) | 0x80000000u)) & 0xffff0000u; PIN1(kl);
          }
          if constexpr (gp == 5) { kl = (lane & 2) ? __float_as_uint(su) : (lane & 1) ? kl : (__float_as_uint(mbv) >> 16) | __float_as_uint(ta); PIN1(kl); }
        }
        if constexpr (jb == ST_JB + 3) {
          if constexpr (gp == 0) { if (!__all(gr.y == tag)) gr = granule_wait((const u64*)((const char*)(flags + FL_AB) + gro), tag); PIN1(gr); }
          if constexpr (gp == 1) { g1 = (z ? -1.0f : 1.0f) / hi_bf(gr.x); PIN1(g1); }
          if constexpr (gp == 2) { g0 = pw < 2 ? -(lo_bf(gr.x) * g1) : __uint_as_float(gr.x); PIN1(g0); }
        }
        if constexpr (gp < 7 && (jb - 1) * 7 + gp < 64) {
          constexpr int n = (jb - 1) * 7 + gp;
          sld<((n >> 1) & 7) * 4096 + (n & 1) * 64>(sd, spb, KROT(n >> 4));
        }
        SB();
      };
      auto step1 = [&](auto JB) __attribute__((always_inline)) {
        constexpr int jb = decltype(JB)::value;
        if constexpr (jb == 1) PAGE(1);
        if constexpr (jb == 7) PAGE(2);
        constexpr int n16 = jb == 12 ? 12 : jb == 11 ? 14 : 16;
        constexpr auto wl = [](int q) { return (q == 3 ? 1 : 0) + (q == ST_JB ? 1 : 0); };
        if constexpr (jb == 1) {
          asm volatile("s_waitcnt vmcnt(%11)" : "+v"(lv[0]), "+v"(lv[1]), "+v"(lw[0]), "+v"(lw[1]), "+v"(ld[0]), "+v"(ld[1]),
                       "+v"(lv2[0]), "+v"(lv2[1]), "+v"(ld2[0]), "+v"(ld2[1]), "+v"(epv) : "i"(16) : "memory");
        } else if constexpr (jb == ST_JB + 3) {
          asm volatile("s_waitcnt vmcnt(%1)" : "+v"(gr) : "i"(n16 + wl(jb - 2) + wl(jb - 1)) : "memory");
        } else wait_vm<n16 + wl(jb - 3) + wl(jb - 2) + wl(jb - 1)>();
        BAR();
        if constexpr (jb == 2) { if (pw == 0 && lane < 16) ((int*)(smem + LDS_SEL))[lane < 8 ? lane : lane + 8] = 0; }
        if constexpr (jb == 4) { if (pw == 0 && lane == 0) ((unsigned*)(smem + LDS_SEL))[5] = tag; }
#define W1(i) work1(JB, IC<i>{})
        if constexpr (jb < 10) G1_STAGE_TILE(jb - 1 + G1_NST, ((jb - 1) % G1_NST) * G1_STAGE, W1);
        else G1_STAGE_A(jb - 1 + G1_NST, ((jb - 1) % G1_NST) * G1_STAGE, W1);
        if constexpr (jb == 12) { G1_STAGE_B(13, G1_STAGE); G1_STAGE_B(14, 2 * G1_STAGE); }
        if constexpr (jb == 3) __builtin_amdgcn_raw_buffer_store_b64((u32x2){kl, tag}, rf, go, 0, 16);
        if constexpr (jb == ST_JB) {
          asm volatile("buffer_load_dwordx2 %0, %1, %2, 0 offen sc1" : "=v"(gr) : "v"(gro), "s"(rf) : "memory");
        }
        touches(IC<100 + jb>{});
        if constexpr (jb <= 7) {
          if (et == jb - 1) {
#pragma unroll
            for (int l = 0; l < WSN; ++l) bld16_nt_io(wv[l], rw, vw, l * F_ * 2);
          }
        }
        SB();
      };
      static_assert(ST_JB + 3 <= 12 && ST_JB >= 6, "schedule");
      step1(IC<1>{}); step1(IC<2>{}); step1(IC<3>{}); step1(IC<4>{}); step1(IC<5>{}); step1(IC<6>{}); step1(IC<7>{}); step1(IC<8>{}); step1(IC<9>{}); step1(IC<10>{}); step1(IC<11>{}); step1(IC<12>{});
    }
#define G2_STAGE_N(kk_, W) do { const int kb = KROT(kk_); constexpr int so = n_off(kk_); SB(); \
    DMA_(rn2, vn2, mq0, so, kb, 0, ""); W(0); DMA_(rn2, vn2, mq1, so, kb, 32 * S_ * 2, ""); W(1); \
    DMA_(rn2, vn2, mq2, so, kb, 64 * S_ * 2, ""); W(2); DMA_(rn2, vn2, mq3, so, kb, 96 * S_ * 2, ""); W(3); \
    DMA_(rn2, vn2, mq0, so + 16384, kb, 128 * S_ * 2, ""); W(4); DMA_(rn2, vn2, mq1, so + 16384, kb, 160 * S_ * 2, ""); W(5); \
    DMA_(rn2, vn2, mq2, so + 16384, kb, 192 * S_ * 2, ""); W(6); DMA_(rn2, vn2, mq3, so + 16384, kb, 224 * S_ * 2, ""); W(7); SB(); } while (0)
#define G2_STAGE_NH(kk_, h) do { const int kb = KROT(kk_); constexpr int so = n_off(kk_) + (h) * 16384; SB(); \
    DMA_(rn2, vn2, mq0, so, kb, (h) * 128 * S_ * 2, ""); DMA_(rn2, vn2, mq1, so, kb, ((h) * 128 + 32) * S_ * 2, ""); \
    DMA_(rn2, vn2, mq2, so, kb, ((h) * 128 + 64) * S_ * 2, ""); DMA_(rn2, vn2, mq3, so, kb, ((h) * 128 + 96) * S_ * 2, ""); SB(); } while (0)
#define G2_STAGE_M(kk_, W) do { const int kb = KROT(kk_); constexpr int so = G2_MRING + m_off(kk_); SB(); \
    DMA_(rm2, vm2, mb0, so, kb, 0, " sc1"); W(8); DMA_(rm2, vm2, mb0, so + 4096, kb, 32 * 8192, " sc1"); W(9); \
    DMA_(rm2, vm2, mb0, so + 8192, kb, 64 * 8192, " sc1"); W(10); DMA_(rm2, vm2, mb0, so + 12288, kb, 96 * 8192, " sc1"); W(11); SB(); } while (0)
    WAIT_VM(16); BAR();
    asm volatile("ds_write_b32 %0, %1" :: "v"(lma), "v"(g0) : "memory");
    if (pw < 2) asm volatile("ds_write_b32 %0, %1 offset:512" :: "v"(lma), "v"(g1) : "memory");
    u32x4 tv[4];
    unsigned ready = 0;
    auto need = [&](int k) __attribute__((always_inline)) {
      const int b = k_blk(k) + (k_ord(k) & 1) * 8;
#pragma unroll 1
      for (int it = 0; !((ready >> b) & 1) && it < SPIN_MAX; ++it) {
        ready |= (unsigned)__ballot(__builtin_amdgcn_raw_buffer_load_b32(rf, abq[k < 5 ? 0 : k < 9 ? 1 : 2], 0, 16) == tag);
        asm volatile("" ::: "memory");
      }
    };
    unsigned ksel, bad = 0;
    int isel;
    unsigned xw;
    auto bvec = [&](unsigned src, const unsigned (&sl)[4]) __attribute__((always_inline)) {
      return (u32x4){__builtin_amdgcn_perm(0, src, sl[0]), __builtin_amdgcn_perm(0, src, sl[1]),
                     __builtin_amdgcn_perm(0, src, sl[2]), __builtin_amdgcn_perm(0, src, sl[3])};
    };
#define BC8(v, p) (unsigned)__builtin_amdgcn_update_dpp(__builtin_amdgcn_update_dpp(0, (int)(v), 0x150 + (p), 0xf, 0x3, false), (int)(v), 0x158 + (p), 0xf, 0xc, false)
    float* lg = (float*)(smem + LDS_GU);
    f32x4 macc = {0.f, 0.f, 0.f, 0.f};
    u32x2 sv;
    unsigned hp, gbad;
    float s0, s1;
    auto work2 = [&](auto JB, auto GP) __attribute__((always_inline)) {
      constexpr int jb = decltype(JB)::value, gp = decltype(GP)::value;
      SB();
      if constexpr (jb == -1 && gp < 4) {
        constexpr int c = gp == 0 ? 0xB1 : gp == 1 ? 0x4E : gp == 2 ? 0x141 : 0x140;
        merge2(k1, k2, dpp_u<c>(k1), dpp_u<c>(k2));
        PIN2(k1, k2);
      }
      if constexpr (jb == -1 && gp == 4) {
        ksel = tk ? k2 : k1;
        isel = (0xffff - (int)(ksel & 0xffffu)) & (S_ - 1);
        k1 = (isel * D_ + d0 + rq * 16 + (lane & 7) * 2) * 2; k2 = O_ST + isel * 8;
        PIN1(ksel);
        xw = gld4_s(x, k1);
        sv = bld8_sc1(rf, k2);
      }
      if constexpr (jb == 0 && gp == 1) wait_xs<3 + 1 + 2>(xw, sv);
      if constexpr (jb == 0) {
        if constexpr (gp == 1) { if (!__all(sv.y == tag)) sv = granule_wait(stat + isel, tag); s0 = __builtin_amdgcn_rcpf(hi_bf(sv.x)); PIN1(s0); }
        if constexpr (gp == 2) { s1 = ln_elem(lo_bf(xw), lo_bf(sv.x), hi_bf(sv.x), s0); PIN1(s1); }
        if constexpr (gp == 3) { xw = pk2(s1, ln_elem(hi_bf(xw), lo_bf(sv.x), hi_bf(sv.x), s0)); PIN1(xw); }
      }
      if constexpr (jb == DG && gp < 7) { PIN1(dv[gp]); macc = MM(dv[gp], bvec(BC8(hp, gp), sel[0]), macc); PIN1(macc); }
      if constexpr ((jb == 0 && gp >= 4) || jb == 1) {
        constexpr int l = jb == 0 ? gp - 4 : gp + 4;
        if constexpr (!(l & 1)) hp = BC8(xw, l >> 1);
        PIN1(wv[l]);
        macc = MM(wv[l], bvec(hp, sel[l & 1]), macc);
        PIN2(macc, hp);
      }
      if constexpr (jb == GV_JB + 2) {
        if constexpr (gp == 0) { gbad = (gv[0].y ^ tag) | (gv[0].w ^ tag) | (gv[1].y ^ tag) | (gv[1].w ^ tag); PIN1(gbad); }
        if constexpr (gp == 1) { gbad |= (gv[2].y ^ tag) | (gv[2].w ^ tag) | (gv[3].y ^ tag) | (gv[3].w ^ tag); PIN1(gbad); }
        if constexpr (gp == 2) {
          s0 = ((__uint_as_float(gv[0].x) + __uint_as_float(gv[1].x)) + __uint_as_float(gv[2].x)) + __uint_as_float(gv[3].x);
          s1 = ((__uint_as_float(gv[0].z) + __uint_as_float(gv[1].z)) + __uint_as_float(gv[2].z)) + __uint_as_float(gv[3].z);
          PIN2(s0, s1);
        }
        if constexpr (gp == 3) {
#pragma unroll 1
          for (int it = 0; !__all(gbad == 0) && it < SPIN_MAX; ++it) {
            u32x4 q[4];
            gbad = 0;
#pragma unroll
            for (int i = 0; i < 4; ++i) { q[i] = __builtin_amdgcn_raw_buffer_load_b128(rgu, vgu, i * F_ * 16, 16); gbad |= (q[i].y ^ tag) | (q[i].w ^ tag); }
            s0 = ((__uint_as_float(q[0].x) + __uint_as_float(q[1].x)) + __uint_as_float(q[2].x)) + __uint_as_float(q[3].x);
            s1 = ((__uint_as_float(q[0].z) + __uint_as_float(q[1].z)) + __uint_as_float(q[2].z)) + __uint_as_float(q[3].z);
            asm volatile("" ::: "memory");
          }
          const unsigned gp2 = pk2(s0, s1);
          const unsigned up2 = dpp_u<0x128>(gp2);
          s0 = tk ? hi_bf(up2) : lo_bf(gp2); s1 = tk ? hi_bf(gp2) : lo_bf(up2);
          PIN2(s0, s1);
        }
        if constexpr (gp == 4) { s0 = rbf(s0 * __builtin_amdgcn_rcpf(1.f + __expf(-s0))); PIN1(s0); }
        if constexpr (gp == 5) { hp = pk2(s0 * s1, 0.f); PIN1(hp); }
      }
      if constexpr (jb >= 3 && jb <= 10 && gp >= 4) {
        constexpr int n = (jb - 3) * 8 + gp - 4;
        sld<((n >> 1) & 7) * 4096 + (n & 1) * 64>(sd, spb, KROT(12 + (n >> 4)));
      }
      if constexpr (jb == 10 && gp == 2) epi_setup();
      if constexpr (jb == 10 && gp == 4) code_touch(sd, (j * 4 + pw) * 256);
      SB();
    };
#define W2M(i) work2(IC<-1>{}, IC<i>{})
#define W20(i) work2(IC<0>{}, IC<i>{})
#define W21(i) work2(IC<1>{}, IC<i>{})
    unsigned fs;
    {
      G1_STAGE_B(15, 3 * G1_STAGE);
      G2_STAGE_NH(0, 0);
      lgkm0(sd);
      WAIT_VM(16); BAR();
      G2_STAGE_NH(0, 1);
      WAIT_VM(10); BAR();
#pragma unroll
      for (int i = 0; i < 4; ++i) kk[i] = bld16_sc1(rf, l16, ko_ + i * 1024);
#pragma unroll
      for (int l = WSN; l < 16; ++l) wv[l] = bld16_nt(rw, vw, l * F_ * 2);
      asm volatile("s_waitcnt vmcnt(%4)" : "+v"(kk[0]), "+v"(kk[1]), "+v"(kk[2]), "+v"(kk[3]) : "i"(16 - WSN) : "memory");
      touches(IC<115>{});
#pragma unroll
      for (int i = 0; i < 4; ++i) {
        k2 = max(k2, min(k1, kk[i].x)); k1 = max(k1, kk[i].x);
        k2 = max(k2, min(k1, kk[i].z)); k1 = max(k1, kk[i].z);
        bad |= (kk[i].y ^ tag) | (kk[i].w ^ tag);
      }
#pragma unroll 1
      for (int it = 0; !__all(bad == 0) && it < SPIN_MAX; ++it) {
        k1 = 0; k2 = 0; bad = 0;
#pragma unroll
        for (int i = 0; i < 4; ++i) {
          const u32x4 q = __builtin_amdgcn_raw_buffer_load_b128(rf, l16, ko_ + i * 1024, 16);
          k2 = max(k2, min(k1, q.x)); k1 = max(k1, q.x);
          k2 = max(k2, min(k1, q.z)); k1 = max(k1, q.z);
          bad |= (q.y ^ tag) | (q.w ^ tag);
        }
        asm volatile("" ::: "memory");
      }
      merge2(k1, k2, dpp_u<0xB1>(k1), dpp_u<0xB1>(k2));
      merge2(k1, k2, dpp_u<0x4E>(k1), dpp_u<0x4E>(k2));
      merge2(k1, k2, dpp_u<0x141>(k1), dpp_u<0x141>(k2));
      merge2(k1, k2, dpp_u<0x140>(k1), dpp_u<0x140>(k2));
      if ((lane & 15) == 0) *(u32x2*)(smem + LDS_KEY + (pw * 4 + rq) * 8) = (u32x2){k1, k2};
      PAGE(3);
      BAR_LGKM();
#pragma unroll
      for (int jj = 0; jj < 4; ++jj) tv[jj] = *(const u32x4*)(smem + G2_MRING + m_off(0) + (jj * 4 + pw) * 1024 + lane * 16);
      { const u32x2 kp = *(const u32x2*)(smem + LDS_KEY + ci * 8); k1 = kp.x; k2 = kp.y; }
#pragma unroll
      for (int jj = 0; jj < 4; ++jj) __builtin_amdgcn_raw_buffer_store_b128(tv[jj], rm2, vm2 + jj * 32 * 8192 + 2 * tt * 128, 0, 16);
      G2_STAGE_N(1, W2M);
      WAIT_VM(10);
      if (lane == tt) __builtin_amdgcn_raw_buffer_store_b32(tag, rf, abo, 0, 16);
      G2_STAGE_N(2, W20);
      __builtin_amdgcn_s_sleep(2);
      WAIT_VM(9);
      fs = bld4_sc1(rf, abq[0]);
    }
    BAR();
#pragma unroll
    for (int jj = 0; jj < 4; ++jj) tv[jj] = *(const u32x4*)(smem + G2_MRING + m_off(1) + (jj * 4 + pw) * 1024 + lane * 16);
    G2_STAGE_N(3, NOW);
    asm volatile("s_waitcnt vmcnt(8)" : "+v"(fs) :: "memory");
    ready = (unsigned)__ballot(fs == tag);
    need(2);
    G2_STAGE_M(2, NOW);
    need(3);
    G2_STAGE_M(3, NOW);
    fs = bld4_sc1(rf, abq[1]);
    touches(IC<1>{});
    W21(0); W21(1); W21(2); W21(3); W21(4); W21(5); W21(6); W21(7); W21(8); W21(9); W21(10); W21(11);
    asm volatile("" : "+v"(tv[0]), "+v"(tv[1]), "+v"(tv[2]), "+v"(tv[3]));
    auto step2 = [&](auto JB) __attribute__((always_inline)) {
      constexpr int jb = decltype(JB)::value;
      if constexpr (jb == 3) PAGE(4);
      if constexpr (jb == 9) PAGE(5);
      constexpr auto must = [](int q) { return (q == 2 ? 4 : 0) + ((FSM >> q) & 1) + ((q == GV_JB || q == GV_JB - 1) ? 2 : 0); };
      if constexpr (jb == 2) wait_vm<4 + must(1)>();
      else wait_vm<dn_cnt(jb - 2) + (jb == 5 ? 1 : 0) + 12 + must(jb - 1) + dn_cnt(jb - 1)>();
      if constexpr (jb == 3) { if (pw == 0 && (lane & ~8) == 0) ((unsigned*)(smem + LDS_SEL))[tk] = ksel; }
      if constexpr ((jb == 2 || jb == DG + 1)) {
        *(f32x4*)(lg + pw * 256 + lane * 4) = macc;
        macc = (f32x4){0.f, 0.f, 0.f, 0.f};
        lgkm0(sd);
      }
      BAR();
      float p0[4], p1[4];
      const int col = (pw & 1) * 64 + lane;
      if constexpr (jb == 3) {
        if (g == 0 && pw == 0 && lane == 0) __hip_atomic_store((unsigned*)flags + FL_EPOCH, epoch + 1u, __ATOMIC_RELAXED, __HIP_MEMORY_SCOPE_AGENT);
      }
      if constexpr (jb == DG + 1) {
        const float* p = lg + (pw & 1) * 32 + (lane >> 4) * 64 + ((2 * lane) & 7) * 4 + ((lane >> 2) & 3);
#pragma unroll
        for (int i = 0; i < 4; ++i) { p0[i] = p[i * 256]; p1[i] = p[i * 256 + 4]; }
      }
      if constexpr (jb == 2) {
        const float* p = lg + (col >> 5) * 64 + (col & 7) * 4 + ((col >> 3) & 3);
#pragma unroll
        for (int i = 0; i < 4; ++i) { p0[i] = p[i * 256]; p1[i] = p[i * 256 + 32]; }
      }
      if constexpr (jb == 4) {
        if (lane == 8 + tt) __builtin_amdgcn_raw_buffer_store_b32(tag, rf, abo, 0, 16);
      }
      if constexpr ((FSM >> (jb - 2)) & 1) {
        asm volatile("" : "+v"(fs));
        ready |= (unsigned)__ballot(fs == tag);
      }
      if constexpr (jb == GV_JB + 2) asm volatile("" : "+v"(gv[0]), "+v"(gv[1]), "+v"(gv[2]), "+v"(gv[3]));
#define W2(i) work2(JB, IC<i>{})
      G2_STAGE_N(jb + 2, W2);
      need(jb + 2);
      G2_STAGE_M(jb + 2, W2);
      if constexpr (jb == 2) {
#pragma unroll
        for (int jj = 0; jj < 4; ++jj) __builtin_amdgcn_raw_buffer_store_b128(tv[jj], rm2, vm2 + jj * 32 * 8192 + (2 * tt + 1) * 128, 0, 16);
      }
      if constexpr ((FSM >> jb) & 1) fs = bld4_sc1(rf, abq[jb < 5 ? 1 : 2]);
      if constexpr ((jb == GV_JB || jb == GV_JB - 1)) {
#pragma unroll
        for (int i = (jb == GV_JB ? 2 : 0); i < (jb == GV_JB ? 4 : 2); ++i) gv[i] = bld16_sc1(rgu, vgu, i * F_ * 16);
      }
      touches(JB);
#pragma unroll
      for (int i = dn_first(jb); i < dn_first(jb) + dn_cnt(jb); ++i) dv[i] = bld16_nt(rdn, vdn, i * D_ * 2);
      if constexpr (jb == 2) asm volatile("" : "+v"(p0[0]), "+v"(p0[1]), "+v"(p0[2]), "+v"(p0[3]), "+v"(p1[0]), "+v"(p1[1]), "+v"(p1[2]), "+v"(p1[3]));
      if constexpr (jb == DG + 1) {
        if (pw < 2)
          __builtin_amdgcn_raw_buffer_store_b128((u32x4){__float_as_uint((p0[0] + p0[1]) + (p0[2] + p0[3])), tag, __float_as_uint((p1[0] + p1[1]) + (p1[2] + p1[3])), tag},
                                                 mk_rsrc(wsp + WS_DN), (((pw * 8 + cg8) * 32 + rgb) * 64 + lane) * 16, 0, 16);
      }
      if constexpr (jb == 2) {
        if (moe && pw < 2)
          __builtin_amdgcn_raw_buffer_store_b128((u32x4){__float_as_uint((p0[0] + p0[1]) + (p0[2] + p0[3])), tag, __float_as_uint((p1[0] + p1[1]) + (p1[2] + p1[3])), tag},
                                                 rgu, ((mat * 4 + dq) * F_ + cg * 128 + col) * 16, 0, 16);
      }
      SB();
    };
    static_assert(DG + 1 <= 12 && DG >= GV_JB + 3 && dn_first(DG - 2) == 7, "schedule");
    step2(IC<2>{}); step2(IC<3>{}); step2(IC<4>{}); step2(IC<5>{}); step2(IC<6>{}); step2(IC<7>{}); step2(IC<8>{}); step2(IC<9>{}); step2(IC<10>{}); step2(IC<11>{}); step2(IC<12>{}); step2(IC<13>{});
    lgkm0(sd);
    PAGE(7);
    WAIT_VM(12); BAR();
    xq[0] = ld16_s<0>(x, xo); xq[1] = ld16_s<64>(x, xo); sq = ld8_s_sc1(stat, xo >> 11 << 3);
    WAIT_VM(3); BAR();
    asm volatile("s_waitcnt vmcnt(0)" : "+v"(xq[0]), "+v"(xq[1]), "+v"(sq) :: "memory");
    __builtin_amdgcn_s_setprio(0);
    key[0] = __builtin_amdgcn_readlane(ksel, 0); key[1] = __builtin_amdgcn_readlane(ksel, 8);
    nf_pre();
#pragma unroll
    for (int n = 0; n < 56; ++n) nf_step(n);
#pragma unroll
    for (int e = 0; e < 16; ++e) nf_unp(e);
#pragma unroll 1
    for (int c = 0; c < 2; ++c) {
      const unsigned kc = __builtin_amdgcn_readlane(ksel, c * 8);
      const int ic = (0xffff - (int)(kc & 0xffffu)) & (S_ - 1);
      if ((ic >> 8) != tt || ((ic >> 5) & 3) != kq) continue;
      const __amdgpu_buffer_rsrc_t rd = mk_rsrc(wsp + WS_DN + (size_t)(c * 8 + et) * 32768);
#pragma unroll 1
      for (int it = 0; it < SPIN_MAX; ++it) {
        if (__all(__builtin_amdgcn_raw_buffer_load_b32(rd, (lane & 31) * 1024 + 1020, 0, 16) == tag)) break;
        __builtin_amdgcn_s_sleep(2);
        asm volatile("" ::: "memory");
      }
      float s0, s1;
      unsigned bad = 1;
#pragma unroll 1
      for (int it = 0; !__all(bad == 0) && it < SPIN_MAX; ++it) {
        bad = 0; s0 = 0.f; s1 = 0.f;
#pragma unroll
        for (int i = 0; i < 8; ++i) {
          const u32x4 q = __builtin_amdgcn_raw_buffer_load_b128(rd, rq * 1024 + pw * 256 + ci * 16, i * 4096, 16);
          bad |= (q.y ^ tag) | (q.w ^ tag); s0 += __uint_as_float(q.x); s1 += __uint_as_float(q.z);
        }
        asm volatile("" ::: "memory");
      }
      s0 = fold32(s0, s1);
      s0 = fold16(s0, s0);
      const float sg = rbf(1.f / (1.f + __expf(-key_score(kc))));
      ((float*)(smem + LDS_FIN))[c * 128 + pw * 32 + ci * 2 + (lane >> 5)] = rbf(rbf(s0) * sg);
      asm volatile("s_waitcnt lgkmcnt(0)" ::: "memory");
      if (lane == 0) asm volatile("ds_add_u32 %0, %1" :: "v"(lds_addr(smem + LDS_SEL + 24 + c * 4)), "v"(1) : "memory");
    }
    asm volatile("v_mov_b32 %0, 0" : "=v"(fx), "=v"(ya[0]), "=v"(ya[1]), "=v"(ya[2]), "=v"(ya[3]));
  } else {
  {
    const int q = j * 4 + wid;
    constexpr int Q1 = WS_MB, Q2 = Q1 + 8, Q3 = Q2 + 8, Q4 = Q3 + 7, Q5 = Q4 + 7, Q6 = Q5 + 7, Q7 = Q6 + 4, Q8 = Q7 + 4, QC = Q8 + 1;
    static_assert(QC <= 128, "warm-up slots");
    unsigned dmy = 0;
    if (q < QC) {
      const char* base; int b0;
      if (q < Q1) { base = wsp; b0 = 0; }
      else if (q < Q2) { base = (const char*)ws_re; b0 = Q1; }
      else if (q < Q3) { base = (const char*)ws_im; b0 = Q2; }
      else if (q < Q4) { base = (const char*)w_gate; b0 = Q3; }
      else if (q < Q5) { base = (const char*)w_up; b0 = Q4; }
      else if (q < Q6) { base = (const char*)w_down; b0 = Q5; }
      else if (q < Q7) { base = (const char*)out; b0 = Q6; }
      else if (q < Q8) { base = (const char*)x; b0 = Q7; }
      else { base = (const char*)w_r; b0 = Q8; }
      base += (size_t)(q - b0) << 20;
      asm volatile("s_load_dword %0, %1, 0" : "+s"(dmy) : "s"(base) : "memory");
    }
    {
      const int w = g * 4 + wid;
      constexpr int R = (int)(WS_FLAGS - WS_DN);
      static_assert(FL_X == 0 && FL_ST == 6144 * 32 && FL_EPOCH == 6528 * 32, "");
      const int o = lane < 6 ? R + (lane * 1024 + w) * 128 : lane > 6 || w >= 896 ? (int)0x80000000 : w < 512 ? w * 1024 + 896 : R + (6144 - 512 + w) * 128;
      asm volatile("s_mov_b32 m0, %0\n\ts_nop 0\n\tbuffer_load_dword %1, %2, 0 offen sc1 lds"
                   :: "s"(lds_addr(smem + LDS_DUMMY) + wid * 1024 + 512), "v"(o), "s"(mk_rsrc(wsp + WS_DN)) : "memory");
    }
    if (wid == 0) { CALL_PG(1); CALL_PG(8); CALL_PG(5); }
    else if (wid == 1) { CALL_PG(2); CALL_PG(9); CALL_PG(6); }
    else if (wid == 2) { CALL_PG(3); CALL_PG(10); CALL_PG(7); }
    else { CALL_PG(4); CALL_PG(11); }
    asm volatile("s_barrier\n\ts_waitcnt lgkmcnt(0)" : "+s"(dmy) :: "memory");
  }
  PAGE(8);
  epi_setup();
  const int wr = wid >> 1, wc = wid & 1;
  const int sw = (fr >> 1) & 7;
  const int ko0 = ((0 + fq) ^ sw) * 16, ko1 = ((4 + fq) ^ sw) * 16;
  const int mrow = (fr >> 2) * 8 + (fr & 3);
  bf16x8 fm[2][4], fn[2][4];
  {
    const char* pm = smem + (wr * 64 + mrow) * 128;
    const char* pn = smem + 16384 + (wc * 64 + mrow) * 128;
    f32x4 acc[4][4];
#pragma unroll
    for (int i = 0; i < 4; ++i)
#pragma unroll
      for (int jn = 0; jn < 4; ++jn)
#pragma unroll
        for (int q = 0; q < 4; ++q) acc[i][jn][q] = 0.f;
#define TO(i) (((i) >> 1) * 4096 + ((i) & 1) * 512)
#define LDSP(T, a) ((__attribute__((address_space(3))) T*)(size_t)(a))
#define SLOTS(n) _Pragma("unroll") for (int q = 0; q < (n); ++q)
#define MFA(c, a, b) asm volatile("v_mfma_f32_16x16x32_bf16 %0, %1, %2, %0" : "+v"(c) : "v"(a), "v"(b))
#define MFA2(c, a, b, a2, b2) asm volatile("v_mfma_f32_16x16x32_bf16 %0, %1, %2, %0\n\tv_mfma_f32_16x16x32_bf16 %0, %3, %4, %0" : "+v"(c) : "v"(a), "v"(b), "v"(a2), "v"(b2))
    unsigned lm[2][2], ln[2][2];
#pragma unroll
    for (int u = 0; u < 2; ++u)
#pragma unroll
      for (int kk = 0; kk < 2; ++kk) {
        lm[u][kk] = lds_addr(pm) + u * 2 * G1_STAGE + (kk ? ko1 : ko0);
        ln[u][kk] = lds_addr(pn) + u * 2 * G1_STAGE + (kk ? ko1 : ko0);
        asm volatile("" : "+v"(lm[u][kk]), "+v"(ln[u][kk]));
      }
    auto ldm = [&](int stg, int i, int kk) __attribute__((always_inline)) { fm[kk][i] = *LDSP(const bf16x8, lm[stg >> 1][kk] + (stg & 1) * G1_STAGE + TO(i)); };
    auto ldn = [&](int stg, int jn, int kk) __attribute__((always_inline)) { fn[kk][jn] = *LDSP(const bf16x8, ln[stg >> 1][kk] + (stg & 1) * G1_STAGE + TO(jn)); };
    auto quad = [&](int a, int c, int d, int s) __attribute__((always_inline)) {
      const int i = (s < 2 || s >= 6) ? a : a + 1, jn = s < 4 ? c : d, kk = (s == 1 || s == 2 || s == 5 || s == 6);
      MFA(acc[i][jn], fm[kk][i], fn[kk][jn]);
    };
    auto quad2 = [&](int a, int c, int d, int s) __attribute__((always_inline)) {
      const int i = (s < 2 || s >= 6) ? a : a + 1, jn = s < 4 ? c : d, kk = (s == 2 || s == 6);
      MFA2(acc[i][jn], fm[kk][i], fn[kk][jn], fm[1 - kk][i], fn[1 - kk][jn]);
    };
    auto half1 = [&](int stg) __attribute__((always_inline)) {
      const int ja = (stg & 1) * 2, jb = 2 - ja;
      SLOTS(8) {
        quad(0, ja, ja + 1, q);
        if (q < 4) ldn(stg, jb + (q >> 1), q & 1); else ldm(stg, 2 + ((q - 4) >> 1), q == 5 || q == 6);
        SB();
      }
      SLOTS(4) { quad2(0, jb, jb + 1, 2 * q); SB(); }
    };
    auto half2 = [&](int stg) __attribute__((always_inline)) {
      const int ja = (stg & 1) * 2, jb = 2 - ja, nx = (stg + 1) & 3;
      SLOTS(8) {
        quad(2, jb + 1, jb, q);
        if (!(q & 1)) ldm(nx, q >> 2, q == 2 || q == 4);
        SB();
      }
      SLOTS(8) {
        quad(2, ja, ja + 1, q);
        if (!(q & 1)) ldn(nx, jb + (q >> 2), (q >> 1) & 1);
        SB();
      }
    };
    SLOTS(16) asm volatile("" : "+v"(acc[q >> 2][q & 3]));
    SB();
    SLOTS(4) { ldm(0, q >> 1, q & 1); ldn(0, q >> 1, q & 1); }
    SB();
#pragma unroll 1
    for (int k4 = 0; k4 < G1_NKT - 4; k4 += 4) {
      half1(0); BAR_LGKM();
      SB(); half2(0);
      half1(1); BAR_LGKM();
      SB(); half2(1);
      half1(2); BAR_LGKM();
      SB(); half2(2);
      half1(3); BAR_LGKM();
      SB(); half2(3);
    }
    bf16x8 gm[2][4], gn[2][8];
    unsigned am[2][2], an[2][2];
#pragma unroll
    for (int u = 0; u < 2; ++u)
#pragma unroll
      for (int kk = 0; kk < 2; ++kk) {
        am[u][kk] = lds_addr(pm) + (u ? 3 : 1) * G1_STAGE + (kk ? ko1 : ko0);
        an[u][kk] = lds_addr(pn) + (u ? 3 : 1) * G1_STAGE + (kk ? ko1 : ko0);
        asm volatile("" : "+v"(am[u][kk]), "+v"(an[u][kk]));
      }
    unsigned al = lds_addr(smem + LDS_MB) + (wr * 32 + fq * 8) * 4, ac = lds_addr(smem + LDS_MB) + (256 + wc * 64 + mrow) * 4;
    unsigned ae = lds_addr(smem + G2_MRING) + (wc * 64 + mrow) * 128 + (((wr * 4 + fq) ^ sw) * 16);
    asm volatile("" : "+v"(al), "+v"(ac), "+v"(ae));
    auto rdg = [&](int set, int t, int h, int r) __attribute__((always_inline)) {
      const int u = t == 15, o = t == 14 ? G1_STAGE : 0;
      if (r == 0 || r == 2 || r == 4 || r == 5) {
        const int i = r >= 4, kk = r == 2 || r == 4;
        gm[set][kk * 2 + i] = *LDSP(const bf16x8, am[u][kk] + o + h * 4096 + i * 512);
      } else {
        const int jn = r < 4 ? 0 : (r - 4) >> 1, kk = r < 4 ? r >> 1 : r & 1;
        gn[set][kk * 4 + jn] = *LDSP(const bf16x8, an[u][kk] + o + TO(jn));
      }
    };
    auto mmg = [&](int set, int h, int q) __attribute__((always_inline)) {
      const int jn = q >> 2, r = q & 3, i = (r >> 1) ^ (jn & 1), kk = r == 1 || r == 2;
      MFA(acc[h * 2 + i][jn], gm[set][kk * 2 + i], gn[set][kk * 4 + jn]);
    };
    f32x4 rsv[2], mrv[2];
    float cev[4], tc[32];
    auto rds = [&](int h, int r) __attribute__((always_inline)) {
      if (r < 2) mrv[r] = *LDSP(const f32x4, al + h * 256 + r * 16); else rsv[r - 2] = *LDSP(const f32x4, al + 512 + h * 256 + (r - 2) * 16);
    };
    auto prep = [&](int n) __attribute__((always_inline)) {
      tc[n] = llvm_fmul_legacy(mrv[(n >> 2) & 1][n & 3], cev[n >> 3]);
      asm volatile("" : "+v"(tc[n]));
    };
    u32x4 ep;
    auto epi = [&](int h, int q) __attribute__((always_inline)) {
      const int jn = q >> 1, i = q & 1;
      asm volatile("v_fmac_f32 %0, %4, %8\n\tv_fmac_f32 %1, %5, %9\n\tv_fmac_f32 %2, %6, %10\n\tv_fmac_f32 %3, %7, %11"
                   : "+v"(tc[q * 4]), "+v"(tc[q * 4 + 1]), "+v"(tc[q * 4 + 2]), "+v"(tc[q * 4 + 3])
                   : "v"(acc[h * 2 + i][jn][0]), "v"(acc[h * 2 + i][jn][1]), "v"(acc[h * 2 + i][jn][2]), "v"(acc[h * 2 + i][jn][3]),
                     "v"(rsv[i][0]), "v"(rsv[i][1]), "v"(rsv[i][2]), "v"(rsv[i][3]));
      ep[i * 2] = pk2(tc[q * 4], tc[q * 4 + 1]); ep[i * 2 + 1] = pk2(tc[q * 4 + 2], tc[q * 4 + 3]);
      if (i) *LDSP(u32x4, ae + m_off(h) + TO(jn)) = ep;
    };
    SB();
    half1(0);
    BAR_LGKM();
    SB();
    SLOTS(16) {
      if (q < 8) quad(2, 3, 2, q); else quad(2, 0, 1, q - 8);
      if (q < 12) rdg(0, 13, 0, q);
      SB();
    }
    BAR();
    SB();
    SLOTS(16) {
      mmg(0, 0, q);
      if (q < 12) rdg(1, 14, 0, q);
      else { rds(0, q - 12); cev[q - 12] = *LDSP(const float, ac + (((q - 12) >> 1) * 32 + ((q - 12) & 1) * 4) * 4); }
      SB();
    }
    PAGE(9);
    BAR();
    SB();
    SLOTS(16) { mmg(1, 0, q); if ((q & 3) < 3) rdg(0, 15, 0, (q >> 2) * 3 + (q & 3)); if (q >= 8) { prep(2 * q - 16); prep(2 * q - 15); } SB(); }
    SLOTS(16) { mmg(0, 0, q); if ((q & 3) < 3) rdg(1, 13, 1, (q >> 2) * 3 + (q & 3)); prep(16 + q); SB(); if (q >= 9 && (q & 1)) { epi(0, (q - 9) >> 1); SB(); } }
    SLOTS(8) { mmg(1, 1, q); if (q < 6) { rdg(0, 14, 1, 2 * q); rdg(0, 14, 1, 2 * q + 1); } SB(); if (q & 1) { epi(0, 4 + (q >> 1)); SB(); } }
    BAR_LGKM();
    SB();
    SLOTS(8) { mmg(1, 1, 8 + q); if (q < 4) rdg(1, 15, 1, q == 0 ? 1 : q == 1 ? 3 : q + 4); else rds(1, q - 4); SB(); }
    SLOTS(16) { mmg(0, 1, q); if (q < 8) rdg(1, 15, 1, q == 0 ? 0 : q == 1 ? 2 : q < 4 ? q + 2 : q + 4); prep(q); SB(); }
    asm volatile("s_waitcnt lgkmcnt(0)" ::: "memory");
    if (lane == 0) asm volatile("ds_add_u32 %0, %1" :: "v"(lds_addr(smem + LDS_SEL + 12)), "v"(1) : "memory");
    SB();
    SLOTS(16) {
      mmg(1, 1, q);
      if (q < 4) fn[0][q] = *(const bf16x8*)(smem + (wr * 128 + fr) * 128 + (q >> 1) * 2048 + ((q & 1) ? ko1 : ko0));
      else if (q < 8) fm[q & 1][(q - 4) >> 1] = *(const bf16x8*)(smem + G2_MRING + m_off(0) + (wc * 64 + mrow) * 128 + TO((q - 4) >> 1) + ((q & 1) ? ko1 : ko0));
      prep(16 + q);
      SB();
    }
    {
      lds_vi rel = (lds_vi)(smem + LDS_SEL + 12);
      int it = 0;
      while (rel[0] != 4 && it < SPIN_MAX * 8) ++it;
      asm volatile("" ::: "memory");
    }
    asm volatile("s_nop 7");
    SB();
    SLOTS(8) { epi(1, q); SB(); }
  }

  const char* pn = smem + (wr * 128 + fr) * 128;
  const char* pm = smem + G2_MRING + (wc * 64 + mrow) * 128;
  f32x4 acc[8][4];
  auto mm = [&](bool imaj, int set, int q2, int s, int mode) __attribute__((always_inline)) {
    int i, jj, kk;
    if (imaj) { i = s >> 2; const int r = s & 3; jj = (r >> 1) ^ (i & 1); kk = (r == 1 || r == 2); }
    else { jj = s >> 3; const int r = s & 7; i = jj ? 3 - (r >> 1) : (r >> 1); kk = ((r + 1) >> 1) & 1; }
    if (mode == 1 && !(s & 1)) asm volatile("v_mfma_f32_16x16x32_bf16 %0, %1, %2, 0" : "=&v"(acc[q2 * 2 + jj][i]) : "v"(fm[kk][i]), "v"(fn[set][jj * 2 + kk]));
    else if (mode) asm volatile("v_mfma_f32_16x16x32_bf16 %0, %1, %2, %0" : "+v"(acc[q2 * 2 + jj][i]) : "v"(fm[kk][i]), "v"(fn[set][jj * 2 + kk]));
    else acc[q2 * 2 + jj][i] = __builtin_amdgcn_mfma_f32_16x16x32_bf16(fm[kk][i], fn[set][jj * 2 + kk], acc[q2 * 2 + jj][i], 0, 0, 0);
  };
  unsigned bn[2], bm[2], an[2], am[2];
#pragma unroll
  for (int kk = 0; kk < 2; ++kk) {
    bn[kk] = lds_addr(pn) + (kk ? ko1 : ko0); bm[kk] = lds_addr(pm) + (kk ? ko1 : ko0);
    asm volatile("" : "+v"(bn[kk]), "+v"(bm[kk]));
    an[kk] = bn[kk]; am[kk] = bm[kk] + m_off(0);
  }
#define RDN(jt, kk, o) (*LDSP(const bf16x8, an[kk] + (o) + (jt) * 2048))
#define RDM(i, kk, o) (*LDSP(const bf16x8, am[kk] + (o) + TO(i)))
  PAGE(10);
  tag = __builtin_amdgcn_readfirstlane(((const unsigned*)(smem + LDS_SEL))[5]);
  {
    int son = 0;
    auto tile = [&](int kt, int zero) __attribute__((always_inline)) {
      SLOTS(16) {
        mm(true, 0, 0, q, zero);
        if (!(q & 1)) {
          if (q < 8) fm[(q >> 1) & 1][2 + (q >> 2)] = RDM(2 + (q >> 2), (q >> 1) & 1, 0);
          else { const int r = (q - 8) >> 1; fn[1][r] = RDN(2 + (r >> 1), r & 1, 0); }
        }
        SB();
      }
      SLOTS(16) {
        mm(false, 1, 1, q, zero);
        if (!(q & 1) && q < 8) fn[0][q >> 1] = RDN(4 + (q >> 2), (q >> 1) & 1, 0);
        SB();
      }
      const int som = son >> 1;
      son = son == 65536 ? 0 : son + 32768;
      SLOTS(16) {
        mm(false, 0, 2, q, zero);
        if (!(q & 1) && q < 8) fn[1][q >> 1] = RDN(6 + (q >> 2), (q >> 1) & 1, 0);
        if (q == 9 || q == 11) { an[q == 11] = bn[q == 11] + son; asm volatile("" : "+v"(an[q == 11])); }
        if (q == 13 || q == 15) { am[q == 15] = bm[q == 15] + som; asm volatile("" : "+v"(am[q == 15])); }
        SB();
      }
      BAR_LGKM();
      SB();
      SLOTS(16) {
        mm(true, 1, 3, q, zero);
        if (!(q & 1)) {
          if (q < 8) fn[0][q >> 1] = RDN(q >> 2, (q >> 1) & 1, 0);
          else fm[(q >> 1) & 1][(q - 8) >> 2] = RDM((q - 8) >> 2, (q >> 1) & 1, 0);
        }
        SB();
      }
    };
    SB();
    tile(0, 1);
#pragma unroll 1
    for (int kt = 1; kt < G2_NKT - 3; ++kt) tile(kt, 0);
  }
  {
    constexpr int NG = 12;
    static constexpr int g_t[NG] = {0, 0, 1, 1, 2, 2, 2, 1, 0, 0, 1, 2};
    static constexpr int g_q[NG] = {0, 1, 0, 1, 0, 1, 2, 2, 2, 3, 3, 3};
    constexpr int G14 = 1, G15 = 3, GP0 = 5, GP1 = 6;
    constexpr auto chp = [](int g) { return g == 0 || g_t[g] != g_t[g - 1]; };
    constexpr auto chn = [](int g) { return g + 1 < NG && g_t[g + 1] != g_t[g]; };
    static_assert(n_off(13) == 32768 && n_off(14) == 65536 && n_off(15) == 0, "");
    unsigned an2[2];
#pragma unroll
    for (int kk = 0; kk < 2; ++kk) {
      an[kk] = bn[kk]; an2[kk] = bn[kk] + 65536; am[kk] = bm[kk];
      asm volatile("" : "+v"(an[kk]), "+v"(an2[kk]), "+v"(am[kk]));
    }
    int vo;
    u32x4 pk;
    u32x2 kv;
    constexpr int NF0 = 152;
    auto publish = [&](int d, int h) __attribute__((always_inline)) {
      if (h == 0) { vo = lane; PINV(vo); vo = vo * 16 + (((d + kq + 1) & 3) * 4 + kq) * 4 * MSG; PINV(vo); }
      const f32x4 v = acc[2 * d + (h >> 2)][h & 3];
      pk[(h & 1) * 2] = pk2(v[0], v[1]); pk[(h & 1) * 2 + 1] = pk2(v[2], v[3]);
      if (h & 1) __builtin_amdgcn_raw_buffer_store_b128(pk, rs, vo + (h >> 1) * 1024, 0, 16);
    };
    int fo = lane == 0 ? kq * 512 : (int)0x80000000;
#define MSG_FLAG(d) __builtin_amdgcn_raw_buffer_store_b32(tag, rc, fo + (((d) + kq + 1) & 3) * 2048, 0, 16)
    auto rdn = [&](int g, int r) __attribute__((always_inline)) {
      const int t = g_t[g], jt = g_q[g] * 2 + (r >> 1);
      fn[g & 1][r] = *LDSP(const bf16x8, (t == 1 ? an2[r & 1] : an[r & 1]) + (t == 0 ? 32768 : 0) + jt * 2048);
    };
    auto rdm = [&](int t, int i, int kk) __attribute__((always_inline)) { fm[kk][i] = *LDSP(const bf16x8, am[kk] + m_off(13 + t) + TO(i)); };
    auto group = [&](auto GI) __attribute__((always_inline)) {
      constexpr int gi = decltype(GI)::value;
      if (gi == G14) { BAR();
        SB(); }
      if (gi == G15) {
        PAGE(11);
        BAR();
        SB();
        xq[0] = ld16_s<0>(x, xo); xq[1] = ld16_s<64>(x, xo);
        sq = ld8_s_sc1(stat, xo >> 11 << 3);
        kv = *(const u32x2*)(smem + LDS_SEL);
        SB();
      }
#pragma unroll
      for (int q = 0; q < 16; ++q) {
        const int n = gi * 16 + q;
        if (n == NF0) {
          wait_xq<12>(xq, sq);
          key[0] = __builtin_amdgcn_readfirstlane(kv.x); key[1] = __builtin_amdgcn_readfirstlane(kv.y);
          nf_pre();
          SB();
        }
        if (n == 112) {
        }
        if (n == 152) {
        }
        if (n == 168) {
          WAIT_VM(8); MSG_FLAG(0); SB(); }
        if (n == 176) { WAIT_VM(5); MSG_FLAG(1); SB(); }
        if (n == 184) {
          WAIT_VM(2);
          MSG_FLAG(2); fx = bld4_sc1(rc, po); SB(); }
        mm(chp(gi) || chn(gi), gi & 1, g_q[gi], q, 2);
        constexpr bool ni = gi + 1 < NG && (chp(gi + 1 < NG ? gi + 1 : gi) || chn(gi + 1 < NG ? gi + 1 : gi));
        auto nr = [](int r) { return ni && r >= 2 ? 5 - r : r; };
        if constexpr (chp(gi) && chn(gi)) {
          if (q < 4) rdm(g_t[gi], 2 + (q >> 1), q & 1); else if (q < 8) rdn(gi + 1, nr(q - 4)); else if (q < 12) rdm(g_t[gi + 1], (q - 8) >> 1, q & 1);
        } else if constexpr (chp(gi)) {
          if (q < 4) rdm(g_t[gi], 2 + (q >> 1), q & 1);
          else if (gi + 1 < NG && q < 12 && !(q & 1)) rdn(gi + 1, nr((q - 4) >> 1));
        } else if constexpr (chn(gi)) {
          if (!(q & 1)) { if (q < 8) rdn(gi + 1, nr(q >> 1)); else rdm(g_t[gi + 1], (q - 8) >> 2, (q >> 1) & 1); }
        } else {
          if (!(q & 1) && q < 8) rdn(gi + 1, nr(q >> 1));
        }
        if (gi == GP0 && q < 8) publish(0, q);
        if (gi == GP1 && q < 8) publish(1, q);
        if (gi == 9 && q < 8) publish(2, q);
        if (n >= NF0) {
          const int m = (n - NF0) * 2;
          if (m < 56) { nf_step(m); nf_step(m + 1); } else if (m < 72) { nf_unp(m - 56); nf_unp(m - 55); }
        }
        SB();
      }
    };
#define GR4(i) group(IC<i>{}); group(IC<i + 1>{}); group(IC<i + 2>{}); group(IC<i + 3>{});
    GR4(0) GR4(4) GR4(8)
    asm volatile("s_nop 7");
    SB();
  }
#pragma unroll
  for (int i = 0; i < 4; ++i) *(f32x4*)(smem + LDS_ACC + wr * 16384 + wc * 4096 + i * 1024 + lane * 16) = acc[7][i];
#pragma unroll
  for (int i = 0; i < 4; ++i) ya[i] = acc[6][i];
  asm volatile("s_waitcnt lgkmcnt(0)" ::: "memory");
  if (lane == 0) sig[0] = 2;
  }

  PAGE(6);
  const unsigned rel0 = 0xffff - (key[0] & 0xffffu) - trow, rel1 = 0xffff - (key[1] & 0xffffu) - trow;
  if (min(rel0, rel1) < 16) {
#pragma unroll 1
    for (int c = 0; c < 2; ++c) {
      const unsigned rel = c ? rel1 : rel0;
      if (rel >= 16) continue;
      lds_vi cnt = (lds_vi)(smem + LDS_SEL + 24 + c * 4);
      int it = 0;
      while (cnt[0] != 4 && it < SPIN_MAX * 8) { __builtin_amdgcn_s_sleep(1); ++it; }
      asm volatile("" ::: "memory");
      if (rel == fr) {
        const float* fp = (const float*)(smem + LDS_FIN + c * 512) + (wid & 1) * 64 + fq * 8;
#pragma unroll
        for (int u4 = 0; u4 < 4; ++u4) {
          const f32x4 v = *(const f32x4*)(fp + (u4 >> 1) * 32 + (u4 & 1) * 4);
#pragma unroll
          for (int k = 0; k < 4; k += 2) {
            const unsigned u = pk2(nf[u4 * 4 + k] + v[k], nf[u4 * 4 + k + 1] + v[k + 1]);
            nf[u4 * 4 + k] = lo_bf(u); nf[u4 * 4 + k + 1] = hi_bf(u);
          }
        }
      }
    }
  }
  u32x4 pp[3][2];
  unsigned e_lo = 0x00003f80u, e_hi = 0x3f800000u;
  asm volatile("" : "+v"(e_lo), "+v"(e_hi));
  __amdgpu_buffer_rsrc_t rs_ = rs, rc_ = rc;
  int mo[3] = {MSG_IDX(0) * 4 * MSG, MSG_IDX(1) * 4 * MSG, MSG_IDX(2) * 4 * MSG}, mv = lane * 16 + ha * 2048, mv2 = mv + 1024;
  asm volatile("" : "+s"(rs_), "+s"(rc_), "+s"(mo[0]), "+s"(mo[1]), "+s"(mo[2]));
  unsigned as_ = lds_addr((const void*)smem) + LDS_SIG + (wid & 3) * 4, aa_ = lds_addr(smem + LDS_ACC) + (wid & 2) * 8192 + (wid & 1) * 4096 + lane * 16;
  asm volatile("" : "+v"(mv), "+v"(mv2), "+v"(as_), "+v"(aa_));
  {
    unsigned sv_, it_ = SPIN_MAX * 8;
    asm volatile("s_cmp_eq_u32 %9, 0\n\ts_cbranch_scc1 2f\n"
                 "1:\n\tds_read_b32 %0, %10\n\ts_waitcnt lgkmcnt(0)\n\tv_cmp_eq_u32 vcc, 2, %0\n\ts_cbranch_vccnz 3f\n\ts_sleep 1\n\ts_sub_u32 %1, %1, 1\n\ts_cbranch_scc0 1b\n"
                 "3:\n\tds_read_b128 %4, %11\n\tds_read_b128 %5, %11 offset:1024\n\tds_read_b128 %6, %11 offset:2048\n\tds_read_b128 %7, %11 offset:3072\n"
                 "2:\n\ts_waitcnt vmcnt(0)\n\tv_cmp_eq_u32 vcc, %8, %3\n\ts_mov_b32 %2, vcc_lo"
                 : "=&v"(sv_), "+s"(it_), "=&s"(seen), "+v"(fx), "+v"(ya[0]), "+v"(ya[1]), "+v"(ya[2]), "+v"(ya[3])
                 : "s"(tag), "s"(ha), "v"(as_), "v"(aa_) : "vcc", "scc", "memory");
  }
#define MSG_POLL(m) do { \
    if (!((seen >> (m)) & 1)) { \
      unsigned pv_, it_ = SPIN_MAX; \
      asm volatile("1:\n\tbuffer_load_dword %0, %3, %4, 0 offen sc1\n\ts_waitcnt vmcnt(0)\n\tv_cmp_eq_u32 vcc, %5, %0\n\ts_mov_b32 %1, vcc_lo\n\t" \
                   "s_bitcmp1_b32 %1, %6\n\ts_cbranch_scc1 2f\n\ts_sub_u32 %2, %2, 1\n\ts_cbranch_scc1 2f\n\ts_sleep 1\n\ts_branch 1b\n2:" \
                   : "=&v"(pv_), "+s"(seen), "+s"(it_) : "v"(po), "s"(rc_), "s"(tag), "i"(m) : "vcc", "scc", "memory"); \
    } \
    pp[m][0] = __builtin_amdgcn_raw_buffer_load_b128(rs_, mv, mo[m], 16); \
    pp[m][1] = __builtin_amdgcn_raw_buffer_load_b128(rs_, mv2, mo[m], 16); } while (0)
#define MSG_ADD(m) do { \
    _Pragma("unroll") for (int u = 0; u < 8; ++u) { \
      ya[u >> 1][(u & 1) * 2] = add_bf(pp[m][u >> 2][u & 3], e_lo, ya[u >> 1][(u & 1) * 2]); ya[u >> 1][(u & 1) * 2 + 1] = add_bf(pp[m][u >> 2][u & 3], e_hi, ya[u >> 1][(u & 1) * 2 + 1]); } } while (0)
  MSG_POLL(2); MSG_POLL(1); MSG_POLL(0);
  asm volatile("s_waitcnt lgkmcnt(0)" : "+v"(ya[0]), "+v"(ya[1]), "+v"(ya[2]), "+v"(ya[3]) :: "memory");
  SB();
  MSG_ADD(2); MSG_ADD(1);
  asm volatile("" : "+v"(ya[0]), "+v"(ya[1]), "+v"(ya[2]), "+v"(ya[3]));
  SB();
  MSG_ADD(0);
  {
    unsigned ov[8];
    SB();
#pragma unroll
    for (int u = 0; u < 8; ++u) ov[u] = pk2(ya[u >> 1][(u & 1) * 2], ya[u >> 1][(u & 1) * 2 + 1]);
    SB();
#pragma unroll
    for (int u = 0; u < 8; ++u) { xf[2 * u] = add_bf(ov[u], e_lo, xf[2 * u]); xf[2 * u + 1] = add_bf(ov[u], e_hi, xf[2 * u + 1]); }
    SB();
#pragma unroll
    for (int u = 0; u < 8; ++u) ov[u] = pk2(xf[2 * u], xf[2 * u + 1]);
    SB();
#pragma unroll
    for (int u = 0; u < 8; ++u) { nf[2 * u] = add_bf(ov[u], e_lo, nf[2 * u]); nf[2 * u + 1] = add_bf(ov[u], e_hi, nf[2 * u + 1]); }
    SB();
#pragma unroll
    for (int u = 0; u < 8; ++u) ov[u] = pk2(nf[2 * u], nf[2 * u + 1]);
    SB();
    *(u32x4*)((char*)out + (unsigned)xo) = (u32x4){ov[0], ov[1], ov[2], ov[3]};
    *(u32x4*)((char*)out + (unsigned)xo + 64) = (u32x4){ov[4], ov[5], ov[6], ov[7]};
  }
}

extern "C" void kernel_launch(void* const* d_in, const int* in_sizes, int n_in, void* d_out, int out_size,
                              void* d_ws, size_t ws_size, hipStream_t stream) {
  const u16* x = (const u16*)d_in[0];
  const u16* ws_re = (const u16*)d_in[1];
  const u16* ws_im = (const u16*)d_in[2];
  const u16* wd_re = (const u16*)d_in[3];
  const u16* wd_im = (const u16*)d_in[4];
  const u16* w_r = (const u16*)d_in[6];
  const u16* w_gate = (const u16*)d_in[7];
  const u16* w_up = (const u16*)d_in[8];
  const u16* w_down = (const u16*)d_in[9];
  k_main<<<256, 512, 0, stream>>>(x, 0x7fffffffu, 0x00020000u, wd_re, 0x7fffffffu, 0x00020000u, wd_im, 0x7fffffffu, 0x00020000u, w_r, (char*)d_ws,
                                  ws_re, ws_im, w_gate, w_up, w_down, (u16*)d_out);
}
```

```cpp
#include <hip/hip_runtime.h>
#include <hip/hip_bf16.h>
#include <stdint.h>
#include <type_traits>

extern "C" __device__ unsigned long long llvm_dispatch_id(void) __asm("llvm.amdgcn.dispatch.id");
extern "C" __device__ float llvm_fmul_legacy(float, float) __asm("llvm.amdgcn.fmul.legacy");
typedef unsigned short u16;
typedef unsigned long long u64;
typedef __attribute__((ext_vector_type(8))) short bf16x8;
typedef __attribute__((ext_vector_type(4))) unsigned u32x4;
typedef __attribute__((ext_vector_type(2))) unsigned u32x2;
typedef __attribute__((ext_vector_type(4))) float f32x4;
typedef __attribute__((ext_vector_type(2))) float f32x2;
typedef __attribute__((ext_vector_type(2))) __bf16 bf16x2;
typedef __attribute__((address_space(3))) void* lds_vp;
typedef volatile __attribute__((address_space(3))) int* lds_vi;

constexpr int S_ = 2048, D_ = 1024, F_ = 3584;
template <int N> using IC = std::integral_constant<int, N>;

static __device__ __forceinline__ float bf2f(u16 u) { return __uint_as_float((unsigned)u << 16); }
static __device__ __forceinline__ u16 f2bf(float f) {
  __hip_bfloat16 b = __float2bfloat16(f);
  return *reinterpret_cast<u16*>(&b);
}
static __device__ __forceinline__ float rbf(float f) { return bf2f(f2bf(f)); }
static __device__ __forceinline__ float lo_bf(unsigned u) { return __uint_as_float(u << 16); }
static __device__ __forceinline__ float hi_bf(unsigned u) { return __uint_as_float(u & 0xffff0000u); }
static __device__ __forceinline__ unsigned pk2(float a, float b) {
  const bf16x2 v = __builtin_convertvector((f32x2){a, b}, bf16x2);
  return __builtin_bit_cast(unsigned, v);
}
static __device__ __forceinline__ float add_bf(unsigned u, unsigned e, float c) {
  return __builtin_amdgcn_fdot2_f32_bf16(__builtin_bit_cast(bf16x2, u), __builtin_bit_cast(bf16x2, e), c, false);
}
static __device__ __forceinline__ float ln_elem(float x, float mb, float sd, float ri) {
  const float d = rbf(x - mb);
  const float q0 = d * ri;
  return rbf(__builtin_fmaf(__builtin_fmaf(-q0, sd, d), ri, q0));
}
template <int CTRL> static __device__ __forceinline__ float dpp_f(float v) {
  return __int_as_float(__builtin_amdgcn_update_dpp(0, __float_as_int(v), CTRL, 0xf, 0xf, true));
}
template <int CTRL> static __device__ __forceinline__ unsigned dpp_u(unsigned v) {
  return (unsigned)__builtin_amdgcn_update_dpp(0, (int)v, CTRL, 0xf, 0xf, true);
}
static __device__ __forceinline__ float row_sum(float v) {
  v += dpp_f<0xB1>(v);
  v += dpp_f<0x4E>(v);
  v += dpp_f<0x141>(v);
  v += dpp_f<0x140>(v);
  return v;
}
static __device__ __forceinline__ float fold32(float a, float b) {
  const auto r = __builtin_amdgcn_permlane32_swap(__float_as_uint(a), __float_as_uint(b), false, false);
  return __uint_as_float(r[0]) + __uint_as_float(r[1]);
}
static __device__ __forceinline__ float fold16(float a, float b) {
  const auto r = __builtin_amdgcn_permlane16_swap(__float_as_uint(a), __float_as_uint(b), false, false);
  return __uint_as_float(r[0]) + __uint_as_float(r[1]);
}
static __device__ __forceinline__ float rdl(float v, int l) { return __int_as_float(__builtin_amdgcn_readlane(__float_as_int(v), l)); }

#define ST_JB 9
#define WSN 10
#define FSM 0xa2
#define GV_JB 7
#define DG 11
#define AB_STRIDE 32
#define SPIN_MAX (1 << 17)

static __device__ __forceinline__ unsigned lds_addr(const void* p) { return (unsigned)(size_t)(lds_vp)p; }

constexpr int MSG = 4096;
constexpr size_t WS_ABT   = 0;
constexpr size_t WS_SLAB  = WS_ABT + (size_t)D_ * 4096 * 2;
constexpr size_t WS_GU    = WS_SLAB + (size_t)64 * 64 * MSG;
constexpr size_t WS_DN    = WS_GU + (size_t)8 * F_ * 16;
constexpr size_t WS_FLAGS = WS_DN + (size_t)2 * 8 * 32 * 64 * 16;
constexpr int FL_X = 0;
constexpr int FL_AB = 64 * 64 * 32;
constexpr int FL_ST = FL_AB + 2048 * AB_STRIDE;
constexpr int FL_CE = FL_ST + 4096;
constexpr int FL_KEY = FL_CE + 4096;
constexpr int FL_EPOCH = FL_KEY + 4096;
constexpr int WS_MB = (int)((WS_FLAGS + (FL_EPOCH + 1) * 4 + 1048575) >> 20);

#define WAIT_VM(n) asm volatile("s_waitcnt vmcnt(" #n ")" ::: "memory")
template <int N> static __device__ __forceinline__ void wait_vm() { asm volatile("s_waitcnt vmcnt(%0)" :: "i"(N) : "memory"); }
#define BAR_LGKM() asm volatile("s_waitcnt lgkmcnt(0)\n\ts_barrier" ::: "memory")
#define BAR() asm volatile("s_barrier" ::: "memory")
#define SB() __builtin_amdgcn_sched_barrier(0)
#define PAGE(n) asm volatile("s_branch 2f\n\t.p2align 12\nk_pg" #n ":\n\ts_setpc_b64 s[42:43]\n2:")
#define CALL_PG(n) asm volatile("s_getpc_b64 s[40:41]\n3:\n\ts_mov_b32 s42, k_pg" #n "-3b\n\ts_ashr_i32 s43, s42, 31\n\ts_add_u32 s40, s40, s42\n\ts_addc_u32 s41, s41, s43\n\t" \
                                "s_swappc_b64 s[42:43], s[40:41]" ::: "s40", "s41", "s42", "s43", "scc", "memory")

static __device__ __forceinline__ __amdgpu_buffer_rsrc_t mk_rsrc(const void* p) {
  return __builtin_amdgcn_make_buffer_rsrc((void*)p, 0, 0x7fffffff, 0x00020000);
}
#define DMA_(rsrc, voff, mbase, MI, sbase, SI, pol) do { unsigned _t; \
    asm volatile("s_add_i32 m0, %2, %3\n\ts_add_i32 %0, %5, %6\n\tbuffer_load_dwordx4 %1, %4, %0 offen" pol " lds" \
                 : "=&s"(_t) : "v"(voff), "s"(mbase), "i"(MI), "s"(rsrc), "s"(sbase), "i"(SI) : "memory", "scc"); } while (0)
#define TOUCH(rsrc, voff, mbase, MI, soff) \
    asm volatile("s_add_i32 m0, %1, %2\n\ts_nop 0\n\tbuffer_load_dword %0, %3, %4 offen lds" \
                 :: "v"(voff), "s"(mbase), "i"(MI), "s"(rsrc), "s"(soff) : "memory", "scc")
template <int OFF> static __device__ __forceinline__ u32x4 ld16_s(const void* p, int voff) {
  u32x4 v;
  asm volatile("global_load_dwordx4 %0, %1, %2 offset:%3" : "=v"(v) : "v"(voff), "s"(p), "i"(OFF) : "memory");
  return v;
}
template <int OFF> static __device__ __forceinline__ void sld(unsigned& sink, const void* p, int soff) {
  asm volatile("s_load_dword %0, %1, %2 offset:%3" : "+s"(sink) : "s"(p), "s"(soff), "i"(OFF) : "memory");
}
static __device__ __forceinline__ unsigned gld4_s(const void* p, unsigned voff) {
  unsigned v;
  asm volatile("global_load_dword %0, %1, %2" : "=v"(v) : "v"(voff), "s"(p) : "memory");
  return v;
}
static __device__ __forceinline__ u32x2 bld8_sc1(__amdgpu_buffer_rsrc_t r, unsigned voff) {
  u32x2 v;
  asm volatile("buffer_load_dwordx2 %0, %1, %2, 0 offen sc1" : "=v"(v) : "v"(voff), "s"(r) : "memory");
  return v;
}
template <int N> static __device__ __forceinline__ void wait_xs(unsigned& a, u32x2& b) { asm volatile("s_waitcnt vmcnt(%2)" : "+v"(a), "+v"(b) : "i"(N) : "memory"); }
template <int N> static __device__ __forceinline__ void wait_xq(u32x4 (&a)[2], u32x2& b) { asm volatile("s_waitcnt vmcnt(%3)" : "+v"(a[0]), "+v"(a[1]), "+v"(b) : "i"(N) : "memory"); }
static __device__ __forceinline__ u32x2 ld8_s_sc1(const void* p, int voff) {
  u32x2 v;
  asm volatile("global_load_dwordx2 %0, %1, %2 sc1" : "=v"(v) : "v"(voff), "s"(p) : "memory");
  return v;
}
static __device__ __forceinline__ void lgkm0(unsigned& sink) { asm volatile("s_waitcnt lgkmcnt(0)" : "+s"(sink) :: "memory"); }
template <typename T> static __device__ __forceinline__ void pin4(T& a, T& b, T& c, T& d) { asm volatile("" : "+s"(a), "+s"(b), "+s"(c), "+s"(d)); }
template <typename T, typename U> static __device__ __forceinline__ void pins(T& a, U& b) { asm volatile("" : "+s"(a), "+s"(b)); }
static __device__ __forceinline__ void launder(int& v, int& a, int& b) { asm volatile("" : "+v"(v)); asm volatile("" : "+s"(a), "+s"(b)); }
static __device__ __forceinline__ unsigned bld4_sc1(__amdgpu_buffer_rsrc_t r, int voff) {
  unsigned v;
  asm volatile("buffer_load_dword %0, %1, %2, 0 offen sc1" : "=v"(v) : "v"(voff), "s"(r) : "memory");
  return v;
}
static __device__ __forceinline__ u32x4 bld16_nt(__amdgpu_buffer_rsrc_t r, int voff, int soff) {
  u32x4 v;
  asm volatile("buffer_load_dwordx4 %0, %1, %2, %3 offen nt" : "=v"(v) : "v"(voff), "s"(r), "s"(soff) : "memory");
  return v;
}
static __device__ __forceinline__ void bld16_nt_io(u32x4& v, __amdgpu_buffer_rsrc_t r, int voff, int soff) {
  asm volatile("buffer_load_dwordx4 %0, %1, %2, %3 offen nt" : "+v"(v) : "v"(voff), "s"(r), "s"(soff) : "memory");
}
static __device__ __forceinline__ u32x4 bld16_sc1(__amdgpu_buffer_rsrc_t r, int voff, int soff) {
  u32x4 v;
  asm volatile("buffer_load_dwordx4 %0, %1, %2, %3 offen sc1" : "=v"(v) : "v"(voff), "s"(r), "s"(soff) : "memory");
  return v;
}
static __device__ __forceinline__ u32x2 granule_wait(const u64* p, unsigned tag) {
  u64 v = 0;
#pragma unroll 1
  for (int it = 0; it < SPIN_MAX; ++it) {
    v = __hip_atomic_load(p, __ATOMIC_RELAXED, __HIP_MEMORY_SCOPE_AGENT);
    if (__all((unsigned)(v >> 32) == tag)) break;
    __builtin_amdgcn_s_sleep(2);
  }
  return (u32x2){(unsigned)v, (unsigned)(v >> 32)};
}
static __device__ __forceinline__ float key_score(unsigned key) {
  const unsigned u = key >> 16;
  return bf2f((u16)((u & 0x8000u) ? (u & 0x7fffu) : (~u & 0xffffu)));
}
static __device__ __forceinline__ void merge2(unsigned& a1, unsigned& a2, unsigned b1, unsigned b2) {
  const unsigned lo = min(a1, b1);
  a1 = max(a1, b1);
  a2 = max(lo, max(a2, b2));
}
static __device__ __forceinline__ float sqdev16(const u32x4 (&v)[2], float mb) {
  f32x2 q2 = {0.f, 0.f};
#pragma unroll
  for (int i = 0; i < 8; ++i) {
    const f32x2 e = (f32x2){lo_bf(v[i >> 2][i & 3]), hi_bf(v[i >> 2][i & 3])} - mb;
    q2 += e * e;
  }
  return q2.x + q2.y;
}

constexpr int G1_NST = 4, G1_NKT = 16, G1_STAGE = 32768;
constexpr int G2_NKT = 16, G2_MRING = 98304;
constexpr int n_off(int k) { return (k % 3) * 32768; }
constexpr int m_off(int k) { return ((k + 2) % 3) * 16384; }
constexpr int k_ord(int k) { return k < 2 ? k : (k < 9 ? 2 * (k - 1) : 2 * (k - 8) + 1); }
constexpr int LDS_DUMMY = G2_MRING + 49152;
constexpr int LDS_GU = LDS_DUMMY + 4096;
constexpr int LDS_FIN = LDS_GU + 4096;
constexpr int LDS_MB = LDS_FIN + 4096;
constexpr int LDS_SEL = LDS_MB + 1536;
constexpr int LDS_SIG = LDS_SEL + 64;
constexpr int LDS_KEY = LDS_SIG + 32;
constexpr int LDS_BYTES = LDS_KEY + 128;
constexpr int LDS_ACC = n_off(G2_NKT - 3);

constexpr int tpos[16] = {0, 0, 0, 0, 106, 110, 115, 2, 2, 3, 4, 5, 0, 0, 0, 0};
constexpr int twho[16] = {0, 0, 0, 0, 7, 7, 7, 0, 1, 2, 6, 5, 0, 0, 0, 0};
constexpr int dn_cnt(int q) { return q == 5 ? 3 : q == 8 ? 4 : 0; }
constexpr int dn_first(int q) { return q <= 5 ? 0 : q <= 8 ? 3 : 7; }

static __device__ __forceinline__ void code_touch(unsigned& sink, unsigned off) {
  asm volatile("s_getpc_b64 s[40:41]\n\tk_main_rt:\n\ts_min_u32 %1, %1, k_main_end-k_main_rt-196\n\t"
               "s_load_dword %0, s[40:41], %1 offset:0\n\ts_load_dword %0, s[40:41], %1 offset:64\n\ts_load_dword %0, s[40:41], %1 offset:128\n\ts_load_dword %0, s[40:41], %1 offset:192"
               : "+s"(sink), "+s"(off) :: "s40", "s41", "scc", "memory");
}
static __device__ __forceinline__ void touch1(__amdgpu_buffer_rsrc_t r, int voff, unsigned mb, int soff) { TOUCH(r, voff, mb, LDS_DUMMY, soff); }
typedef const __attribute__((address_space(1))) u16* gptr;
typedef __attribute__((ext_vector_type(8))) u64 u64x8;
extern "C" __global__ __launch_bounds__(512, 2) void k_main(const u16* x, unsigned x2, unsigned x3, const u16* wd_re, unsigned r2, unsigned r3,
                                                 const u16* wd_im, unsigned i2, unsigned i3, const u16* w_r,
                                                 char* wsp, const u16* ws_re, const u16* ws_im,
                                                 const u16* w_gate, const u16* w_up, const u16* w_down, u16* out) {
  __shared__ __attribute__((aligned(16))) char smem[LDS_BYTES];
  u64x8 ka;
  u32x4 rm, rn;
  asm volatile("s_load_dwordx4 %0, %3, 0\n\ts_load_dwordx4 %1, %3, %4 offset:16\n\ts_load_dwordx16 %2, %3, 48"
               : "=&s"(rm), "=&s"(rn), "=&s"(ka) : "s"(__builtin_amdgcn_kernarg_segment_ptr()), "s"((blockIdx.x & 4) * 4) : "memory");
  const int tid = threadIdx.x, lane = tid & 63;
  const int wid = __builtin_amdgcn_readfirstlane(tid >> 6);
  const int g = blockIdx.x, xcd = g & 7, j = g >> 3;
  if (wid < 4) {
    unsigned cw, co = (j * 4 + wid) * 448;
    asm volatile(".subsection 1\nk_main_end:\n.subsection 0\n\ts_min_u32 %1, %1, k_main_end-k_main-480\n\ts_getpc_b64 s[40:41]\n\ts_add_u32 s40, s40, %1\n\ts_addc_u32 s41, s41, 0\n\t"
                 "s_load_dword %0, s[40:41], 0\n\ts_load_dword %0, s[40:41], 64\n\ts_load_dword %0, s[40:41], 128\n\t"
                 "s_load_dword %0, s[40:41], 192\n\ts_load_dword %0, s[40:41], 256\n\ts_load_dword %0, s[40:41], 320\n\ts_load_dword %0, s[40:41], 384\n\ts_waitcnt lgkmcnt(0)"
                 : "=&s"(cw), "+s"(co) :: "s40", "s41", "scc", "memory");
  }
  const int pw = wid - 4;
  const int row0 = (wid & 3) * 8 + (lane >> 3);
  const int cm = ((lane & 7) ^ (((row0 >> 1) & 1) | ((wid & 3) << 1))) * 16;
  int vm = row0 * (D_ * 2) + cm;
  asm volatile("" : "+v"(vm));
  const int kq = xcd >> 1;
  const int tt = (xcd & 1) * 4 + (j >> 3);
  const int et = j & 7;
  const int z = kq >> 1, st = (kq & 1) * 8 + tt;
  unsigned o_m = st * 128 * D_ * 2, o_n = et * 128 * D_ * 2, mb0 = lds_addr(smem) + pw * 1024;
  asm volatile("" : "+s"(o_m), "+s"(o_n), "+s"(mb0));
#define G1_X(kt, so, q) DMA_(rm, vm, mb0, (so) + (q) * 4096, o_m, (kt) * 128 + (((q) & 1) * 64 + ((q) >> 1) * 32) * D_ * 2, "")
#define G1_W(kt, so, q) DMA_(rn, vm, mb0, (so) + 16384 + (q) * 4096, o_n, (kt) * 128 + (q) * 32 * D_ * 2, "")
#define G1_STAGE_TILE(kt, so, W) do { SB(); G1_X(kt, so, 0); W(0); G1_X(kt, so, 1); W(1); G1_X(kt, so, 2); W(2); G1_X(kt, so, 3); W(3); \
    G1_W(kt, so, 0); W(4); G1_W(kt, so, 1); W(5); G1_W(kt, so, 2); W(6); G1_W(kt, so, 3); W(7); SB(); } while (0)
#define G1_STAGE_A(kt, so, W) do { SB(); G1_X(kt, so, 0); W(0); G1_X(kt, so, 2); W(1); \
    G1_W(kt, so, 0); W(2); G1_W(kt, so, 1); W(3); G1_W(kt, so, 2); W(4); G1_W(kt, so, 3); W(5); W(6); W(7); SB(); } while (0)
#define G1_STAGE_B(kt, so) do { SB(); G1_X(kt, so, 1); G1_X(kt, so, 3); SB(); } while (0)
#define NOW(i)
  asm volatile("s_waitcnt lgkmcnt(0)" : "+s"(ka), "+s"(rm), "+s"(rn) :: "memory");
  int lrow, drow, l16;
  unsigned o_l, o_d;
  auto rows = [&](int i) __attribute__((always_inline)) {
    int g_ = g, l_ = lane;
    if (i == 0) { asm volatile("" : "+s"(g_)); drow = (g_ & ~7) + pw * 2; lrow = drow + (g_ & 3) * 512 + (g_ & 4) * 64; asm volatile("" : "+s"(drow), "+s"(lrow)); }
    if (i == 1) { asm volatile("" : "+s"(g_)); drow += (g_ & 3) * 256; o_l = lrow * D_ * 2; o_d = drow * D_ * 2; asm volatile("" : "+s"(drow), "+s"(o_l), "+s"(o_d)); }
    if (i == 2) { asm volatile("" : "+v"(l_)); l16 = l_ * 16; asm volatile("" : "+v"(l16)); }
  };
  if (wid >= 4) {
    __builtin_amdgcn_s_setprio(3);
    G1_STAGE_TILE(0, 0, rows);
  }
  x = (const u16*)(gptr)((u64)rm[0] | (u64)rm[1] << 32);
  const u16* wdz = (const u16*)(gptr)((u64)rn[0] | (u64)rn[1] << 32);
  w_r = (const u16*)(gptr)ka[0]; wsp = (char*)(gptr)ka[1]; ws_re = (const u16*)(gptr)ka[2]; ws_im = (const u16*)(gptr)ka[3];
  w_gate = (const u16*)(gptr)ka[4]; w_up = (const u16*)(gptr)ka[5]; w_down = (const u16*)(gptr)ka[6]; out = (u16*)(gptr)ka[7];
  int* flags = (int*)(wsp + WS_FLAGS);
  u64* stat = (u64*)(flags + FL_ST);
  const int fr = lane & 15, fq = lane >> 4;
  const int ha = wid >> 2;
  int trow, xo, po;
  const char *prs, *prc;
  auto epi_setup = [&]() __attribute__((always_inline)) {
    int t_ = tt, l_ = lane;
    asm volatile("" : "+s"(t_));
    asm volatile("" : "+v"(l_));
    trow = t_ * 256 + ((wid >> 1) & 1) * 128 + kq * 32 + ha * 16;
    xo = ((trow + (l_ & 15)) * D_ + et * 128 + (wid & 1) * 64 + (l_ >> 4) * 8) * 2;
    prs = wsp + WS_SLAB + (size_t)((t_ * 8 + et) * 64 + (wid & 3)) * MSG;
    prc = (const char*)(flags + FL_X + ((t_ * 8 + et) * 64 + (wid & 3)) * 32);
    asm volatile("" : "+s"(trow), "+s"(prs), "+s"(prc));
    po = l_ < 3 ? (kq * 4 + ((kq + 1 + l_) & 3)) * 512 : (int)0x80000000;
    asm volatile("" : "+v"(xo), "+v"(po));
  };
#define rs mk_rsrc(prs)
#define rc mk_rsrc(prc)
  lds_vi sig = (lds_vi)(smem + LDS_SIG + (wid & 3) * 4);
  unsigned tag;
  u32x4 xq[2];
  u32x2 sq;
  f32x4 ya[4];
  unsigned key[2];
  float xf[16], nf[16], lmb, lsd, lri, la, lb, lqa, lqb;
  unsigned nfp[8];
#define PINV(a) asm volatile("" : "+v"(a))
  auto nf_pre = [&]() __attribute__((always_inline)) {
    if (__builtin_expect(!__all(sq.y == tag), 0)) sq = granule_wait(stat + (xo >> 11), tag);
    lmb = lo_bf(sq.x); lsd = hi_bf(sq.x); lri = __builtin_amdgcn_rcpf(lsd);
    PINV(lmb); PINV(lsd); PINV(lri);
  };
  auto nf_step = [&](int n) __attribute__((always_inline)) {
    const int u = n / 7;
    switch (n % 7) {
      case 0: la = lo_bf(xq[u >> 2][u & 3]); PINV(la); la -= lmb; PINV(la); break;
      case 1: lb = hi_bf(xq[u >> 2][u & 3]); PINV(lb); lb -= lmb; PINV(lb); break;
      case 2: nfp[u] = pk2(la, lb); la = lo_bf(nfp[u]); PINV(la); PINV(nfp[u]); break;
      case 3: lqa = la * lri; PINV(lqa); lb = hi_bf(nfp[u]); PINV(lb); break;
      case 4: la = __builtin_fmaf(-lqa, lsd, la); PINV(la); lqb = lb * lri; PINV(lqb); break;
      case 5: lqa = __builtin_fmaf(la, lri, lqa); PINV(lqa); lb = __builtin_fmaf(-lqb, lsd, lb); PINV(lb); break;
      default: lqb = __builtin_fmaf(lb, lri, lqb); nfp[u] = pk2(lqa, lqb); PINV(nfp[u]); break;
    }
  };
  auto nf_unp = [&](int e) __attribute__((always_inline)) {
    const int u = e >> 1;
    if (e == 0) { PINV(xq[0]); PINV(xq[1]); }
    xf[e] = (e & 1) ? hi_bf(xq[u >> 2][u & 3]) : lo_bf(xq[u >> 2][u & 3]);
    nf[e] = (e & 1) ? hi_bf(nfp[u]) : lo_bf(nfp[u]);
    PINV(xf[e]); PINV(nf[e]);
  };
#define MSG_IDX(m) (kq * 4 + ((kq + 1 + (m)) & 3))
  unsigned seen, fx;

  if (__builtin_expect(wid >= 4, 1)) {
    constexpr int O_ST = (FL_ST - FL_AB) * 4, O_CE = (FL_CE - FL_AB) * 4, O_KEY = (FL_KEY - FL_AB) * 4, O_EP = (FL_EPOCH - FL_AB) * 4;
    const __amdgpu_buffer_rsrc_t rf = mk_rsrc(wsp + WS_FLAGS + FL_AB * 4);
    u32x4 lv[2], lv2[2], lw[2], ld[2], ld2[2];
    unsigned epv;
    {
      const char* xrow = (const char*)x + o_l;
      const char* wdrow = (const char*)wdz + o_d;
      lv[0] = ld16_s<0>(xrow, l16); lv[1] = ld16_s<1024>(xrow, l16);
      lv2[0] = ld16_s<2048>(xrow, l16); lv2[1] = ld16_s<3072>(xrow, l16);
      ld[0] = ld16_s<0>(wdrow, l16); ld[1] = ld16_s<1024>(wdrow, l16);
      ld2[0] = ld16_s<2048>(wdrow, l16); ld2[1] = ld16_s<3072>(wdrow, l16);
      lw[0] = ld16_s<0>(w_r, l16); lw[1] = ld16_s<1024>(w_r, l16);
      asm volatile("buffer_load_dword %0, off, %1, %2 sc1" : "=v"(epv) : "s"(rf), "s"(O_EP) : "memory");
    }
    SB();
    const unsigned long long did = llvm_dispatch_id();
    const unsigned dmix = (unsigned)did * 0x85EBCA6Bu + (unsigned)(did >> 32) * 0xC2B2AE35u + 0x27D4EB2Fu;
    const bool moe = et != 7;
    unsigned epoch;
    const int ci = lane & 15, rq = lane >> 4, tk = (lane >> 3) & 1;
    int mat, cg, dq, d0;
    u32x4 wv[16];
    const u16 *pgu, *pn2;
    const char *pm2, *spb;
#define rw __builtin_amdgcn_make_buffer_rsrc((void*)pgu, 0, moe ? 0x7fffffff : 0, 0x00020000)
#define rn2 mk_rsrc(pn2)
#define rm2 mk_rsrc(pm2)
    int vw, vn2, vm2;
    int ord2;
    auto k_blk = [&](int k) __attribute__((always_inline)) { return k >= 9 ? (ord2 >> (4 * (k - 9))) & 7 : tt ^ (k_ord(k) >> 1); };
#define KROT(kk_) (k_blk(kk_) * 256 + (k_ord(kk_) & 1) * 128)
    int tv2;
    unsigned sd = 0;
    auto touches = [&](auto P) __attribute__((always_inline)) {
      constexpr int p = decltype(P)::value;
      int e = et;
      asm volatile("" : "+s"(e));
#define TCH(k) if constexpr (tpos[k] == p) { if (e == twho[k]) { const int kb = KROT(k); touch1(rn2, tv2, mb0, kb); } }
      TCH(0) TCH(1) TCH(2) TCH(3) TCH(4) TCH(5) TCH(6) TCH(7) TCH(8) TCH(9) TCH(10) TCH(11) TCH(12) TCH(13) TCH(14) TCH(15)
    };
    unsigned mq0, mq1, mq2, mq3;
    const int cg8 = g & 7, rgb = g >> 3;
    const __amdgpu_buffer_rsrc_t rdn = mk_rsrc(w_down);
    const __amdgpu_buffer_rsrc_t rgu = mk_rsrc(wsp + WS_GU);
    int vdn, vgu;
    u32x4 dv[7], gv[4];
    int abo, abq[3];
    unsigned lma;
    unsigned sel[2][4];
    float g0, g1;
    u32x4 kk[4];
    unsigned k1 = 0, k2 = 0;
    const int ko_ = O_KEY + pw * 4096;
#define MM(a, b, c) __builtin_amdgcn_mfma_f32_16x16x32_bf16(__builtin_bit_cast(bf16x8, a), __builtin_bit_cast(bf16x8, b), c, 0, 0, 0)
#define PIN1(a) asm volatile("" : "+v"(a))
#define PIN2(a, b) asm volatile("" : "+v"(a), "+v"(b))
#define PIN3(a, b, c) asm volatile("" : "+v"(a), "+v"(b), "+v"(c))
    {
      int gro, go;
      u32x2 gr;
      u32x4 oh[5];
      f32x4 sa4, gw[2], gx[2];
      float su, gs, ta, tb, sx, mbv;
      unsigned dk[4], kl;
      const int hb = lane >> 5;
#pragma unroll
      for (int l = 0; l < WSN; ++l) asm volatile("" : "=v"(wv[l]));
      auto diag = [&](const f32x4& m) __attribute__((always_inline)) {
        return __uint_as_float((__float_as_uint(m[0]) & dk[0]) | (__float_as_uint(m[1]) & dk[1]) | (__float_as_uint(m[2]) & dk[2]) | (__float_as_uint(m[3]) & dk[3]));
      };
      auto setup = [&](auto JB, auto GP) __attribute__((always_inline)) {
        constexpr int jb = decltype(JB)::value, gp = decltype(GP)::value;
        SB();
        {
          int ln = lane, jj = j, p_ = pw;
          launder(ln, jj, p_);
          const int c_ = ln & 15, r_ = ln >> 4, e_ = jj & 7, t_ = (xcd & 1) * 4 + (jj >> 3);
          if constexpr (jb == 1 && gp == 0) {
            pn2 = ((kq >> 1) ? ws_im : ws_re) + (long)t_ * 256 * S_ + (kq & 1) * 1024;
            spb = (const char*)(pn2 + (e_ * 4 + p_) * 8 * S_);
            pins(pn2, spb);
          }
          if constexpr (jb == 1 && gp >= 1 && gp < 6) {
            const unsigned o = c_ == gp - 1 ? 0x3f803f80u : 0u;
            oh[gp - 1] = (u32x4){o, o, o, o};
            PIN1(oh[gp - 1]);
          }
          if constexpr (jb == 1 && gp == 6) {
            const int gm = moe ? (jj - (jj >> 3)) * 8 + xcd : 0;
            mat = gm / 112; cg = (gm % 112) >> 2; dq = gm & 3; d0 = dq * 256 + p_ * 64;
            pin4(mat, cg, dq, d0);
          }
          if constexpr (jb == 2 && gp == 0) {
            pgu = mat ? w_up : w_gate;
            pm2 = wsp + WS_ABT + ((long)e_ * 128 * 4096 + kq * 1024) * 2;
            pins(pgu, pm2);
            vw = ((d0 + r_ * 16) * F_ + cg * 128 + c_ * 8) * 2;
            PIN1(vw);
          }
          if constexpr (jb == 2 && (gp == 1 || gp == 2)) {
            dk[gp * 2 - 2] = c_ == r_ * 4 + gp * 2 - 2 ? ~0u : 0u; dk[gp * 2 - 1] = c_ == r_ * 4 + gp * 2 - 1 ? ~0u : 0u;
            PIN2(dk[gp * 2 - 2], dk[gp * 2 - 1]);
          }
          if constexpr (jb == 2 && gp == 3) { kl = lrow + (ln >> 5); go = (ln & 30) ? (int)0x80000000 : ((ln & 1) ? O_KEY : O_ST) + (int)kl * 8; PIN2(kl, go); }
          if constexpr (jb == 2 && gp == 4) { kl = 0xffff - kl; go = (ln & ~1) == 2 ? O_CE + (z * D_ + drow + (ln & 1)) * 8 : go; PIN2(kl, go); }
          if constexpr (jb == 2 && gp == 5) {
            gro = (p_ < 2 ? O_ST + (st * 128 + p_ * 64) * 8 : O_CE + (z * D_ + e_ * 128 + (p_ - 2) * 64) * 8) + ln * 8;
            tv2 = (p_ * 64 + ln) * 4096;
            lma = lds_addr(smem + LDS_MB) + (p_ < 2 ? p_ * 256 : 512 + p_ * 256) + ln * 4;
            PIN3(gro, tv2, lma);
          }
          if constexpr (jb == 2 && gp == 6) {
            const u64 lo = (t_ & 2) ? ((t_ & 4) ? 0x0150234601503247ull : 0x0514670205147603ull) : ((t_ & 4) ? 0x0123067401532076ull : 0x0567423005176432ull);
            ord2 = (int)(lo >> ((t_ & 1) * 32));
            pins(ord2, p_);
          }
          if constexpr (jb == 7 && gp == 0) {
            const int r0 = p_ * 8 + (ln >> 3);
            vn2 = r0 * (S_ * 2) + ((ln & 7) ^ ((r0 >> 1) & 7)) * 16; vm2 = r0 * 8192 + (vm & 127);
            PIN2(vn2, vm2);
          }
          if constexpr (jb == 7 && gp == 1) { vdn = jj * 112 + p_ * 28 + r_ * 7; vgu = (((ln >> 3) & 1) * 4 * F_ + vdn + min(ln & 7, 6)) * 16; PIN2(vdn, vgu); }
          if constexpr (jb == 7 && gp == 2) { vdn = (vdn * D_ + cg8 * 128 + c_ * 8) * 2; PIN1(vdn); }
          if constexpr (jb == 7 && gp == 3) {
            abo = (((kq * 8 + e_) * 4 + p_) * 16 + c_) * AB_STRIDE * 4;
            abq[2] = ln >= 16 || (ln & 7) == t_ ? (int)0x80000000 : abo;
            PIN2(abo, abq[2]);
          }
          if constexpr (jb == 7 && gp == 4) {
            abq[1] = ln >= 8 ? (int)0x80000000 : abq[2];
            abq[0] = ((ln ^ t_) & 4) ? (int)0x80000000 : abq[1];
            PIN2(abq[0], abq[1]);
          }
          if constexpr (jb == 7 && gp == 5) {
            const unsigned mb = lds_addr(smem) + p_ * 1024;
            mq0 = mb + ((3 - kq) & 3) * 4096; mq1 = mb + ((4 - kq) & 3) * 4096; mq2 = mb + ((5 - kq) & 3) * 4096; mq3 = mb + ((6 - kq) & 3) * 4096;
            pin4(mq0, mq1, mq2, mq3);
          }
          if constexpr (jb == 8 && gp < 4) {
#pragma unroll
            for (int h = 0; h < 2; ++h) {
              const unsigned hv = h ? 0x0302u : 0x0100u;
              sel[h][gp] = ((ln & 7) >> 1) == gp ? ((ln & 1) ? (hv << 16) | 0x0c0cu : 0x0c0c0000u | hv) : 0x0c0c0c0cu;
            }
            PIN2(sel[0][gp], sel[1][gp]);
          }
        }
        SB();
      };
#define WS1(i) setup(IC<1>{}, IC<i>{})
#define WS2(i) setup(IC<2>{}, IC<i>{})
#define WS3(i) do { setup(IC<7>{}, IC<i>{}); if (i >= 4) setup(IC<8>{}, IC<(i) & 3>{}); } while (0)
      G1_STAGE_TILE(1, G1_STAGE, WS1);
      WAIT_VM(19); BAR();
      G1_STAGE_TILE(2, 2 * G1_STAGE, WS2);
      G1_STAGE_TILE(3, 3 * G1_STAGE, WS3);
      auto work1 = [&](auto JB, auto GP) __attribute__((always_inline)) {
        constexpr int jb = decltype(JB)::value, gp = decltype(GP)::value;
        SB();
        if constexpr (jb == 1) {
          const f32x4 z4 = {0.f, 0.f, 0.f, 0.f};
          if constexpr (gp == 0) { sa4 = MM(lv[0], oh[0], z4); gw[0] = MM(lv[0], lw[0], z4); gx[0] = MM(lv[0], lv[0], z4); PIN3(sa4, gw[0], gx[0]); }
          if constexpr (gp == 1) { sa4 = MM(lv2[0], oh[1], sa4); gw[1] = MM(lv2[0], lw[0], z4); gx[1] = MM(lv2[0], lv2[0], z4); PIN3(sa4, gw[1], gx[1]); }
          if constexpr (gp == 2) { sa4 = MM(lv[1], oh[0], sa4); gw[0] = MM(lv[1], lw[1], gw[0]); gx[0] = MM(lv[1], lv[1], gx[0]); PIN3(sa4, gw[0], gx[0]); }
          if constexpr (gp == 3) { sa4 = MM(lv2[1], oh[1], sa4); gw[1] = MM(lv2[1], lw[1], gw[1]); gx[1] = MM(lv2[1], lv2[1], gx[1]); PIN3(sa4, gw[1], gx[1]); }
          if constexpr (gp == 4) { sa4 = MM(ld[0], oh[2], sa4); sa4 = MM(ld[1], oh[2], sa4); PIN1(sa4); }
          if constexpr (gp == 5) { sa4 = MM(ld2[0], oh[3], sa4); sa4 = MM(ld2[1], oh[3], sa4); PIN1(sa4); }
          if constexpr (gp == 6) { sa4 = MM(lw[0], oh[4], sa4); sa4 = MM(lw[1], oh[4], sa4); PIN1(sa4);
            epoch = __builtin_amdgcn_readfirstlane(epv); tag = ((epoch + 1u) * 0x9E3779B1u ^ dmix) | 1u; }
        }
        if constexpr (jb == 2) {
          if constexpr (gp == 0) { su = (sa4[0] + sa4[1]) + (sa4[2] + sa4[3]); ta = diag(gw[0]); PIN2(su, ta); }
          if constexpr (gp == 1) { su = fold32(su, su); tb = diag(gw[1]); PIN2(su, tb); }
          if constexpr (gp == 2) { su = fold16(su, su); gs = fold32(ta, tb); PIN2(su, gs); }
          if constexpr (gp == 3) { ta = diag(gx[0]); tb = diag(gx[1]); PIN2(ta, tb); }
          if constexpr (gp == 4) { gs = fold16(gs, fold32(ta, tb)); PIN1(gs); }
          if constexpr (gp == 5) { gs += dpp_f<0xB1>(gs); gs += dpp_f<0x4E>(gs); PIN1(gs); }
          if constexpr (gp == 6) { gs += dpp_f<0x141>(gs); gs += dpp_f<0x140>(gs); PIN1(gs); }
        }
        if constexpr (jb == 3) {
          if constexpr (gp == 0) {
            const float s0 = rdl(su, 0), s1 = rdl(su, 1), q0 = rdl(gs, 16), q1 = rdl(gs, 48);
            sx = hb ? s1 : s0; ta = hb ? q1 : q0; PIN2(sx, ta);
          }
          if constexpr (gp == 1) {
            const float mean = sx * (1.0f / D_);
            mbv = rbf(mean);
            ta -= sx * mean;
            if (__builtin_expect(__any(!(sx * mean <= 16.f * ta)), 0)) {
              float q = fold32(sqdev16(lv, rdl(mbv, 0)), sqdev16(lv2, rdl(mbv, 32)));
              q = row_sum(fold16(q, q));
              const float dm = mean - mbv;
              ta = fmaxf(q - (float)D_ * dm * dm, 0.f);
            }
            PIN2(mbv, ta);
          }
          if constexpr (gp == 2) { ta = rbf(rbf(ta * (1.0f / D_)) + 1e-5f); gs -= mbv * rdl(su, 4); PIN2(ta, gs); }
          if constexpr (gp == 3) { ta = rbf(__builtin_amdgcn_sqrtf(ta)); tb = __builtin_amdgcn_rcpf(ta); PIN2(ta, tb); }
          if constexpr (gp == 4) {
            const unsigned rb = __float_as_uint(rbf(gs * tb));
            kl |= (rb ^ ((unsigned)((int)rb >> 31) | 0x80000000u)) & 0xffff0000u; PIN1(kl);
          }
          if constexpr (gp == 5) { kl = (lane & 2) ? __float_as_uint(su) : (lane & 1) ? kl : (__float_as_uint(mbv) >> 16) | __float_as_uint(ta); PIN1(kl); }
        }
        if constexpr (jb == ST_JB + 3) {
          if constexpr (gp == 0) { if (!__all(gr.y == tag)) gr = granule_wait((const u64*)((const char*)(flags + FL_AB) + gro), tag); PIN1(gr); }
          if constexpr (gp == 1) { g1 = (z ? -1.0f : 1.0f) / hi_bf(gr.x); PIN1(g1); }
          if constexpr (gp == 2) { g0 = pw < 2 ? -(lo_bf(gr.x) * g1) : __uint_as_float(gr.x); PIN1(g0); }
        }
        if constexpr (gp < 7 && (jb - 1) * 7 + gp < 64) {
          constexpr int n = (jb - 1) * 7 + gp;
          sld<((n >> 1) & 7) * 4096 + (n & 1) * 64>(sd, spb, KROT(n >> 4));
        }
        SB();
      };
      auto step1 = [&](auto JB) __attribute__((always_inline)) {
        constexpr int jb = decltype(JB)::value;
        if constexpr (jb == 1) PAGE(1);
        if constexpr (jb == 7) PAGE(2);
        constexpr int n16 = jb == 12 ? 12 : jb == 11 ? 14 : 16;
        constexpr auto wl = [](int q) { return (q == 3 ? 1 : 0) + (q == ST_JB ? 1 : 0); };
        if constexpr (jb == 1) {
          asm volatile("s_waitcnt vmcnt(%11)" : "+v"(lv[0]), "+v"(lv[1]), "+v"(lw[0]), "+v"(lw[1]), "+v"(ld[0]), "+v"(ld[1]),
                       "+v"(lv2[0]), "+v"(lv2[1]), "+v"(ld2[0]), "+v"(ld2[1]), "+v"(epv) : "i"(16) : "memory");
        } else if constexpr (jb == ST_JB + 3) {
          asm volatile("s_waitcnt vmcnt(%1)" : "+v"(gr) : "i"(n16 + wl(jb - 2) + wl(jb - 1)) : "memory");
        } else wait_vm<n16 + wl(jb - 3) + wl(jb - 2) + wl(jb - 1)>();
        BAR();
        if constexpr (jb == 2) { if (pw == 0 && lane < 16) ((int*)(smem + LDS_SEL))[lane < 8 ? lane : lane + 8] = 0; }
        if constexpr (jb == 4) { if (pw == 0 && lane == 0) ((unsigned*)(smem + LDS_SEL))[5] = tag; }
#define W1(i) work1(JB, IC<i>{})
        if constexpr (jb < 10) G1_STAGE_TILE(jb - 1 + G1_NST, ((jb - 1) % G1_NST) * G1_STAGE, W1);
        else G1_STAGE_A(jb - 1 + G1_NST, ((jb - 1) % G1_NST) * G1_STAGE, W1);
        if constexpr (jb == 12) { G1_STAGE_B(13, G1_STAGE); G1_STAGE_B(14, 2 * G1_STAGE); }
        if constexpr (jb == 3) __builtin_amdgcn_raw_buffer_store_b64((u32x2){kl, tag}, rf, go, 0, 16);
        if constexpr (jb == ST_JB) {
          asm volatile("buffer_load_dwordx2 %0, %1, %2, 0 offen sc1" : "=v"(gr) : "v"(gro), "s"(rf) : "memory");
        }
        touches(IC<100 + jb>{});
        if constexpr (jb <= 7) {
          if (et == jb - 1) {
#pragma unroll
            for (int l = 0; l < WSN; ++l) bld16_nt_io(wv[l], rw, vw, l * F_ * 2);
          }
        }
        SB();
      };
      static_assert(ST_JB + 3 <= 12 && ST_JB >= 6, "schedule");
      step1(IC<1>{}); step1(IC<2>{}); step1(IC<3>{}); step1(IC<4>{}); step1(IC<5>{}); step1(IC<6>{}); step1(IC<7>{}); step1(IC<8>{}); step1(IC<9>{}); step1(IC<10>{}); step1(IC<11>{}); step1(IC<12>{});
    }
#define G2_STAGE_N(kk_, W) do { const int kb = KROT(kk_); constexpr int so = n_off(kk_); SB(); \
    DMA_(rn2, vn2, mq0, so, kb, 0, ""); W(0); DMA_(rn2, vn2, mq1, so, kb, 32 * S_ * 2, ""); W(1); \
    DMA_(rn2, vn2, mq2, so, kb, 64 * S_ * 2, ""); W(2); DMA_(rn2, vn2, mq3, so, kb, 96 * S_ * 2, ""); W(3); \
    DMA_(rn2, vn2, mq0, so + 16384, kb, 128 * S_ * 2, ""); W(4); DMA_(rn2, vn2, mq1, so + 16384, kb, 160 * S_ * 2, ""); W(5); \
    DMA_(rn2, vn2, mq2, so + 16384, kb, 192 * S_ * 2, ""); W(6); DMA_(rn2, vn2, mq3, so + 16384, kb, 224 * S_ * 2, ""); W(7); SB(); } while (0)
#define G2_STAGE_NH(kk_, h) do { const int kb = KROT(kk_); constexpr int so = n_off(kk_) + (h) * 16384; SB(); \
    DMA_(rn2, vn2, mq0, so, kb, (h) * 128 * S_ * 2, ""); DMA_(rn2, vn2, mq1, so, kb, ((h) * 128 + 32) * S_ * 2, ""); \
    DMA_(rn2, vn2, mq2, so, kb, ((h) * 128 + 64) * S_ * 2, ""); DMA_(rn2, vn2, mq3, so, kb, ((h) * 128 + 96) * S_ * 2, ""); SB(); } while (0)
#define G2_STAGE_M(kk_, W) do { const int kb = KROT(kk_); constexpr int so = G2_MRING + m_off(kk_); SB(); \
    DMA_(rm2, vm2, mb0, so, kb, 0, " sc1"); W(8); DMA_(rm2, vm2, mb0, so + 4096, kb, 32 * 8192, " sc1"); W(9); \
    DMA_(rm2, vm2, mb0, so + 8192, kb, 64 * 8192, " sc1"); W(10); DMA_(rm2, vm2, mb0, so + 12288, kb, 96 * 8192, " sc1"); W(11); SB(); } while (0)
    WAIT_VM(16); BAR();
    asm volatile("ds_write_b32 %0, %1" :: "v"(lma), "v"(g0) : "memory");
    if (pw < 2) asm volatile("ds_write_b32 %0, %1 offset:512" :: "v"(lma), "v"(g1) : "memory");
    u32x4 tv[4];
    unsigned ready = 0;
    auto need = [&](int k) __attribute__((always_inline)) {
      const int b = k_blk(k) + (k_ord(k) & 1) * 8;
#pragma unroll 1
      for (int it = 0; !((ready >> b) & 1) && it < SPIN_MAX; ++it) {
        ready |= (unsigned)__ballot(__builtin_amdgcn_raw_buffer_load_b32(rf, abq[k < 5 ? 0 : k < 9 ? 1 : 2], 0, 16) == tag);
        asm volatile("" ::: "memory");
      }
    };
    unsigned ksel, bad = 0;
    int isel;
    unsigned xw;
    auto bvec = [&](unsigned src, const unsigned (&sl)[4]) __attribute__((always_inline)) {
      return (u32x4){__builtin_amdgcn_perm(0, src, sl[0]), __builtin_amdgcn_perm(0, src, sl[1]),
                     __builtin_amdgcn_perm(0, src, sl[2]), __builtin_amdgcn_perm(0, src, sl[3])};
    };
#define BC8(v, p) (unsigned)__builtin_amdgcn_update_dpp(__builtin_amdgcn_update_dpp(0, (int)(v), 0x150 + (p), 0xf, 0x3, false), (int)(v), 0x158 + (p), 0xf, 0xc, false)
    float* lg = (float*)(smem + LDS_GU);
    f32x4 macc = {0.f, 0.f, 0.f, 0.f};
    u32x2 sv;
    unsigned hp, gbad;
    float s0, s1;
    auto work2 = [&](auto JB, auto GP) __attribute__((always_inline)) {
      constexpr int jb = decltype(JB)::value, gp = decltype(GP)::value;
      SB();
      if constexpr (jb == -1 && gp < 4) {
        constexpr int c = gp == 0 ? 0xB1 : gp == 1 ? 0x4E : gp == 2 ? 0x141 : 0x140;
        merge2(k1, k2, dpp_u<c>(k1), dpp_u<c>(k2));
        PIN2(k1, k2);
      }
      if constexpr (jb == -1 && gp == 4) {
        ksel = tk ? k2 : k1;
        isel = (0xffff - (int)(ksel & 0xffffu)) & (S_ - 1);
        k1 = (isel * D_ + d0 + rq * 16 + (lane & 7) * 2) * 2; k2 = O_ST + isel * 8;
        PIN1(ksel);
        xw = gld4_s(x, k1);
        sv = bld8_sc1(rf, k2);
      }
      if constexpr (jb == 0 && gp == 1) wait_xs<3 + 1 + 2>(xw, sv);
      if constexpr (jb == 0) {
        if constexpr (gp == 1) { if (!__all(sv.y == tag)) sv = granule_wait(stat + isel, tag); s0 = __builtin_amdgcn_rcpf(hi_bf(sv.x)); PIN1(s0); }
        if constexpr (gp == 2) { s1 = ln_elem(lo_bf(xw), lo_bf(sv.x), hi_bf(sv.x), s0); PIN1(s1); }
        if constexpr (gp == 3) { xw = pk2(s1, ln_elem(hi_bf(xw), lo_bf(sv.x), hi_bf(sv.x), s0)); PIN1(xw); }
      }
      if constexpr (jb == DG && gp < 7) { PIN1(dv[gp]); macc = MM(dv[gp], bvec(BC8(hp, gp), sel[0]), macc); PIN1(macc); }
      if constexpr ((jb == 0 && gp >= 4) || jb == 1) {
        constexpr int l = jb == 0 ? gp - 4 : gp + 4;
        if constexpr (!(l & 1)) hp = BC8(xw, l >> 1);
        PIN1(wv[l]);
        macc = MM(wv[l], bvec(hp, sel[l & 1]), macc);
        PIN2(macc, hp);
      }
      if constexpr (jb == GV_JB + 2) {
        if constexpr (gp == 0) { gbad = (gv[0].y ^ tag) | (gv[0].w ^ tag) | (gv[1].y ^ tag) | (gv[1].w ^ tag); PIN1(gbad); }
        if constexpr (gp == 1) { gbad |= (gv[2].y ^ tag) | (gv[2].w ^ tag) | (gv[3].y ^ tag) | (gv[3].w ^ tag); PIN1(gbad); }
        if constexpr (gp == 2) {
          s0 = ((__uint_as_float(gv[0].x) + __uint_as_float(gv[1].x)) + __uint_as_float(gv[2].x)) + __uint_as_float(gv[3].x);
          s1 = ((__uint_as_float(gv[0].z) + __uint_as_float(gv[1].z)) + __uint_as_float(gv[2].z)) + __uint_as_float(gv[3].z);
          PIN2(s0, s1);
        }
        if constexpr (gp == 3) {
#pragma unroll 1
          for (int it = 0; !__all(gbad == 0) && it < SPIN_MAX; ++it) {
            u32x4 q[4];
            gbad = 0;
#pragma unroll
            for (int i = 0; i < 4; ++i) { q[i] = __builtin_amdgcn_raw_buffer_load_b128(rgu, vgu, i * F_ * 16, 16); gbad |= (q[i].y ^ tag) | (q[i].w ^ tag); }
            s0 = ((__uint_as_float(q[0].x) + __uint_as_float(q[1].x)) + __uint_as_float(q[2].x)) + __uint_as_float(q[3].x);
            s1 = ((__uint_as_float(q[0].z) + __uint_as_float(q[1].z)) + __uint_as_float(q[2].z)) + __uint_as_float(q[3].z);
            asm volatile("" ::: "memory");
          }
          const unsigned gp2 = pk2(s0, s1);
          const unsigned up2 = dpp_u<0x128>(gp2);
          s0 = tk ? hi_bf(up2) : lo_bf(gp2); s1 = tk ? hi_bf(gp2) : lo_bf(up2);
          PIN2(s0, s1);
        }
        if constexpr (gp == 4) { s0 = rbf(s0 * __builtin_amdgcn_rcpf(1.f + __expf(-s0))); PIN1(s0); }
        if constexpr (gp == 5) { hp = pk2(s0 * s1, 0.f); PIN1(hp); }
      }
      if constexpr (jb >= 3 && jb <= 10 && gp >= 4) {
        constexpr int n = (jb - 3) * 8 + gp - 4;
        sld<((n >> 1) & 7) * 4096 + (n & 1) * 64>(sd, spb, KROT(12 + (n >> 4)));
      }
      if constexpr (jb == 10 && gp == 2) epi_setup();
      if constexpr (jb == 10 && gp == 4) code_touch(sd, (j * 4 + pw) * 256);
      SB();
    };
#define W2M(i) work2(IC<-1>{}, IC<i>{})
#define W20(i) work2(IC<0>{}, IC<i>{})
#define W21(i) work2(IC<1>{}, IC<i>{})
    unsigned fs;
    {
      G1_STAGE_B(15, 3 * G1_STAGE);
      G2_STAGE_NH(0, 0);
      lgkm0(sd);
      WAIT_VM(16); BAR();
      G2_STAGE_NH(0, 1);
      WAIT_VM(10); BAR();
#pragma unroll
      for (int i = 0; i < 4; ++i) kk[i] = bld16_sc1(rf, l16, ko_ + i * 1024);
#pragma unroll
      for (int l = WSN; l < 16; ++l) wv[l] = bld16_nt(rw, vw, l * F_ * 2);
      asm volatile("s_waitcnt vmcnt(%4)" : "+v"(kk[0]), "+v"(kk[1]), "+v"(kk[2]), "+v"(kk[3]) : "i"(16 - WSN) : "memory");
      touches(IC<115>{});
#pragma unroll
      for (int i = 0; i < 4; ++i) {
        k2 = max(k2, min(k1, kk[i].x)); k1 = max(k1, kk[i].x);
        k2 = max(k2, min(k1, kk[i].z)); k1 = max(k1, kk[i].z);
        bad |= (kk[i].y ^ tag) | (kk[i].w ^ tag);
      }
#pragma unroll 1
      for (int it = 0; !__all(bad == 0) && it < SPIN_MAX; ++it) {
        k1 = 0; k2 = 0; bad = 0;
#pragma unroll
        for (int i = 0; i < 4; ++i) {
          const u32x4 q = __builtin_amdgcn_raw_buffer_load_b128(rf, l16, ko_ + i * 1024, 16);
          k2 = max(k2, min(k1, q.x)); k1 = max(k1, q.x);
          k2 = max(k2, min(k1, q.z)); k1 = max(k1, q.z);
          bad |= (q.y ^ tag) | (q.w ^ tag);
        }
        asm volatile("" ::: "memory");
      }
      merge2(k1, k2, dpp_u<0xB1>(k1), dpp_u<0xB1>(k2));
      merge2(k1, k2, dpp_u<0x4E>(k1), dpp_u<0x4E>(k2));
      merge2(k1, k2, dpp_u<0x141>(k1), dpp_u<0x141>(k2));
      merge2(k1, k2, dpp_u<0x140>(k1), dpp_u<0x140>(k2));
      if ((lane & 15) == 0) *(u32x2*)(smem + LDS_KEY + (pw * 4 + rq) * 8) = (u32x2){k1, k2};
      PAGE(3);
      BAR_LGKM();
#pragma unroll
      for (int jj = 0; jj < 4; ++jj) tv[jj] = *(const u32x4*)(smem + G2_MRING + m_off(0) + (jj * 4 + pw) * 1024 + lane * 16);
      { const u32x2 kp = *(const u32x2*)(smem + LDS_KEY + ci * 8); k1 = kp.x; k2 = kp.y; }
#pragma unroll
      for (int jj = 0; jj < 4; ++jj) __builtin_amdgcn_raw_buffer_store_b128(tv[jj], rm2, vm2 + jj * 32 * 8192 + 2 * tt * 128, 0, 16);
      G2_STAGE_N(1, W2M);
      WAIT_VM(10);
      if (lane == tt) __builtin_amdgcn_raw_buffer_store_b32(tag, rf, abo, 0, 16);
      G2_STAGE_N(2, W20);
      __builtin_amdgcn_s_sleep(2);
      WAIT_VM(9);
      fs = bld4_sc1(rf, abq[0]);
    }
    BAR();
#pragma unroll
    for (int jj = 0; jj < 4; ++jj) tv[jj] = *(const u32x4*)(smem + G2_MRING + m_off(1) + (jj * 4 + pw) * 1024 + lane * 16);
    G2_STAGE_N(3, NOW);
    asm volatile("s_waitcnt vmcnt(8)" : "+v"(fs) :: "memory");
    ready = (unsigned)__ballot(fs == tag);
    need(2);
    G2_STAGE_M(2, NOW);
    need(3);
    G2_STAGE_M(3, NOW);
    fs = bld4_sc1(rf, abq[1]);
    touches(IC<1>{});
    W21(0); W21(1); W21(2); W21(3); W21(4); W21(5); W21(6); W21(7); W21(8); W21(9); W21(10); W21(11);
    asm volatile("" : "+v"(tv[0]), "+v"(tv[1]), "+v"(tv[2]), "+v"(tv[3]));
    auto step2 = [&](auto JB) __attribute__((always_inline)) {
      constexpr int jb = decltype(JB)::value;
      if constexpr (jb == 3) PAGE(4);
      if constexpr (jb == 9) PAGE(5);
      constexpr auto must = [](int q) { return (q == 2 ? 4 : 0) + ((FSM >> q) & 1) + ((q == GV_JB || q == GV_JB - 1) ? 2 : 0); };
      if constexpr (jb == 2) wait_vm<4 + must(1)>();
      else wait_vm<dn_cnt(jb - 2) + (jb == 5 ? 1 : 0) + 12 + must(jb - 1) + dn_cnt(jb - 1)>();
      if constexpr (jb == 3) { if (pw == 0 && (lane & ~8) == 0) ((unsigned*)(smem + LDS_SEL))[tk] = ksel; }
      if constexpr ((jb == 2 || jb == DG + 1)) {
        *(f32x4*)(lg + pw * 256 + lane * 4) = macc;
        macc = (f32x4){0.f, 0.f, 0.f, 0.f};
        lgkm0(sd);
      }
      BAR();
      float p0[4], p1[4];
      const int col = (pw & 1) * 64 + lane;
      if constexpr (jb == 3) {
        if (g == 0 && pw == 0 && lane == 0) __hip_atomic_store((unsigned*)flags + FL_EPOCH, epoch + 1u, __ATOMIC_RELAXED, __HIP_MEMORY_SCOPE_AGENT);
      }
      if constexpr (jb == DG + 1) {
        const float* p = lg + (pw & 1) * 32 + (lane >> 4) * 64 + ((2 * lane) & 7) * 4 + ((lane >> 2) & 3);
#pragma unroll
        for (int i = 0; i < 4; ++i) { p0[i] = p[i * 256]; p1[i] = p[i * 256 + 4]; }
      }
      if constexpr (jb == 2) {
        const float* p = lg + (col >> 5) * 64 + (col & 7) * 4 + ((col >> 3) & 3);
#pragma unroll
        for (int i = 0; i < 4; ++i) { p0[i] = p[i * 256]; p1[i] = p[i * 256 + 32]; }
      }
      if constexpr (jb == 4) {
        if (lane == 8 + tt) __builtin_amdgcn_raw_buffer_store_b32(tag, rf, abo, 0, 16);
      }
      if constexpr ((FSM >> (jb - 2)) & 1) {
        asm volatile("" : "+v"(fs));
        ready |= (unsigned)__ballot(fs == tag);
      }
      if constexpr (jb == GV_JB + 2) asm volatile("" : "+v"(gv[0]), "+v"(gv[1]), "+v"(gv[2]), "+v"(gv[3]));
#define W2(i) work2(JB, IC<i>{})
      G2_STAGE_N(jb + 2, W2);
      need(jb + 2);
      G2_STAGE_M(jb + 2, W2);
      if constexpr (jb == 2) {
#pragma unroll
        for (int jj = 0; jj < 4; ++jj) __builtin_amdgcn_raw_buffer_store_b128(tv[jj], rm2, vm2 + jj * 32 * 8192 + (2 * tt + 1) * 128, 0, 16);
      }
      if constexpr ((FSM >> jb) & 1) fs = bld4_sc1(rf, abq[jb < 5 ? 1 : 2]);
      if constexpr ((jb == GV_JB || jb == GV_JB - 1)) {
#pragma unroll
        for (int i = (jb == GV_JB ? 2 : 0); i < (jb == GV_JB ? 4 : 2); ++i) gv[i] = bld16_sc1(rgu, vgu, i * F_ * 16);
      }
      touches(JB);
#pragma unroll
      for (int i = dn_first(jb); i < dn_first(jb) + dn_cnt(jb); ++i) dv[i] = bld16_nt(rdn, vdn, i * D_ * 2);
      if constexpr (jb == 2) asm volatile("" : "+v"(p0[0]), "+v"(p0[1]), "+v"(p0[2]), "+v"(p0[3]), "+v"(p1[0]), "+v"(p1[1]), "+v"(p1[2]), "+v"(p1[3]));
      if constexpr (jb == DG + 1) {
        if (pw < 2)
          __builtin_amdgcn_raw_buffer_store_b128((u32x4){__float_as_uint((p0[0] + p0[1]) + (p0[2] + p0[3])), tag, __float_as_uint((p1[0] + p1[1]) + (p1[2] + p1[3])), tag},
                                                 mk_rsrc(wsp + WS_DN), (((pw * 8 + cg8) * 32 + rgb) * 64 + lane) * 16, 0, 16);
      }
      if constexpr (jb == 2) {
        if (moe && pw < 2)
          __builtin_amdgcn_raw_buffer_store_b128((u32x4){__float_as_uint((p0[0] + p0[1]) + (p0[2] + p0[3])), tag, __float_as_uint((p1[0] + p1[1]) + (p1[2] + p1[3])), tag},
                                                 rgu, ((mat * 4 + dq) * F_ + cg * 128 + col) * 16, 0, 16);
      }
      SB();
    };
    static_assert(DG + 1 <= 12 && DG >= GV_JB + 3 && dn_first(DG - 2) == 7, "schedule");
    step2(IC<2>{}); step2(IC<3>{}); step2(IC<4>{}); step2(IC<5>{}); step2(IC<6>{}); step2(IC<7>{}); step2(IC<8>{}); step2(IC<9>{}); step2(IC<10>{}); step2(IC<11>{}); step2(IC<12>{}); step2(IC<13>{});
    lgkm0(sd);
    PAGE(7);
    WAIT_VM(12); BAR();
    xq[0] = ld16_s<0>(x, xo); xq[1] = ld16_s<64>(x, xo); sq = ld8_s_sc1(stat, xo >> 11 << 3);
    WAIT_VM(3); BAR();
    asm volatile("s_waitcnt vmcnt(0)" : "+v"(xq[0]), "+v"(xq[1]), "+v"(sq) :: "memory");
    __builtin_amdgcn_s_setprio(0);
    key[0] = __builtin_amdgcn_readlane(ksel, 0); key[1] = __builtin_amdgcn_readlane(ksel, 8);
    nf_pre();
#pragma unroll
    for (int n = 0; n < 56; ++n) nf_step(n);
#pragma unroll
    for (int e = 0; e < 16; ++e) nf_unp(e);
#pragma unroll 1
    for (int c = 0; c < 2; ++c) {
      const unsigned kc = __builtin_amdgcn_readlane(ksel, c * 8);
      const int ic = (0xffff - (int)(kc & 0xffffu)) & (S_ - 1);
      if ((ic >> 8) != tt || ((ic >> 5) & 3) != kq) continue;
      const __amdgpu_buffer_rsrc_t rd = mk_rsrc(wsp + WS_DN + (size_t)(c * 8 + et) * 32768);
#pragma unroll 1
      for (int it = 0; it < SPIN_MAX; ++it) {
        if (__all(__builtin_amdgcn_raw_buffer_load_b32(rd, (lane & 31) * 1024 + 1020, 0, 16) == tag)) break;
        __builtin_amdgcn_s_sleep(2);
        asm volatile("" ::: "memory");
      }
      float s0, s1;
      unsigned bad = 1;
#pragma unroll 1
      for (int it = 0; !__all(bad == 0) && it < SPIN_MAX; ++it) {
        bad = 0; s0 = 0.f; s1 = 0.f;
#pragma unroll
        for (int i = 0; i < 8; ++i) {
          const u32x4 q = __builtin_amdgcn_raw_buffer_load_b128(rd, rq * 1024 + pw * 256 + ci * 16, i * 4096, 16);
          bad |= (q.y ^ tag) | (q.w ^ tag); s0 += __uint_as_float(q.x); s1 += __uint_as_float(q.z);
        }
        asm volatile("" ::: "memory");
      }
      s0 = fold32(s0, s1);
      s0 = fold16(s0, s0);
      const float sg = rbf(1.f / (1.f + __expf(-key_score(kc))));
      ((float*)(smem + LDS_FIN))[c * 128 + pw * 32 + ci * 2 + (lane >> 5)] = rbf(rbf(s0) * sg);
      asm volatile("s_waitcnt lgkmcnt(0)" ::: "memory");
      if (lane == 0) asm volatile("ds_add_u32 %0, %1" :: "v"(lds_addr(smem + LDS_SEL + 24 + c * 4)), "v"(1) : "memory");
    }
    asm volatile("v_mov_b32 %0, 0" : "=v"(fx), "=v"(ya[0]), "=v"(ya[1]), "=v"(ya[2]), "=v"(ya[3]));
  } else {
  {
    const int q = j * 4 + wid;
    constexpr int Q1 = WS_MB, Q2 = Q1 + 8, Q3 = Q2 + 8, Q4 = Q3 + 7, Q5 = Q4 + 7, Q6 = Q5 + 7, Q7 = Q6 + 4, Q8 = Q7 + 4, QC = Q8 + 1;
    static_assert(QC <= 128, "warm-up slots");
    unsigned dmy = 0;
    if (q < QC) {
      const char* base; int b0;
      if (q < Q1) { base = wsp; b0 = 0; }
      else if (q < Q2) { base = (const char*)ws_re; b0 = Q1; }
      else if (q < Q3) { base = (const char*)ws_im; b0 = Q2; }
      else if (q < Q4) { base = (const char*)w_gate; b0 = Q3; }
      else if (q < Q5) { base = (const char*)w_up; b0 = Q4; }
      else if (q < Q6) { base = (const char*)w_down; b0 = Q5; }
      else if (q < Q7) { base = (const char*)out; b0 = Q6; }
      else if (q < Q8) { base = (const char*)x; b0 = Q7; }
      else { base = (const char*)w_r; b0 = Q8; }
      base += (size_t)(q - b0) << 20;
      asm volatile("s_load_dword %0, %1, 0" : "+s"(dmy) : "s"(base) : "memory");
    }
    {
      const int w = g * 4 + wid;
      constexpr int R = (int)(WS_FLAGS - WS_DN);
      static_assert(FL_X == 0 && FL_ST == 6144 * 32 && FL_EPOCH == 6528 * 32, "");
      const int o = lane < 6 ? R + (lane * 1024 + w) * 128 : lane > 6 || w >= 896 ? (int)0x80000000 : w < 512 ? w * 1024 + 896 : R + (6144 - 512 + w) * 128;
      asm volatile("s_mov_b32 m0, %0\n\ts_nop 0\n\tbuffer_load_dword %1, %2, 0 offen sc1 lds"
                   :: "s"(lds_addr(smem + LDS_DUMMY) + wid * 1024 + 512), "v"(o), "s"(mk_rsrc(wsp + WS_DN)) : "memory");
    }
    if (wid == 0) { CALL_PG(1); CALL_PG(8); CALL_PG(5); }
    else if (wid == 1) { CALL_PG(2); CALL_PG(9); CALL_PG(6); }
    else if (wid == 2) { CALL_PG(3); CALL_PG(10); CALL_PG(7); }
    else { CALL_PG(4); CALL_PG(11); }
    asm volatile("s_barrier\n\ts_waitcnt lgkmcnt(0)" : "+s"(dmy) :: "memory");
  }
  PAGE(8);
  epi_setup();
  const int wr = wid >> 1, wc = wid & 1;
  const int sw = (fr >> 1) & 7;
  const int ko0 = ((0 + fq) ^ sw) * 16, ko1 = ((4 + fq) ^ sw) * 16;
  const int mrow = (fr >> 2) * 8 + (fr & 3);
  bf16x8 fm[2][4], fn[2][4];
  {
    const char* pm = smem + (wr * 64 + mrow) * 128;
    const char* pn = smem + 16384 + (wc * 64 + mrow) * 128;
    f32x4 acc[4][4];
#pragma unroll
    for (int i = 0; i < 4; ++i)
#pragma unroll
      for (int jn = 0; jn < 4; ++jn)
#pragma unroll
        for (int q = 0; q < 4; ++q) acc[i][jn][q] = 0.f;
#define TO(i) (((i) >> 1) * 4096 + ((i) & 1) * 512)
#define LDSP(T, a) ((__attribute__((address_space(3))) T*)(size_t)(a))
#define SLOTS(n) _Pragma("unroll") for (int q = 0; q < (n); ++q)
#define MFA(c, a, b) asm volatile("v_mfma_f32_16x16x32_bf16 %0, %1, %2, %0" : "+v"(c) : "v"(a), "v"(b))
#define MFA2(c, a, b, a2, b2) asm volatile("v_mfma_f32_16x16x32_bf16 %0, %1, %2, %0\n\tv_mfma_f32_16x16x32_bf16 %0, %3, %4, %0" : "+v"(c) : "v"(a), "v"(b), "v"(a2), "v"(b2))
    unsigned lm[2][2], ln[2][2];
#pragma unroll
    for (int u = 0; u < 2; ++u)
#pragma unroll
      for (int kk = 0; kk < 2; ++kk) {
        lm[u][kk] = lds_addr(pm) + u * 2 * G1_STAGE + (kk ? ko1 : ko0);
        ln[u][kk] = lds_addr(pn) + u * 2 * G1_STAGE + (kk ? ko1 : ko0);
        asm volatile("" : "+v"(lm[u][kk]), "+v"(ln[u][kk]));
      }
    auto ldm = [&](int stg, int i, int kk) __attribute__((always_inline)) { fm[kk][i] = *LDSP(const bf16x8, lm[stg >> 1][kk] + (stg & 1) * G1_STAGE + TO(i)); };
    auto ldn = [&](int stg, int jn, int kk) __attribute__((always_inline)) { fn[kk][jn] = *LDSP(const bf16x8, ln[stg >> 1][kk] + (stg & 1) * G1_STAGE + TO(jn)); };
    auto quad = [&](int a, int c, int d, int s) __attribute__((always_inline)) {
      const int i = (s < 2 || s >= 6) ? a : a + 1, jn = s < 4 ? c : d, kk = (s == 1 || s == 2 || s == 5 || s == 6);
      MFA(acc[i][jn], fm[kk][i], fn[kk][jn]);
    };
    auto quad2 = [&](int a, int c, int d, int s) __attribute__((always_inline)) {
      const int i = (s < 2 || s >= 6) ? a : a + 1, jn = s < 4 ? c : d, kk = (s == 2 || s == 6);
      MFA2(acc[i][jn], fm[kk][i], fn[kk][jn], fm[1 - kk][i], fn[1 - kk][jn]);
    };
    auto half1 = [&](int stg) __attribute__((always_inline)) {
      const int ja = (stg & 1) * 2, jb = 2 - ja;
      SLOTS(8) {
        quad(0, ja, ja + 1, q);
        if (q < 4) ldn(stg, jb + (q >> 1), q & 1); else ldm(stg, 2 + ((q - 4) >> 1), q == 5 || q == 6);
        SB();
      }
      SLOTS(4) { quad2(0, jb, jb + 1, 2 * q); SB(); }
    };
    auto half2 = [&](int stg) __attribute__((always_inline)) {
      const int ja = (stg & 1) * 2, jb = 2 - ja, nx = (stg + 1) & 3;
      SLOTS(8) {
        quad(2, jb + 1, jb, q);
        if (!(q & 1)) ldm(nx, q >> 2, q == 2 || q == 4);
        SB();
      }
      SLOTS(8) {
        quad(2, ja, ja + 1, q);
        if (!(q & 1)) ldn(nx, jb + (q >> 2), (q >> 1) & 1);
        SB();
      }
    };
    SLOTS(16) asm volatile("" : "+v"(acc[q >> 2][q & 3]));
    SB();
    SLOTS(4) { ldm(0, q >> 1, q & 1); ldn(0, q >> 1, q & 1); }
    SB();
#pragma unroll 1
    for (int k4 = 0; k4 < G1_NKT - 4; k4 += 4) {
      half1(0); BAR_LGKM();
      SB(); half2(0);
      half1(1); BAR_LGKM();
      SB(); half2(1);
      half1(2); BAR_LGKM();
      SB(); half2(2);
      half1(3); BAR_LGKM();
      SB(); half2(3);
    }
    bf16x8 gm[2][4], gn[2][8];
    unsigned am[2][2], an[2][2];
#pragma unroll
    for (int u = 0; u < 2; ++u)
#pragma unroll
      for (int kk = 0; kk < 2; ++kk) {
        am[u][kk] = lds_addr(pm) + (u ? 3 : 1) * G1_STAGE + (kk ? ko1 : ko0);
        an[u][kk] = lds_addr(pn) + (u ? 3 : 1) * G1_STAGE + (kk ? ko1 : ko0);
        asm volatile("" : "+v"(am[u][kk]), "+v"(an[u][kk]));
      }
    unsigned al = lds_addr(smem + LDS_MB) + (wr * 32 + fq * 8) * 4, ac = lds_addr(smem + LDS_MB) + (256 + wc * 64 + mrow) * 4;
    unsigned ae = lds_addr(smem + G2_MRING) + (wc * 64 + mrow) * 128 + (((wr * 4 + fq) ^ sw) * 16);
    asm volatile("" : "+v"(al), "+v"(ac), "+v"(ae));
    auto rdg = [&](int set, int t, int h, int r) __attribute__((always_inline)) {
      const int u = t == 15, o = t == 14 ? G1_STAGE : 0;
      if (r == 0 || r == 2 || r == 4 || r == 5) {
        const int i = r >= 4, kk = r == 2 || r == 4;
        gm[set][kk * 2 + i] = *LDSP(const bf16x8, am[u][kk] + o + h * 4096 + i * 512);
      } else {
        const int jn = r < 4 ? 0 : (r - 4) >> 1, kk = r < 4 ? r >> 1 : r & 1;
        gn[set][kk * 4 + jn] = *LDSP(const bf16x8, an[u][kk] + o + TO(jn));
      }
    };
    auto mmg = [&](int set, int h, int q) __attribute__((always_inline)) {
      const int jn = q >> 2, r = q & 3, i = (r >> 1) ^ (jn & 1), kk = r == 1 || r == 2;
      MFA(acc[h * 2 + i][jn], gm[set][kk * 2 + i], gn[set][kk * 4 + jn]);
    };
    f32x4 rsv[2], mrv[2];
    float cev[4], tc[32];
    auto rds = [&](int h, int r) __attribute__((always_inline)) {
      if (r < 2) mrv[r] = *LDSP(const f32x4, al + h * 256 + r * 16); else rsv[r - 2] = *LDSP(const f32x4, al + 512 + h * 256 + (r - 2) * 16);
    };
    auto prep = [&](int n) __attribute__((always_inline)) {
      tc[n] = llvm_fmul_legacy(mrv[(n >> 2) & 1][n & 3], cev[n >> 3]);
      asm volatile("" : "+v"(tc[n]));
    };
    u32x4 ep;
    auto epi = [&](int h, int q) __attribute__((always_inline)) {
      const int jn = q >> 1, i = q & 1;
      asm volatile("v_fmac_f32 %0, %4, %8\n\tv_fmac_f32 %1, %5, %9\n\tv_fmac_f32 %2, %6, %10\n\tv_fmac_f32 %3, %7, %11"
                   : "+v"(tc[q * 4]), "+v"(tc[q * 4 + 1]), "+v"(tc[q * 4 + 2]), "+v"(tc[q * 4 + 3])
                   : "v"(acc[h * 2 + i][jn][0]), "v"(acc[h * 2 + i][jn][1]), "v"(acc[h * 2 + i][jn][2]), "v"(acc[h * 2 + i][jn][3]),
                     "v"(rsv[i][0]), "v"(rsv[i][1]), "v"(rsv[i][2]), "v"(rsv[i][3]));
      ep[i * 2] = pk2(tc[q * 4], tc[q * 4 + 1]); ep[i * 2 + 1] = pk2(tc[q * 4 + 2], tc[q * 4 + 3]);
      if (i) *LDSP(u32x4, ae + m_off(h) + TO(jn)) = ep;
    };
    SB();
    half1(0);
    BAR_LGKM();
    SB();
    SLOTS(16) {
      if (q < 8) quad(2, 3, 2, q); else quad(2, 0, 1, q - 8);
      if (q < 12) rdg(0, 13, 0, q);
      SB();
    }
    BAR();
    SB();
    SLOTS(16) {
      mmg(0, 0, q);
      if (q < 12) rdg(1, 14, 0, q);
      else { rds(0, q - 12); cev[q - 12] = *LDSP(const float, ac + (((q - 12) >> 1) * 32 + ((q - 12) & 1) * 4) * 4); }
      SB();
    }
    PAGE(9);
    BAR();
    SB();
    SLOTS(16) { mmg(1, 0, q); if ((q & 3) < 3) rdg(0, 15, 0, (q >> 2) * 3 + (q & 3)); if (q >= 8) { prep(2 * q - 16); prep(2 * q - 15); } SB(); }
    SLOTS(16) { mmg(0, 0, q); if ((q & 3) < 3) rdg(1, 13, 1, (q >> 2) * 3 + (q & 3)); prep(16 + q); SB(); if (q >= 9 && (q & 1)) { epi(0, (q - 9) >> 1); SB(); } }
    SLOTS(8) { mmg(1, 1, q); if (q < 6) { rdg(0, 14, 1, 2 * q); rdg(0, 14, 1, 2 * q + 1); } SB(); if (q & 1) { epi(0, 4 + (q >> 1)); SB(); } }
    BAR_LGKM();
    SB();
    SLOTS(8) { mmg(1, 1, 8 + q); if (q < 4) rdg(1, 15, 1, q == 0 ? 1 : q == 1 ? 3 : q + 4); else rds(1, q - 4); SB(); }
    SLOTS(16) { mmg(0, 1, q); if (q < 8) rdg(1, 15, 1, q == 0 ? 0 : q == 1 ? 2 : q < 4 ? q + 2 : q + 4); prep(q); SB(); }
    asm volatile("s_waitcnt lgkmcnt(0)" ::: "memory");
    if (lane == 0) asm volatile("ds_add_u32 %0, %1" :: "v"(lds_addr(smem + LDS_SEL + 12)), "v"(1) : "memory");
    SB();
    SLOTS(16) {
      mmg(1, 1, q);
      if (q < 4) fn[0][q] = *(const bf16x8*)(smem + (wr * 128 + fr) * 128 + (q >> 1) * 2048 + ((q & 1) ? ko1 : ko0));
      else if (q < 8) fm[q & 1][(q - 4) >> 1] = *(const bf16x8*)(smem + G2_MRING + m_off(0) + (wc * 64 + mrow) * 128 + TO((q - 4) >> 1) + ((q & 1) ? ko1 : ko0));
      prep(16 + q);
      SB();
    }
    {
      lds_vi rel = (lds_vi)(smem + LDS_SEL + 12);
      int it = 0;
      while (rel[0] != 4 && it < SPIN_MAX * 8) ++it;
      asm volatile("" ::: "memory");
    }
    asm volatile("s_nop 7");
    SB();
    SLOTS(8) { epi(1, q); SB(); }
  }

  const char* pn = smem + (wr * 128 + fr) * 128;
  const char* pm = smem + G2_MRING + (wc * 64 + mrow) * 128;
  f32x4 acc[8][4];
  auto mm = [&](bool imaj, int set, int q2, int s, int mode) __attribute__((always_inline)) {
    int i, jj, kk;
    if (imaj) { i = s >> 2; const int r = s & 3; jj = (r >> 1) ^ (i & 1); kk = (r == 1 || r == 2); }
    else { jj = s >> 3; const int r = s & 7; i = jj ? 3 - (r >> 1) : (r >> 1); kk = ((r + 1) >> 1) & 1; }
    if (mode == 1 && !(s & 1)) asm volatile("v_mfma_f32_16x16x32_bf16 %0, %1, %2, 0" : "=&v"(acc[q2 * 2 + jj][i]) : "v"(fm[kk][i]), "v"(fn[set][jj * 2 + kk]));
    else if (mode) asm volatile("v_mfma_f32_16x16x32_bf16 %0, %1, %2, %0" : "+v"(acc[q2 * 2 + jj][i]) : "v"(fm[kk][i]), "v"(fn[set][jj * 2 + kk]));
    else acc[q2 * 2 + jj][i] = __builtin_amdgcn_mfma_f32_16x16x32_bf16(fm[kk][i], fn[set][jj * 2 + kk], acc[q2 * 2 + jj][i], 0, 0, 0);
  };
  unsigned bn[2], bm[2], an[2], am[2];
#pragma unroll
  for (int kk = 0; kk < 2; ++kk) {
    bn[kk] = lds_addr(pn) + (kk ? ko1 : ko0); bm[kk] = lds_addr(pm) + (kk ? ko1 : ko0);
    asm volatile("" : "+v"(bn[kk]), "+v"(bm[kk]));
    an[kk] = bn[kk]; am[kk] = bm[kk] + m_off(0);
  }
#define RDN(jt, kk, o) (*LDSP(const bf16x8, an[kk] + (o) + (jt) * 2048))
#define RDM(i, kk, o) (*LDSP(const bf16x8, am[kk] + (o) + TO(i)))
  PAGE(10);
  tag = __builtin_amdgcn_readfirstlane(((const unsigned*)(smem + LDS_SEL))[5]);
  {
    int son = 0;
    auto tile = [&](int kt, int zero) __attribute__((always_inline)) {
      SLOTS(16) {
        mm(true, 0, 0, q, zero);
        if (!(q & 1)) {
          if (q < 8) fm[(q >> 1) & 1][2 + (q >> 2)] = RDM(2 + (q >> 2), (q >> 1) & 1, 0);
          else { const int r = (q - 8) >> 1; fn[1][r] = RDN(2 + (r >> 1), r & 1, 0); }
        }
        SB();
      }
      SLOTS(16) {
        mm(false, 1, 1, q, zero);
        if (!(q & 1) && q < 8) fn[0][q >> 1] = RDN(4 + (q >> 2), (q >> 1) & 1, 0);
        SB();
      }
      const int som = son >> 1;
      son = son == 65536 ? 0 : son + 32768;
      SLOTS(16) {
        mm(false, 0, 2, q, zero);
        if (!(q & 1) && q < 8) fn[1][q >> 1] = RDN(6 + (q >> 2), (q >> 1) & 1, 0);
        if (q == 9 || q == 11) { an[q == 11] = bn[q == 11] + son; asm volatile("" : "+v"(an[q == 11])); }
        if (q == 13 || q == 15) { am[q == 15] = bm[q == 15] + som; asm volatile("" : "+v"(am[q == 15])); }
        SB();
      }
      BAR_LGKM();
      SB();
      SLOTS(16) {
        mm(true, 1, 3, q, zero);
        if (!(q & 1)) {
          if (q < 8) fn[0][q >> 1] = RDN(q >> 2, (q >> 1) & 1, 0);
          else fm[(q >> 1) & 1][(q - 8) >> 2] = RDM((q - 8) >> 2, (q >> 1) & 1, 0);
        }
        SB();
      }
    };
    SB();
    tile(0, 1);
#pragma unroll 1
    for (int kt = 1; kt < G2_NKT - 3; ++kt) tile(kt, 0);
  }
  {
    constexpr int NG = 12;
    static constexpr int g_t[NG] = {0, 0, 1, 1, 2, 2, 2, 1, 0, 0, 1, 2};
    static constexpr int g_q[NG] = {0, 1, 0, 1, 0, 1, 2, 2, 2, 3, 3, 3};
    constexpr int G14 = 1, G15 = 3, GP0 = 5, GP1 = 6;
    constexpr auto chp = [](int g) { return g == 0 || g_t[g] != g_t[g - 1]; };
    constexpr auto chn = [](int g) { return g + 1 < NG && g_t[g + 1] != g_t[g]; };
    static_assert(n_off(13) == 32768 && n_off(14) == 65536 && n_off(15) == 0, "");
    unsigned an2[2];
#pragma unroll
    for (int kk = 0; kk < 2; ++kk) {
      an[kk] = bn[kk]; an2[kk] = bn[kk] + 65536; am[kk] = bm[kk];
      asm volatile("" : "+v"(an[kk]), "+v"(an2[kk]), "+v"(am[kk]));
    }
    int vo;
    u32x4 pk;
    u32x2 kv;
    constexpr int NF0 = 152;
    auto publish = [&](int d, int h) __attribute__((always_inline)) {
      if (h == 0) { vo = lane; PINV(vo); vo = vo * 16 + (((d + kq + 1) & 3) * 4 + kq) * 4 * MSG; PINV(vo); }
      const f32x4 v = acc[2 * d + (h >> 2)][h & 3];
      pk[(h & 1) * 2] = pk2(v[0], v[1]); pk[(h & 1) * 2 + 1] = pk2(v[2], v[3]);
      if (h & 1) __builtin_amdgcn_raw_buffer_store_b128(pk, rs, vo + (h >> 1) * 1024, 0, 16);
    };
    int fo = lane == 0 ? kq * 512 : (int)0x80000000;
#define MSG_FLAG(d) __builtin_amdgcn_raw_buffer_store_b32(tag, rc, fo + (((d) + kq + 1) & 3) * 2048, 0, 16)
    auto rdn = [&](int g, int r) __attribute__((always_inline)) {
      const int t = g_t[g], jt = g_q[g] * 2 + (r >> 1);
      fn[g & 1][r] = *LDSP(const bf16x8, (t == 1 ? an2[r & 1] : an[r & 1]) + (t == 0 ? 32768 : 0) + jt * 2048);
    };
    auto rdm = [&](int t, int i, int kk) __attribute__((always_inline)) { fm[kk][i] = *LDSP(const bf16x8, am[kk] + m_off(13 + t) + TO(i)); };
    auto group = [&](auto GI) __attribute__((always_inline)) {
      constexpr int gi = decltype(GI)::value;
      if (gi == G14) { BAR();
        SB(); }
      if (gi == G15) {
        PAGE(11);
        BAR();
        SB();
        xq[0] = ld16_s<0>(x, xo); xq[1] = ld16_s<64>(x, xo);
        sq = ld8_s_sc1(stat, xo >> 11 << 3);
        kv = *(const u32x2*)(smem + LDS_SEL);
        SB();
      }
#pragma unroll
      for (int q = 0; q < 16; ++q) {
        const int n = gi * 16 + q;
        if (n == NF0) {
          wait_xq<12>(xq, sq);
          key[0] = __builtin_amdgcn_readfirstlane(kv.x); key[1] = __builtin_amdgcn_readfirstlane(kv.y);
          nf_pre();
          SB();
        }
        if (n == 112) {
        }
        if (n == 152) {
        }
        if (n == 168) {
          WAIT_VM(8); MSG_FLAG(0); SB(); }
        if (n == 176) { WAIT_VM(5); MSG_FLAG(1); SB(); }
        if (n == 184) {
          WAIT_VM(2);
          MSG_FLAG(2); fx = bld4_sc1(rc, po); SB(); }
        mm(chp(gi) || chn(gi), gi & 1, g_q[gi], q, 2);
        constexpr bool ni = gi + 1 < NG && (chp(gi + 1 < NG ? gi + 1 : gi) || chn(gi + 1 < NG ? gi + 1 : gi));
        auto nr = [](int r) { return ni && r >= 2 ? 5 - r : r; };
        if constexpr (chp(gi) && chn(gi)) {
          if (q < 4) rdm(g_t[gi], 2 + (q >> 1), q & 1); else if (q < 8) rdn(gi + 1, nr(q - 4)); else if (q < 12) rdm(g_t[gi + 1], (q - 8) >> 1, q & 1);
        } else if constexpr (chp(gi)) {
          if (q < 4) rdm(g_t[gi], 2 + (q >> 1), q & 1);
          else if (gi + 1 < NG && q < 12 && !(q & 1)) rdn(gi + 1, nr((q - 4) >> 1));
        } else if constexpr (chn(gi)) {
          if (!(q & 1)) { if (q < 8) rdn(gi + 1, nr(q >> 1)); else rdm(g_t[gi + 1], (q - 8) >> 2, (q >> 1) & 1); }
        } else {
          if (!(q & 1) && q < 8) rdn(gi + 1, nr(q >> 1));
        }
        if (gi == GP0 && q < 8) publish(0, q);
        if (gi == GP1 && q < 8) publish(1, q);
        if (gi == 9 && q < 8) publish(2, q);
        if (n >= NF0) {
          const int m = (n - NF0) * 2;
          if (m < 56) { nf_step(m); nf_step(m + 1); } else if (m < 72) { nf_unp(m - 56); nf_unp(m - 55); }
        }
        SB();
      }
    };
#define GR4(i) group(IC<i>{}); group(IC<i + 1>{}); group(IC<i + 2>{}); group(IC<i + 3>{});
    GR4(0) GR4(4) GR4(8)
    asm volatile("s_nop 7");
    SB();
  }
#pragma unroll
  for (int i = 0; i < 4; ++i) *(f32x4*)(smem + LDS_ACC + wr * 16384 + wc * 4096 + i * 1024 + lane * 16) = acc[7][i];
#pragma unroll
  for (int i = 0; i < 4; ++i) ya[i] = acc[6][i];
  asm volatile("s_waitcnt lgkmcnt(0)" ::: "memory");
  if (lane == 0) sig[0] = 2;
  }

  PAGE(6);
  const unsigned rel0 = 0xffff - (key[0] & 0xffffu) - trow, rel1 = 0xffff - (key[1] & 0xffffu) - trow;
  if (min(rel0, rel1) < 16) {
#pragma unroll 1
    for (int c = 0; c < 2; ++c) {
      const unsigned rel = c ? rel1 : rel0;
      if (rel >= 16) continue;
      lds_vi cnt = (lds_vi)(smem + LDS_SEL + 24 + c * 4);
      int it = 0;
      while (cnt[0] != 4 && it < SPIN_MAX * 8) { __builtin_amdgcn_s_sleep(1); ++it; }
      asm volatile("" ::: "memory");
      if (rel == fr) {
        const float* fp = (const float*)(smem + LDS_FIN + c * 512) + (wid & 1) * 64 + fq * 8;
#pragma unroll
        for (int u4 = 0; u4 < 4; ++u4) {
          const f32x4 v = *(const f32x4*)(fp + (u4 >> 1) * 32 + (u4 & 1) * 4);
#pragma unroll
          for (int k = 0; k < 4; k += 2) {
            const unsigned u = pk2(nf[u4 * 4 + k] + v[k], nf[u4 * 4 + k + 1] + v[k + 1]);
            nf[u4 * 4 + k] = lo_bf(u); nf[u4 * 4 + k + 1] = hi_bf(u);
          }
        }
      }
    }
  }
  u32x4 pp[3][2];
  unsigned e_lo = 0x00003f80u, e_hi = 0x3f800000u;
  asm volatile("" : "+v"(e_lo), "+v"(e_hi));
  __amdgpu_buffer_rsrc_t rs_ = rs, rc_ = rc, ro_ = mk_rsrc(out);
  int mo[3] = {MSG_IDX(0) * 4 * MSG, MSG_IDX(1) * 4 * MSG, MSG_IDX(2) * 4 * MSG}, mv = lane * 16 + ha * 2048, mv2 = mv + 1024;
  asm volatile("" : "+s"(rs_), "+s"(rc_), "+s"(ro_), "+s"(mo[0]), "+s"(mo[1]), "+s"(mo[2]));
  unsigned as_ = lds_addr((const void*)smem) + LDS_SIG + (wid & 3) * 4, aa_ = lds_addr(smem + LDS_ACC) + (wid & 2) * 8192 + (wid & 1) * 4096 + lane * 16;
  int xs = ((trow + (lane >> 3)) * D_ + et * 128 + (wid & 1) * 64) * 2 + (lane & 7) * 16, xs2 = xs + 8 * D_ * 2;
  const unsigned mt = lds_addr(smem) + (wid & 2) * 8192 + (ha ? LDS_ACC + (wid & 1) * 4096 : n_off(14) + (wid & 1) * 2048);
  unsigned aw_ = mt + fr * 128 + ((fq ^ (fr & 7)) * 16), aw2 = aw_ ^ 64, ar_ = mt + (lane >> 3) * 128 + (((lane ^ (lane >> 3)) & 7) * 16);
  asm volatile("" : "+v"(mv), "+v"(mv2), "+v"(as_), "+v"(aa_), "+v"(xs), "+v"(xs2), "+v"(aw_), "+v"(aw2), "+v"(ar_));
  {
    unsigned sv_, it_ = SPIN_MAX * 8;
    asm volatile("s_cmp_eq_u32 %9, 0\n\ts_cbranch_scc1 2f\n"
                 "1:\n\tds_read_b32 %0, %10\n\ts_waitcnt lgkmcnt(0)\n\tv_cmp_eq_u32 vcc, 2, %0\n\ts_cbranch_vccnz 3f\n\ts_sleep 1\n\ts_sub_u32 %1, %1, 1\n\ts_cbranch_scc0 1b\n"
                 "3:\n\tds_read_b128 %4, %11\n\tds_read_b128 %5, %11 offset:1024\n\tds_read_b128 %6, %11 offset:2048\n\tds_read_b128 %7, %11 offset:3072\n"
                 "2:\n\ts_waitcnt vmcnt(0)\n\tv_cmp_eq_u32 vcc, %8, %3\n\ts_mov_b32 %2, vcc_lo"
                 : "=&v"(sv_), "+s"(it_), "=&s"(seen), "+v"(fx), "+v"(ya[0]), "+v"(ya[1]), "+v"(ya[2]), "+v"(ya[3])
                 : "s"(tag), "s"(ha), "v"(as_), "v"(aa_) : "vcc", "scc", "memory");
  }
#define MSG_POLL(m) do { \
    if (!((seen >> (m)) & 1)) { \
      unsigned pv_, it_ = SPIN_MAX; \
      asm volatile("1:\n\tbuffer_load_dword %0, %3, %4, 0 offen sc1\n\ts_waitcnt vmcnt(0)\n\tv_cmp_eq_u32 vcc, %5, %0\n\ts_mov_b32 %1, vcc_lo\n\t" \
                   "s_bitcmp1_b32 %1, %6\n\ts_cbranch_scc1 2f\n\ts_sub_u32 %2, %2, 1\n\ts_cbranch_scc1 2f\n\ts_sleep 1\n\ts_branch 1b\n2:" \
                   : "=&v"(pv_), "+s"(seen), "+s"(it_) : "v"(po), "s"(rc_), "s"(tag), "i"(m) : "vcc", "scc", "memory"); \
    } \
    pp[m][0] = __builtin_amdgcn_raw_buffer_load_b128(rs_, mv, mo[m], 16); \
    pp[m][1] = __builtin_amdgcn_raw_buffer_load_b128(rs_, mv2, mo[m], 16); } while (0)
#define MSG_ADD(m) do { \
    _Pragma("unroll") for (int u = 0; u < 8; ++u) { \
      ya[u >> 1][(u & 1) * 2] = add_bf(pp[m][u >> 2][u & 3], e_lo, ya[u >> 1][(u & 1) * 2]); ya[u >> 1][(u & 1) * 2 + 1] = add_bf(pp[m][u >> 2][u & 3], e_hi, ya[u >> 1][(u & 1) * 2 + 1]); } } while (0)
  MSG_POLL(2); MSG_POLL(1); MSG_POLL(0);
  asm volatile("s_waitcnt lgkmcnt(0)" : "+v"(ya[0]), "+v"(ya[1]), "+v"(ya[2]), "+v"(ya[3]) :: "memory");
  SB();
  MSG_ADD(2); MSG_ADD(1);
  asm volatile("" : "+v"(ya[0]), "+v"(ya[1]), "+v"(ya[2]), "+v"(ya[3]));
  SB();
  MSG_ADD(0);
  {
    unsigned ov[8];
    SB();
#pragma unroll
    for (int u = 0; u < 8; ++u) ov[u] = pk2(ya[u >> 1][(u & 1) * 2], ya[u >> 1][(u & 1) * 2 + 1]);
    SB();
#pragma unroll
    for (int u = 0; u < 8; ++u) { xf[2 * u] = add_bf(ov[u], e_lo, xf[2 * u]); xf[2 * u + 1] = add_bf(ov[u], e_hi, xf[2 * u + 1]); }
    SB();
#pragma unroll
    for (int u = 0; u < 8; ++u) ov[u] = pk2(xf[2 * u], xf[2 * u + 1]);
    SB();
#pragma unroll
    for (int u = 0; u < 8; ++u) { nf[2 * u] = add_bf(ov[u], e_lo, nf[2 * u]); nf[2 * u + 1] = add_bf(ov[u], e_hi, nf[2 * u + 1]); }
    SB();
#pragma unroll
    for (int u = 0; u < 8; ++u) ov[u] = pk2(nf[2 * u], nf[2 * u + 1]);
    SB();
    *LDSP(u32x4, aw_) = (u32x4){ov[0], ov[1], ov[2], ov[3]};
    *LDSP(u32x4, aw2) = (u32x4){ov[4], ov[5], ov[6], ov[7]};
    asm volatile("" ::: "memory");
    __builtin_amdgcn_raw_buffer_store_b128(*LDSP(const u32x4, ar_), ro_, xs, 0, 16);
    __builtin_amdgcn_raw_buffer_store_b128(*LDSP(const u32x4, ar_ + 1024), ro_, xs2, 0, 16);
  }
}

extern "C" void kernel_launch(void* const* d_in, const int* in_sizes, int n_in, void* d_out, int out_size,
                              void* d_ws, size_t ws_size, hipStream_t stream) {
  const u16* x = (const u16*)d_in[0];
  const u16* ws_re = (const u16*)d_in[1];
  const u16* ws_im = (const u16*)d_in[2];
  const u16* wd_re = (const u16*)d_in[3];
  const u16* wd_im = (const u16*)d_in[4];
  const u16* w_r = (const u16*)d_in[6];
  const u16* w_gate = (const u16*)d_in[7];
  const u16* w_up = (const u16*)d_in[8];
  const u16* w_down = (const u16*)d_in[9];
  k_main<<<256, 512, 0, stream>>>(x, 0x7fffffffu, 0x00020000u, wd_re, 0x7fffffffu, 0x00020000u, wd_im, 0x7fffffffu, 0x00020000u, w_r, (char*)d_ws,
                                  ws_re, ws_im, w_gate, w_up, w_down, (u16*)d_out);
}
```

```cpp
#include <hip/hip_runtime.h>
#include <hip/hip_bf16.h>
#include <stdint.h>
#include <type_traits>

extern "C" __device__ unsigned long long llvm_dispatch_id(void) __asm("llvm.amdgcn.dispatch.id");
extern "C" __device__ float llvm_fmul_legacy(float, float) __asm("llvm.amdgcn.fmul.legacy");
typedef unsigned short u16;
typedef unsigned long long u64;
typedef __attribute__((ext_vector_type(8))) short bf16x8;
typedef __attribute__((ext_vector_type(4))) unsigned u32x4;
typedef __attribute__((ext_vector_type(2))) unsigned u32x2;
typedef __attribute__((ext_vector_type(4))) float f32x4;
typedef __attribute__((ext_vector_type(2))) float f32x2;
typedef __attribute__((ext_vector_type(2))) __bf16 bf16x2;
typedef __attribute__((address_space(3))) void* lds_vp;
typedef volatile __attribute__((address_space(3))) int* lds_vi;

constexpr int S_ = 2048, D_ = 1024, F_ = 3584;
template <int N> using IC = std::integral_constant<int, N>;

static __device__ __forceinline__ float bf2f(u16 u) { return __uint_as_float((unsigned)u << 16); }
static __device__ __forceinline__ u16 f2bf(float f) {
  __hip_bfloat16 b = __float2bfloat16(f);
  return *reinterpret_cast<u16*>(&b);
}
static __device__ __forceinline__ float rbf(float f) { return bf2f(f2bf(f)); }
static __device__ __forceinline__ float lo_bf(unsigned u) { return __uint_as_float(u << 16); }
static __device__ __forceinline__ float hi_bf(unsigned u) { return __uint_as_float(u & 0xffff0000u); }
static __device__ __forceinline__ unsigned pk2(float a, float b) {
  const bf16x2 v = __builtin_convertvector((f32x2){a, b}, bf16x2);
  return __builtin_bit_cast(unsigned, v);
}
static __device__ __forceinline__ float add_bf(unsigned u, unsigned e, float c) {
  return __builtin_amdgcn_fdot2_f32_bf16(__builtin_bit_cast(bf16x2, u), __builtin_bit_cast(bf16x2, e), c, false);
}
static __device__ __forceinline__ float ln_elem(float x, float mb, float sd, float ri) {
  const float d = rbf(x - mb);
  const float q0 = d * ri;
  return rbf(__builtin_fmaf(__builtin_fmaf(-q0, sd, d), ri, q0));
}
template <int CTRL> static __device__ __forceinline__ float dpp_f(float v) {
  return __int_as_float(__builtin_amdgcn_update_dpp(0, __float_as_int(v), CTRL, 0xf, 0xf, true));
}
template <int CTRL> static __device__ __forceinline__ unsigned dpp_u(unsigned v) {
  return (unsigned)__builtin_amdgcn_update_dpp(0, (int)v, CTRL, 0xf, 0xf, true);
}
static __device__ __forceinline__ float row_sum(float v) {
  v += dpp_f<0xB1>(v);
  v += dpp_f<0x4E>(v);
  v += dpp_f<0x141>(v);
  v += dpp_f<0x140>(v);
  return v;
}
static __device__ __forceinline__ float fold32(float a, float b) {
  const auto r = __builtin_amdgcn_permlane32_swap(__float_as_uint(a), __float_as_uint(b), false, false);
  return __uint_as_float(r[0]) + __uint_as_float(r[1]);
}
static __device__ __forceinline__ float fold16(float a, float b) {
  const auto r = __builtin_amdgcn_permlane16_swap(__float_as_uint(a), __float_as_uint(b), false, false);
  return __uint_as_float(r[0]) + __uint_as_float(r[1]);
}
static __device__ __forceinline__ float rdl(float v, int l) { return __int_as_float(__builtin_amdgcn_readlane(__float_as_int(v), l)); }

#define ST_JB 9
#define WSN 10
#define FSM 0xa2
#define GV_JB 7
#define DG 11
#define AB_STRIDE 32
#define SPIN_MAX (1 << 17)

static __device__ __forceinline__ unsigned lds_addr(const void* p) { return (unsigned)(size_t)(lds_vp)p; }

constexpr int MSG = 4096;
constexpr size_t WS_ABT   = 0;
constexpr size_t WS_SLAB  = WS_ABT + (size_t)D_ * 4096 * 2;
constexpr size_t WS_GU    = WS_SLAB + (size_t)64 * 64 * MSG;
constexpr size_t WS_DN    = WS_GU + (size_t)8 * F_ * 16;
constexpr size_t WS_FLAGS = WS_DN + (size_t)2 * 8 * 32 * 64 * 16;
constexpr int FL_X = 0;
constexpr int FL_AB = 64 * 64 * 32;
constexpr int FL_ST = FL_AB + 2048 * AB_STRIDE;
constexpr int FL_CE = FL_ST + 4096;
constexpr int FL_KEY = FL_CE + 4096;
constexpr int FL_EPOCH = FL_KEY + 4096;
constexpr int WS_MB = (int)((WS_FLAGS + (FL_EPOCH + 1) * 4 + 1048575) >> 20);

#define WAIT_VM(n) asm volatile("s_waitcnt vmcnt(" #n ")" ::: "memory")
template <int N> static __device__ __forceinline__ void wait_vm() { asm volatile("s_waitcnt vmcnt(%0)" :: "i"(N) : "memory"); }
#define BAR_LGKM() asm volatile("s_waitcnt lgkmcnt(0)\n\ts_barrier" ::: "memory")
#define BAR() asm volatile("s_barrier" ::: "memory")
#define SB() __builtin_amdgcn_sched_barrier(0)
#define PAGE(n) asm volatile("s_branch 2f\n\t.p2align 12\nk_pg" #n ":\n\ts_setpc_b64 s[42:43]\n2:")
#define CALL_PG(n) asm volatile("s_getpc_b64 s[40:41]\n3:\n\ts_mov_b32 s42, k_pg" #n "-3b\n\ts_ashr_i32 s43, s42, 31\n\ts_add_u32 s40, s40, s42\n\ts_addc_u32 s41, s41, s43\n\t" \
                                "s_swappc_b64 s[42:43], s[40:41]" ::: "s40", "s41", "s42", "s43", "scc", "memory")

static __device__ __forceinline__ __amdgpu_buffer_rsrc_t mk_rsrc(const void* p) {
  return __builtin_amdgcn_make_buffer_rsrc((void*)p, 0, 0x7fffffff, 0x00020000);
}
#define DMA_(rsrc, voff, mbase, MI, sbase, SI, pol) do { unsigned _t; \
    asm volatile("s_add_i32 m0, %2, %3\n\ts_add_i32 %0, %5, %6\n\tbuffer_load_dwordx4 %1, %4, %0 offen" pol " lds" \
                 : "=&s"(_t) : "v"(voff), "s"(mbase), "i"(MI), "s"(rsrc), "s"(sbase), "i"(SI) : "memory", "scc"); } while (0)
#define TOUCH(rsrc, voff, mbase, MI, soff) \
    asm volatile("s_add_i32 m0, %1, %2\n\ts_nop 0\n\tbuffer_load_dword %0, %3, %4 offen lds" \
                 :: "v"(voff), "s"(mbase), "i"(MI), "s"(rsrc), "s"(soff) : "memory", "scc")
template <int OFF> static __device__ __forceinline__ u32x4 ld16_s(const void* p, int voff) {
  u32x4 v;
  asm volatile("global_load_dwordx4 %0, %1, %2 offset:%3" : "=v"(v) : "v"(voff), "s"(p), "i"(OFF) : "memory");
  return v;
}
template <int OFF> static __device__ __forceinline__ void sld(unsigned& sink, const void* p, int soff) {
  asm volatile("s_load_dword %0, %1, %2 offset:%3" : "+s"(sink) : "s"(p), "s"(soff), "i"(OFF) : "memory");
}
static __device__ __forceinline__ unsigned gld4_s(const void* p, unsigned voff) {
  unsigned v;
  asm volatile("global_load_dword %0, %1, %2" : "=v"(v) : "v"(voff), "s"(p) : "memory");
  return v;
}
static __device__ __forceinline__ u32x2 bld8_sc1(__amdgpu_buffer_rsrc_t r, unsigned voff) {
  u32x2 v;
  asm volatile("buffer_load_dwordx2 %0, %1, %2, 0 offen sc1" : "=v"(v) : "v"(voff), "s"(r) : "memory");
  return v;
}
template <int N> static __device__ __forceinline__ void wait_xs(unsigned& a, u32x2& b) { asm volatile("s_waitcnt vmcnt(%2)" : "+v"(a), "+v"(b) : "i"(N) : "memory"); }
template <int N> static __device__ __forceinline__ void wait_xq(u32x4 (&a)[2], u32x2& b) { asm volatile("s_waitcnt vmcnt(%3)" : "+v"(a[0]), "+v"(a[1]), "+v"(b) : "i"(N) : "memory"); }
static __device__ __forceinline__ u32x2 ld8_s_sc1(const void* p, int voff) {
  u32x2 v;
  asm volatile("global_load_dwordx2 %0, %1, %2 sc1" : "=v"(v) : "v"(voff), "s"(p) : "memory");
  return v;
}
static __device__ __forceinline__ void lgkm0(unsigned& sink) { asm volatile("s_waitcnt lgkmcnt(0)" : "+s"(sink) :: "memory"); }
template <typename T> static __device__ __forceinline__ void pin4(T& a, T& b, T& c, T& d) { asm volatile("" : "+s"(a), "+s"(b), "+s"(c), "+s"(d)); }
template <typename T, typename U> static __device__ __forceinline__ void pins(T& a, U& b) { asm volatile("" : "+s"(a), "+s"(b)); }
static __device__ __forceinline__ void launder(int& v, int& a, int& b) { asm volatile("" : "+v"(v)); asm volatile("" : "+s"(a), "+s"(b)); }
static __device__ __forceinline__ unsigned bld4_sc1(__amdgpu_buffer_rsrc_t r, int voff) {
  unsigned v;
  asm volatile("buffer_load_dword %0, %1, %2, 0 offen sc1" : "=v"(v) : "v"(voff), "s"(r) : "memory");
  return v;
}
static __device__ __forceinline__ u32x4 bld16_nt(__amdgpu_buffer_rsrc_t r, int voff, int soff) {
  u32x4 v;
  asm volatile("buffer_load_dwordx4 %0, %1, %2, %3 offen nt" : "=v"(v) : "v"(voff), "s"(r), "s"(soff) : "memory");
  return v;
}
static __device__ __forceinline__ void bld16_nt_io(u32x4& v, __amdgpu_buffer_rsrc_t r, int voff, int soff) {
  asm volatile("buffer_load_dwordx4 %0, %1, %2, %3 offen nt" : "+v"(v) : "v"(voff), "s"(r), "s"(soff) : "memory");
}
static __device__ __forceinline__ u32x4 bld16_sc1(__amdgpu_buffer_rsrc_t r, int voff, int soff) {
  u32x4 v;
  asm volatile("buffer_load_dwordx4 %0, %1, %2, %3 offen sc1" : "=v"(v) : "v"(voff), "s"(r), "s"(soff) : "memory");
  return v;
}
static __device__ __forceinline__ u32x2 granule_wait(const u64* p, unsigned tag) {
  u64 v = 0;
#pragma unroll 1
  for (int it = 0; it < SPIN_MAX; ++it) {
    v = __hip_atomic_load(p, __ATOMIC_RELAXED, __HIP_MEMORY_SCOPE_AGENT);
    if (__all((unsigned)(v >> 32) == tag)) break;
    __builtin_amdgcn_s_sleep(2);
  }
  return (u32x2){(unsigned)v, (unsigned)(v >> 32)};
}
static __device__ __forceinline__ float key_score(unsigned key) {
  const unsigned u = key >> 16;
  return bf2f((u16)((u & 0x8000u) ? (u & 0x7fffu) : (~u & 0xffffu)));
}
static __device__ __forceinline__ void merge2(unsigned& a1, unsigned& a2, unsigned b1, unsigned b2) {
  const unsigned lo = min(a1, b1);
  a1 = max(a1, b1);
  a2 = max(lo, max(a2, b2));
}
static __device__ __forceinline__ float sqdev16(const u32x4 (&v)[2], float mb) {
  f32x2 q2 = {0.f, 0.f};
#pragma unroll
  for (int i = 0; i < 8; ++i) {
    const f32x2 e = (f32x2){lo_bf(v[i >> 2][i & 3]), hi_bf(v[i >> 2][i & 3])} - mb;
    q2 += e * e;
  }
  return q2.x + q2.y;
}

constexpr int G1_NST = 4, G1_NKT = 16, G1_STAGE = 32768;
constexpr int G2_NKT = 16, G2_MRING = 98304;
constexpr int n_off(int k) { return (k % 3) * 32768; }
constexpr int m_off(int k) { return ((k + 2) % 3) * 16384; }
constexpr int k_ord(int k) { return k < 2 ? k : (k < 9 ? 2 * (k - 1) : 2 * (k - 8) + 1); }
constexpr int LDS_DUMMY = G2_MRING + 49152;
constexpr int LDS_GU = LDS_DUMMY + 4096;
constexpr int LDS_FIN = LDS_GU + 4096;
constexpr int LDS_MB = LDS_FIN + 4096;
constexpr int LDS_SEL = LDS_MB + 1536;
constexpr int LDS_SIG = LDS_SEL + 64;
constexpr int LDS_KEY = LDS_SIG + 32;
constexpr int LDS_BYTES = LDS_KEY + 128;
constexpr int LDS_ACC = n_off(G2_NKT - 3);

constexpr int tpos[16] = {0, 0, 0, 0, 106, 110, 115, 2, 2, 3, 4, 5, 0, 0, 0, 0};
constexpr int twho[16] = {0, 0, 0, 0, 7, 7, 7, 0, 1, 2, 6, 5, 0, 0, 0, 0};
constexpr int dn_cnt(int q) { return q == 5 ? 3 : q == 8 ? 4 : 0; }
constexpr int dn_first(int q) { return q <= 5 ? 0 : q <= 8 ? 3 : 7; }

static __device__ __forceinline__ void code_touch(unsigned& sink, unsigned off) {
  asm volatile("s_getpc_b64 s[40:41]\n\tk_main_rt:\n\ts_min_u32 %1, %1, k_main_end-k_main_rt-196\n\t"
               "s_load_dword %0, s[40:41], %1 offset:0\n\ts_load_dword %0, s[40:41], %1 offset:64\n\ts_load_dword %0, s[40:41], %1 offset:128\n\ts_load_dword %0, s[40:41], %1 offset:192"
               : "+s"(sink), "+s"(off) :: "s40", "s41", "scc", "memory");
}
static __device__ __forceinline__ void touch1(__amdgpu_buffer_rsrc_t r, int voff, unsigned mb, int soff) { TOUCH(r, voff, mb, LDS_DUMMY, soff); }
typedef const __attribute__((address_space(1))) u16* gptr;
typedef __attribute__((ext_vector_type(8))) u64 u64x8;
extern "C" __global__ __launch_bounds__(512, 2) void k_main(const u16* x, unsigned x2, unsigned x3, const u16* wd_re, unsigned r2, unsigned r3,
                                                 const u16* wd_im, unsigned i2, unsigned i3, const u16* w_r,
                                                 char* wsp, const u16* ws_re, const u16* ws_im,
                                                 const u16* w_gate, const u16* w_up, const u16* w_down, u16* out) {
  __shared__ __attribute__((aligned(16))) char smem[LDS_BYTES];
  u64x8 ka;
  u32x4 rm, rn;
  unsigned kz;
  asm volatile("s_load_dwordx4 %0, %4, 0\n\ts_load_dwordx16 %2, %4, 48\n\ts_andn2_b32 %3, 4, %5\n\ts_lshl_b32 %3, %3, 2\n\ts_load_dwordx4 %1, %4, %3 offset:16"
               : "=&s"(rm), "=&s"(rn), "=&s"(ka), "=&s"(kz) : "s"(__builtin_amdgcn_kernarg_segment_ptr()), "s"(blockIdx.x) : "scc", "memory");
  SB();
  const int tid = threadIdx.x, lane = tid & 63;
  const int wid = __builtin_amdgcn_readfirstlane(tid >> 6);
  const int g = blockIdx.x ^ 6, xcd = g & 7, j = g >> 3;
  if (wid < 4) {
    unsigned co = ((j * 4 + wid) * 4 + (lane & 3)) * 128;
    asm volatile(".subsection 1\nk_main_end:\n.subsection 0\n\tv_min_u32 %0, k_main_end-k_main-132, %0\n\ts_getpc_b64 s[40:41]\n\ts_mov_b32 m0, %1\n\ts_mov_b64 exec, 15\n\t"
                 "global_load_lds_dword %0, s[40:41]\n\ts_mov_b64 exec, -1"
                 : "+v"(co) : "i"(LDS_DUMMY + 768) : "s40", "s41", "memory");
  }
  const int pw = wid - 4;
  const int row0 = (wid & 3) * 8 + (lane >> 3);
  const int cm = ((lane & 7) ^ (((row0 >> 1) & 1) | ((wid & 3) << 1))) * 16;
  int vm = row0 * (D_ * 2) + cm;
  asm volatile("" : "+v"(vm));
  const int kq = xcd >> 1;
  const int tt = (xcd & 1) * 4 + (j >> 3);
  const int et = j & 7;
  const int z = kq >> 1, st = (kq & 1) * 8 + tt;
  unsigned o_m = st * 128 * D_ * 2, o_n = et * 128 * D_ * 2, mb0 = lds_addr(smem) + pw * 1024;
  asm volatile("" : "+s"(o_m), "+s"(o_n), "+s"(mb0));
#define G1_X(kt, so, q) DMA_(rm, vm, mb0, (so) + (q) * 4096, o_m, (kt) * 128 + (((q) & 1) * 64 + ((q) >> 1) * 32) * D_ * 2, "")
#define G1_W(kt, so, q) DMA_(rn, vm, mb0, (so) + 16384 + (q) * 4096, o_n, (kt) * 128 + (q) * 32 * D_ * 2, "")
#define G1_STAGE_TILE(kt, so, W) do { SB(); G1_X(kt, so, 0); W(0); G1_X(kt, so, 1); W(1); G1_X(kt, so, 2); W(2); G1_X(kt, so, 3); W(3); \
    G1_W(kt, so, 0); W(4); G1_W(kt, so, 1); W(5); G1_W(kt, so, 2); W(6); G1_W(kt, so, 3); W(7); SB(); } while (0)
#define G1_STAGE_A(kt, so, W) do { SB(); G1_X(kt, so, 0); W(0); G1_X(kt, so, 2); W(1); \
    G1_W(kt, so, 0); W(2); G1_W(kt, so, 1); W(3); G1_W(kt, so, 2); W(4); G1_W(kt, so, 3); W(5); W(6); W(7); SB(); } while (0)
#define G1_STAGE_B(kt, so) do { SB(); G1_X(kt, so, 1); G1_X(kt, so, 3); SB(); } while (0)
#define NOW(i)
  asm volatile("s_waitcnt lgkmcnt(0)" : "+s"(ka), "+s"(rm), "+s"(rn) :: "memory");
  int lrow, drow, l16;
  unsigned o_l, o_d;
  auto rows = [&](int i) __attribute__((always_inline)) {
    int g_ = g, l_ = lane;
    if (i == 0) { asm volatile("" : "+s"(g_)); drow = (g_ & ~7) + pw * 2; lrow = drow + (g_ & 3) * 512 + (g_ & 4) * 64; asm volatile("" : "+s"(drow), "+s"(lrow)); }
    if (i == 1) { asm volatile("" : "+s"(g_)); drow += (g_ & 3) * 256; o_l = lrow * D_ * 2; o_d = drow * D_ * 2; asm volatile("" : "+s"(drow), "+s"(o_l), "+s"(o_d)); }
    if (i == 2) { asm volatile("" : "+v"(l_)); l16 = l_ * 16; asm volatile("" : "+v"(l16)); }
  };
  if (__builtin_expect(wid >= 4, 1)) {
    G1_STAGE_TILE(0, 0, rows);
    __builtin_amdgcn_s_setprio(3);
  }
  x = (const u16*)(gptr)((u64)rm[0] | (u64)rm[1] << 32);
  const u16* wdz = (const u16*)(gptr)((u64)rn[0] | (u64)rn[1] << 32);
  w_r = (const u16*)(gptr)ka[0]; wsp = (char*)(gptr)ka[1]; ws_re = (const u16*)(gptr)ka[2]; ws_im = (const u16*)(gptr)ka[3];
  w_gate = (const u16*)(gptr)ka[4]; w_up = (const u16*)(gptr)ka[5]; w_down = (const u16*)(gptr)ka[6]; out = (u16*)(gptr)ka[7];
  int* flags = (int*)(wsp + WS_FLAGS);
  u64* stat = (u64*)(flags + FL_ST);
  const int fr = lane & 15, fq = lane >> 4;
  const int ha = wid >> 2;
  int trow, xo, po;
  const char *prs, *prc;
  auto epi_setup = [&]() __attribute__((always_inline)) {
    int t_ = tt, l_ = lane;
    asm volatile("" : "+s"(t_));
    asm volatile("" : "+v"(l_));
    trow = t_ * 256 + ((wid >> 1) & 1) * 128 + kq * 32 + ha * 16;
    xo = ((trow + (l_ & 15)) * D_ + et * 128 + (wid & 1) * 64 + (l_ >> 4) * 8) * 2;
    prs = wsp + WS_SLAB + (size_t)((t_ * 8 + et) * 64 + (wid & 3)) * MSG;
    prc = (const char*)(flags + FL_X + ((t_ * 8 + et) * 64 + (wid & 3)) * 32);
    asm volatile("" : "+s"(trow), "+s"(prs), "+s"(prc));
    po = l_ < 3 ? (kq * 4 + ((kq + 1 + l_) & 3)) * 512 : (int)0x80000000;
    asm volatile("" : "+v"(xo), "+v"(po));
  };
#define rs mk_rsrc(prs)
#define rc mk_rsrc(prc)
  lds_vi sig = (lds_vi)(smem + LDS_SIG + (wid & 3) * 4);
  unsigned tag;
  u32x4 xq[2];
  u32x2 sq;
  f32x4 ya[4];
  unsigned key[2];
  float xf[16], nf[16], lmb, lsd, lri, la, lb, lqa, lqb;
  unsigned nfp[8];
#define PINV(a) asm volatile("" : "+v"(a))
  auto nf_pre = [&]() __attribute__((always_inline)) {
    if (__builtin_expect(!__all(sq.y == tag), 0)) sq = granule_wait(stat + (xo >> 11), tag);
    lmb = lo_bf(sq.x); lsd = hi_bf(sq.x); lri = __builtin_amdgcn_rcpf(lsd);
    PINV(lmb); PINV(lsd); PINV(lri);
  };
  auto nf_step = [&](int n) __attribute__((always_inline)) {
    const int u = n / 7;
    switch (n % 7) {
      case 0: la = lo_bf(xq[u >> 2][u & 3]); PINV(la); la -= lmb; PINV(la); break;
      case 1: lb = hi_bf(xq[u >> 2][u & 3]); PINV(lb); lb -= lmb; PINV(lb); break;
      case 2: nfp[u] = pk2(la, lb); la = lo_bf(nfp[u]); PINV(la); PINV(nfp[u]); break;
      case 3: lqa = la * lri; PINV(lqa); lb = hi_bf(nfp[u]); PINV(lb); break;
      case 4: la = __builtin_fmaf(-lqa, lsd, la); PINV(la); lqb = lb * lri; PINV(lqb); break;
      case 5: lqa = __builtin_fmaf(la, lri, lqa); PINV(lqa); lb = __builtin_fmaf(-lqb, lsd, lb); PINV(lb); break;
      default: lqb = __builtin_fmaf(lb, lri, lqb); nfp[u] = pk2(lqa, lqb); PINV(nfp[u]); break;
    }
  };
  auto nf_unp = [&](int e) __attribute__((always_inline)) {
    const int u = e >> 1;
    if (e == 0) { PINV(xq[0]); PINV(xq[1]); }
    xf[e] = (e & 1) ? hi_bf(xq[u >> 2][u & 3]) : lo_bf(xq[u >> 2][u & 3]);
    nf[e] = (e & 1) ? hi_bf(nfp[u]) : lo_bf(nfp[u]);
    PINV(xf[e]); PINV(nf[e]);
  };
#define MSG_IDX(m) (kq * 4 + ((kq + 1 + (m)) & 3))
  unsigned seen, fx;

  if (__builtin_expect(wid >= 4, 1)) {
    constexpr int O_ST = (FL_ST - FL_AB) * 4, O_CE = (FL_CE - FL_AB) * 4, O_KEY = (FL_KEY - FL_AB) * 4, O_EP = (FL_EPOCH - FL_AB) * 4;
    const __amdgpu_buffer_rsrc_t rf = mk_rsrc(wsp + WS_FLAGS + FL_AB * 4);
    u32x4 lv[2], lv2[2], lw[2], ld[2], ld2[2];
    unsigned epv;
    {
      const char* xrow = (const char*)x + o_l;
      const char* wdrow = (const char*)wdz + o_d;
      lv[0] = ld16_s<0>(xrow, l16); lv[1] = ld16_s<1024>(xrow, l16);
      lv2[0] = ld16_s<2048>(xrow, l16); lv2[1] = ld16_s<3072>(xrow, l16);
      ld[0] = ld16_s<0>(wdrow, l16); ld[1] = ld16_s<1024>(wdrow, l16);
      ld2[0] = ld16_s<2048>(wdrow, l16); ld2[1] = ld16_s<3072>(wdrow, l16);
      lw[0] = ld16_s<0>(w_r, l16); lw[1] = ld16_s<1024>(w_r, l16);
      asm volatile("buffer_load_dword %0, off, %1, %2 sc1" : "=v"(epv) : "s"(rf), "s"(O_EP) : "memory");
    }
    SB();
    const unsigned long long did = llvm_dispatch_id();
    const unsigned dmix = (unsigned)did * 0x85EBCA6Bu + (unsigned)(did >> 32) * 0xC2B2AE35u + 0x27D4EB2Fu;
    const bool moe = et != 7;
    unsigned epoch;
    const int ci = lane & 15, rq = lane >> 4, tk = (lane >> 3) & 1;
    int mat, cg, dq, d0;
    u32x4 wv[16];
    const u16 *pgu, *pn2;
    const char *pm2, *spb;
#define rw __builtin_amdgcn_make_buffer_rsrc((void*)pgu, 0, moe ? 0x7fffffff : 0, 0x00020000)
#define rn2 mk_rsrc(pn2)
#define rm2 mk_rsrc(pm2)
    int vw, vn2, vm2;
    int ord2;
    auto k_blk = [&](int k) __attribute__((always_inline)) { return k >= 9 ? (ord2 >> (4 * (k - 9))) & 7 : tt ^ (k_ord(k) >> 1); };
#define KROT(kk_) (k_blk(kk_) * 256 + (k_ord(kk_) & 1) * 128)
    int tv2;
    unsigned sd = 0;
    auto touches = [&](auto P) __attribute__((always_inline)) {
      constexpr int p = decltype(P)::value;
      int e = et;
      asm volatile("" : "+s"(e));
#define TCH(k) if constexpr (tpos[k] == p) { if (e == twho[k]) { const int kb = KROT(k); touch1(rn2, tv2, mb0, kb); } }
      TCH(0) TCH(1) TCH(2) TCH(3) TCH(4) TCH(5) TCH(6) TCH(7) TCH(8) TCH(9) TCH(10) TCH(11) TCH(12) TCH(13) TCH(14) TCH(15)
    };
    unsigned mq0, mq1, mq2, mq3;
    const int cg8 = g & 7, rgb = g >> 3;
    const __amdgpu_buffer_rsrc_t rdn = mk_rsrc(w_down);
    const __amdgpu_buffer_rsrc_t rgu = mk_rsrc(wsp + WS_GU);
    int vdn, vgu;
    u32x4 dv[7], gv[4];
    int abo, abq[3];
    unsigned lma;
    unsigned sel[2][4];
    float g0, g1;
    u32x4 kk[4];
    unsigned k1 = 0, k2 = 0;
    const int ko_ = O_KEY + pw * 4096;
#define MM(a, b, c) __builtin_amdgcn_mfma_f32_16x16x32_bf16(__builtin_bit_cast(bf16x8, a), __builtin_bit_cast(bf16x8, b), c, 0, 0, 0)
#define PIN1(a) asm volatile("" : "+v"(a))
#define PIN2(a, b) asm volatile("" : "+v"(a), "+v"(b))
#define PIN3(a, b, c) asm volatile("" : "+v"(a), "+v"(b), "+v"(c))
    {
      int gro, go;
      u32x2 gr;
      u32x4 oh[5];
      f32x4 sa4, gw[2], gx[2];
      float su, gs, ta, tb, sx, mbv;
      unsigned dk[4], kl;
      const int hb = lane >> 5;
#pragma unroll
      for (int l = 0; l < WSN; ++l) asm volatile("" : "=v"(wv[l]));
      auto diag = [&](const f32x4& m) __attribute__((always_inline)) {
        return __uint_as_float((__float_as_uint(m[0]) & dk[0]) | (__float_as_uint(m[1]) & dk[1]) | (__float_as_uint(m[2]) & dk[2]) | (__float_as_uint(m[3]) & dk[3]));
      };
      auto setup = [&](auto JB, auto GP) __attribute__((always_inline)) {
        constexpr int jb = decltype(JB)::value, gp = decltype(GP)::value;
        SB();
        {
          int ln = lane, jj = j, p_ = pw;
          launder(ln, jj, p_);
          const int c_ = ln & 15, r_ = ln >> 4, e_ = jj & 7, t_ = (xcd & 1) * 4 + (jj >> 3);
          if constexpr (jb == 1 && gp == 0) {
            pn2 = ((kq >> 1) ? ws_im : ws_re) + (long)t_ * 256 * S_ + (kq & 1) * 1024;
            spb = (const char*)(pn2 + (e_ * 4 + p_) * 8 * S_);
            pins(pn2, spb);
          }
          if constexpr (jb == 1 && gp >= 1 && gp < 6) {
            const unsigned o = c_ == gp - 1 ? 0x3f803f80u : 0u;
            oh[gp - 1] = (u32x4){o, o, o, o};
            PIN1(oh[gp - 1]);
          }
          if constexpr (jb == 1 && gp == 6) {
            const int gm = moe ? (jj - (jj >> 3)) * 8 + xcd : 0;
            mat = gm / 112; cg = (gm % 112) >> 2; dq = gm & 3; d0 = dq * 256 + p_ * 64;
            pin4(mat, cg, dq, d0);
          }
          if constexpr (jb == 2 && gp == 0) {
            pgu = mat ? w_up : w_gate;
            pm2 = wsp + WS_ABT + ((long)e_ * 128 * 4096 + kq * 1024) * 2;
            pins(pgu, pm2);
            vw = ((d0 + r_ * 16) * F_ + cg * 128 + c_ * 8) * 2;
            PIN1(vw);
          }
          if constexpr (jb == 2 && (gp == 1 || gp == 2)) {
            dk[gp * 2 - 2] = c_ == r_ * 4 + gp * 2 - 2 ? ~0u : 0u; dk[gp * 2 - 1] = c_ == r_ * 4 + gp * 2 - 1 ? ~0u : 0u;
            PIN2(dk[gp * 2 - 2], dk[gp * 2 - 1]);
          }
          if constexpr (jb == 2 && gp == 3) { kl = lrow + (ln >> 5); go = (ln & 30) ? (int)0x80000000 : ((ln & 1) ? O_KEY : O_ST) + (int)kl * 8; PIN2(kl, go); }
          if constexpr (jb == 2 && gp == 4) { kl = 0xffff - kl; go = (ln & ~1) == 2 ? O_CE + (z * D_ + drow + (ln & 1)) * 8 : go; PIN2(kl, go); }
          if constexpr (jb == 2 && gp == 5) {
            gro = (p_ < 2 ? O_ST + (st * 128 + p_ * 64) * 8 : O_CE + (z * D_ + e_ * 128 + (p_ - 2) * 64) * 8) + ln * 8;
            tv2 = (p_ * 64 + ln) * 4096;
            lma = lds_addr(smem + LDS_MB) + (p_ < 2 ? p_ * 256 : 512 + p_ * 256) + ln * 4;
            PIN3(gro, tv2, lma);
          }
          if constexpr (jb == 2 && gp == 6) {
            const u64 lo = (t_ & 2) ? ((t_ & 4) ? 0x0150234601503247ull : 0x0514670205147603ull) : ((t_ & 4) ? 0x0123067401532076ull : 0x0567423005176432ull);
            ord2 = (int)(lo >> ((t_ & 1) * 32));
            pins(ord2, p_);
          }
          if constexpr (jb == 7 && gp == 0) {
            const int r0 = p_ * 8 + (ln >> 3);
            vn2 = r0 * (S_ * 2) + ((ln & 7) ^ ((r0 >> 1) & 7)) * 16; vm2 = r0 * 8192 + (vm & 127);
            PIN2(vn2, vm2);
          }
          if constexpr (jb == 7 && gp == 1) { vdn = jj * 112 + p_ * 28 + r_ * 7; vgu = (((ln >> 3) & 1) * 4 * F_ + vdn + min(ln & 7, 6)) * 16; PIN2(vdn, vgu); }
          if constexpr (jb == 7 && gp == 2) { vdn = (vdn * D_ + cg8 * 128 + c_ * 8) * 2; PIN1(vdn); }
          if constexpr (jb == 7 && gp == 3) {
            abo = (((kq * 8 + e_) * 4 + p_) * 16 + c_) * AB_STRIDE * 4;
            abq[2] = ln >= 16 || (ln & 7) == t_ ? (int)0x80000000 : abo;
            PIN2(abo, abq[2]);
          }
          if constexpr (jb == 7 && gp == 4) {
            abq[1] = ln >= 8 ? (int)0x80000000 : abq[2];
            abq[0] = ((ln ^ t_) & 4) ? (int)0x80000000 : abq[1];
            PIN2(abq[0], abq[1]);
          }
          if constexpr (jb == 7 && gp == 5) {
            const unsigned mb = lds_addr(smem) + p_ * 1024;
            mq0 = mb + ((3 - kq) & 3) * 4096; mq1 = mb + ((4 - kq) & 3) * 4096; mq2 = mb + ((5 - kq) & 3) * 4096; mq3 = mb + ((6 - kq) & 3) * 4096;
            pin4(mq0, mq1, mq2, mq3);
          }
          if constexpr (jb == 8 && gp < 4) {
#pragma unroll
            for (int h = 0; h < 2; ++h) {
              const unsigned hv = h ? 0x0302u : 0x0100u;
              sel[h][gp] = ((ln & 7) >> 1) == gp ? ((ln & 1) ? (hv << 16) | 0x0c0cu : 0x0c0c0000u | hv) : 0x0c0c0c0cu;
            }
            PIN2(sel[0][gp], sel[1][gp]);
          }
        }
        SB();
      };
#define WS1(i) setup(IC<1>{}, IC<i>{})
#define WS2(i) setup(IC<2>{}, IC<i>{})
#define WS3(i) do { setup(IC<7>{}, IC<i>{}); if (i >= 4) setup(IC<8>{}, IC<(i) & 3>{}); } while (0)
      G1_STAGE_TILE(1, G1_STAGE, WS1);
      WAIT_VM(19); BAR();
      G1_STAGE_TILE(2, 2 * G1_STAGE, WS2);
      G1_STAGE_TILE(3, 3 * G1_STAGE, WS3);
      auto work1 = [&](auto JB, auto GP) __attribute__((always_inline)) {
        constexpr int jb = decltype(JB)::value, gp = decltype(GP)::value;
        SB();
        if constexpr (jb == 1) {
          const f32x4 z4 = {0.f, 0.f, 0.f, 0.f};
          if constexpr (gp == 0) { sa4 = MM(lv[0], oh[0], z4); gw[0] = MM(lv[0], lw[0], z4); gx[0] = MM(lv[0], lv[0], z4); PIN3(sa4, gw[0], gx[0]); }
          if constexpr (gp == 1) { sa4 = MM(lv2[0], oh[1], sa4); gw[1] = MM(lv2[0], lw[0], z4); gx[1] = MM(lv2[0], lv2[0], z4); PIN3(sa4, gw[1], gx[1]); }
          if constexpr (gp == 2) { sa4 = MM(lv[1], oh[0], sa4); gw[0] = MM(lv[1], lw[1], gw[0]); gx[0] = MM(lv[1], lv[1], gx[0]); PIN3(sa4, gw[0], gx[0]); }
          if constexpr (gp == 3) { sa4 = MM(lv2[1], oh[1], sa4); gw[1] = MM(lv2[1], lw[1], gw[1]); gx[1] = MM(lv2[1], lv2[1], gx[1]); PIN3(sa4, gw[1], gx[1]); }
          if constexpr (gp == 4) { sa4 = MM(ld[0], oh[2], sa4); sa4 = MM(ld[1], oh[2], sa4); PIN1(sa4); }
          if constexpr (gp == 5) { sa4 = MM(ld2[0], oh[3], sa4); sa4 = MM(ld2[1], oh[3], sa4); PIN1(sa4); }
          if constexpr (gp == 6) { sa4 = MM(lw[0], oh[4], sa4); sa4 = MM(lw[1], oh[4], sa4); PIN1(sa4);
            epoch = __builtin_amdgcn_readfirstlane(epv); tag = ((epoch + 1u) * 0x9E3779B1u ^ dmix) | 1u; }
        }
        if constexpr (jb == 2) {
          if constexpr (gp == 0) { su = (sa4[0] + sa4[1]) + (sa4[2] + sa4[3]); ta = diag(gw[0]); PIN2(su, ta); }
          if constexpr (gp == 1) { su = fold32(su, su); tb = diag(gw[1]); PIN2(su, tb); }
          if constexpr (gp == 2) { su = fold16(su, su); gs = fold32(ta, tb); PIN2(su, gs); }
          if constexpr (gp == 3) { ta = diag(gx[0]); tb = diag(gx[1]); PIN2(ta, tb); }
          if constexpr (gp == 4) { gs = fold16(gs, fold32(ta, tb)); PIN1(gs); }
          if constexpr (gp == 5) { gs += dpp_f<0xB1>(gs); gs += dpp_f<0x4E>(gs); PIN1(gs); }
          if constexpr (gp == 6) { gs += dpp_f<0x141>(gs); gs += dpp_f<0x140>(gs); PIN1(gs); }
        }
        if constexpr (jb == 3) {
          if constexpr (gp == 0) {
            const float s0 = rdl(su, 0), s1 = rdl(su, 1), q0 = rdl(gs, 16), q1 = rdl(gs, 48);
            sx = hb ? s1 : s0; ta = hb ? q1 : q0; PIN2(sx, ta);
          }
          if constexpr (gp == 1) {
            const float mean = sx * (1.0f / D_);
            mbv = rbf(mean);
            ta -= sx * mean;
            if (__builtin_expect(__any(!(sx * mean <= 16.f * ta)), 0)) {
              float q = fold32(sqdev16(lv, rdl(mbv, 0)), sqdev16(lv2, rdl(mbv, 32)));
              q = row_sum(fold16(q, q));
              const float dm = mean - mbv;
              ta = fmaxf(q - (float)D_ * dm * dm, 0.f);
            }
            PIN2(mbv, ta);
          }
          if constexpr (gp == 2) { ta = rbf(rbf(ta * (1.0f / D_)) + 1e-5f); gs -= mbv * rdl(su, 4); PIN2(ta, gs); }
          if constexpr (gp == 3) { ta = rbf(__builtin_amdgcn_sqrtf(ta)); tb = __builtin_amdgcn_rcpf(ta); PIN2(ta, tb); }
          if constexpr (gp == 4) {
            const unsigned rb = __float_as_uint(rbf(gs * tb));
            kl |= (rb ^ ((unsigned)((int)rb >> 31) | 0x80000000u)) & 0xffff0000u; PIN1(kl);
          }
          if constexpr (gp == 5) { kl = (lane & 2) ? __float_as_uint(su) : (lane & 1) ? kl : (__float_as_uint(mbv) >> 16) | __float_as_uint(ta); PIN1(kl); }
        }
        if constexpr (jb == ST_JB + 3) {
          if constexpr (gp == 0) { if (!__all(gr.y == tag)) gr = granule_wait((const u64*)((const char*)(flags + FL_AB) + gro), tag); PIN1(gr); }
          if constexpr (gp == 1) { g1 = (z ? -1.0f : 1.0f) / hi_bf(gr.x); PIN1(g1); }
          if constexpr (gp == 2) { g0 = pw < 2 ? -(lo_bf(gr.x) * g1) : __uint_as_float(gr.x); PIN1(g0); }
        }
        if constexpr (gp < 7 && (jb - 1) * 7 + gp < 64) {
          constexpr int n = (jb - 1) * 7 + gp;
          sld<((n >> 1) & 7) * 4096 + (n & 1) * 64>(sd, spb, KROT(n >> 4));
        }
        SB();
      };
      auto step1 = [&](auto JB) __attribute__((always_inline)) {
        constexpr int jb = decltype(JB)::value;
        if constexpr (jb == 1) PAGE(1);
        if constexpr (jb == 7) PAGE(2);
        constexpr int n16 = jb == 12 ? 12 : jb == 11 ? 14 : 16;
        constexpr auto wl = [](int q) { return (q == 3 ? 1 : 0) + (q == ST_JB ? 1 : 0); };
        if constexpr (jb == 1) {
          asm volatile("s_waitcnt vmcnt(%11)" : "+v"(lv[0]), "+v"(lv[1]), "+v"(lw[0]), "+v"(lw[1]), "+v"(ld[0]), "+v"(ld[1]),
                       "+v"(lv2[0]), "+v"(lv2[1]), "+v"(ld2[0]), "+v"(ld2[1]), "+v"(epv) : "i"(16) : "memory");
        } else if constexpr (jb == ST_JB + 3) {
          asm volatile("s_waitcnt vmcnt(%1)" : "+v"(gr) : "i"(n16 + wl(jb - 2) + wl(jb - 1)) : "memory");
        } else wait_vm<n16 + wl(jb - 3) + wl(jb - 2) + wl(jb - 1)>();
        BAR();
        if constexpr (jb == 2) { if (pw == 0 && lane < 16) ((int*)(smem + LDS_SEL))[lane < 8 ? lane : lane + 8] = 0; }
        if constexpr (jb == 4) { if (pw == 0 && lane == 0) ((unsigned*)(smem + LDS_SEL))[5] = tag; }
#define W1(i) work1(JB, IC<i>{})
        if constexpr (jb < 10) G1_STAGE_TILE(jb - 1 + G1_NST, ((jb - 1) % G1_NST) * G1_STAGE, W1);
        else G1_STAGE_A(jb - 1 + G1_NST, ((jb - 1) % G1_NST) * G1_STAGE, W1);
        if constexpr (jb == 12) { G1_STAGE_B(13, G1_STAGE); G1_STAGE_B(14, 2 * G1_STAGE); }
        if constexpr (jb == 3) __builtin_amdgcn_raw_buffer_store_b64((u32x2){kl, tag}, rf, go, 0, 16);
        if constexpr (jb == ST_JB) {
          asm volatile("buffer_load_dwordx2 %0, %1, %2, 0 offen sc1" : "=v"(gr) : "v"(gro), "s"(rf) : "memory");
        }
        touches(IC<100 + jb>{});
        if constexpr (jb <= 7) {
          if (et == jb - 1) {
#pragma unroll
            for (int l = 0; l < WSN; ++l) bld16_nt_io(wv[l], rw, vw, l * F_ * 2);
          }
        }
        SB();
      };
      static_assert(ST_JB + 3 <= 12 && ST_JB >= 6, "schedule");
      step1(IC<1>{}); step1(IC<2>{}); step1(IC<3>{}); step1(IC<4>{}); step1(IC<5>{}); step1(IC<6>{}); step1(IC<7>{}); step1(IC<8>{}); step1(IC<9>{}); step1(IC<10>{}); step1(IC<11>{}); step1(IC<12>{});
    }
#define G2_STAGE_N(kk_, W) do { const int kb = KROT(kk_); constexpr int so = n_off(kk_); SB(); \
    DMA_(rn2, vn2, mq0, so, kb, 0, ""); W(0); DMA_(rn2, vn2, mq1, so, kb, 32 * S_ * 2, ""); W(1); \
    DMA_(rn2, vn2, mq2, so, kb, 64 * S_ * 2, ""); W(2); DMA_(rn2, vn2, mq3, so, kb, 96 * S_ * 2, ""); W(3); \
    DMA_(rn2, vn2, mq0, so + 16384, kb, 128 * S_ * 2, ""); W(4); DMA_(rn2, vn2, mq1, so + 16384, kb, 160 * S_ * 2, ""); W(5); \
    DMA_(rn2, vn2, mq2, so + 16384, kb, 192 * S_ * 2, ""); W(6); DMA_(rn2, vn2, mq3, so + 16384, kb, 224 * S_ * 2, ""); W(7); SB(); } while (0)
#define G2_STAGE_NH(kk_, h) do { const int kb = KROT(kk_); constexpr int so = n_off(kk_) + (h) * 16384; SB(); \
    DMA_(rn2, vn2, mq0, so, kb, (h) * 128 * S_ * 2, ""); DMA_(rn2, vn2, mq1, so, kb, ((h) * 128 + 32) * S_ * 2, ""); \
    DMA_(rn2, vn2, mq2, so, kb, ((h) * 128 + 64) * S_ * 2, ""); DMA_(rn2, vn2, mq3, so, kb, ((h) * 128 + 96) * S_ * 2, ""); SB(); } while (0)
#define G2_STAGE_M(kk_, W) do { const int kb = KROT(kk_); constexpr int so = G2_MRING + m_off(kk_); SB(); \
    DMA_(rm2, vm2, mb0, so, kb, 0, " sc1"); W(8); DMA_(rm2, vm2, mb0, so + 4096, kb, 32 * 8192, " sc1"); W(9); \
    DMA_(rm2, vm2, mb0, so + 8192, kb, 64 * 8192, " sc1"); W(10); DMA_(rm2, vm2, mb0, so + 12288, kb, 96 * 8192, " sc1"); W(11); SB(); } while (0)
    WAIT_VM(16); BAR();
    asm volatile("ds_write_b32 %0, %1" :: "v"(lma), "v"(g0) : "memory");
    if (pw < 2) asm volatile("ds_write_b32 %0, %1 offset:512" :: "v"(lma), "v"(g1) : "memory");
    u32x4 tv[4];
    unsigned ready = 0;
    auto need = [&](int k) __attribute__((always_inline)) {
      const int b = k_blk(k) + (k_ord(k) & 1) * 8;
#pragma unroll 1
      for (int it = 0; !((ready >> b) & 1) && it < SPIN_MAX; ++it) {
        ready |= (unsigned)__ballot(__builtin_amdgcn_raw_buffer_load_b32(rf, abq[k < 5 ? 0 : k < 9 ? 1 : 2], 0, 16) == tag);
        asm volatile("" ::: "memory");
      }
    };
    unsigned ksel, bad = 0;
    int isel;
    unsigned xw;
    auto bvec = [&](unsigned src, const unsigned (&sl)[4]) __attribute__((always_inline)) {
      return (u32x4){__builtin_amdgcn_perm(0, src, sl[0]), __builtin_amdgcn_perm(0, src, sl[1]),
                     __builtin_amdgcn_perm(0, src, sl[2]), __builtin_amdgcn_perm(0, src, sl[3])};
    };
#define BC8(v, p) (unsigned)__builtin_amdgcn_update_dpp(__builtin_amdgcn_update_dpp(0, (int)(v), 0x150 + (p), 0xf, 0x3, false), (int)(v), 0x158 + (p), 0xf, 0xc, false)
    float* lg = (float*)(smem + LDS_GU);
    f32x4 macc = {0.f, 0.f, 0.f, 0.f};
    u32x2 sv;
    unsigned hp, gbad;
    float s0, s1;
    auto work2 = [&](auto JB, auto GP) __attribute__((always_inline)) {
      constexpr int jb = decltype(JB)::value, gp = decltype(GP)::value;
      SB();
      if constexpr (jb == -1 && gp < 4) {
        constexpr int c = gp == 0 ? 0xB1 : gp == 1 ? 0x4E : gp == 2 ? 0x141 : 0x140;
        merge2(k1, k2, dpp_u<c>(k1), dpp_u<c>(k2));
        PIN2(k1, k2);
      }
      if constexpr (jb == -1 && gp == 4) {
        ksel = tk ? k2 : k1;
        isel = (0xffff - (int)(ksel & 0xffffu)) & (S_ - 1);
        k1 = (isel * D_ + d0 + rq * 16 + (lane & 7) * 2) * 2; k2 = O_ST + isel * 8;
        PIN1(ksel);
        xw = gld4_s(x, k1);
        sv = bld8_sc1(rf, k2);
      }
      if constexpr (jb == 0 && gp == 1) wait_xs<3 + 1 + 2>(xw, sv);
      if constexpr (jb == 0) {
        if constexpr (gp == 1) { if (!__all(sv.y == tag)) sv = granule_wait(stat + isel, tag); s0 = __builtin_amdgcn_rcpf(hi_bf(sv.x)); PIN1(s0); }
        if constexpr (gp == 2) { s1 = ln_elem(lo_bf(xw), lo_bf(sv.x), hi_bf(sv.x), s0); PIN1(s1); }
        if constexpr (gp == 3) { xw = pk2(s1, ln_elem(hi_bf(xw), lo_bf(sv.x), hi_bf(sv.x), s0)); PIN1(xw); }
      }
      if constexpr (jb == DG && gp < 7) { PIN1(dv[gp]); macc = MM(dv[gp], bvec(BC8(hp, gp), sel[0]), macc); PIN1(macc); }
      if constexpr ((jb == 0 && gp >= 4) || jb == 1) {
        constexpr int l = jb == 0 ? gp - 4 : gp + 4;
        if constexpr (!(l & 1)) hp = BC8(xw, l >> 1);
        PIN1(wv[l]);
        macc = MM(wv[l], bvec(hp, sel[l & 1]), macc);
        PIN2(macc, hp);
      }
      if constexpr (jb == GV_JB + 2) {
        if constexpr (gp == 0) { gbad = (gv[0].y ^ tag) | (gv[0].w ^ tag) | (gv[1].y ^ tag) | (gv[1].w ^ tag); PIN1(gbad); }
        if constexpr (gp == 1) { gbad |= (gv[2].y ^ tag) | (gv[2].w ^ tag) | (gv[3].y ^ tag) | (gv[3].w ^ tag); PIN1(gbad); }
        if constexpr (gp == 2) {
          s0 = ((__uint_as_float(gv[0].x) + __uint_as_float(gv[1].x)) + __uint_as_float(gv[2].x)) + __uint_as_float(gv[3].x);
          s1 = ((__uint_as_float(gv[0].z) + __uint_as_float(gv[1].z)) + __uint_as_float(gv[2].z)) + __uint_as_float(gv[3].z);
          PIN2(s0, s1);
        }
        if constexpr (gp == 3) {
#pragma unroll 1
          for (int it = 0; !__all(gbad == 0) && it < SPIN_MAX; ++it) {
            u32x4 q[4];
            gbad = 0;
#pragma unroll
            for (int i = 0; i < 4; ++i) { q[i] = __builtin_amdgcn_raw_buffer_load_b128(rgu, vgu, i * F_ * 16, 16); gbad |= (q[i].y ^ tag) | (q[i].w ^ tag); }
            s0 = ((__uint_as_float(q[0].x) + __uint_as_float(q[1].x)) + __uint_as_float(q[2].x)) + __uint_as_float(q[3].x);
            s1 = ((__uint_as_float(q[0].z) + __uint_as_float(q[1].z)) + __uint_as_float(q[2].z)) + __uint_as_float(q[3].z);
            asm volatile("" ::: "memory");
          }
          const unsigned gp2 = pk2(s0, s1);
          const unsigned up2 = dpp_u<0x128>(gp2);
          s0 = tk ? hi_bf(up2) : lo_bf(gp2); s1 = tk ? hi_bf(gp2) : lo_bf(up2);
          PIN2(s0, s1);
        }
        if constexpr (gp == 4) { s0 = rbf(s0 * __builtin_amdgcn_rcpf(1.f + __expf(-s0))); PIN1(s0); }
        if constexpr (gp == 5) { hp = pk2(s0 * s1, 0.f); PIN1(hp); }
      }
      if constexpr (jb >= 3 && jb <= 10 && gp >= 4) {
        constexpr int n = (jb - 3) * 8 + gp - 4;
        sld<((n >> 1) & 7) * 4096 + (n & 1) * 64>(sd, spb, KROT(12 + (n >> 4)));
      }
      if constexpr (jb == 10 && gp == 2) epi_setup();
      if constexpr (jb == 10 && gp == 4) code_touch(sd, (j * 4 + pw) * 256);
      SB();
    };
#define W2M(i) work2(IC<-1>{}, IC<i>{})
#define W20(i) work2(IC<0>{}, IC<i>{})
#define W21(i) work2(IC<1>{}, IC<i>{})
    unsigned fs;
    {
      G1_STAGE_B(15, 3 * G1_STAGE);
      G2_STAGE_NH(0, 0);
      lgkm0(sd);
      WAIT_VM(16); BAR();
      G2_STAGE_NH(0, 1);
      WAIT_VM(10); BAR();
#pragma unroll
      for (int i = 0; i < 4; ++i) kk[i] = bld16_sc1(rf, l16, ko_ + i * 1024);
#pragma unroll
      for (int l = WSN; l < 16; ++l) wv[l] = bld16_nt(rw, vw, l * F_ * 2);
      asm volatile("s_waitcnt vmcnt(%4)" : "+v"(kk[0]), "+v"(kk[1]), "+v"(kk[2]), "+v"(kk[3]) : "i"(16 - WSN) : "memory");
      touches(IC<115>{});
#pragma unroll
      for (int i = 0; i < 4; ++i) {
        k2 = max(k2, min(k1, kk[i].x)); k1 = max(k1, kk[i].x);
        k2 = max(k2, min(k1, kk[i].z)); k1 = max(k1, kk[i].z);
        bad |= (kk[i].y ^ tag) | (kk[i].w ^ tag);
      }
#pragma unroll 1
      for (int it = 0; !__all(bad == 0) && it < SPIN_MAX; ++it) {
        k1 = 0; k2 = 0; bad = 0;
#pragma unroll
        for (int i = 0; i < 4; ++i) {
          const u32x4 q = __builtin_amdgcn_raw_buffer_load_b128(rf, l16, ko_ + i * 1024, 16);
          k2 = max(k2, min(k1, q.x)); k1 = max(k1, q.x);
          k2 = max(k2, min(k1, q.z)); k1 = max(k1, q.z);
          bad |= (q.y ^ tag) | (q.w ^ tag);
        }
        asm volatile("" ::: "memory");
      }
      merge2(k1, k2, dpp_u<0xB1>(k1), dpp_u<0xB1>(k2));
      merge2(k1, k2, dpp_u<0x4E>(k1), dpp_u<0x4E>(k2));
      merge2(k1, k2, dpp_u<0x141>(k1), dpp_u<0x141>(k2));
      merge2(k1, k2, dpp_u<0x140>(k1), dpp_u<0x140>(k2));
      if ((lane & 15) == 0) *(u32x2*)(smem + LDS_KEY + (pw * 4 + rq) * 8) = (u32x2){k1, k2};
      PAGE(3);
      BAR_LGKM();
#pragma unroll
      for (int jj = 0; jj < 4; ++jj) tv[jj] = *(const u32x4*)(smem + G2_MRING + m_off(0) + (jj * 4 + pw) * 1024 + lane * 16);
      { const u32x2 kp = *(const u32x2*)(smem + LDS_KEY + ci * 8); k1 = kp.x; k2 = kp.y; }
#pragma unroll
      for (int jj = 0; jj < 4; ++jj) __builtin_amdgcn_raw_buffer_store_b128(tv[jj], rm2, vm2 + jj * 32 * 8192 + 2 * tt * 128, 0, 16);
      G2_STAGE_N(1, W2M);
      WAIT_VM(10);
      if (lane == tt) __builtin_amdgcn_raw_buffer_store_b32(tag, rf, abo, 0, 16);
      G2_STAGE_N(2, W20);
      __builtin_amdgcn_s_sleep(2);
      WAIT_VM(9);
      fs = bld4_sc1(rf, abq[0]);
    }
    BAR();
#pragma unroll
    for (int jj = 0; jj < 4; ++jj) tv[jj] = *(const u32x4*)(smem + G2_MRING + m_off(1) + (jj * 4 + pw) * 1024 + lane * 16);
    G2_STAGE_N(3, NOW);
    asm volatile("s_waitcnt vmcnt(8)" : "+v"(fs) :: "memory");
    ready = (unsigned)__ballot(fs == tag);
    need(2);
    G2_STAGE_M(2, NOW);
    need(3);
    G2_STAGE_M(3, NOW);
    fs = bld4_sc1(rf, abq[1]);
    touches(IC<1>{});
    W21(0); W21(1); W21(2); W21(3); W21(4); W21(5); W21(6); W21(7); W21(8); W21(9); W21(10); W21(11);
    asm volatile("" : "+v"(tv[0]), "+v"(tv[1]), "+v"(tv[2]), "+v"(tv[3]));
    auto step2 = [&](auto JB) __attribute__((always_inline)) {
      constexpr int jb = decltype(JB)::value;
      if constexpr (jb == 3) PAGE(4);
      if constexpr (jb == 9) PAGE(5);
      constexpr auto must = [](int q) { return (q == 2 ? 4 : 0) + ((FSM >> q) & 1) + ((q == GV_JB || q == GV_JB - 1) ? 2 : 0); };
      if constexpr (jb == 2) wait_vm<4 + must(1)>();
      else wait_vm<dn_cnt(jb - 2) + (jb == 5 ? 1 : 0) + 12 + must(jb - 1) + dn_cnt(jb - 1)>();
      if constexpr (jb == 3) { if (pw == 0 && (lane & ~8) == 0) ((unsigned*)(smem + LDS_SEL))[tk] = ksel; }
      if constexpr ((jb == 2 || jb == DG + 1)) {
        *(f32x4*)(lg + pw * 256 + lane * 4) = macc;
        macc = (f32x4){0.f, 0.f, 0.f, 0.f};
        lgkm0(sd);
      }
      BAR();
      float p0[4], p1[4];
      const int col = (pw & 1) * 64 + lane;
      if constexpr (jb == 3) {
        if (g == 0 && pw == 0 && lane == 0) __hip_atomic_store((unsigned*)flags + FL_EPOCH, epoch + 1u, __ATOMIC_RELAXED, __HIP_MEMORY_SCOPE_AGENT);
      }
      if constexpr (jb == DG + 1) {
        const float* p = lg + (pw & 1) * 32 + (lane >> 4) * 64 + ((2 * lane) & 7) * 4 + ((lane >> 2) & 3);
#pragma unroll
        for (int i = 0; i < 4; ++i) { p0[i] = p[i * 256]; p1[i] = p[i * 256 + 4]; }
      }
      if constexpr (jb == 2) {
        const float* p = lg + (col >> 5) * 64 + (col & 7) * 4 + ((col >> 3) & 3);
#pragma unroll
        for (int i = 0; i < 4; ++i) { p0[i] = p[i * 256]; p1[i] = p[i * 256 + 32]; }
      }
      if constexpr (jb == 4) {
        if (lane == 8 + tt) __builtin_amdgcn_raw_buffer_store_b32(tag, rf, abo, 0, 16);
      }
      if constexpr ((FSM >> (jb - 2)) & 1) {
        asm volatile("" : "+v"(fs));
        ready |= (unsigned)__ballot(fs == tag);
      }
      if constexpr (jb == GV_JB + 2) asm volatile("" : "+v"(gv[0]), "+v"(gv[1]), "+v"(gv[2]), "+v"(gv[3]));
#define W2(i) work2(JB, IC<i>{})
      G2_STAGE_N(jb + 2, W2);
      need(jb + 2);
      G2_STAGE_M(jb + 2, W2);
      if constexpr (jb == 2) {
#pragma unroll
        for (int jj = 0; jj < 4; ++jj) __builtin_amdgcn_raw_buffer_store_b128(tv[jj], rm2, vm2 + jj * 32 * 8192 + (2 * tt + 1) * 128, 0, 16);
      }
      if constexpr ((FSM >> jb) & 1) fs = bld4_sc1(rf, abq[jb < 5 ? 1 : 2]);
      if constexpr ((jb == GV_JB || jb == GV_JB - 1)) {
#pragma unroll
        for (int i = (jb == GV_JB ? 2 : 0); i < (jb == GV_JB ? 4 : 2); ++i) gv[i] = bld16_sc1(rgu, vgu, i * F_ * 16);
      }
      touches(JB);
#pragma unroll
      for (int i = dn_first(jb); i < dn_first(jb) + dn_cnt(jb); ++i) dv[i] = bld16_nt(rdn, vdn, i * D_ * 2);
      if constexpr (jb == 2) asm volatile("" : "+v"(p0[0]), "+v"(p0[1]), "+v"(p0[2]), "+v"(p0[3]), "+v"(p1[0]), "+v"(p1[1]), "+v"(p1[2]), "+v"(p1[3]));
      if constexpr (jb == DG + 1) {
        if (pw < 2)
          __builtin_amdgcn_raw_buffer_store_b128((u32x4){__float_as_uint((p0[0] + p0[1]) + (p0[2] + p0[3])), tag, __float_as_uint((p1[0] + p1[1]) + (p1[2] + p1[3])), tag},
                                                 mk_rsrc(wsp + WS_DN), (((pw * 8 + cg8) * 32 + rgb) * 64 + lane) * 16, 0, 16);
      }
      if constexpr (jb == 2) {
        if (moe && pw < 2)
          __builtin_amdgcn_raw_buffer_store_b128((u32x4){__float_as_uint((p0[0] + p0[1]) + (p0[2] + p0[3])), tag, __float_as_uint((p1[0] + p1[1]) + (p1[2] + p1[3])), tag},
                                                 rgu, ((mat * 4 + dq) * F_ + cg * 128 + col) * 16, 0, 16);
      }
      SB();
    };
    static_assert(DG + 1 <= 12 && DG >= GV_JB + 3 && dn_first(DG - 2) == 7, "schedule");
    step2(IC<2>{}); step2(IC<3>{}); step2(IC<4>{}); step2(IC<5>{}); step2(IC<6>{}); step2(IC<7>{}); step2(IC<8>{}); step2(IC<9>{}); step2(IC<10>{}); step2(IC<11>{}); step2(IC<12>{}); step2(IC<13>{});
    lgkm0(sd);
    PAGE(7);
    WAIT_VM(12); BAR();
    xq[0] = ld16_s<0>(x, xo); xq[1] = ld16_s<64>(x, xo); sq = ld8_s_sc1(stat, xo >> 11 << 3);
    WAIT_VM(3); BAR();
    asm volatile("s_waitcnt vmcnt(0)" : "+v"(xq[0]), "+v"(xq[1]), "+v"(sq) :: "memory");
    __builtin_amdgcn_s_setprio(0);
    key[0] = __builtin_amdgcn_readlane(ksel, 0); key[1] = __builtin_amdgcn_readlane(ksel, 8);
    nf_pre();
#pragma unroll
    for (int n = 0; n < 56; ++n) nf_step(n);
#pragma unroll
    for (int e = 0; e < 16; ++e) nf_unp(e);
#pragma unroll 1
    for (int c = 0; c < 2; ++c) {
      const unsigned kc = __builtin_amdgcn_readlane(ksel, c * 8);
      const int ic = (0xffff - (int)(kc & 0xffffu)) & (S_ - 1);
      if ((ic >> 8) != tt || ((ic >> 5) & 3) != kq) continue;
      const __amdgpu_buffer_rsrc_t rd = mk_rsrc(wsp + WS_DN + (size_t)(c * 8 + et) * 32768);
#pragma unroll 1
      for (int it = 0; it < SPIN_MAX; ++it) {
        if (__all(__builtin_amdgcn_raw_buffer_load_b32(rd, (lane & 31) * 1024 + 1020, 0, 16) == tag)) break;
        __builtin_amdgcn_s_sleep(2);
        asm volatile("" ::: "memory");
      }
      float s0, s1;
      unsigned bad = 1;
#pragma unroll 1
      for (int it = 0; !__all(bad == 0) && it < SPIN_MAX; ++it) {
        bad = 0; s0 = 0.f; s1 = 0.f;
#pragma unroll
        for (int i = 0; i < 8; ++i) {
          const u32x4 q = __builtin_amdgcn_raw_buffer_load_b128(rd, rq * 1024 + pw * 256 + ci * 16, i * 4096, 16);
          bad |= (q.y ^ tag) | (q.w ^ tag); s0 += __uint_as_float(q.x); s1 += __uint_as_float(q.z);
        }
        asm volatile("" ::: "memory");
      }
      s0 = fold32(s0, s1);
      s0 = fold16(s0, s0);
      const float sg = rbf(1.f / (1.f + __expf(-key_score(kc))));
      ((float*)(smem + LDS_FIN))[c * 128 + pw * 32 + ci * 2 + (lane >> 5)] = rbf(rbf(s0) * sg);
      asm volatile("s_waitcnt lgkmcnt(0)" ::: "memory");
      if (lane == 0) asm volatile("ds_add_u32 %0, %1" :: "v"(lds_addr(smem + LDS_SEL + 24 + c * 4)), "v"(1) : "memory");
    }
    asm volatile("v_mov_b32 %0, 0" : "=v"(fx), "=v"(ya[0]), "=v"(ya[1]), "=v"(ya[2]), "=v"(ya[3]));
  } else {
  {
    const int q = j * 4 + wid;
    constexpr int Q1 = WS_MB, Q2 = Q1 + 8, Q3 = Q2 + 8, Q4 = Q3 + 7, Q5 = Q4 + 7, Q6 = Q5 + 7, Q7 = Q6 + 4, Q8 = Q7 + 4, QC = Q8 + 1;
    static_assert(QC <= 128, "warm-up slots");
    unsigned dmy = 0;
    if (q < QC) {
      const char* base; int b0;
      if (q < Q1) { base = wsp; b0 = 0; }
      else if (q < Q2) { base = (const char*)ws_re; b0 = Q1; }
      else if (q < Q3) { base = (const char*)ws_im; b0 = Q2; }
      else if (q < Q4) { base = (const char*)w_gate; b0 = Q3; }
      else if (q < Q5) { base = (const char*)w_up; b0 = Q4; }
      else if (q < Q6) { base = (const char*)w_down; b0 = Q5; }
      else if (q < Q7) { base = (const char*)out; b0 = Q6; }
      else if (q < Q8) { base = (const char*)x; b0 = Q7; }
      else { base = (const char*)w_r; b0 = Q8; }
      base += (size_t)(q - b0) << 20;
      asm volatile("s_load_dword %0, %1, 0" : "+s"(dmy) : "s"(base) : "memory");
    }
    {
      const int w = g * 4 + wid;
      constexpr int R = (int)(WS_FLAGS - WS_DN);
      static_assert(FL_X == 0 && FL_ST == 6144 * 32 && FL_EPOCH == 6528 * 32, "");
      const int o = lane < 6 ? R + (lane * 1024 + w) * 128 : lane > 6 || w >= 896 ? (int)0x80000000 : w < 512 ? w * 1024 + 896 : R + (6144 - 512 + w) * 128;
      asm volatile("s_mov_b32 m0, %0\n\ts_nop 0\n\tbuffer_load_dword %1, %2, 0 offen sc1 lds"
                   :: "s"(lds_addr(smem + LDS_DUMMY) + wid * 1024 + 512), "v"(o), "s"(mk_rsrc(wsp + WS_DN)) : "memory");
    }
    WAIT_VM(1);
    if (wid == 0) { CALL_PG(1); CALL_PG(8); CALL_PG(5); }
    else if (wid == 1) { CALL_PG(2); CALL_PG(9); CALL_PG(6); }
    else if (wid == 2) { CALL_PG(3); CALL_PG(10); CALL_PG(7); }
    else { CALL_PG(4); CALL_PG(11); }
    asm volatile("s_barrier\n\ts_waitcnt lgkmcnt(0)" : "+s"(dmy) :: "memory");
  }
  PAGE(8);
  epi_setup();
  const int wr = wid >> 1, wc = wid & 1;
  const int sw = (fr >> 1) & 7;
  const int ko0 = ((0 + fq) ^ sw) * 16, ko1 = ((4 + fq) ^ sw) * 16;
  const int mrow = (fr >> 2) * 8 + (fr & 3);
  bf16x8 fm[2][4], fn[2][4];
  {
    const char* pm = smem + (wr * 64 + mrow) * 128;
    const char* pn = smem + 16384 + (wc * 64 + mrow) * 128;
    f32x4 acc[4][4];
#pragma unroll
    for (int i = 0; i < 4; ++i)
#pragma unroll
      for (int jn = 0; jn < 4; ++jn)
#pragma unroll
        for (int q = 0; q < 4; ++q) acc[i][jn][q] = 0.f;
#define TO(i) (((i) >> 1) * 4096 + ((i) & 1) * 512)
#define LDSP(T, a) ((__attribute__((address_space(3))) T*)(size_t)(a))
#define SLOTS(n) _Pragma("unroll") for (int q = 0; q < (n); ++q)
#define MFA(c, a, b) asm volatile("v_mfma_f32_16x16x32_bf16 %0, %1, %2, %0" : "+v"(c) : "v"(a), "v"(b))
#define MFA2(c, a, b, a2, b2) asm volatile("v_mfma_f32_16x16x32_bf16 %0, %1, %2, %0\n\tv_mfma_f32_16x16x32_bf16 %0, %3, %4, %0" : "+v"(c) : "v"(a), "v"(b), "v"(a2), "v"(b2))
    unsigned lm[2][2], ln[2][2];
#pragma unroll
    for (int u = 0; u < 2; ++u)
#pragma unroll
      for (int kk = 0; kk < 2; ++kk) {
        lm[u][kk] = lds_addr(pm) + u * 2 * G1_STAGE + (kk ? ko1 : ko0);
        ln[u][kk] = lds_addr(pn) + u * 2 * G1_STAGE + (kk ? ko1 : ko0);
        asm volatile("" : "+v"(lm[u][kk]), "+v"(ln[u][kk]));
      }
    auto ldm = [&](int stg, int i, int kk) __attribute__((always_inline)) { fm[kk][i] = *LDSP(const bf16x8, lm[stg >> 1][kk] + (stg & 1) * G1_STAGE + TO(i)); };
    auto ldn = [&](int stg, int jn, int kk) __attribute__((always_inline)) { fn[kk][jn] = *LDSP(const bf16x8, ln[stg >> 1][kk] + (stg & 1) * G1_STAGE + TO(jn)); };
    auto quad = [&](int a, int c, int d, int s) __attribute__((always_inline)) {
      const int i = (s < 2 || s >= 6) ? a : a + 1, jn = s < 4 ? c : d, kk = (s == 1 || s == 2 || s == 5 || s == 6);
      MFA(acc[i][jn], fm[kk][i], fn[kk][jn]);
    };
    auto quad2 = [&](int a, int c, int d, int s) __attribute__((always_inline)) {
      const int i = (s < 2 || s >= 6) ? a : a + 1, jn = s < 4 ? c : d, kk = (s == 2 || s == 6);
      MFA2(acc[i][jn], fm[kk][i], fn[kk][jn], fm[1 - kk][i], fn[1 - kk][jn]);
    };
    auto half1 = [&](int stg) __attribute__((always_inline)) {
      const int ja = (stg & 1) * 2, jb = 2 - ja;
      SLOTS(8) {
        quad(0, ja, ja + 1, q);
        if (q < 4) ldn(stg, jb + (q >> 1), q & 1); else ldm(stg, 2 + ((q - 4) >> 1), q == 5 || q == 6);
        SB();
      }
      SLOTS(4) { quad2(0, jb, jb + 1, 2 * q); SB(); }
    };
    auto half2 = [&](int stg) __attribute__((always_inline)) {
      const int ja = (stg & 1) * 2, jb = 2 - ja, nx = (stg + 1) & 3;
      SLOTS(8) {
        quad(2, jb + 1, jb, q);
        if (!(q & 1)) ldm(nx, q >> 2, q == 2 || q == 4);
        SB();
      }
      SLOTS(8) {
        quad(2, ja, ja + 1, q);
        if (!(q & 1)) ldn(nx, jb + (q >> 2), (q >> 1) & 1);
        SB();
      }
    };
    SLOTS(16) asm volatile("" : "+v"(acc[q >> 2][q & 3]));
    SB();
    SLOTS(4) { ldm(0, q >> 1, q & 1); ldn(0, q >> 1, q & 1); }
    SB();
#pragma unroll 1
    for (int k4 = 0; k4 < G1_NKT - 4; k4 += 4) {
      half1(0); BAR_LGKM();
      SB(); half2(0);
      half1(1); BAR_LGKM();
      SB(); half2(1);
      half1(2); BAR_LGKM();
      SB(); half2(2);
      half1(3); BAR_LGKM();
      SB(); half2(3);
    }
    bf16x8 gm[2][4], gn[2][8];
    unsigned am[2][2], an[2][2];
#pragma unroll
    for (int u = 0; u < 2; ++u)
#pragma unroll
      for (int kk = 0; kk < 2; ++kk) {
        am[u][kk] = lds_addr(pm) + (u ? 3 : 1) * G1_STAGE + (kk ? ko1 : ko0);
        an[u][kk] = lds_addr(pn) + (u ? 3 : 1) * G1_STAGE + (kk ? ko1 : ko0);
        asm volatile("" : "+v"(am[u][kk]), "+v"(an[u][kk]));
      }
    unsigned al = lds_addr(smem + LDS_MB) + (wr * 32 + fq * 8) * 4, ac = lds_addr(smem + LDS_MB) + (256 + wc * 64 + mrow) * 4;
    unsigned ae = lds_addr(smem + G2_MRING) + (wc * 64 + mrow) * 128 + (((wr * 4 + fq) ^ sw) * 16);
    asm volatile("" : "+v"(al), "+v"(ac), "+v"(ae));
    auto rdg = [&](int set, int t, int h, int r) __attribute__((always_inline)) {
      const int u = t == 15, o = t == 14 ? G1_STAGE : 0;
      if (r == 0 || r == 2 || r == 4 || r == 5) {
        const int i = r >= 4, kk = r == 2 || r == 4;
        gm[set][kk * 2 + i] = *LDSP(const bf16x8, am[u][kk] + o + h * 4096 + i * 512);
      } else {
        const int jn = r < 4 ? 0 : (r - 4) >> 1, kk = r < 4 ? r >> 1 : r & 1;
        gn[set][kk * 4 + jn] = *LDSP(const bf16x8, an[u][kk] + o + TO(jn));
      }
    };
    auto mmg = [&](int set, int h, int q) __attribute__((always_inline)) {
      const int jn = q >> 2, r = q & 3, i = (r >> 1) ^ (jn & 1), kk = r == 1 || r == 2;
      MFA(acc[h * 2 + i][jn], gm[set][kk * 2 + i], gn[set][kk * 4 + jn]);
    };
    f32x4 rsv[2], mrv[2];
    float cev[4], tc[32];
    auto rds = [&](int h, int r) __attribute__((always_inline)) {
      if (r < 2) mrv[r] = *LDSP(const f32x4, al + h * 256 + r * 16); else rsv[r - 2] = *LDSP(const f32x4, al + 512 + h * 256 + (r - 2) * 16);
    };
    auto prep = [&](int n) __attribute__((always_inline)) {
      tc[n] = llvm_fmul_legacy(mrv[(n >> 2) & 1][n & 3], cev[n >> 3]);
      asm volatile("" : "+v"(tc[n]));
    };
    u32x4 ep;
    auto epi = [&](int h, int q) __attribute__((always_inline)) {
      const int jn = q >> 1, i = q & 1;
      asm volatile("v_fmac_f32 %0, %4, %8\n\tv_fmac_f32 %1, %5, %9\n\tv_fmac_f32 %2, %6, %10\n\tv_fmac_f32 %3, %7, %11"
                   : "+v"(tc[q * 4]), "+v"(tc[q * 4 + 1]), "+v"(tc[q * 4 + 2]), "+v"(tc[q * 4 + 3])
                   : "v"(acc[h * 2 + i][jn][0]), "v"(acc[h * 2 + i][jn][1]), "v"(acc[h * 2 + i][jn][2]), "v"(acc[h * 2 + i][jn][3]),
                     "v"(rsv[i][0]), "v"(rsv[i][1]), "v"(rsv[i][2]), "v"(rsv[i][3]));
      ep[i * 2] = pk2(tc[q * 4], tc[q * 4 + 1]); ep[i * 2 + 1] = pk2(tc[q * 4 + 2], tc[q * 4 + 3]);
      if (i) *LDSP(u32x4, ae + m_off(h) + TO(jn)) = ep;
    };
    SB();
    half1(0);
    BAR_LGKM();
    SB();
    SLOTS(16) {
      if (q < 8) quad(2, 3, 2, q); else quad(2, 0, 1, q - 8);
      if (q < 12) rdg(0, 13, 0, q);
      SB();
    }
    BAR();
    SB();
    SLOTS(16) {
      mmg(0, 0, q);
      if (q < 12) rdg(1, 14, 0, q);
      else { rds(0, q - 12); cev[q - 12] = *LDSP(const float, ac + (((q - 12) >> 1) * 32 + ((q - 12) & 1) * 4) * 4); }
      SB();
    }
    PAGE(9);
    BAR();
    SB();
    SLOTS(16) { mmg(1, 0, q); if ((q & 3) < 3) rdg(0, 15, 0, (q >> 2) * 3 + (q & 3)); if (q >= 8) { prep(2 * q - 16); prep(2 * q - 15); } SB(); }
    SLOTS(16) { mmg(0, 0, q); if ((q & 3) < 3) rdg(1, 13, 1, (q >> 2) * 3 + (q & 3)); prep(16 + q); SB(); if (q >= 9 && (q & 1)) { epi(0, (q - 9) >> 1); SB(); } }
    SLOTS(8) { mmg(1, 1, q); if (q < 6) { rdg(0, 14, 1, 2 * q); rdg(0, 14, 1, 2 * q + 1); } SB(); if (q & 1) { epi(0, 4 + (q >> 1)); SB(); } }
    BAR_LGKM();
    SB();
    SLOTS(8) { mmg(1, 1, 8 + q); if (q < 4) rdg(1, 15, 1, q == 0 ? 1 : q == 1 ? 3 : q + 4); else rds(1, q - 4); SB(); }
    SLOTS(16) { mmg(0, 1, q); if (q < 8) rdg(1, 15, 1, q == 0 ? 0 : q == 1 ? 2 : q < 4 ? q + 2 : q + 4); prep(q); SB(); }
    asm volatile("s_waitcnt lgkmcnt(0)" ::: "memory");
    if (lane == 0) asm volatile("ds_add_u32 %0, %1" :: "v"(lds_addr(smem + LDS_SEL + 12)), "v"(1) : "memory");
    SB();
    SLOTS(16) {
      mmg(1, 1, q);
      if (q < 4) fn[0][q] = *(const bf16x8*)(smem + (wr * 128 + fr) * 128 + (q >> 1) * 2048 + ((q & 1) ? ko1 : ko0));
      else if (q < 8) fm[q & 1][(q - 4) >> 1] = *(const bf16x8*)(smem + G2_MRING + m_off(0) + (wc * 64 + mrow) * 128 + TO((q - 4) >> 1) + ((q & 1) ? ko1 : ko0));
      prep(16 + q);
      SB();
    }
    {
      lds_vi rel = (lds_vi)(smem + LDS_SEL + 12);
      int it = 0;
      while (rel[0] != 4 && it < SPIN_MAX * 8) ++it;
      asm volatile("" ::: "memory");
    }
    asm volatile("s_nop 7");
    SB();
    SLOTS(8) { epi(1, q); SB(); }
  }

  const char* pn = smem + (wr * 128 + fr) * 128;
  const char* pm = smem + G2_MRING + (wc * 64 + mrow) * 128;
  f32x4 acc[8][4];
  auto mm = [&](bool imaj, int set, int q2, int s, int mode) __attribute__((always_inline)) {
    int i, jj, kk;
    if (imaj) { i = s >> 2; const int r = s & 3; jj = (r >> 1) ^ (i & 1); kk = (r == 1 || r == 2); }
    else { jj = s >> 3; const int r = s & 7; i = jj ? 3 - (r >> 1) : (r >> 1); kk = ((r + 1) >> 1) & 1; }
    if (mode == 1 && !(s & 1)) asm volatile("v_mfma_f32_16x16x32_bf16 %0, %1, %2, 0" : "=&v"(acc[q2 * 2 + jj][i]) : "v"(fm[kk][i]), "v"(fn[set][jj * 2 + kk]));
    else if (mode) asm volatile("v_mfma_f32_16x16x32_bf16 %0, %1, %2, %0" : "+v"(acc[q2 * 2 + jj][i]) : "v"(fm[kk][i]), "v"(fn[set][jj * 2 + kk]));
    else acc[q2 * 2 + jj][i] = __builtin_amdgcn_mfma_f32_16x16x32_bf16(fm[kk][i], fn[set][jj * 2 + kk], acc[q2 * 2 + jj][i], 0, 0, 0);
  };
  unsigned bn[2], bm[2], an[2], am[2];
#pragma unroll
  for (int kk = 0; kk < 2; ++kk) {
    bn[kk] = lds_addr(pn) + (kk ? ko1 : ko0); bm[kk] = lds_addr(pm) + (kk ? ko1 : ko0);
    asm volatile("" : "+v"(bn[kk]), "+v"(bm[kk]));
    an[kk] = bn[kk]; am[kk] = bm[kk] + m_off(0);
  }
#define RDN(jt, kk, o) (*LDSP(const bf16x8, an[kk] + (o) + (jt) * 2048))
#define RDM(i, kk, o) (*LDSP(const bf16x8, am[kk] + (o) + TO(i)))
  PAGE(10);
  tag = __builtin_amdgcn_readfirstlane(((const unsigned*)(smem + LDS_SEL))[5]);
  {
    int son = 0;
    auto tile = [&](int kt, int zero) __attribute__((always_inline)) {
      SLOTS(16) {
        mm(true, 0, 0, q, zero);
        if (!(q & 1)) {
          if (q < 8) fm[(q >> 1) & 1][2 + (q >> 2)] = RDM(2 + (q >> 2), (q >> 1) & 1, 0);
          else { const int r = (q - 8) >> 1; fn[1][r] = RDN(2 + (r >> 1), r & 1, 0); }
        }
        SB();
      }
      SLOTS(16) {
        mm(false, 1, 1, q, zero);
        if (!(q & 1) && q < 8) fn[0][q >> 1] = RDN(4 + (q >> 2), (q >> 1) & 1, 0);
        SB();
      }
      const int som = son >> 1;
      son = son == 65536 ? 0 : son + 32768;
      SLOTS(16) {
        mm(false, 0, 2, q, zero);
        if (!(q & 1) && q < 8) fn[1][q >> 1] = RDN(6 + (q >> 2), (q >> 1) & 1, 0);
        if (q == 9 || q == 11) { an[q == 11] = bn[q == 11] + son; asm volatile("" : "+v"(an[q == 11])); }
        if (q == 13 || q == 15) { am[q == 15] = bm[q == 15] + som; asm volatile("" : "+v"(am[q == 15])); }
        SB();
      }
      BAR_LGKM();
      SB();
      SLOTS(16) {
        mm(true, 1, 3, q, zero);
        if (!(q & 1)) {
          if (q < 8) fn[0][q >> 1] = RDN(q >> 2, (q >> 1) & 1, 0);
          else fm[(q >> 1) & 1][(q - 8) >> 2] = RDM((q - 8) >> 2, (q >> 1) & 1, 0);
        }
        SB();
      }
    };
    SB();
    tile(0, 1);
#pragma unroll 1
    for (int kt = 1; kt < G2_NKT - 3; ++kt) tile(kt, 0);
  }
  {
    constexpr int NG = 12;
    static constexpr int g_t[NG] = {0, 0, 1, 1, 2, 2, 2, 1, 0, 0, 1, 2};
    static constexpr int g_q[NG] = {0, 1, 0, 1, 0, 1, 2, 2, 2, 3, 3, 3};
    constexpr int G14 = 1, G15 = 3, GP0 = 5, GP1 = 6;
    constexpr auto chp = [](int g) { return g == 0 || g_t[g] != g_t[g - 1]; };
    constexpr auto chn = [](int g) { return g + 1 < NG && g_t[g + 1] != g_t[g]; };
    static_assert(n_off(13) == 32768 && n_off(14) == 65536 && n_off(15) == 0, "");
    unsigned an2[2];
#pragma unroll
    for (int kk = 0; kk < 2; ++kk) {
      an[kk] = bn[kk]; an2[kk] = bn[kk] + 65536; am[kk] = bm[kk];
      asm volatile("" : "+v"(an[kk]), "+v"(an2[kk]), "+v"(am[kk]));
    }
    int vo;
    u32x4 pk;
    u32x2 kv;
    constexpr int NF0 = 152;
    auto publish = [&](int d, int h) __attribute__((always_inline)) {
      if (h == 0) { vo = lane; PINV(vo); vo = vo * 16 + (((d + kq + 1) & 3) * 4 + kq) * 4 * MSG; PINV(vo); }
      const f32x4 v = acc[2 * d + (h >> 2)][h & 3];
      pk[(h & 1) * 2] = pk2(v[0], v[1]); pk[(h & 1) * 2 + 1] = pk2(v[2], v[3]);
      if (h & 1) __builtin_amdgcn_raw_buffer_store_b128(pk, rs, vo + (h >> 1) * 1024, 0, 16);
    };
    int fo = lane == 0 ? kq * 512 : (int)0x80000000;
#define MSG_FLAG(d) __builtin_amdgcn_raw_buffer_store_b32(tag, rc, fo + (((d) + kq + 1) & 3) * 2048, 0, 16)
    auto rdn = [&](int g, int r) __attribute__((always_inline)) {
      const int t = g_t[g], jt = g_q[g] * 2 + (r >> 1);
      fn[g & 1][r] = *LDSP(const bf16x8, (t == 1 ? an2[r & 1] : an[r & 1]) + (t == 0 ? 32768 : 0) + jt * 2048);
    };
    auto rdm = [&](int t, int i, int kk) __attribute__((always_inline)) { fm[kk][i] = *LDSP(const bf16x8, am[kk] + m_off(13 + t) + TO(i)); };
    auto group = [&](auto GI) __attribute__((always_inline)) {
      constexpr int gi = decltype(GI)::value;
      if (gi == G14) { BAR();
        SB(); }
      if (gi == G15) {
        PAGE(11);
        BAR();
        SB();
        xq[0] = ld16_s<0>(x, xo); xq[1] = ld16_s<64>(x, xo);
        sq = ld8_s_sc1(stat, xo >> 11 << 3);
        kv = *(const u32x2*)(smem + LDS_SEL);
        SB();
      }
#pragma unroll
      for (int q = 0; q < 16; ++q) {
        const int n = gi * 16 + q;
        if (n == NF0) {
          wait_xq<12>(xq, sq);
          key[0] = __builtin_amdgcn_readfirstlane(kv.x); key[1] = __builtin_amdgcn_readfirstlane(kv.y);
          nf_pre();
          SB();
        }
        if (n == 112) {
        }
        if (n == 152) {
        }
        if (n == 168) {
          WAIT_VM(8); MSG_FLAG(0); SB(); }
        if (n == 176) { WAIT_VM(5); MSG_FLAG(1); SB(); }
        if (n == 184) {
          WAIT_VM(2);
          MSG_FLAG(2); fx = bld4_sc1(rc, po); SB(); }
        mm(chp(gi) || chn(gi), gi & 1, g_q[gi], q, 2);
        constexpr bool ni = gi + 1 < NG && (chp(gi + 1 < NG ? gi + 1 : gi) || chn(gi + 1 < NG ? gi + 1 : gi));
        auto nr = [](int r) { return ni && r >= 2 ? 5 - r : r; };
        if constexpr (chp(gi) && chn(gi)) {
          if (q < 4) rdm(g_t[gi], 2 + (q >> 1), q & 1); else if (q < 8) rdn(gi + 1, nr(q - 4)); else if (q < 12) rdm(g_t[gi + 1], (q - 8) >> 1, q & 1);
        } else if constexpr (chp(gi)) {
          if (q < 4) rdm(g_t[gi], 2 + (q >> 1), q & 1);
          else if (gi + 1 < NG && q < 12 && !(q & 1)) rdn(gi + 1, nr((q - 4) >> 1));
        } else if constexpr (chn(gi)) {
          if (!(q & 1)) { if (q < 8) rdn(gi + 1, nr(q >> 1)); else rdm(g_t[gi + 1], (q - 8) >> 2, (q >> 1) & 1); }
        } else {
          if (!(q & 1) && q < 8) rdn(gi + 1, nr(q >> 1));
        }
        if (gi == GP0 && q < 8) publish(0, q);
        if (gi == GP1 && q < 8) publish(1, q);
        if (gi == 9 && q < 8) publish(2, q);
        if (n >= NF0) {
          const int m = (n - NF0) * 2;
          if (m < 56) { nf_step(m); nf_step(m + 1); } else if (m < 72) { nf_unp(m - 56); nf_unp(m - 55); }
        }
        SB();
      }
    };
#define GR4(i) group(IC<i>{}); group(IC<i + 1>{}); group(IC<i + 2>{}); group(IC<i + 3>{});
    GR4(0) GR4(4) GR4(8)
    asm volatile("s_nop 7");
    SB();
  }
#pragma unroll
  for (int i = 0; i < 4; ++i) *(f32x4*)(smem + LDS_ACC + wr * 16384 + wc * 4096 + i * 1024 + lane * 16) = acc[7][i];
#pragma unroll
  for (int i = 0; i < 4; ++i) ya[i] = acc[6][i];
  asm volatile("s_waitcnt lgkmcnt(0)" ::: "memory");
  if (lane == 0) sig[0] = 2;
  }

  PAGE(6);
  const unsigned rel0 = 0xffff - (key[0] & 0xffffu) - trow, rel1 = 0xffff - (key[1] & 0xffffu) - trow;
  if (min(rel0, rel1) < 16) {
#pragma unroll 1
    for (int c = 0; c < 2; ++c) {
      const unsigned rel = c ? rel1 : rel0;
      if (rel >= 16) continue;
      lds_vi cnt = (lds_vi)(smem + LDS_SEL + 24 + c * 4);
      int it = 0;
      while (cnt[0] != 4 && it < SPIN_MAX * 8) { __builtin_amdgcn_s_sleep(1); ++it; }
      asm volatile("" ::: "memory");
      if (rel == fr) {
        const float* fp = (const float*)(smem + LDS_FIN + c * 512) + (wid & 1) * 64 + fq * 8;
#pragma unroll
        for (int u4 = 0; u4 < 4; ++u4) {
          const f32x4 v = *(const f32x4*)(fp + (u4 >> 1) * 32 + (u4 & 1) * 4);
#pragma unroll
          for (int k = 0; k < 4; k += 2) {
            const unsigned u = pk2(nf[u4 * 4 + k] + v[k], nf[u4 * 4 + k + 1] + v[k + 1]);
            nf[u4 * 4 + k] = lo_bf(u); nf[u4 * 4 + k + 1] = hi_bf(u);
          }
        }
      }
    }
  }
  u32x4 pp[3][2];
  unsigned e_lo = 0x00003f80u, e_hi = 0x3f800000u;
  asm volatile("" : "+v"(e_lo), "+v"(e_hi));
  __amdgpu_buffer_rsrc_t rs_ = rs, rc_ = rc, ro_ = mk_rsrc(out);
  int mo[3] = {MSG_IDX(0) * 4 * MSG, MSG_IDX(1) * 4 * MSG, MSG_IDX(2) * 4 * MSG}, mv = lane * 16 + ha * 2048, mv2 = mv + 1024;
  asm volatile("" : "+s"(rs_), "+s"(rc_), "+s"(ro_), "+s"(mo[0]), "+s"(mo[1]), "+s"(mo[2]));
  unsigned as_ = lds_addr((const void*)smem) + LDS_SIG + (wid & 3) * 4, aa_ = lds_addr(smem + LDS_ACC) + (wid & 2) * 8192 + (wid & 1) * 4096 + lane * 16;
  int xs = ((trow + (lane >> 3)) * D_ + et * 128 + (wid & 1) * 64) * 2 + (lane & 7) * 16, xs2 = xs + 8 * D_ * 2;
  const unsigned mt = lds_addr(smem) + (wid & 2) * 8192 + (ha ? LDS_ACC + (wid & 1) * 4096 : n_off(14) + (wid & 1) * 2048);
  unsigned aw_ = mt + fr * 128 + ((fq ^ (fr & 7)) * 16), aw2 = aw_ ^ 64, ar_ = mt + (lane >> 3) * 128 + (((lane ^ (lane >> 3)) & 7) * 16);
  asm volatile("" : "+v"(mv), "+v"(mv2), "+v"(as_), "+v"(aa_), "+v"(xs), "+v"(xs2), "+v"(aw_), "+v"(aw2), "+v"(ar_));
  {
    unsigned sv_, it_ = SPIN_MAX * 8;
    asm volatile("s_cmp_eq_u32 %9, 0\n\ts_cbranch_scc1 2f\n"
                 "1:\n\tds_read_b32 %0, %10\n\ts_waitcnt lgkmcnt(0)\n\tv_cmp_eq_u32 vcc, 2, %0\n\ts_cbranch_vccnz 3f\n\ts_sleep 1\n\ts_sub_u32 %1, %1, 1\n\ts_cbranch_scc0 1b\n"
                 "3:\n\tds_read_b128 %4, %11\n\tds_read_b128 %5, %11 offset:1024\n\tds_read_b128 %6, %11 offset:2048\n\tds_read_b128 %7, %11 offset:3072\n"
                 "2:\n\ts_waitcnt vmcnt(0)\n\tv_cmp_eq_u32 vcc, %8, %3\n\ts_mov_b32 %2, vcc_lo"
                 : "=&v"(sv_), "+s"(it_), "=&s"(seen), "+v"(fx), "+v"(ya[0]), "+v"(ya[1]), "+v"(ya[2]), "+v"(ya[3])
                 : "s"(tag), "s"(ha), "v"(as_), "v"(aa_) : "vcc", "scc", "memory");
  }
#define MSG_POLL(m) do { \
    if (!((seen >> (m)) & 1)) { \
      unsigned pv_, it_ = SPIN_MAX; \
      asm volatile("1:\n\tbuffer_load_dword %0, %3, %4, 0 offen sc1\n\ts_waitcnt vmcnt(0)\n\tv_cmp_eq_u32 vcc, %5, %0\n\ts_mov_b32 %1, vcc_lo\n\t" \
                   "s_bitcmp1_b32 %1, %6\n\ts_cbranch_scc1 2f\n\ts_sub_u32 %2, %2, 1\n\ts_cbranch_scc1 2f\n\ts_sleep 1\n\ts_branch 1b\n2:" \
                   : "=&v"(pv_), "+s"(seen), "+s"(it_) : "v"(po), "s"(rc_), "s"(tag), "i"(m) : "vcc", "scc", "memory"); \
    } \
    pp[m][0] = __builtin_amdgcn_raw_buffer_load_b128(rs_, mv, mo[m], 16); \
    pp[m][1] = __builtin_amdgcn_raw_buffer_load_b128(rs_, mv2, mo[m], 16); } while (0)
#define MSG_ADD(m) do { \
    _Pragma("unroll") for (int u = 0; u < 8; ++u) { \
      ya[u >> 1][(u & 1) * 2] = add_bf(pp[m][u >> 2][u & 3], e_lo, ya[u >> 1][(u & 1) * 2]); ya[u >> 1][(u & 1) * 2 + 1] = add_bf(pp[m][u >> 2][u & 3], e_hi, ya[u >> 1][(u & 1) * 2 + 1]); } } while (0)
  MSG_POLL(2); MSG_POLL(1); MSG_POLL(0);
  asm volatile("s_waitcnt lgkmcnt(0)" : "+v"(ya[0]), "+v"(ya[1]), "+v"(ya[2]), "+v"(ya[3]) :: "memory");
  SB();
  MSG_ADD(2); MSG_ADD(1);
  asm volatile("" : "+v"(ya[0]), "+v"(ya[1]), "+v"(ya[2]), "+v"(ya[3]));
  SB();
  MSG_ADD(0);
  {
    unsigned ov[8];
    SB();
#pragma unroll
    for (int u = 0; u < 8; ++u) ov[u] = pk2(ya[u >> 1][(u & 1) * 2], ya[u >> 1][(u & 1) * 2 + 1]);
    SB();
#pragma unroll
    for (int u = 0; u < 8; ++u) { xf[2 * u] = add_bf(ov[u], e_lo, xf[2 * u]); xf[2 * u + 1] = add_bf(ov[u], e_hi, xf[2 * u + 1]); }
    SB();
#pragma unroll
    for (int u = 0; u < 8; ++u) ov[u] = pk2(xf[2 * u], xf[2 * u + 1]);
    SB();
#pragma unroll
    for (int u = 0; u < 8; ++u) { nf[2 * u] = add_bf(ov[u], e_lo, nf[2 * u]); nf[2 * u + 1] = add_bf(ov[u], e_hi, nf[2 * u + 1]); }
    SB();
#pragma unroll
    for (int u = 0; u < 8; ++u) ov[u] = pk2(nf[2 * u], nf[2 * u + 1]);
    SB();
    *LDSP(u32x4, aw_) = (u32x4){ov[0], ov[1], ov[2], ov[3]};
    *LDSP(u32x4, aw2) = (u32x4){ov[4], ov[5], ov[6], ov[7]};
    asm volatile("" ::: "memory");
    __builtin_amdgcn_raw_buffer_store_b128(*LDSP(const u32x4, ar_), ro_, xs, 0, 16);
    __builtin_amdgcn_raw_buffer_store_b128(*LDSP(const u32x4, ar_ + 1024), ro_, xs2, 0, 16);
  }
}

extern "C" void kernel_launch(void* const* d_in, const int* in_sizes, int n_in, void* d_out, int out_size,
                              void* d_ws, size_t ws_size, hipStream_t stream) {
  const u16* x = (const u16*)d_in[0];
  const u16* ws_re = (const u16*)d_in[1];
  const u16* ws_im = (const u16*)d_in[2];
  const u16* wd_re = (const u16*)d_in[3];
  const u16* wd_im = (const u16*)d_in[4];
  const u16* w_r = (const u16*)d_in[6];
  const u16* w_gate = (const u16*)d_in[7];
  const u16* w_up = (const u16*)d_in[8];
  const u16* w_down = (const u16*)d_in[9];
  k_main<<<256, 512, 0, stream>>>(x, 0x7fffffffu, 0x00020000u, wd_re, 0x7fffffffu, 0x00020000u, wd_im, 0x7fffffffu, 0x00020000u, w_r, (char*)d_ws,
                                  ws_re, ws_im, w_gate, w_up, w_down, (u16*)d_out);
}
```

```cpp
#include <hip/hip_runtime.h>
#include <hip/hip_bf16.h>
#include <stdint.h>
#include <type_traits>

extern "C" __device__ unsigned long long llvm_dispatch_id(void) __asm("llvm.amdgcn.dispatch.id");
extern "C" __device__ float llvm_fmul_legacy(float, float) __asm("llvm.amdgcn.fmul.legacy");
typedef unsigned short u16;
typedef unsigned long long u64;
typedef __attribute__((ext_vector_type(8))) short bf16x8;
typedef __attribute__((ext_vector_type(4))) unsigned u32x4;
typedef __attribute__((ext_vector_type(2))) unsigned u32x2;
typedef __attribute__((ext_vector_type(4))) float f32x4;
typedef __attribute__((ext_vector_type(2))) float f32x2;
typedef __attribute__((ext_vector_type(2))) __bf16 bf16x2;
typedef __attribute__((address_space(3))) void* lds_vp;
typedef volatile __attribute__((address_space(3))) int* lds_vi;

constexpr int S_ = 2048, D_ = 1024, F_ = 3584;
template <int N> using IC = std::integral_constant<int, N>;

static __device__ __forceinline__ float bf2f(u16 u) { return __uint_as_float((unsigned)u << 16); }
static __device__ __forceinline__ u16 f2bf(float f) {
  __hip_bfloat16 b = __float2bfloat16(f);
  return *reinterpret_cast<u16*>(&b);
}
static __device__ __forceinline__ float rbf(float f) { return bf2f(f2bf(f)); }
static __device__ __forceinline__ float lo_bf(unsigned u) { return __uint_as_float(u << 16); }
static __device__ __forceinline__ float hi_bf(unsigned u) { return __uint_as_float(u & 0xffff0000u); }
static __device__ __forceinline__ unsigned pk2(float a, float b) {
  const bf16x2 v = __builtin_convertvector((f32x2){a, b}, bf16x2);
  return __builtin_bit_cast(unsigned, v);
}
static __device__ __forceinline__ float add_bf(unsigned u, unsigned e, float c) {
  return __builtin_amdgcn_fdot2_f32_bf16(__builtin_bit_cast(bf16x2, u), __builtin_bit_cast(bf16x2, e), c, false);
}
static __device__ __forceinline__ float ln_elem(float x, float mb, float sd, float ri) {
  const float d = rbf(x - mb);
  const float q0 = d * ri;
  return rbf(__builtin_fmaf(__builtin_fmaf(-q0, sd, d), ri, q0));
}
template <int CTRL> static __device__ __forceinline__ float dpp_f(float v) {
  return __int_as_float(__builtin_amdgcn_update_dpp(0, __float_as_int(v), CTRL, 0xf, 0xf, true));
}
template <int CTRL> static __device__ __forceinline__ unsigned dpp_u(unsigned v) {
  return (unsigned)__builtin_amdgcn_update_dpp(0, (int)v, CTRL, 0xf, 0xf, true);
}
static __device__ __forceinline__ float row_sum(float v) {
  v += dpp_f<0xB1>(v);
  v += dpp_f<0x4E>(v);
  v += dpp_f<0x141>(v);
  v += dpp_f<0x140>(v);
  return v;
}
static __device__ __forceinline__ float fold32(float a, float b) {
  const auto r = __builtin_amdgcn_permlane32_swap(__float_as_uint(a), __float_as_uint(b), false, false);
  return __uint_as_float(r[0]) + __uint_as_float(r[1]);
}
static __device__ __forceinline__ float fold16(float a, float b) {
  const auto r = __builtin_amdgcn_permlane16_swap(__float_as_uint(a), __float_as_uint(b), false, false);
  return __uint_as_float(r[0]) + __uint_as_float(r[1]);
}
static __device__ __forceinline__ float rdl(float v, int l) { return __int_as_float(__builtin_amdgcn_readlane(__float_as_int(v), l)); }

#define ST_JB 9
#define WSN 10
#define FSM 0xa2
#define GV_JB 7
#define DG 11
#define AB_STRIDE 32
#define SPIN_MAX (1 << 17)

static __device__ __forceinline__ unsigned lds_addr(const void* p) { return (unsigned)(size_t)(lds_vp)p; }

constexpr int MSG = 4096;
constexpr size_t WS_ABT   = 0;
constexpr size_t WS_SLAB  = WS_ABT + (size_t)D_ * 4096 * 2;
constexpr size_t WS_GU    = WS_SLAB + (size_t)64 * 64 * MSG;
constexpr size_t WS_DN    = WS_GU + (size_t)8 * F_ * 16;
constexpr size_t WS_FLAGS = WS_DN + (size_t)2 * 8 * 32 * 64 * 16;
constexpr int FL_X = 0;
constexpr int FL_AB = 64 * 64 * 32;
constexpr int FL_ST = FL_AB + 2048 * AB_STRIDE;
constexpr int FL_CE = FL_ST + 4096;
constexpr int FL_KEY = FL_CE + 4096;
constexpr int FL_EPOCH = FL_KEY + 4096;
constexpr int WS_MB = (int)((WS_FLAGS + (FL_EPOCH + 1) * 4 + 1048575) >> 20);

#define WAIT_VM(n) asm volatile("s_waitcnt vmcnt(" #n ")" ::: "memory")
template <int N> static __device__ __forceinline__ void wait_vm() { asm volatile("s_waitcnt vmcnt(%0)" :: "i"(N) : "memory"); }
#define BAR_LGKM() asm volatile("s_waitcnt lgkmcnt(0)\n\ts_barrier" ::: "memory")
#define BAR() asm volatile("s_barrier" ::: "memory")
#define SB() __builtin_amdgcn_sched_barrier(0)
#define PAGE(n) asm volatile("s_branch 2f\n\t.p2align 12\nk_pg" #n ":\n\ts_setpc_b64 s[42:43]\n2:")
#define CALL_PG(n) asm volatile("s_getpc_b64 s[40:41]\n3:\n\ts_mov_b32 s42, k_pg" #n "-3b\n\ts_ashr_i32 s43, s42, 31\n\ts_add_u32 s40, s40, s42\n\ts_addc_u32 s41, s41, s43\n\t" \
                                "s_swappc_b64 s[42:43], s[40:41]" ::: "s40", "s41", "s42", "s43", "scc", "memory")

static __device__ __forceinline__ __amdgpu_buffer_rsrc_t mk_rsrc(const void* p) {
  return __builtin_amdgcn_make_buffer_rsrc((void*)p, 0, 0x7fffffff, 0x00020000);
}
#define DMA_(rsrc, voff, mbase, MI, sbase, SI, pol) do { unsigned _t; \
    asm volatile("s_add_i32 m0, %2, %3\n\ts_add_i32 %0, %5, %6\n\tbuffer_load_dwordx4 %1, %4, %0 offen" pol " lds" \
                 : "=&s"(_t) : "v"(voff), "s"(mbase), "i"(MI), "s"(rsrc), "s"(sbase), "i"(SI) : "memory", "scc"); } while (0)
#define TOUCH(rsrc, voff, mbase, MI, soff) \
    asm volatile("s_add_i32 m0, %1, %2\n\ts_nop 0\n\tbuffer_load_dword %0, %3, %4 offen lds" \
                 :: "v"(voff), "s"(mbase), "i"(MI), "s"(rsrc), "s"(soff) : "memory", "scc")
template <int OFF> static __device__ __forceinline__ u32x4 ld16_s(const void* p, int voff) {
  u32x4 v;
  asm volatile("global_load_dwordx4 %0, %1, %2 offset:%3" : "=v"(v) : "v"(voff), "s"(p), "i"(OFF) : "memory");
  return v;
}
template <int OFF> static __device__ __forceinline__ void sld(unsigned& sink, const void* p, int soff) {
  asm volatile("s_load_dword %0, %1, %2 offset:%3" : "+s"(sink) : "s"(p), "s"(soff), "i"(OFF) : "memory");
}
static __device__ __forceinline__ unsigned gld4_s(const void* p, unsigned voff) {
  unsigned v;
  asm volatile("global_load_dword %0, %1, %2" : "=v"(v) : "v"(voff), "s"(p) : "memory");
  return v;
}
static __device__ __forceinline__ u32x2 bld8_sc1(__amdgpu_buffer_rsrc_t r, unsigned voff) {
  u32x2 v;
  asm volatile("buffer_load_dwordx2 %0, %1, %2, 0 offen sc1" : "=v"(v) : "v"(voff), "s"(r) : "memory");
  return v;
}
template <int N> static __device__ __forceinline__ void wait_xs(unsigned& a, u32x2& b) { asm volatile("s_waitcnt vmcnt(%2)" : "+v"(a), "+v"(b) : "i"(N) : "memory"); }
template <int N> static __device__ __forceinline__ void wait_xq(u32x4 (&a)[2], u32x2& b) { asm volatile("s_waitcnt vmcnt(%3)" : "+v"(a[0]), "+v"(a[1]), "+v"(b) : "i"(N) : "memory"); }
static __device__ __forceinline__ u32x2 ld8_s_sc1(const void* p, int voff) {
  u32x2 v;
  asm volatile("global_load_dwordx2 %0, %1, %2 sc1" : "=v"(v) : "v"(voff), "s"(p) : "memory");
  return v;
}
static __device__ __forceinline__ void lgkm0(unsigned& sink) { asm volatile("s_waitcnt lgkmcnt(0)" : "+s"(sink) :: "memory"); }
template <typename T> static __device__ __forceinline__ void pin4(T& a, T& b, T& c, T& d) { asm volatile("" : "+s"(a), "+s"(b), "+s"(c), "+s"(d)); }
template <typename T, typename U> static __device__ __forceinline__ void pins(T& a, U& b) { asm volatile("" : "+s"(a), "+s"(b)); }
static __device__ __forceinline__ void launder(int& v, int& a, int& b) { asm volatile("" : "+v"(v)); asm volatile("" : "+s"(a), "+s"(b)); }
static __device__ __forceinline__ unsigned bld4_sc1(__amdgpu_buffer_rsrc_t r, int voff) {
  unsigned v;
  asm volatile("buffer_load_dword %0, %1, %2, 0 offen sc1" : "=v"(v) : "v"(voff), "s"(r) : "memory");
  return v;
}
static __device__ __forceinline__ u32x4 bld16_nt(__amdgpu_buffer_rsrc_t r, int voff, int soff) {
  u32x4 v;
  asm volatile("buffer_load_dwordx4 %0, %1, %2, %3 offen nt" : "=v"(v) : "v"(voff), "s"(r), "s"(soff) : "memory");
  return v;
}
static __device__ __forceinline__ void bld16_nt_io(u32x4& v, __amdgpu_buffer_rsrc_t r, int voff, int soff) {
  asm volatile("buffer_load_dwordx4 %0, %1, %2, %3 offen nt" : "+v"(v) : "v"(voff), "s"(r), "s"(soff) : "memory");
}
static __device__ __forceinline__ u32x4 bld16_sc1(__amdgpu_buffer_rsrc_t r, int voff, int soff) {
  u32x4 v;
  asm volatile("buffer_load_dwordx4 %0, %1, %2, %3 offen sc1" : "=v"(v) : "v"(voff), "s"(r), "s"(soff) : "memory");
  return v;
}
static __device__ __forceinline__ u32x2 granule_wait(const u64* p, unsigned tag) {
  u64 v = 0;
#pragma unroll 1
  for (int it = 0; it < SPIN_MAX; ++it) {
    v = __hip_atomic_load(p, __ATOMIC_RELAXED, __HIP_MEMORY_SCOPE_AGENT);
    if (__all((unsigned)(v >> 32) == tag)) break;
    __builtin_amdgcn_s_sleep(2);
  }
  return (u32x2){(unsigned)v, (unsigned)(v >> 32)};
}
static __device__ __forceinline__ float key_score(unsigned key) {
  const unsigned u = key >> 16;
  return bf2f((u16)((u & 0x8000u) ? (u & 0x7fffu) : (~u & 0xffffu)));
}
static __device__ __forceinline__ void merge2(unsigned& a1, unsigned& a2, unsigned b1, unsigned b2) {
  const unsigned lo = min(a1, b1);
  a1 = max(a1, b1);
  a2 = max(lo, max(a2, b2));
}
static __device__ __forceinline__ float sqdev16(const u32x4 (&v)[2], float mb) {
  f32x2 q2 = {0.f, 0.f};
#pragma unroll
  for (int i = 0; i < 8; ++i) {
    const f32x2 e = (f32x2){lo_bf(v[i >> 2][i & 3]), hi_bf(v[i >> 2][i & 3])} - mb;
    q2 += e * e;
  }
  return q2.x + q2.y;
}

constexpr int G1_NST = 4, G1_NKT = 16, G1_STAGE = 32768;
constexpr int G2_NKT = 16, G2_MRING = 98304;
constexpr int n_off(int k) { return (k % 3) * 32768; }
constexpr int m_off(int k) { return ((k + 2) % 3) * 16384; }
constexpr int k_ord(int k) { return k < 2 ? k : (k < 9 ? 2 * (k - 1) : 2 * (k - 8) + 1); }
constexpr int LDS_DUMMY = G2_MRING + 49152;
constexpr int LDS_GU = LDS_DUMMY + 4096;
constexpr int LDS_FIN = LDS_GU + 4096;
constexpr int LDS_MB = LDS_FIN + 4096;
constexpr int LDS_SEL = LDS_MB + 1536;
constexpr int LDS_SIG = LDS_SEL + 64;
constexpr int LDS_KEY = LDS_SIG + 32;
constexpr int LDS_BYTES = LDS_KEY + 128;
constexpr int LDS_ACC = n_off(G2_NKT - 3);

constexpr int tpos[16] = {0, 0, 0, 0, 106, 110, 115, 2, 2, 3, 4, 5, 0, 0, 0, 0};
constexpr int twho[16] = {0, 0, 0, 0, 7, 7, 7, 0, 1, 2, 6, 5, 0, 0, 0, 0};
constexpr int dn_cnt(int q) { return q == 5 ? 3 : q == 8 ? 4 : 0; }
constexpr int dn_first(int q) { return q <= 5 ? 0 : q <= 8 ? 3 : 7; }

static __device__ __forceinline__ void code_touch(unsigned& sink, unsigned off) {
  asm volatile("s_getpc_b64 s[40:41]\n\tk_main_rt:\n\ts_min_u32 %1, %1, k_main_end-k_main_rt-196\n\t"
               "s_load_dword %0, s[40:41], %1 offset:0\n\ts_load_dword %0, s[40:41], %1 offset:64\n\ts_load_dword %0, s[40:41], %1 offset:128\n\ts_load_dword %0, s[40:41], %1 offset:192"
               : "+s"(sink), "+s"(off) :: "s40", "s41", "scc", "memory");
}
static __device__ __forceinline__ void touch1(__amdgpu_buffer_rsrc_t r, int voff, unsigned mb, int soff) { TOUCH(r, voff, mb, LDS_DUMMY, soff); }
typedef const __attribute__((address_space(1))) u16* gptr;
typedef __attribute__((ext_vector_type(8))) u64 u64x8;
extern "C" __global__ __launch_bounds__(512, 2) void k_main(const u16* x, unsigned x2, unsigned x3, const u16* wd_re, unsigned r2, unsigned r3,
                                                 const u16* wd_im, unsigned i2, unsigned i3, const u16* w_r,
                                                 char* wsp, const u16* ws_re, const u16* ws_im,
                                                 const u16* w_gate, const u16* w_up, const u16* w_down, u16* out) {
  __shared__ __attribute__((aligned(16))) char smem[LDS_BYTES];
  u64x8 ka;
  u32x4 rm, rn;
  unsigned kz;
  asm volatile("s_load_dwordx4 %0, %4, 0\n\ts_load_dwordx16 %2, %4, 48\n\ts_andn2_b32 %3, 4, %5\n\ts_lshl_b32 %3, %3, 2\n\ts_load_dwordx4 %1, %4, %3 offset:16"
               : "=&s"(rm), "=&s"(rn), "=&s"(ka), "=&s"(kz) : "s"(__builtin_amdgcn_kernarg_segment_ptr()), "s"(blockIdx.x) : "scc", "memory");
  SB();
  const int tid = threadIdx.x, lane = tid & 63;
  const int wid = __builtin_amdgcn_readfirstlane(tid >> 6);
  const int g = blockIdx.x ^ 6, xcd = g & 7, j = g >> 3;
  if (wid < 4) {
    unsigned co = ((j * 4 + wid) * 4 + (lane & 3)) * 128;
    asm volatile(".subsection 1\nk_main_end:\n.subsection 0\n\tv_min_u32 %0, k_main_end-k_main-132, %0\n\ts_getpc_b64 s[40:41]\n\ts_mov_b32 m0, %1\n\ts_mov_b64 exec, 15\n\t"
                 "global_load_lds_dword %0, s[40:41]\n\ts_mov_b64 exec, -1"
                 : "+v"(co) : "i"(LDS_DUMMY + 768) : "s40", "s41", "memory");
  }
  const int pw = wid - 4;
  const int row0 = (wid & 3) * 8 + (lane >> 3);
  const int cm = ((lane & 7) ^ (((row0 >> 1) & 1) | ((wid & 3) << 1))) * 16;
  int vm = row0 * (D_ * 2) + cm;
  asm volatile("" : "+v"(vm));
  const int kq = xcd >> 1;
  const int tt = (xcd & 1) * 4 + (j >> 3);
  const int et = j & 7;
  const int z = kq >> 1, st = (kq & 1) * 8 + tt;
  unsigned o_m = st * 128 * D_ * 2, o_n = et * 128 * D_ * 2, mb0 = lds_addr(smem) + pw * 1024;
  asm volatile("" : "+s"(o_m), "+s"(o_n), "+s"(mb0));
#define G1_X(kt, so, q) DMA_(rm, vm, mb0, (so) + (q) * 4096, o_m, (kt) * 128 + (((q) & 1) * 64 + ((q) >> 1) * 32) * D_ * 2, "")
#define G1_W(kt, so, q) DMA_(rn, vm, mb0, (so) + 16384 + (q) * 4096, o_n, (kt) * 128 + (q) * 32 * D_ * 2, "")
#define G1_STAGE_TILE(kt, so, W) do { SB(); G1_X(kt, so, 0); W(0); G1_X(kt, so, 1); W(1); G1_X(kt, so, 2); W(2); G1_X(kt, so, 3); W(3); \
    G1_W(kt, so, 0); W(4); G1_W(kt, so, 1); W(5); G1_W(kt, so, 2); W(6); G1_W(kt, so, 3); W(7); SB(); } while (0)
#define G1_STAGE_A(kt, so, W) do { SB(); G1_X(kt, so, 0); W(0); G1_X(kt, so, 2); W(1); \
    G1_W(kt, so, 0); W(2); G1_W(kt, so, 1); W(3); G1_W(kt, so, 2); W(4); G1_W(kt, so, 3); W(5); W(6); W(7); SB(); } while (0)
#define G1_STAGE_B(kt, so) do { SB(); G1_X(kt, so, 1); G1_X(kt, so, 3); SB(); } while (0)
#define NOW(i)
  asm volatile("s_waitcnt lgkmcnt(0)" : "+s"(ka), "+s"(rm), "+s"(rn) :: "memory");
  int lrow, drow, l16;
  unsigned o_l, o_d;
  auto rows = [&](int i) __attribute__((always_inline)) {
    int g_ = g, l_ = lane;
    if (i == 0) { asm volatile("" : "+s"(g_)); drow = (g_ & ~7) + pw * 2; lrow = drow + (g_ & 3) * 512 + (g_ & 4) * 64; asm volatile("" : "+s"(drow), "+s"(lrow)); }
    if (i == 1) { asm volatile("" : "+s"(g_)); drow += (g_ & 3) * 256; o_l = lrow * D_ * 2; o_d = drow * D_ * 2; asm volatile("" : "+s"(drow), "+s"(o_l), "+s"(o_d)); }
    if (i == 2) { asm volatile("" : "+v"(l_)); l16 = l_ * 16; asm volatile("" : "+v"(l16)); }
  };
  if (__builtin_expect(wid >= 4, 1)) {
    G1_STAGE_TILE(0, 0, rows);
    __builtin_amdgcn_s_setprio(3);
  }
  x = (const u16*)(gptr)((u64)rm[0] | (u64)rm[1] << 32);
  const u16* wdz = (const u16*)(gptr)((u64)rn[0] | (u64)rn[1] << 32);
  w_r = (const u16*)(gptr)ka[0]; wsp = (char*)(gptr)ka[1]; ws_re = (const u16*)(gptr)ka[2]; ws_im = (const u16*)(gptr)ka[3];
  w_gate = (const u16*)(gptr)ka[4]; w_up = (const u16*)(gptr)ka[5]; w_down = (const u16*)(gptr)ka[6]; out = (u16*)(gptr)ka[7];
  int* flags = (int*)(wsp + WS_FLAGS);
  u64* stat = (u64*)(flags + FL_ST);
  const int fr = lane & 15, fq = lane >> 4;
  const int ha = wid >> 2;
  int trow, xo, po;
  const char *prs, *prc;
  auto epi_setup = [&]() __attribute__((always_inline)) {
    int t_ = tt, l_ = lane;
    asm volatile("" : "+s"(t_));
    asm volatile("" : "+v"(l_));
    trow = t_ * 256 + ((wid >> 1) & 1) * 128 + kq * 32 + ha * 16;
    xo = ((trow + (l_ & 15)) * D_ + et * 128 + (wid & 1) * 64 + (l_ >> 4) * 8) * 2;
    prs = wsp + WS_SLAB + (size_t)((t_ * 8 + et) * 64 + (wid & 3)) * MSG;
    prc = (const char*)(flags + FL_X + ((t_ * 8 + et) * 64 + (wid & 3)) * 32);
    asm volatile("" : "+s"(trow), "+s"(prs), "+s"(prc));
    po = l_ < 3 ? (kq * 4 + ((kq + 1 + l_) & 3)) * 512 : (int)0x80000000;
    asm volatile("" : "+v"(xo), "+v"(po));
  };
#define rs mk_rsrc(prs)
#define rc mk_rsrc(prc)
  lds_vi sig = (lds_vi)(smem + LDS_SIG + (wid & 3) * 4);
  unsigned tag;
  u32x4 xq[2];
  u32x2 sq;
  f32x4 ya[4];
  unsigned key[2];
  float nf[16], la0, la1;
#define PINV(a) asm volatile("" : "+v"(a))
  auto nf_pre = [&]() __attribute__((always_inline)) {
    if (__builtin_expect(!__all(sq.y == tag), 0)) sq = granule_wait(stat + (xo >> 11), tag);
    const float ri = __builtin_amdgcn_rcpf(hi_bf(sq.x));
    la0 = -(lo_bf(sq.x) * ri); la1 = 1.f + ri;
    PINV(la0); PINV(la1);
  };
  auto nf_step = [&](int e) __attribute__((always_inline)) {
    const int u = e >> 1;
    nf[e] = (e & 1) ? hi_bf(xq[u >> 2][u & 3]) : lo_bf(xq[u >> 2][u & 3]);
    PINV(nf[e]);
    nf[e] = __builtin_fmaf(nf[e], la1, la0);
    PINV(nf[e]);
  };
#define MSG_IDX(m) (kq * 4 + ((kq + 1 + (m)) & 3))
  unsigned seen, fx;

  if (__builtin_expect(wid >= 4, 1)) {
    constexpr int O_ST = (FL_ST - FL_AB) * 4, O_CE = (FL_CE - FL_AB) * 4, O_KEY = (FL_KEY - FL_AB) * 4, O_EP = (FL_EPOCH - FL_AB) * 4;
    const __amdgpu_buffer_rsrc_t rf = mk_rsrc(wsp + WS_FLAGS + FL_AB * 4);
    u32x4 lv[2], lv2[2], lw[2], ld[2], ld2[2];
    unsigned epv;
    {
      const char* xrow = (const char*)x + o_l;
      const char* wdrow = (const char*)wdz + o_d;
      lv[0] = ld16_s<0>(xrow, l16); lv[1] = ld16_s<1024>(xrow, l16);
      lv2[0] = ld16_s<2048>(xrow, l16); lv2[1] = ld16_s<3072>(xrow, l16);
      ld[0] = ld16_s<0>(wdrow, l16); ld[1] = ld16_s<1024>(wdrow, l16);
      ld2[0] = ld16_s<2048>(wdrow, l16); ld2[1] = ld16_s<3072>(wdrow, l16);
      lw[0] = ld16_s<0>(w_r, l16); lw[1] = ld16_s<1024>(w_r, l16);
      asm volatile("buffer_load_dword %0, off, %1, %2 sc1" : "=v"(epv) : "s"(rf), "s"(O_EP) : "memory");
    }
    SB();
    const unsigned long long did = llvm_dispatch_id();
    const unsigned dmix = (unsigned)did * 0x85EBCA6Bu + (unsigned)(did >> 32) * 0xC2B2AE35u + 0x27D4EB2Fu;
    const bool moe = et != 7;
    unsigned epoch;
    const int ci = lane & 15, rq = lane >> 4, tk = (lane >> 3) & 1;
    int mat, cg, dq, d0;
    u32x4 wv[16];
    const u16 *pgu, *pn2;
    const char *pm2, *spb;
#define rw __builtin_amdgcn_make_buffer_rsrc((void*)pgu, 0, moe ? 0x7fffffff : 0, 0x00020000)
#define rn2 mk_rsrc(pn2)
#define rm2 mk_rsrc(pm2)
    int vw, vn2, vm2;
    int ord2;
    auto k_blk = [&](int k) __attribute__((always_inline)) { return k >= 9 ? (ord2 >> (4 * (k - 9))) & 7 : tt ^ (k_ord(k) >> 1); };
#define KROT(kk_) (k_blk(kk_) * 256 + (k_ord(kk_) & 1) * 128)
    int tv2;
    unsigned sd = 0;
    auto touches = [&](auto P) __attribute__((always_inline)) {
      constexpr int p = decltype(P)::value;
      int e = et;
      asm volatile("" : "+s"(e));
#define TCH(k) if constexpr (tpos[k] == p) { if (e == twho[k]) { const int kb = KROT(k); touch1(rn2, tv2, mb0, kb); } }
      TCH(0) TCH(1) TCH(2) TCH(3) TCH(4) TCH(5) TCH(6) TCH(7) TCH(8) TCH(9) TCH(10) TCH(11) TCH(12) TCH(13) TCH(14) TCH(15)
    };
    unsigned mq0, mq1, mq2, mq3;
    const int cg8 = g & 7, rgb = g >> 3;
    const __amdgpu_buffer_rsrc_t rdn = mk_rsrc(w_down);
    const __amdgpu_buffer_rsrc_t rgu = mk_rsrc(wsp + WS_GU);
    int vdn, vgu;
    u32x4 dv[7], gv[4];
    int abo, abq[3];
    unsigned lma;
    unsigned sel[2][4];
    float g0, g1;
    u32x4 kk[4];
    unsigned k1 = 0, k2 = 0;
    const int ko_ = O_KEY + pw * 4096;
#define MM(a, b, c) __builtin_amdgcn_mfma_f32_16x16x32_bf16(__builtin_bit_cast(bf16x8, a), __builtin_bit_cast(bf16x8, b), c, 0, 0, 0)
#define PIN1(a) asm volatile("" : "+v"(a))
#define PIN2(a, b) asm volatile("" : "+v"(a), "+v"(b))
#define PIN3(a, b, c) asm volatile("" : "+v"(a), "+v"(b), "+v"(c))
    {
      int gro, go;
      u32x2 gr;
      u32x4 oh[5];
      f32x4 sa4, gw[2], gx[2];
      float su, gs, ta, tb, sx, mbv;
      unsigned dk[4], kl;
      const int hb = lane >> 5;
#pragma unroll
      for (int l = 0; l < WSN; ++l) asm volatile("" : "=v"(wv[l]));
      auto diag = [&](const f32x4& m) __attribute__((always_inline)) {
        return __uint_as_float((__float_as_uint(m[0]) & dk[0]) | (__float_as_uint(m[1]) & dk[1]) | (__float_as_uint(m[2]) & dk[2]) | (__float_as_uint(m[3]) & dk[3]));
      };
      auto setup = [&](auto JB, auto GP) __attribute__((always_inline)) {
        constexpr int jb = decltype(JB)::value, gp = decltype(GP)::value;
        SB();
        {
          int ln = lane, jj = j, p_ = pw;
          launder(ln, jj, p_);
          const int c_ = ln & 15, r_ = ln >> 4, e_ = jj & 7, t_ = (xcd & 1) * 4 + (jj >> 3);
          if constexpr (jb == 1 && gp == 0) {
            pn2 = ((kq >> 1) ? ws_im : ws_re) + (long)t_ * 256 * S_ + (kq & 1) * 1024;
            spb = (const char*)(pn2 + (e_ * 4 + p_) * 8 * S_);
            pins(pn2, spb);
          }
          if constexpr (jb == 1 && gp >= 1 && gp < 6) {
            const unsigned o = c_ == gp - 1 ? 0x3f803f80u : 0u;
            oh[gp - 1] = (u32x4){o, o, o, o};
            PIN1(oh[gp - 1]);
          }
          if constexpr (jb == 1 && gp == 6) {
            const int gm = moe ? (jj - (jj >> 3)) * 8 + xcd : 0;
            mat = gm / 112; cg = (gm % 112) >> 2; dq = gm & 3; d0 = dq * 256 + p_ * 64;
            pin4(mat, cg, dq, d0);
          }
          if constexpr (jb == 2 && gp == 0) {
            pgu = mat ? w_up : w_gate;
            pm2 = wsp + WS_ABT + ((long)e_ * 128 * 4096 + kq * 1024) * 2;
            pins(pgu, pm2);
            vw = ((d0 + r_ * 16) * F_ + cg * 128 + c_ * 8) * 2;
            PIN1(vw);
          }
          if constexpr (jb == 2 && (gp == 1 || gp == 2)) {
            dk[gp * 2 - 2] = c_ == r_ * 4 + gp * 2 - 2 ? ~0u : 0u; dk[gp * 2 - 1] = c_ == r_ * 4 + gp * 2 - 1 ? ~0u : 0u;
            PIN2(dk[gp * 2 - 2], dk[gp * 2 - 1]);
          }
          if constexpr (jb == 2 && gp == 3) { kl = lrow + (ln >> 5); go = (ln & 30) ? (int)0x80000000 : ((ln & 1) ? O_KEY : O_ST) + (int)kl * 8; PIN2(kl, go); }
          if constexpr (jb == 2 && gp == 4) { kl = 0xffff - kl; go = (ln & ~1) == 2 ? O_CE + (z * D_ + drow + (ln & 1)) * 8 : go; PIN2(kl, go); }
          if constexpr (jb == 2 && gp == 5) {
            gro = (p_ < 2 ? O_ST + (st * 128 + p_ * 64) * 8 : O_CE + (z * D_ + e_ * 128 + (p_ - 2) * 64) * 8) + ln * 8;
            tv2 = (p_ * 64 + ln) * 4096;
            lma = lds_addr(smem + LDS_MB) + (p_ < 2 ? p_ * 256 : 512 + p_ * 256) + ln * 4;
            PIN3(gro, tv2, lma);
          }
          if constexpr (jb == 2 && gp == 6) {
            const u64 lo = (t_ & 2) ? ((t_ & 4) ? 0x0150234601503247ull : 0x0514670205147603ull) : ((t_ & 4) ? 0x0123067401532076ull : 0x0567423005176432ull);
            ord2 = (int)(lo >> ((t_ & 1) * 32));
            pins(ord2, p_);
          }
          if constexpr (jb == 7 && gp == 0) {
            const int r0 = p_ * 8 + (ln >> 3);
            vn2 = r0 * (S_ * 2) + ((ln & 7) ^ ((r0 >> 1) & 7)) * 16; vm2 = r0 * 8192 + (vm & 127);
            PIN2(vn2, vm2);
          }
          if constexpr (jb == 7 && gp == 1) { vdn = jj * 112 + p_ * 28 + r_ * 7; vgu = (((ln >> 3) & 1) * 4 * F_ + vdn + min(ln & 7, 6)) * 16; PIN2(vdn, vgu); }
          if constexpr (jb == 7 && gp == 2) { vdn = (vdn * D_ + cg8 * 128 + c_ * 8) * 2; PIN1(vdn); }
          if constexpr (jb == 7 && gp == 3) {
            abo = (((kq * 8 + e_) * 4 + p_) * 16 + c_) * AB_STRIDE * 4;
            abq[2] = ln >= 16 || (ln & 7) == t_ ? (int)0x80000000 : abo;
            PIN2(abo, abq[2]);
          }
          if constexpr (jb == 7 && gp == 4) {
            abq[1] = ln >= 8 ? (int)0x80000000 : abq[2];
            abq[0] = ((ln ^ t_) & 4) ? (int)0x80000000 : abq[1];
            PIN2(abq[0], abq[1]);
          }
          if constexpr (jb == 7 && gp == 5) {
            const unsigned mb = lds_addr(smem) + p_ * 1024;
            mq0 = mb + ((3 - kq) & 3) * 4096; mq1 = mb + ((4 - kq) & 3) * 4096; mq2 = mb + ((5 - kq) & 3) * 4096; mq3 = mb + ((6 - kq) & 3) * 4096;
            pin4(mq0, mq1, mq2, mq3);
          }
          if constexpr (jb == 8 && gp < 4) {
#pragma unroll
            for (int h = 0; h < 2; ++h) {
              const unsigned hv = h ? 0x0302u : 0x0100u;
              sel[h][gp] = ((ln & 7) >> 1) == gp ? ((ln & 1) ? (hv << 16) | 0x0c0cu : 0x0c0c0000u | hv) : 0x0c0c0c0cu;
            }
            PIN2(sel[0][gp], sel[1][gp]);
          }
        }
        SB();
      };
#define WS1(i) setup(IC<1>{}, IC<i>{})
#define WS2(i) setup(IC<2>{}, IC<i>{})
#define WS3(i) do { setup(IC<7>{}, IC<i>{}); if (i >= 4) setup(IC<8>{}, IC<(i) & 3>{}); } while (0)
      G1_STAGE_TILE(1, G1_STAGE, WS1);
      WAIT_VM(19); BAR();
      G1_STAGE_TILE(2, 2 * G1_STAGE, WS2);
      G1_STAGE_TILE(3, 3 * G1_STAGE, WS3);
      auto work1 = [&](auto JB, auto GP) __attribute__((always_inline)) {
        constexpr int jb = decltype(JB)::value, gp = decltype(GP)::value;
        SB();
        if constexpr (jb == 1) {
          const f32x4 z4 = {0.f, 0.f, 0.f, 0.f};
          if constexpr (gp == 0) { sa4 = MM(lv[0], oh[0], z4); gw[0] = MM(lv[0], lw[0], z4); gx[0] = MM(lv[0], lv[0], z4); PIN3(sa4, gw[0], gx[0]); }
          if constexpr (gp == 1) { sa4 = MM(lv2[0], oh[1], sa4); gw[1] = MM(lv2[0], lw[0], z4); gx[1] = MM(lv2[0], lv2[0], z4); PIN3(sa4, gw[1], gx[1]); }
          if constexpr (gp == 2) { sa4 = MM(lv[1], oh[0], sa4); gw[0] = MM(lv[1], lw[1], gw[0]); gx[0] = MM(lv[1], lv[1], gx[0]); PIN3(sa4, gw[0], gx[0]); }
          if constexpr (gp == 3) { sa4 = MM(lv2[1], oh[1], sa4); gw[1] = MM(lv2[1], lw[1], gw[1]); gx[1] = MM(lv2[1], lv2[1], gx[1]); PIN3(sa4, gw[1], gx[1]); }
          if constexpr (gp == 4) { sa4 = MM(ld[0], oh[2], sa4); sa4 = MM(ld[1], oh[2], sa4); PIN1(sa4); }
          if constexpr (gp == 5) { sa4 = MM(ld2[0], oh[3], sa4); sa4 = MM(ld2[1], oh[3], sa4); PIN1(sa4); }
          if constexpr (gp == 6) { sa4 = MM(lw[0], oh[4], sa4); sa4 = MM(lw[1], oh[4], sa4); PIN1(sa4);
            epoch = __builtin_amdgcn_readfirstlane(epv); tag = ((epoch + 1u) * 0x9E3779B1u ^ dmix) | 1u; }
        }
        if constexpr (jb == 2) {
          if constexpr (gp == 0) { su = (sa4[0] + sa4[1]) + (sa4[2] + sa4[3]); ta = diag(gw[0]); PIN2(su, ta); }
          if constexpr (gp == 1) { su = fold32(su, su); tb = diag(gw[1]); PIN2(su, tb); }
          if constexpr (gp == 2) { su = fold16(su, su); gs = fold32(ta, tb); PIN2(su, gs); }
          if constexpr (gp == 3) { ta = diag(gx[0]); tb = diag(gx[1]); PIN2(ta, tb); }
          if constexpr (gp == 4) { gs = fold16(gs, fold32(ta, tb)); PIN1(gs); }
          if constexpr (gp == 5) { gs += dpp_f<0xB1>(gs); gs += dpp_f<0x4E>(gs); PIN1(gs); }
          if constexpr (gp == 6) { gs += dpp_f<0x141>(gs); gs += dpp_f<0x140>(gs); PIN1(gs); }
        }
        if constexpr (jb == 3) {
          if constexpr (gp == 0) {
            const float s0 = rdl(su, 0), s1 = rdl(su, 1), q0 = rdl(gs, 16), q1 = rdl(gs, 48);
            sx = hb ? s1 : s0; ta = hb ? q1 : q0; PIN2(sx, ta);
          }
          if constexpr (gp == 1) {
            const float mean = sx * (1.0f / D_);
            mbv = rbf(mean);
            ta -= sx * mean;
            if (__builtin_expect(__any(!(sx * mean <= 16.f * ta)), 0)) {
              float q = fold32(sqdev16(lv, rdl(mbv, 0)), sqdev16(lv2, rdl(mbv, 32)));
              q = row_sum(fold16(q, q));
              const float dm = mean - mbv;
              ta = fmaxf(q - (float)D_ * dm * dm, 0.f);
            }
            PIN2(mbv, ta);
          }
          if constexpr (gp == 2) { ta = rbf(rbf(ta * (1.0f / D_)) + 1e-5f); gs -= mbv * rdl(su, 4); PIN2(ta, gs); }
          if constexpr (gp == 3) { ta = rbf(__builtin_amdgcn_sqrtf(ta)); tb = __builtin_amdgcn_rcpf(ta); PIN2(ta, tb); }
          if constexpr (gp == 4) {
            const unsigned rb = __float_as_uint(rbf(gs * tb));
            kl |= (rb ^ ((unsigned)((int)rb >> 31) | 0x80000000u)) & 0xffff0000u; PIN1(kl);
          }
          if constexpr (gp == 5) { kl = (lane & 2) ? __float_as_uint(su) : (lane & 1) ? kl : (__float_as_uint(mbv) >> 16) | __float_as_uint(ta); PIN1(kl); }
        }
        if constexpr (jb == ST_JB + 3) {
          if constexpr (gp == 0) { if (!__all(gr.y == tag)) gr = granule_wait((const u64*)((const char*)(flags + FL_AB) + gro), tag); PIN1(gr); }
          if constexpr (gp == 1) { g1 = (z ? -1.0f : 1.0f) / hi_bf(gr.x); PIN1(g1); }
          if constexpr (gp == 2) { g0 = pw < 2 ? -(lo_bf(gr.x) * g1) : __uint_as_float(gr.x); PIN1(g0); }
        }
        if constexpr (gp < 7 && (jb - 1) * 7 + gp < 64) {
          constexpr int n = (jb - 1) * 7 + gp;
          sld<((n >> 1) & 7) * 4096 + (n & 1) * 64>(sd, spb, KROT(n >> 4));
        }
        SB();
      };
      auto step1 = [&](auto JB) __attribute__((always_inline)) {
        constexpr int jb = decltype(JB)::value;
        if constexpr (jb == 1) PAGE(1);
        if constexpr (jb == 7) PAGE(2);
        constexpr int n16 = jb == 12 ? 12 : jb == 11 ? 14 : 16;
        constexpr auto wl = [](int q) { return (q == 3 ? 1 : 0) + (q == ST_JB ? 1 : 0); };
        if constexpr (jb == 1) {
          asm volatile("s_waitcnt vmcnt(%11)" : "+v"(lv[0]), "+v"(lv[1]), "+v"(lw[0]), "+v"(lw[1]), "+v"(ld[0]), "+v"(ld[1]),
                       "+v"(lv2[0]), "+v"(lv2[1]), "+v"(ld2[0]), "+v"(ld2[1]), "+v"(epv) : "i"(16) : "memory");
        } else if constexpr (jb == ST_JB + 3) {
          asm volatile("s_waitcnt vmcnt(%1)" : "+v"(gr) : "i"(n16 + wl(jb - 2) + wl(jb - 1)) : "memory");
        } else wait_vm<n16 + wl(jb - 3) + wl(jb - 2) + wl(jb - 1)>();
        BAR();
        if constexpr (jb == 2) { if (pw == 0 && lane < 16) ((int*)(smem + LDS_SEL))[lane < 8 ? lane : lane + 8] = 0; }
        if constexpr (jb == 4) { if (pw == 0 && lane == 0) ((unsigned*)(smem + LDS_SEL))[5] = tag; }
#define W1(i) work1(JB, IC<i>{})
        if constexpr (jb < 10) G1_STAGE_TILE(jb - 1 + G1_NST, ((jb - 1) % G1_NST) * G1_STAGE, W1);
        else G1_STAGE_A(jb - 1 + G1_NST, ((jb - 1) % G1_NST) * G1_STAGE, W1);
        if constexpr (jb == 12) { G1_STAGE_B(13, G1_STAGE); G1_STAGE_B(14, 2 * G1_STAGE); }
        if constexpr (jb == 3) __builtin_amdgcn_raw_buffer_store_b64((u32x2){kl, tag}, rf, go, 0, 16);
        if constexpr (jb == ST_JB) {
          asm volatile("buffer_load_dwordx2 %0, %1, %2, 0 offen sc1" : "=v"(gr) : "v"(gro), "s"(rf) : "memory");
        }
        touches(IC<100 + jb>{});
        if constexpr (jb <= 7) {
          if (et == jb - 1) {
#pragma unroll
            for (int l = 0; l < WSN; ++l) bld16_nt_io(wv[l], rw, vw, l * F_ * 2);
          }
        }
        SB();
      };
      static_assert(ST_JB + 3 <= 12 && ST_JB >= 6, "schedule");
      step1(IC<1>{}); step1(IC<2>{}); step1(IC<3>{}); step1(IC<4>{}); step1(IC<5>{}); step1(IC<6>{}); step1(IC<7>{}); step1(IC<8>{}); step1(IC<9>{}); step1(IC<10>{}); step1(IC<11>{}); step1(IC<12>{});
    }
#define G2_STAGE_N(kk_, W) do { const int kb = KROT(kk_); constexpr int so = n_off(kk_); SB(); \
    DMA_(rn2, vn2, mq0, so, kb, 0, ""); W(0); DMA_(rn2, vn2, mq1, so, kb, 32 * S_ * 2, ""); W(1); \
    DMA_(rn2, vn2, mq2, so, kb, 64 * S_ * 2, ""); W(2); DMA_(rn2, vn2, mq3, so, kb, 96 * S_ * 2, ""); W(3); \
    DMA_(rn2, vn2, mq0, so + 16384, kb, 128 * S_ * 2, ""); W(4); DMA_(rn2, vn2, mq1, so + 16384, kb, 160 * S_ * 2, ""); W(5); \
    DMA_(rn2, vn2, mq2, so + 16384, kb, 192 * S_ * 2, ""); W(6); DMA_(rn2, vn2, mq3, so + 16384, kb, 224 * S_ * 2, ""); W(7); SB(); } while (0)
#define G2_STAGE_NH(kk_, h) do { const int kb = KROT(kk_); constexpr int so = n_off(kk_) + (h) * 16384; SB(); \
    DMA_(rn2, vn2, mq0, so, kb, (h) * 128 * S_ * 2, ""); DMA_(rn2, vn2, mq1, so, kb, ((h) * 128 + 32) * S_ * 2, ""); \
    DMA_(rn2, vn2, mq2, so, kb, ((h) * 128 + 64) * S_ * 2, ""); DMA_(rn2, vn2, mq3, so, kb, ((h) * 128 + 96) * S_ * 2, ""); SB(); } while (0)
#define G2_STAGE_M(kk_, W) do { const int kb = KROT(kk_); constexpr int so = G2_MRING + m_off(kk_); SB(); \
    DMA_(rm2, vm2, mb0, so, kb, 0, " sc1"); W(8); DMA_(rm2, vm2, mb0, so + 4096, kb, 32 * 8192, " sc1"); W(9); \
    DMA_(rm2, vm2, mb0, so + 8192, kb, 64 * 8192, " sc1"); W(10); DMA_(rm2, vm2, mb0, so + 12288, kb, 96 * 8192, " sc1"); W(11); SB(); } while (0)
    WAIT_VM(16); BAR();
    asm volatile("ds_write_b32 %0, %1" :: "v"(lma), "v"(g0) : "memory");
    if (pw < 2) asm volatile("ds_write_b32 %0, %1 offset:512" :: "v"(lma), "v"(g1) : "memory");
    u32x4 tv[4];
    unsigned ready = 0;
    auto need = [&](int k) __attribute__((always_inline)) {
      const int b = k_blk(k) + (k_ord(k) & 1) * 8;
#pragma unroll 1
      for (int it = 0; !((ready >> b) & 1) && it < SPIN_MAX; ++it) {
        ready |= (unsigned)__ballot(__builtin_amdgcn_raw_buffer_load_b32(rf, abq[k < 5 ? 0 : k < 9 ? 1 : 2], 0, 16) == tag);
        asm volatile("" ::: "memory");
      }
    };
    unsigned ksel, bad = 0;
    int isel;
    unsigned xw;
    auto bvec = [&](unsigned src, const unsigned (&sl)[4]) __attribute__((always_inline)) {
      return (u32x4){__builtin_amdgcn_perm(0, src, sl[0]), __builtin_amdgcn_perm(0, src, sl[1]),
                     __builtin_amdgcn_perm(0, src, sl[2]), __builtin_amdgcn_perm(0, src, sl[3])};
    };
#define BC8(v, p) (unsigned)__builtin_amdgcn_update_dpp(__builtin_amdgcn_update_dpp(0, (int)(v), 0x150 + (p), 0xf, 0x3, false), (int)(v), 0x158 + (p), 0xf, 0xc, false)
    float* lg = (float*)(smem + LDS_GU);
    f32x4 macc = {0.f, 0.f, 0.f, 0.f};
    u32x2 sv;
    unsigned hp, gbad;
    float s0, s1;
    auto work2 = [&](auto JB, auto GP) __attribute__((always_inline)) {
      constexpr int jb = decltype(JB)::value, gp = decltype(GP)::value;
      SB();
      if constexpr (jb == -1 && gp < 4) {
        constexpr int c = gp == 0 ? 0xB1 : gp == 1 ? 0x4E : gp == 2 ? 0x141 : 0x140;
        merge2(k1, k2, dpp_u<c>(k1), dpp_u<c>(k2));
        PIN2(k1, k2);
      }
      if constexpr (jb == -1 && gp == 4) {
        ksel = tk ? k2 : k1;
        isel = (0xffff - (int)(ksel & 0xffffu)) & (S_ - 1);
        k1 = (isel * D_ + d0 + rq * 16 + (lane & 7) * 2) * 2; k2 = O_ST + isel * 8;
        PIN1(ksel);
        xw = gld4_s(x, k1);
        sv = bld8_sc1(rf, k2);
      }
      if constexpr (jb == 0 && gp == 1) wait_xs<3 + 1 + 2>(xw, sv);
      if constexpr (jb == 0) {
        if constexpr (gp == 1) { if (!__all(sv.y == tag)) sv = granule_wait(stat + isel, tag); s0 = __builtin_amdgcn_rcpf(hi_bf(sv.x)); PIN1(s0); }
        if constexpr (gp == 2) { s1 = ln_elem(lo_bf(xw), lo_bf(sv.x), hi_bf(sv.x), s0); PIN1(s1); }
        if constexpr (gp == 3) { xw = pk2(s1, ln_elem(hi_bf(xw), lo_bf(sv.x), hi_bf(sv.x), s0)); PIN1(xw); }
      }
      if constexpr (jb == DG && gp < 7) { PIN1(dv[gp]); macc = MM(dv[gp], bvec(BC8(hp, gp), sel[0]), macc); PIN1(macc); }
      if constexpr ((jb == 0 && gp >= 4) || jb == 1) {
        constexpr int l = jb == 0 ? gp - 4 : gp + 4;
        if constexpr (!(l & 1)) hp = BC8(xw, l >> 1);
        PIN1(wv[l]);
        macc = MM(wv[l], bvec(hp, sel[l & 1]), macc);
        PIN2(macc, hp);
      }
      if constexpr (jb == GV_JB + 2) {
        if constexpr (gp == 0) { gbad = (gv[0].y ^ tag) | (gv[0].w ^ tag) | (gv[1].y ^ tag) | (gv[1].w ^ tag); PIN1(gbad); }
        if constexpr (gp == 1) { gbad |= (gv[2].y ^ tag) | (gv[2].w ^ tag) | (gv[3].y ^ tag) | (gv[3].w ^ tag); PIN1(gbad); }
        if constexpr (gp == 2) {
          s0 = ((__uint_as_float(gv[0].x) + __uint_as_float(gv[1].x)) + __uint_as_float(gv[2].x)) + __uint_as_float(gv[3].x);
          s1 = ((__uint_as_float(gv[0].z) + __uint_as_float(gv[1].z)) + __uint_as_float(gv[2].z)) + __uint_as_float(gv[3].z);
          PIN2(s0, s1);
        }
        if constexpr (gp == 3) {
#pragma unroll 1
          for (int it = 0; !__all(gbad == 0) && it < SPIN_MAX; ++it) {
            u32x4 q[4];
            gbad = 0;
#pragma unroll
            for (int i = 0; i < 4; ++i) { q[i] = __builtin_amdgcn_raw_buffer_load_b128(rgu, vgu, i * F_ * 16, 16); gbad |= (q[i].y ^ tag) | (q[i].w ^ tag); }
            s0 = ((__uint_as_float(q[0].x) + __uint_as_float(q[1].x)) + __uint_as_float(q[2].x)) + __uint_as_float(q[3].x);
            s1 = ((__uint_as_float(q[0].z) + __uint_as_float(q[1].z)) + __uint_as_float(q[2].z)) + __uint_as_float(q[3].z);
            asm volatile("" ::: "memory");
          }
          const unsigned gp2 = pk2(s0, s1);
          const unsigned up2 = dpp_u<0x128>(gp2);
          s0 = tk ? hi_bf(up2) : lo_bf(gp2); s1 = tk ? hi_bf(gp2) : lo_bf(up2);
          PIN2(s0, s1);
        }
        if constexpr (gp == 4) { s0 = rbf(s0 * __builtin_amdgcn_rcpf(1.f + __expf(-s0))); PIN1(s0); }
        if constexpr (gp == 5) { hp = pk2(s0 * s1, 0.f); PIN1(hp); }
      }
      if constexpr (jb >= 3 && jb <= 10 && gp >= 4) {
        constexpr int n = (jb - 3) * 8 + gp - 4;
        sld<((n >> 1) & 7) * 4096 + (n & 1) * 64>(sd, spb, KROT(12 + (n >> 4)));
      }
      if constexpr (jb == 10 && gp == 2) epi_setup();
      if constexpr (jb == 10 && gp == 4) code_touch(sd, (j * 4 + pw) * 256);
      SB();
    };
#define W2M(i) work2(IC<-1>{}, IC<i>{})
#define W20(i) work2(IC<0>{}, IC<i>{})
#define W21(i) work2(IC<1>{}, IC<i>{})
    unsigned fs;
    {
      G1_STAGE_B(15, 3 * G1_STAGE);
      G2_STAGE_NH(0, 0);
      lgkm0(sd);
      WAIT_VM(16); BAR();
      G2_STAGE_NH(0, 1);
      WAIT_VM(10); BAR();
#pragma unroll
      for (int i = 0; i < 4; ++i) kk[i] = bld16_sc1(rf, l16, ko_ + i * 1024);
#pragma unroll
      for (int l = WSN; l < 16; ++l) wv[l] = bld16_nt(rw, vw, l * F_ * 2);
      asm volatile("s_waitcnt vmcnt(%4)" : "+v"(kk[0]), "+v"(kk[1]), "+v"(kk[2]), "+v"(kk[3]) : "i"(16 - WSN) : "memory");
      touches(IC<115>{});
#pragma unroll
      for (int i = 0; i < 4; ++i) {
        k2 = max(k2, min(k1, kk[i].x)); k1 = max(k1, kk[i].x);
        k2 = max(k2, min(k1, kk[i].z)); k1 = max(k1, kk[i].z);
        bad |= (kk[i].y ^ tag) | (kk[i].w ^ tag);
      }
#pragma unroll 1
      for (int it = 0; !__all(bad == 0) && it < SPIN_MAX; ++it) {
        k1 = 0; k2 = 0; bad = 0;
#pragma unroll
        for (int i = 0; i < 4; ++i) {
          const u32x4 q = __builtin_amdgcn_raw_buffer_load_b128(rf, l16, ko_ + i * 1024, 16);
          k2 = max(k2, min(k1, q.x)); k1 = max(k1, q.x);
          k2 = max(k2, min(k1, q.z)); k1 = max(k1, q.z);
          bad |= (q.y ^ tag) | (q.w ^ tag);
        }
        asm volatile("" ::: "memory");
      }
      merge2(k1, k2, dpp_u<0xB1>(k1), dpp_u<0xB1>(k2));
      merge2(k1, k2, dpp_u<0x4E>(k1), dpp_u<0x4E>(k2));
      merge2(k1, k2, dpp_u<0x141>(k1), dpp_u<0x141>(k2));
      merge2(k1, k2, dpp_u<0x140>(k1), dpp_u<0x140>(k2));
      if ((lane & 15) == 0) *(u32x2*)(smem + LDS_KEY + (pw * 4 + rq) * 8) = (u32x2){k1, k2};
      PAGE(3);
      BAR_LGKM();
#pragma unroll
      for (int jj = 0; jj < 4; ++jj) tv[jj] = *(const u32x4*)(smem + G2_MRING + m_off(0) + (jj * 4 + pw) * 1024 + lane * 16);
      { const u32x2 kp = *(const u32x2*)(smem + LDS_KEY + ci * 8); k1 = kp.x; k2 = kp.y; }
#pragma unroll
      for (int jj = 0; jj < 4; ++jj) __builtin_amdgcn_raw_buffer_store_b128(tv[jj], rm2, vm2 + jj * 32 * 8192 + 2 * tt * 128, 0, 16);
      G2_STAGE_N(1, W2M);
      WAIT_VM(10);
      if (lane == tt) __builtin_amdgcn_raw_buffer_store_b32(tag, rf, abo, 0, 16);
      G2_STAGE_N(2, W20);
      __builtin_amdgcn_s_sleep(2);
      WAIT_VM(9);
      fs = bld4_sc1(rf, abq[0]);
    }
    BAR();
#pragma unroll
    for (int jj = 0; jj < 4; ++jj) tv[jj] = *(const u32x4*)(smem + G2_MRING + m_off(1) + (jj * 4 + pw) * 1024 + lane * 16);
    G2_STAGE_N(3, NOW);
    asm volatile("s_waitcnt vmcnt(8)" : "+v"(fs) :: "memory");
    ready = (unsigned)__ballot(fs == tag);
    need(2);
    G2_STAGE_M(2, NOW);
    need(3);
    G2_STAGE_M(3, NOW);
    fs = bld4_sc1(rf, abq[1]);
    touches(IC<1>{});
    W21(0); W21(1); W21(2); W21(3); W21(4); W21(5); W21(6); W21(7); W21(8); W21(9); W21(10); W21(11);
    asm volatile("" : "+v"(tv[0]), "+v"(tv[1]), "+v"(tv[2]), "+v"(tv[3]));
    auto step2 = [&](auto JB) __attribute__((always_inline)) {
      constexpr int jb = decltype(JB)::value;
      if constexpr (jb == 3) PAGE(4);
      if constexpr (jb == 9) PAGE(5);
      constexpr auto must = [](int q) { return (q == 2 ? 4 : 0) + ((FSM >> q) & 1) + ((q == GV_JB || q == GV_JB - 1) ? 2 : 0); };
      if constexpr (jb == 2) wait_vm<4 + must(1)>();
      else wait_vm<dn_cnt(jb - 2) + (jb == 5 ? 1 : 0) + 12 + must(jb - 1) + dn_cnt(jb - 1)>();
      if constexpr (jb == 3) { if (pw == 0 && (lane & ~8) == 0) ((unsigned*)(smem + LDS_SEL))[tk] = ksel; }
      if constexpr ((jb == 2 || jb == DG + 1)) {
        *(f32x4*)(lg + pw * 256 + lane * 4) = macc;
        macc = (f32x4){0.f, 0.f, 0.f, 0.f};
        lgkm0(sd);
      }
      BAR();
      float p0[4], p1[4];
      const int col = (pw & 1) * 64 + lane;
      if constexpr (jb == 3) {
        if (g == 0 && pw == 0 && lane == 0) __hip_atomic_store((unsigned*)flags + FL_EPOCH, epoch + 1u, __ATOMIC_RELAXED, __HIP_MEMORY_SCOPE_AGENT);
      }
      if constexpr (jb == DG + 1) {
        const float* p = lg + (pw & 1) * 32 + (lane >> 4) * 64 + ((2 * lane) & 7) * 4 + ((lane >> 2) & 3);
#pragma unroll
        for (int i = 0; i < 4; ++i) { p0[i] = p[i * 256]; p1[i] = p[i * 256 + 4]; }
      }
      if constexpr (jb == 2) {
        const float* p = lg + (col >> 5) * 64 + (col & 7) * 4 + ((col >> 3) & 3);
#pragma unroll
        for (int i = 0; i < 4; ++i) { p0[i] = p[i * 256]; p1[i] = p[i * 256 + 32]; }
      }
      if constexpr (jb == 4) {
        if (lane == 8 + tt) __builtin_amdgcn_raw_buffer_store_b32(tag, rf, abo, 0, 16);
      }
      if constexpr ((FSM >> (jb - 2)) & 1) {
        asm volatile("" : "+v"(fs));
        ready |= (unsigned)__ballot(fs == tag);
      }
      if constexpr (jb == GV_JB + 2) asm volatile("" : "+v"(gv[0]), "+v"(gv[1]), "+v"(gv[2]), "+v"(gv[3]));
#define W2(i) work2(JB, IC<i>{})
      G2_STAGE_N(jb + 2, W2);
      need(jb + 2);
      G2_STAGE_M(jb + 2, W2);
      if constexpr (jb == 2) {
#pragma unroll
        for (int jj = 0; jj < 4; ++jj) __builtin_amdgcn_raw_buffer_store_b128(tv[jj], rm2, vm2 + jj * 32 * 8192 + (2 * tt + 1) * 128, 0, 16);
      }
      if constexpr ((FSM >> jb) & 1) fs = bld4_sc1(rf, abq[jb < 5 ? 1 : 2]);
      if constexpr ((jb == GV_JB || jb == GV_JB - 1)) {
#pragma unroll
        for (int i = (jb == GV_JB ? 2 : 0); i < (jb == GV_JB ? 4 : 2); ++i) gv[i] = bld16_sc1(rgu, vgu, i * F_ * 16);
      }
      touches(JB);
#pragma unroll
      for (int i = dn_first(jb); i < dn_first(jb) + dn_cnt(jb); ++i) dv[i] = bld16_nt(rdn, vdn, i * D_ * 2);
      if constexpr (jb == 2) asm volatile("" : "+v"(p0[0]), "+v"(p0[1]), "+v"(p0[2]), "+v"(p0[3]), "+v"(p1[0]), "+v"(p1[1]), "+v"(p1[2]), "+v"(p1[3]));
      if constexpr (jb == DG + 1) {
        if (pw < 2)
          __builtin_amdgcn_raw_buffer_store_b128((u32x4){__float_as_uint((p0[0] + p0[1]) + (p0[2] + p0[3])), tag, __float_as_uint((p1[0] + p1[1]) + (p1[2] + p1[3])), tag},
                                                 mk_rsrc(wsp + WS_DN), (((pw * 8 + cg8) * 32 + rgb) * 64 + lane) * 16, 0, 16);
      }
      if constexpr (jb == 2) {
        if (moe && pw < 2)
          __builtin_amdgcn_raw_buffer_store_b128((u32x4){__float_as_uint((p0[0] + p0[1]) + (p0[2] + p0[3])), tag, __float_as_uint((p1[0] + p1[1]) + (p1[2] + p1[3])), tag},
                                                 rgu, ((mat * 4 + dq) * F_ + cg * 128 + col) * 16, 0, 16);
      }
      SB();
    };
    static_assert(DG + 1 <= 12 && DG >= GV_JB + 3 && dn_first(DG - 2) == 7, "schedule");
    step2(IC<2>{}); step2(IC<3>{}); step2(IC<4>{}); step2(IC<5>{}); step2(IC<6>{}); step2(IC<7>{}); step2(IC<8>{}); step2(IC<9>{}); step2(IC<10>{}); step2(IC<11>{}); step2(IC<12>{}); step2(IC<13>{});
    lgkm0(sd);
    PAGE(7);
    WAIT_VM(12); BAR();
    xq[0] = ld16_s<0>(x, xo); xq[1] = ld16_s<64>(x, xo); sq = ld8_s_sc1(stat, xo >> 11 << 3);
    WAIT_VM(3); BAR();
    asm volatile("s_waitcnt vmcnt(0)" : "+v"(xq[0]), "+v"(xq[1]), "+v"(sq) :: "memory");
    key[0] = __builtin_amdgcn_readlane(ksel, 0); key[1] = __builtin_amdgcn_readlane(ksel, 8);
    nf_pre();
#pragma unroll
    for (int n = 0; n < 16; ++n) nf_step(n);
#pragma unroll 1
    for (int c = 0; c < 2; ++c) {
      const unsigned kc = __builtin_amdgcn_readlane(ksel, c * 8);
      const int ic = (0xffff - (int)(kc & 0xffffu)) & (S_ - 1);
      if ((ic >> 8) != tt || ((ic >> 5) & 3) != kq) continue;
      const __amdgpu_buffer_rsrc_t rd = mk_rsrc(wsp + WS_DN + (size_t)(c * 8 + et) * 32768);
#pragma unroll 1
      for (int it = 0; it < SPIN_MAX; ++it) {
        if (__all(__builtin_amdgcn_raw_buffer_load_b32(rd, (lane & 31) * 1024 + 1020, 0, 16) == tag)) break;
        __builtin_amdgcn_s_sleep(2);
        asm volatile("" ::: "memory");
      }
      float s0, s1;
      unsigned bad = 1;
#pragma unroll 1
      for (int it = 0; !__all(bad == 0) && it < SPIN_MAX; ++it) {
        bad = 0; s0 = 0.f; s1 = 0.f;
#pragma unroll
        for (int i = 0; i < 8; ++i) {
          const u32x4 q = __builtin_amdgcn_raw_buffer_load_b128(rd, rq * 1024 + pw * 256 + ci * 16, i * 4096, 16);
          bad |= (q.y ^ tag) | (q.w ^ tag); s0 += __uint_as_float(q.x); s1 += __uint_as_float(q.z);
        }
        asm volatile("" ::: "memory");
      }
      s0 = fold32(s0, s1);
      s0 = fold16(s0, s0);
      const float sg = rbf(1.f / (1.f + __expf(-key_score(kc))));
      ((float*)(smem + LDS_FIN))[c * 128 + pw * 32 + ci * 2 + (lane >> 5)] = rbf(rbf(s0) * sg);
      asm volatile("s_waitcnt lgkmcnt(0)" ::: "memory");
      if (lane == 0) asm volatile("ds_add_u32 %0, %1" :: "v"(lds_addr(smem + LDS_SEL + 24 + c * 4)), "v"(1) : "memory");
    }
    asm volatile("v_mov_b32 %0, 0" : "=v"(fx), "=v"(ya[0]), "=v"(ya[1]), "=v"(ya[2]), "=v"(ya[3]));
  } else {
  {
    const int q = j * 4 + wid;
    constexpr int Q1 = WS_MB, Q2 = Q1 + 8, Q3 = Q2 + 8, Q4 = Q3 + 7, Q5 = Q4 + 7, Q6 = Q5 + 7, Q7 = Q6 + 4, Q8 = Q7 + 4, QC = Q8 + 1;
    static_assert(QC <= 128, "warm-up slots");
    unsigned dmy = 0;
    if (q < QC) {
      const char* base; int b0;
      if (q < Q1) { base = wsp; b0 = 0; }
      else if (q < Q2) { base = (const char*)ws_re; b0 = Q1; }
      else if (q < Q3) { base = (const char*)ws_im; b0 = Q2; }
      else if (q < Q4) { base = (const char*)w_gate; b0 = Q3; }
      else if (q < Q5) { base = (const char*)w_up; b0 = Q4; }
      else if (q < Q6) { base = (const char*)w_down; b0 = Q5; }
      else if (q < Q7) { base = (const char*)out; b0 = Q6; }
      else if (q < Q8) { base = (const char*)x; b0 = Q7; }
      else { base = (const char*)w_r; b0 = Q8; }
      base += (size_t)(q - b0) << 20;
      asm volatile("s_load_dword %0, %1, 0" : "+s"(dmy) : "s"(base) : "memory");
    }
    {
      const int w = g * 4 + wid;
      constexpr int R = (int)(WS_FLAGS - WS_DN);
      static_assert(FL_X == 0 && FL_ST == 6144 * 32 && FL_EPOCH == 6528 * 32, "");
      const int o = lane < 6 ? R + (lane * 1024 + w) * 128 : lane > 6 || w >= 896 ? (int)0x80000000 : w < 512 ? w * 1024 + 896 : R + (6144 - 512 + w) * 128;
      asm volatile("s_mov_b32 m0, %0\n\ts_nop 0\n\tbuffer_load_dword %1, %2, 0 offen sc1 lds"
                   :: "s"(lds_addr(smem + LDS_DUMMY) + wid * 1024 + 512), "v"(o), "s"(mk_rsrc(wsp + WS_DN)) : "memory");
    }
    WAIT_VM(1);
    if (wid == 0) { CALL_PG(1); CALL_PG(8); CALL_PG(5); }
    else if (wid == 1) { CALL_PG(2); CALL_PG(9); CALL_PG(6); }
    else if (wid == 2) { CALL_PG(3); CALL_PG(10); CALL_PG(7); }
    else { CALL_PG(4); CALL_PG(11); }
    asm volatile("s_barrier\n\ts_waitcnt lgkmcnt(0)" : "+s"(dmy) :: "memory");
  }
  PAGE(8);
  epi_setup();
  const int wr = wid >> 1, wc = wid & 1;
  const int sw = (fr >> 1) & 7;
  const int ko0 = ((0 + fq) ^ sw) * 16, ko1 = ((4 + fq) ^ sw) * 16;
  const int mrow = (fr >> 2) * 8 + (fr & 3);
  bf16x8 fm[2][4], fn[2][4];
  {
    const char* pm = smem + (wr * 64 + mrow) * 128;
    const char* pn = smem + 16384 + (wc * 64 + mrow) * 128;
    f32x4 acc[4][4];
#pragma unroll
    for (int i = 0; i < 4; ++i)
#pragma unroll
      for (int jn = 0; jn < 4; ++jn)
#pragma unroll
        for (int q = 0; q < 4; ++q) acc[i][jn][q] = 0.f;
#define TO(i) (((i) >> 1) * 4096 + ((i) & 1) * 512)
#define LDSP(T, a) ((__attribute__((address_space(3))) T*)(size_t)(a))
#define SLOTS(n) _Pragma("unroll") for (int q = 0; q < (n); ++q)
#define MFA(c, a, b) asm volatile("v_mfma_f32_16x16x32_bf16 %0, %1, %2, %0" : "+v"(c) : "v"(a), "v"(b))
#define MFA2(c, a, b, a2, b2) asm volatile("v_mfma_f32_16x16x32_bf16 %0, %1, %2, %0\n\tv_mfma_f32_16x16x32_bf16 %0, %3, %4, %0" : "+v"(c) : "v"(a), "v"(b), "v"(a2), "v"(b2))
    unsigned lm[2][2], ln[2][2];
#pragma unroll
    for (int u = 0; u < 2; ++u)
#pragma unroll
      for (int kk = 0; kk < 2; ++kk) {
        lm[u][kk] = lds_addr(pm) + u * 2 * G1_STAGE + (kk ? ko1 : ko0);
        ln[u][kk] = lds_addr(pn) + u * 2 * G1_STAGE + (kk ? ko1 : ko0);
        asm volatile("" : "+v"(lm[u][kk]), "+v"(ln[u][kk]));
      }
    auto ldm = [&](int stg, int i, int kk) __attribute__((always_inline)) { fm[kk][i] = *LDSP(const bf16x8, lm[stg >> 1][kk] + (stg & 1) * G1_STAGE + TO(i)); };
    auto ldn = [&](int stg, int jn, int kk) __attribute__((always_inline)) { fn[kk][jn] = *LDSP(const bf16x8, ln[stg >> 1][kk] + (stg & 1) * G1_STAGE + TO(jn)); };
    auto quad = [&](int a, int c, int d, int s) __attribute__((always_inline)) {
      const int i = (s < 2 || s >= 6) ? a : a + 1, jn = s < 4 ? c : d, kk = (s == 1 || s == 2 || s == 5 || s == 6);
      MFA(acc[i][jn], fm[kk][i], fn[kk][jn]);
    };
    auto quad2 = [&](int a, int c, int d, int s) __attribute__((always_inline)) {
      const int i = (s < 2 || s >= 6) ? a : a + 1, jn = s < 4 ? c : d, kk = (s == 2 || s == 6);
      MFA2(acc[i][jn], fm[kk][i], fn[kk][jn], fm[1 - kk][i], fn[1 - kk][jn]);
    };
    auto half1 = [&](int stg) __attribute__((always_inline)) {
      const int ja = (stg & 1) * 2, jb = 2 - ja;
      SLOTS(8) {
        quad(0, ja, ja + 1, q);
        if (q < 4) ldn(stg, jb + (q >> 1), q & 1); else ldm(stg, 2 + ((q - 4) >> 1), q == 5 || q == 6);
        SB();
      }
      SLOTS(4) { quad2(0, jb, jb + 1, 2 * q); SB(); }
    };
    auto half2 = [&](int stg) __attribute__((always_inline)) {
      const int ja = (stg & 1) * 2, jb = 2 - ja, nx = (stg + 1) & 3;
      SLOTS(8) {
        quad(2, jb + 1, jb, q);
        if (!(q & 1)) ldm(nx, q >> 2, q == 2 || q == 4);
        SB();
      }
      SLOTS(8) {
        quad(2, ja, ja + 1, q);
        if (!(q & 1)) ldn(nx, jb + (q >> 2), (q >> 1) & 1);
        SB();
      }
    };
    SLOTS(16) asm volatile("" : "+v"(acc[q >> 2][q & 3]));
    SB();
    SLOTS(4) { ldm(0, q >> 1, q & 1); ldn(0, q >> 1, q & 1); }
    SB();
#pragma unroll 1
    for (int k4 = 0; k4 < G1_NKT - 4; k4 += 4) {
      half1(0); BAR_LGKM();
      SB(); half2(0);
      half1(1); BAR_LGKM();
      SB(); half2(1);
      half1(2); BAR_LGKM();
      SB(); half2(2);
      half1(3); BAR_LGKM();
      SB(); half2(3);
    }
    bf16x8 gm[2][4], gn[2][8];
    unsigned am[2][2], an[2][2];
#pragma unroll
    for (int u = 0; u < 2; ++u)
#pragma unroll
      for (int kk = 0; kk < 2; ++kk) {
        am[u][kk] = lds_addr(pm) + (u ? 3 : 1) * G1_STAGE + (kk ? ko1 : ko0);
        an[u][kk] = lds_addr(pn) + (u ? 3 : 1) * G1_STAGE + (kk ? ko1 : ko0);
        asm volatile("" : "+v"(am[u][kk]), "+v"(an[u][kk]));
      }
    unsigned al = lds_addr(smem + LDS_MB) + (wr * 32 + fq * 8) * 4, ac = lds_addr(smem + LDS_MB) + (256 + wc * 64 + mrow) * 4;
    unsigned ae = lds_addr(smem + G2_MRING) + (wc * 64 + mrow) * 128 + (((wr * 4 + fq) ^ sw) * 16);
    asm volatile("" : "+v"(al), "+v"(ac), "+v"(ae));
    auto rdg = [&](int set, int t, int h, int r) __attribute__((always_inline)) {
      const int u = t == 15, o = t == 14 ? G1_STAGE : 0;
      if (r == 0 || r == 2 || r == 4 || r == 5) {
        const int i = r >= 4, kk = r == 2 || r == 4;
        gm[set][kk * 2 + i] = *LDSP(const bf16x8, am[u][kk] + o + h * 4096 + i * 512);
      } else {
        const int jn = r < 4 ? 0 : (r - 4) >> 1, kk = r < 4 ? r >> 1 : r & 1;
        gn[set][kk * 4 + jn] = *LDSP(const bf16x8, an[u][kk] + o + TO(jn));
      }
    };
    auto mmg = [&](int set, int h, int q) __attribute__((always_inline)) {
      const int jn = q >> 2, r = q & 3, i = (r >> 1) ^ (jn & 1), kk = r == 1 || r == 2;
      MFA(acc[h * 2 + i][jn], gm[set][kk * 2 + i], gn[set][kk * 4 + jn]);
    };
    f32x4 rsv[2], mrv[2];
    float cev[4], tc[32];
    auto rds = [&](int h, int r) __attribute__((always_inline)) {
      if (r < 2) mrv[r] = *LDSP(const f32x4, al + h * 256 + r * 16); else rsv[r - 2] = *LDSP(const f32x4, al + 512 + h * 256 + (r - 2) * 16);
    };
    auto prep = [&](int n) __attribute__((always_inline)) {
      tc[n] = llvm_fmul_legacy(mrv[(n >> 2) & 1][n & 3], cev[n >> 3]);
      asm volatile("" : "+v"(tc[n]));
    };
    u32x4 ep;
    auto epi = [&](int h, int q) __attribute__((always_inline)) {
      const int jn = q >> 1, i = q & 1;
      asm volatile("v_fmac_f32 %0, %4, %8\n\tv_fmac_f32 %1, %5, %9\n\tv_fmac_f32 %2, %6, %10\n\tv_fmac_f32 %3, %7, %11"
                   : "+v"(tc[q * 4]), "+v"(tc[q * 4 + 1]), "+v"(tc[q * 4 + 2]), "+v"(tc[q * 4 + 3])
                   : "v"(acc[h * 2 + i][jn][0]), "v"(acc[h * 2 + i][jn][1]), "v"(acc[h * 2 + i][jn][2]), "v"(acc[h * 2 + i][jn][3]),
                     "v"(rsv[i][0]), "v"(rsv[i][1]), "v"(rsv[i][2]), "v"(rsv[i][3]));
      ep[i * 2] = pk2(tc[q * 4], tc[q * 4 + 1]); ep[i * 2 + 1] = pk2(tc[q * 4 + 2], tc[q * 4 + 3]);
      if (i) *LDSP(u32x4, ae + m_off(h) + TO(jn)) = ep;
    };
    SB();
    half1(0);
    BAR_LGKM();
    SB();
    SLOTS(16) {
      if (q < 8) quad(2, 3, 2, q); else quad(2, 0, 1, q - 8);
      if (q < 12) rdg(0, 13, 0, q);
      SB();
    }
    BAR();
    SB();
    SLOTS(16) {
      mmg(0, 0, q);
      if (q < 12) rdg(1, 14, 0, q);
      else { rds(0, q - 12); cev[q - 12] = *LDSP(const float, ac + (((q - 12) >> 1) * 32 + ((q - 12) & 1) * 4) * 4); }
      SB();
    }
    PAGE(9);
    BAR();
    SB();
    SLOTS(16) { mmg(1, 0, q); if ((q & 3) < 3) rdg(0, 15, 0, (q >> 2) * 3 + (q & 3)); if (q >= 8) { prep(2 * q - 16); prep(2 * q - 15); } SB(); }
    SLOTS(16) { mmg(0, 0, q); if ((q & 3) < 3) rdg(1, 13, 1, (q >> 2) * 3 + (q & 3)); prep(16 + q); SB(); if (q >= 9 && (q & 1)) { epi(0, (q - 9) >> 1); SB(); } }
    SLOTS(8) { mmg(1, 1, q); if (q < 6) { rdg(0, 14, 1, 2 * q); rdg(0, 14, 1, 2 * q + 1); } SB(); if (q & 1) { epi(0, 4 + (q >> 1)); SB(); } }
    BAR_LGKM();
    SB();
    SLOTS(8) { mmg(1, 1, 8 + q); if (q < 4) rdg(1, 15, 1, q == 0 ? 1 : q == 1 ? 3 : q + 4); else rds(1, q - 4); SB(); }
    SLOTS(16) { mmg(0, 1, q); if (q < 8) rdg(1, 15, 1, q == 0 ? 0 : q == 1 ? 2 : q < 4 ? q + 2 : q + 4); prep(q); SB(); }
    asm volatile("s_waitcnt lgkmcnt(0)" ::: "memory");
    if (lane == 0) asm volatile("ds_add_u32 %0, %1" :: "v"(lds_addr(smem + LDS_SEL + 12)), "v"(1) : "memory");
    SB();
    SLOTS(16) {
      mmg(1, 1, q);
      if (q < 4) fn[0][q] = *(const bf16x8*)(smem + (wr * 128 + fr) * 128 + (q >> 1) * 2048 + ((q & 1) ? ko1 : ko0));
      else if (q < 8) fm[q & 1][(q - 4) >> 1] = *(const bf16x8*)(smem + G2_MRING + m_off(0) + (wc * 64 + mrow) * 128 + TO((q - 4) >> 1) + ((q & 1) ? ko1 : ko0));
      prep(16 + q);
      SB();
    }
    {
      lds_vi rel = (lds_vi)(smem + LDS_SEL + 12);
      int it = 0;
      while (rel[0] != 4 && it < SPIN_MAX * 8) ++it;
      asm volatile("" ::: "memory");
    }
    asm volatile("s_nop 7");
    SB();
    SLOTS(8) { epi(1, q); SB(); }
  }

  const char* pn = smem + (wr * 128 + fr) * 128;
  const char* pm = smem + G2_MRING + (wc * 64 + mrow) * 128;
  f32x4 acc[8][4];
  auto mm = [&](bool imaj, int set, int q2, int s, int mode) __attribute__((always_inline)) {
    int i, jj, kk;
    if (imaj) { i = s >> 2; const int r = s & 3; jj = (r >> 1) ^ (i & 1); kk = (r == 1 || r == 2); }
    else { jj = s >> 3; const int r = s & 7; i = jj ? 3 - (r >> 1) : (r >> 1); kk = ((r + 1) >> 1) & 1; }
    if (mode == 1 && !(s & 1)) asm volatile("v_mfma_f32_16x16x32_bf16 %0, %1, %2, 0" : "=&v"(acc[q2 * 2 + jj][i]) : "v"(fm[kk][i]), "v"(fn[set][jj * 2 + kk]));
    else if (mode) asm volatile("v_mfma_f32_16x16x32_bf16 %0, %1, %2, %0" : "+v"(acc[q2 * 2 + jj][i]) : "v"(fm[kk][i]), "v"(fn[set][jj * 2 + kk]));
    else acc[q2 * 2 + jj][i] = __builtin_amdgcn_mfma_f32_16x16x32_bf16(fm[kk][i], fn[set][jj * 2 + kk], acc[q2 * 2 + jj][i], 0, 0, 0);
  };
  unsigned bn[2], bm[2], an[2], am[2];
#pragma unroll
  for (int kk = 0; kk < 2; ++kk) {
    bn[kk] = lds_addr(pn) + (kk ? ko1 : ko0); bm[kk] = lds_addr(pm) + (kk ? ko1 : ko0);
    asm volatile("" : "+v"(bn[kk]), "+v"(bm[kk]));
    an[kk] = bn[kk]; am[kk] = bm[kk] + m_off(0);
  }
#define RDN(jt, kk, o) (*LDSP(const bf16x8, an[kk] + (o) + (jt) * 2048))
#define RDM(i, kk, o) (*LDSP(const bf16x8, am[kk] + (o) + TO(i)))
  PAGE(10);
  tag = __builtin_amdgcn_readfirstlane(((const unsigned*)(smem + LDS_SEL))[5]);
  {
    int son = 0;
    auto tile = [&](int kt, int zero) __attribute__((always_inline)) {
      SLOTS(16) {
        mm(true, 0, 0, q, zero);
        if (!(q & 1)) {
          if (q < 8) fm[(q >> 1) & 1][2 + (q >> 2)] = RDM(2 + (q >> 2), (q >> 1) & 1, 0);
          else { const int r = (q - 8) >> 1; fn[1][r] = RDN(2 + (r >> 1), r & 1, 0); }
        }
        SB();
      }
      SLOTS(16) {
        mm(false, 1, 1, q, zero);
        if (!(q & 1) && q < 8) fn[0][q >> 1] = RDN(4 + (q >> 2), (q >> 1) & 1, 0);
        SB();
      }
      const int som = son >> 1;
      son = son == 65536 ? 0 : son + 32768;
      SLOTS(16) {
        mm(false, 0, 2, q, zero);
        if (!(q & 1) && q < 8) fn[1][q >> 1] = RDN(6 + (q >> 2), (q >> 1) & 1, 0);
        if (q == 9 || q == 11) { an[q == 11] = bn[q == 11] + son; asm volatile("" : "+v"(an[q == 11])); }
        if (q == 13 || q == 15) { am[q == 15] = bm[q == 15] + som; asm volatile("" : "+v"(am[q == 15])); }
        SB();
      }
      BAR_LGKM();
      SB();
      SLOTS(16) {
        mm(true, 1, 3, q, zero);
        if (!(q & 1)) {
          if (q < 8) fn[0][q >> 1] = RDN(q >> 2, (q >> 1) & 1, 0);
          else fm[(q >> 1) & 1][(q - 8) >> 2] = RDM((q - 8) >> 2, (q >> 1) & 1, 0);
        }
        SB();
      }
    };
    SB();
    tile(0, 1);
#pragma unroll 1
    for (int kt = 1; kt < G2_NKT - 3; ++kt) tile(kt, 0);
  }
  {
    constexpr int NG = 12;
    static constexpr int g_t[NG] = {0, 0, 1, 1, 2, 2, 2, 1, 0, 0, 1, 2};
    static constexpr int g_q[NG] = {0, 1, 0, 1, 0, 1, 2, 2, 2, 3, 3, 3};
    constexpr int G14 = 1, G15 = 3, GP0 = 5, GP1 = 6;
    constexpr auto chp = [](int g) { return g == 0 || g_t[g] != g_t[g - 1]; };
    constexpr auto chn = [](int g) { return g + 1 < NG && g_t[g + 1] != g_t[g]; };
    static_assert(n_off(13) == 32768 && n_off(14) == 65536 && n_off(15) == 0, "");
    unsigned an2[2];
#pragma unroll
    for (int kk = 0; kk < 2; ++kk) {
      an[kk] = bn[kk]; an2[kk] = bn[kk] + 65536; am[kk] = bm[kk];
      asm volatile("" : "+v"(an[kk]), "+v"(an2[kk]), "+v"(am[kk]));
    }
    int vo;
    u32x4 pk;
    u32x2 kv;
    constexpr int NF0 = 152, F0 = 160, F1 = 172, F2 = 188;
    auto publish = [&](int d, int h) __attribute__((always_inline)) {
      if (h == 0) { vo = lane; PINV(vo); vo = vo * 16 + (((d + kq + 1) & 3) * 4 + kq) * 4 * MSG; PINV(vo); }
      const f32x4 v = acc[2 * d + (h >> 2)][h & 3];
      pk[(h & 1) * 2] = pk2(v[0], v[1]); pk[(h & 1) * 2 + 1] = pk2(v[2], v[3]);
      if (h & 1) __builtin_amdgcn_raw_buffer_store_b128(pk, rs, vo + (h >> 1) * 1024, 0, 16);
    };
    int fo = lane == 0 ? kq * 512 : (int)0x80000000;
#define MSG_FLAG(d) __builtin_amdgcn_raw_buffer_store_b32(tag, rc, fo + (((d) + kq + 1) & 3) * 2048, 0, 16)
    auto rdn = [&](int g, int r) __attribute__((always_inline)) {
      const int t = g_t[g], jt = g_q[g] * 2 + (r >> 1);
      fn[g & 1][r] = *LDSP(const bf16x8, (t == 1 ? an2[r & 1] : an[r & 1]) + (t == 0 ? 32768 : 0) + jt * 2048);
    };
    auto rdm = [&](int t, int i, int kk) __attribute__((always_inline)) { fm[kk][i] = *LDSP(const bf16x8, am[kk] + m_off(13 + t) + TO(i)); };
    auto group = [&](auto GI) __attribute__((always_inline)) {
      constexpr int gi = decltype(GI)::value;
      if (gi == G14) { BAR();
        SB(); }
      if (gi == G15) {
        PAGE(11);
        BAR();
        SB();
        xq[0] = ld16_s<0>(x, xo); xq[1] = ld16_s<64>(x, xo);
        sq = ld8_s_sc1(stat, xo >> 11 << 3);
        kv = *(const u32x2*)(smem + LDS_SEL);
        SB();
      }
#pragma unroll
      for (int q = 0; q < 16; ++q) {
        const int n = gi * 16 + q;
        if (n == NF0) {
          wait_xq<12>(xq, sq);
          key[0] = __builtin_amdgcn_readfirstlane(kv.x); key[1] = __builtin_amdgcn_readfirstlane(kv.y);
          nf_pre();
          SB();
        }
        if (n == 112) {
        }
        if (n == 152) {
        }
        if (n == F0) {
          WAIT_VM(8); MSG_FLAG(0); SB(); }
        if (n == F1) { WAIT_VM(5); MSG_FLAG(1); SB(); }
        if (n == F2) {
          WAIT_VM(2);
          MSG_FLAG(2); fx = bld4_sc1(rc, po); SB(); }
        mm(chp(gi) || chn(gi), gi & 1, g_q[gi], q, 2);
        constexpr bool ni = gi + 1 < NG && (chp(gi + 1 < NG ? gi + 1 : gi) || chn(gi + 1 < NG ? gi + 1 : gi));
        auto nr = [](int r) { return ni && r >= 2 ? 5 - r : r; };
        if constexpr (chp(gi) && chn(gi)) {
          if (q < 4) rdm(g_t[gi], 2 + (q >> 1), q & 1); else if (q < 8) rdn(gi + 1, nr(q - 4)); else if (q < 12) rdm(g_t[gi + 1], (q - 8) >> 1, q & 1);
        } else if constexpr (chp(gi)) {
          if (q < 4) rdm(g_t[gi], 2 + (q >> 1), q & 1);
          else if (gi + 1 < NG && q < 12 && !(q & 1)) rdn(gi + 1, nr((q - 4) >> 1));
        } else if constexpr (chn(gi)) {
          if (!(q & 1)) { if (q < 8) rdn(gi + 1, nr(q >> 1)); else rdm(g_t[gi + 1], (q - 8) >> 2, (q >> 1) & 1); }
        } else {
          if (!(q & 1) && q < 8) rdn(gi + 1, nr(q >> 1));
        }
        if (gi == GP0 && q < 8) publish(0, q);
        if (gi == GP1 && q < 8) publish(1, q);
        if (gi == 9 && q < 8) publish(2, q);
        if (n >= NF0 && n < NF0 + 16) nf_step(n - NF0);
        SB();
      }
    };
#define GR4(i) group(IC<i>{}); group(IC<i + 1>{}); group(IC<i + 2>{}); group(IC<i + 3>{});
    GR4(0) GR4(4) GR4(8)
    asm volatile("s_nop 7");
    SB();
  }
#pragma unroll
  for (int i = 0; i < 4; ++i) *(f32x4*)(smem + LDS_ACC + wr * 16384 + wc * 4096 + i * 1024 + lane * 16) = acc[7][i];
#pragma unroll
  for (int i = 0; i < 4; ++i) ya[i] = acc[6][i];
  asm volatile("s_waitcnt lgkmcnt(0)" ::: "memory");
  if (lane == 0) sig[0] = 2;
  }

  PAGE(6);
  const unsigned rel0 = 0xffff - (key[0] & 0xffffu) - trow, rel1 = 0xffff - (key[1] & 0xffffu) - trow;
  if (min(rel0, rel1) < 16) {
#pragma unroll 1
    for (int c = 0; c < 2; ++c) {
      const unsigned rel = c ? rel1 : rel0;
      if (rel >= 16) continue;
      lds_vi cnt = (lds_vi)(smem + LDS_SEL + 24 + c * 4);
      int it = 0;
      while (cnt[0] != 4 && it < SPIN_MAX * 8) { __builtin_amdgcn_s_sleep(1); ++it; }
      asm volatile("" ::: "memory");
      if (rel == fr) {
        const float* fp = (const float*)(smem + LDS_FIN + c * 512) + (wid & 1) * 64 + fq * 8;
#pragma unroll
        for (int u4 = 0; u4 < 4; ++u4) {
          const f32x4 v = *(const f32x4*)(fp + (u4 >> 1) * 32 + (u4 & 1) * 4);
#pragma unroll
          for (int k = 0; k < 4; ++k) nf[u4 * 4 + k] += v[k];
        }
      }
    }
  }
  u32x4 pp[3][2];
  unsigned e_lo = 0x00003f80u, e_hi = 0x3f800000u;
  asm volatile("" : "+v"(e_lo), "+v"(e_hi));
  __amdgpu_buffer_rsrc_t rs_ = rs, rc_ = rc, ro_ = mk_rsrc(out);
  int mo[3] = {MSG_IDX(0) * 4 * MSG, MSG_IDX(1) * 4 * MSG, MSG_IDX(2) * 4 * MSG}, mv = lane * 16 + ha * 2048, mv2 = mv + 1024;
  asm volatile("" : "+s"(rs_), "+s"(rc_), "+s"(ro_), "+s"(mo[0]), "+s"(mo[1]), "+s"(mo[2]));
  unsigned as_ = lds_addr((const void*)smem) + LDS_SIG + (wid & 3) * 4, aa_ = lds_addr(smem + LDS_ACC) + (wid & 2) * 8192 + (wid & 1) * 4096 + lane * 16;
  int xs = ((trow + (lane >> 3)) * D_ + et * 128 + (wid & 1) * 64) * 2 + (lane & 7) * 16, xs2 = xs + 8 * D_ * 2;
  const unsigned mt = lds_addr(smem) + (wid & 2) * 8192 + (ha ? LDS_ACC + (wid & 1) * 4096 : n_off(14) + (wid & 1) * 2048);
  unsigned aw_ = mt + fr * 128 + ((fq ^ (fr & 7)) * 16), aw2 = aw_ ^ 64, ar_ = mt + (lane >> 3) * 128 + (((lane ^ (lane >> 3)) & 7) * 16);
  asm volatile("" : "+v"(mv), "+v"(mv2), "+v"(as_), "+v"(aa_), "+v"(xs), "+v"(xs2), "+v"(aw_), "+v"(aw2), "+v"(ar_));
  {
    unsigned sv_, it_ = SPIN_MAX * 8;
    asm volatile("s_cmp_eq_u32 %9, 0\n\ts_cbranch_scc1 2f\n"
                 "1:\n\tds_read_b32 %0, %10\n\ts_waitcnt lgkmcnt(0)\n\tv_cmp_eq_u32 vcc, 2, %0\n\ts_cbranch_vccnz 3f\n\ts_sleep 1\n\ts_sub_u32 %1, %1, 1\n\ts_cbranch_scc0 1b\n"
                 "3:\n\tds_read_b128 %4, %11\n\tds_read_b128 %5, %11 offset:1024\n\tds_read_b128 %6, %11 offset:2048\n\tds_read_b128 %7, %11 offset:3072\n"
                 "2:\n\ts_waitcnt vmcnt(0)\n\tv_cmp_eq_u32 vcc, %8, %3\n\ts_mov_b32 %2, vcc_lo"
                 : "=&v"(sv_), "+s"(it_), "=&s"(seen), "+v"(fx), "+v"(ya[0]), "+v"(ya[1]), "+v"(ya[2]), "+v"(ya[3])
                 : "s"(tag), "s"(ha), "v"(as_), "v"(aa_) : "vcc", "scc", "memory");
  }
#define MSG_POLL(m) do { \
    if (!((seen >> (m)) & 1)) { \
      unsigned pv_, it_ = SPIN_MAX; \
      asm volatile("1:\n\tbuffer_load_dword %0, %3, %4, 0 offen sc1\n\ts_waitcnt vmcnt(0)\n\tv_cmp_eq_u32 vcc, %5, %0\n\ts_mov_b32 %1, vcc_lo\n\t" \
                   "s_bitcmp1_b32 %1, %6\n\ts_cbranch_scc1 2f\n\ts_sub_u32 %2, %2, 1\n\ts_cbranch_scc1 2f\n\ts_sleep 1\n\ts_branch 1b\n2:" \
                   : "=&v"(pv_), "+s"(seen), "+s"(it_) : "v"(po), "s"(rc_), "s"(tag), "i"(m) : "vcc", "scc", "memory"); \
    } \
    pp[m][0] = __builtin_amdgcn_raw_buffer_load_b128(rs_, mv, mo[m], 16); \
    pp[m][1] = __builtin_amdgcn_raw_buffer_load_b128(rs_, mv2, mo[m], 16); } while (0)
#define MSG_ADD(m) do { \
    _Pragma("unroll") for (int u = 0; u < 8; ++u) { \
      ya[u >> 1][(u & 1) * 2] = add_bf(pp[m][u >> 2][u & 3], e_lo, ya[u >> 1][(u & 1) * 2]); ya[u >> 1][(u & 1) * 2 + 1] = add_bf(pp[m][u >> 2][u & 3], e_hi, ya[u >> 1][(u & 1) * 2 + 1]); } } while (0)
  MSG_POLL(2); MSG_POLL(1); MSG_POLL(0);
  asm volatile("s_waitcnt lgkmcnt(0)" : "+v"(ya[0]), "+v"(ya[1]), "+v"(ya[2]), "+v"(ya[3]) :: "memory");
  SB();
#pragma unroll
  for (int e = 0; e < 16; ++e) { float t = ya[e >> 2][e & 3] + nf[e]; PINV(t); ya[e >> 2][e & 3] = t; }
  SB();
  MSG_ADD(2); MSG_ADD(1);
  asm volatile("" : "+v"(ya[0]), "+v"(ya[1]), "+v"(ya[2]), "+v"(ya[3]));
  SB();
  MSG_ADD(0);
  {
    unsigned ov[8];
    SB();
#pragma unroll
    for (int u = 0; u < 8; ++u) ov[u] = pk2(ya[u >> 1][(u & 1) * 2], ya[u >> 1][(u & 1) * 2 + 1]);
    SB();
    *LDSP(u32x4, aw_) = (u32x4){ov[0], ov[1], ov[2], ov[3]};
    *LDSP(u32x4, aw2) = (u32x4){ov[4], ov[5], ov[6], ov[7]};
    asm volatile("" ::: "memory");
    __builtin_amdgcn_raw_buffer_store_b128(*LDSP(const u32x4, ar_), ro_, xs, 0, 16);
    __builtin_amdgcn_raw_buffer_store_b128(*LDSP(const u32x4, ar_ + 1024), ro_, xs2, 0, 16);
  }
}

extern "C" void kernel_launch(void* const* d_in, const int* in_sizes, int n_in, void* d_out, int out_size,
                              void* d_ws, size_t ws_size, hipStream_t stream) {
  const u16* x = (const u16*)d_in[0];
  const u16* ws_re = (const u16*)d_in[1];
  const u16* ws_im = (const u16*)d_in[2];
  const u16* wd_re = (const u16*)d_in[3];
  const u16* wd_im = (const u16*)d_in[4];
  const u16* w_r = (const u16*)d_in[6];
  const u16* w_gate = (const u16*)d_in[7];
  const u16* w_up = (const u16*)d_in[8];
  const u16* w_down = (const u16*)d_in[9];
  k_main<<<256, 512, 0, stream>>>(x, 0x7fffffffu, 0x00020000u, wd_re, 0x7fffffffu, 0x00020000u, wd_im, 0x7fffffffu, 0x00020000u, w_r, (char*)d_ws,
                                  ws_re, ws_im, w_gate, w_up, w_down, (u16*)d_out);
}
```

```cpp
#include <hip/hip_runtime.h>
#include <hip/hip_bf16.h>
#include <stdint.h>
#include <type_traits>

extern "C" __device__ unsigned long long llvm_dispatch_id(void) __asm("llvm.amdgcn.dispatch.id");
extern "C" __device__ float llvm_fmul_legacy(float, float) __asm("llvm.amdgcn.fmul.legacy");
typedef unsigned short u16;
typedef unsigned long long u64;
typedef __attribute__((ext_vector_type(8))) short bf16x8;
typedef __attribute__((ext_vector_type(4))) unsigned u32x4;
typedef __attribute__((ext_vector_type(2))) unsigned u32x2;
typedef __attribute__((ext_vector_type(4))) float f32x4;
typedef __attribute__((ext_vector_type(2))) float f32x2;
typedef __attribute__((ext_vector_type(2))) __bf16 bf16x2;
typedef __attribute__((address_space(3))) void* lds_vp;
typedef volatile __attribute__((address_space(3))) int* lds_vi;

constexpr int S_ = 2048, D_ = 1024, F_ = 3584;
template <int N> using IC = std::integral_constant<int, N>;

static __device__ __forceinline__ float bf2f(u16 u) { return __uint_as_float((unsigned)u << 16); }
static __device__ __forceinline__ u16 f2bf(float f) {
  __hip_bfloat16 b = __float2bfloat16(f);
  return *reinterpret_cast<u16*>(&b);
}
static __device__ __forceinline__ float rbf(float f) { return bf2f(f2bf(f)); }
static __device__ __forceinline__ float lo_bf(unsigned u) { return __uint_as_float(u << 16); }
static __device__ __forceinline__ float hi_bf(unsigned u) { return __uint_as_float(u & 0xffff0000u); }
static __device__ __forceinline__ unsigned pk2(float a, float b) {
  const bf16x2 v = __builtin_convertvector((f32x2){a, b}, bf16x2);
  return __builtin_bit_cast(unsigned, v);
}
static __device__ __forceinline__ float add_bf(unsigned u, unsigned e, float c) {
  return __builtin_amdgcn_fdot2_f32_bf16(__builtin_bit_cast(bf16x2, u), __builtin_bit_cast(bf16x2, e), c, false);
}
static __device__ __forceinline__ float ln_elem(float x, float mb, float sd, float ri) {
  const float d = rbf(x - mb);
  const float q0 = d * ri;
  return rbf(__builtin_fmaf(__builtin_fmaf(-q0, sd, d), ri, q0));
}
template <int CTRL> static __device__ __forceinline__ float dpp_f(float v) {
  return __int_as_float(__builtin_amdgcn_update_dpp(0, __float_as_int(v), CTRL, 0xf, 0xf, true));
}
template <int CTRL> static __device__ __forceinline__ unsigned dpp_u(unsigned v) {
  return (unsigned)__builtin_amdgcn_update_dpp(0, (int)v, CTRL, 0xf, 0xf, true);
}
static __device__ __forceinline__ float row_sum(float v) {
  v += dpp_f<0xB1>(v);
  v += dpp_f<0x4E>(v);
  v += dpp_f<0x141>(v);
  v += dpp_f<0x140>(v);
  return v;
}
static __device__ __forceinline__ float fold32(float a, float b) {
  const auto r = __builtin_amdgcn_permlane32_swap(__float_as_uint(a), __float_as_uint(b), false, false);
  return __uint_as_float(r[0]) + __uint_as_float(r[1]);
}
static __device__ __forceinline__ float fold16(float a, float b) {
  const auto r = __builtin_amdgcn_permlane16_swap(__float_as_uint(a), __float_as_uint(b), false, false);
  return __uint_as_float(r[0]) + __uint_as_float(r[1]);
}
static __device__ __forceinline__ float rdl(float v, int l) { return __int_as_float(__builtin_amdgcn_readlane(__float_as_int(v), l)); }

#define ST_JB 9
#define WSN 10
#define FSM 0xa2
#define GV_JB 7
#define DG 11
#define B14P 9
#define AB_STRIDE 32
#define SPIN_MAX (1 << 17)

static __device__ __forceinline__ unsigned lds_addr(const void* p) { return (unsigned)(size_t)(lds_vp)p; }

constexpr int MSG = 4096;
constexpr size_t WS_ABT   = 0;
constexpr size_t WS_SLAB  = WS_ABT + (size_t)D_ * 4096 * 2;
constexpr size_t WS_GU    = WS_SLAB + (size_t)64 * 64 * MSG;
constexpr size_t WS_DN    = WS_GU + (size_t)8 * F_ * 16;
constexpr size_t WS_FLAGS = WS_DN + (size_t)2 * 8 * 32 * 64 * 16;
constexpr int FL_X = 0;
constexpr int FL_AB = 64 * 64 * 32;
constexpr int FL_ST = FL_AB + 2048 * AB_STRIDE;
constexpr int FL_CE = FL_ST + 4096;
constexpr int FL_KEY = FL_CE + 4096;
constexpr int FL_EPOCH = FL_KEY + 4096;
constexpr int WS_MB = (int)((WS_FLAGS + (FL_EPOCH + 1) * 4 + 1048575) >> 20);

#define WAIT_VM(n) asm volatile("s_waitcnt vmcnt(" #n ")" ::: "memory")
template <int N> static __device__ __forceinline__ void wait_vm() { asm volatile("s_waitcnt vmcnt(%0)" :: "i"(N) : "memory"); }
#define BAR_LGKM() asm volatile("s_waitcnt lgkmcnt(0)\n\ts_barrier" ::: "memory")
#define BAR() asm volatile("s_barrier" ::: "memory")
#define SB() __builtin_amdgcn_sched_barrier(0)
#define PAGE(n) asm volatile("s_branch 2f\n\t.p2align 12\nk_pg" #n ":\n\ts_setpc_b64 s[42:43]\n2:")
#define CALL_PG(n) asm volatile("s_getpc_b64 s[40:41]\n3:\n\ts_mov_b32 s42, k_pg" #n "-3b\n\ts_ashr_i32 s43, s42, 31\n\ts_add_u32 s40, s40, s42\n\ts_addc_u32 s41, s41, s43\n\t" \
                                "s_swappc_b64 s[42:43], s[40:41]" ::: "s40", "s41", "s42", "s43", "scc", "memory")

static __device__ __forceinline__ __amdgpu_buffer_rsrc_t mk_rsrc(const void* p) {
  return __builtin_amdgcn_make_buffer_rsrc((void*)p, 0, 0x7fffffff, 0x00020000);
}
#define DMA_(rsrc, voff, mbase, MI, sbase, SI, pol) do { unsigned _t; \
    asm volatile("s_add_i32 m0, %2, %3\n\ts_add_i32 %0, %5, %6\n\tbuffer_load_dwordx4 %1, %4, %0 offen" pol " lds" \
                 : "=&s"(_t) : "v"(voff), "s"(mbase), "i"(MI), "s"(rsrc), "s"(sbase), "i"(SI) : "memory", "scc"); } while (0)
#define TOUCH(rsrc, voff, mbase, MI, soff) \
    asm volatile("s_add_i32 m0, %1, %2\n\ts_nop 0\n\tbuffer_load_dword %0, %3, %4 offen lds" \
                 :: "v"(voff), "s"(mbase), "i"(MI), "s"(rsrc), "s"(soff) : "memory", "scc")
template <int OFF> static __device__ __forceinline__ u32x4 ld16_s(const void* p, int voff) {
  u32x4 v;
  asm volatile("global_load_dwordx4 %0, %1, %2 offset:%3" : "=v"(v) : "v"(voff), "s"(p), "i"(OFF) : "memory");
  return v;
}
template <int OFF> static __device__ __forceinline__ void sld(unsigned& sink, const void* p, int soff) {
  asm volatile("s_load_dword %0, %1, %2 offset:%3" : "+s"(sink) : "s"(p), "s"(soff), "i"(OFF) : "memory");
}
static __device__ __forceinline__ unsigned gld4_s(const void* p, unsigned voff) {
  unsigned v;
  asm volatile("global_load_dword %0, %1, %2" : "=v"(v) : "v"(voff), "s"(p) : "memory");
  return v;
}
static __device__ __forceinline__ u32x2 bld8_sc1(__amdgpu_buffer_rsrc_t r, unsigned voff) {
  u32x2 v;
  asm volatile("buffer_load_dwordx2 %0, %1, %2, 0 offen sc1" : "=v"(v) : "v"(voff), "s"(r) : "memory");
  return v;
}
template <int N> static __device__ __forceinline__ void wait_xs(unsigned& a, u32x2& b) { asm volatile("s_waitcnt vmcnt(%2)" : "+v"(a), "+v"(b) : "i"(N) : "memory"); }
template <int N> static __device__ __forceinline__ void wait_xq(u32x4 (&a)[2], u32x2& b) { asm volatile("s_waitcnt vmcnt(%3)" : "+v"(a[0]), "+v"(a[1]), "+v"(b) : "i"(N) : "memory"); }
static __device__ __forceinline__ u32x2 ld8_s_sc1(const void* p, int voff) {
  u32x2 v;
  asm volatile("global_load_dwordx2 %0, %1, %2 sc1" : "=v"(v) : "v"(voff), "s"(p) : "memory");
  return v;
}
static __device__ __forceinline__ void lgkm0(unsigned& sink) { asm volatile("s_waitcnt lgkmcnt(0)" : "+s"(sink) :: "memory"); }
template <typename T> static __device__ __forceinline__ void pin4(T& a, T& b, T& c, T& d) { asm volatile("" : "+s"(a), "+s"(b), "+s"(c), "+s"(d)); }
template <typename T, typename U> static __device__ __forceinline__ void pins(T& a, U& b) { asm volatile("" : "+s"(a), "+s"(b)); }
static __device__ __forceinline__ void launder(int& v, int& a, int& b) { asm volatile("" : "+v"(v)); asm volatile("" : "+s"(a), "+s"(b)); }
static __device__ __forceinline__ unsigned bld4_sc1(__amdgpu_buffer_rsrc_t r, int voff) {
  unsigned v;
  asm volatile("buffer_load_dword %0, %1, %2, 0 offen sc1" : "=v"(v) : "v"(voff), "s"(r) : "memory");
  return v;
}
static __device__ __forceinline__ u32x4 bld16_nt(__amdgpu_buffer_rsrc_t r, int voff, int soff) {
  u32x4 v;
  asm volatile("buffer_load_dwordx4 %0, %1, %2, %3 offen nt" : "=v"(v) : "v"(voff), "s"(r), "s"(soff) : "memory");
  return v;
}
static __device__ __forceinline__ void bld16_nt_io(u32x4& v, __amdgpu_buffer_rsrc_t r, int voff, int soff) {
  asm volatile("buffer_load_dwordx4 %0, %1, %2, %3 offen nt" : "+v"(v) : "v"(voff), "s"(r), "s"(soff) : "memory");
}
static __device__ __forceinline__ u32x4 bld16_sc1(__amdgpu_buffer_rsrc_t r, int voff, int soff) {
  u32x4 v;
  asm volatile("buffer_load_dwordx4 %0, %1, %2, %3 offen sc1" : "=v"(v) : "v"(voff), "s"(r), "s"(soff) : "memory");
  return v;
}
static __device__ __forceinline__ u32x2 granule_wait(const u64* p, unsigned tag) {
  u64 v = 0;
#pragma unroll 1
  for (int it = 0; it < SPIN_MAX; ++it) {
    v = __hip_atomic_load(p, __ATOMIC_RELAXED, __HIP_MEMORY_SCOPE_AGENT);
    if (__all((unsigned)(v >> 32) == tag)) break;
    __builtin_amdgcn_s_sleep(2);
  }
  return (u32x2){(unsigned)v, (unsigned)(v >> 32)};
}
static __device__ __forceinline__ float key_score(unsigned key) {
  const unsigned u = key >> 16;
  return bf2f((u16)((u & 0x8000u) ? (u & 0x7fffu) : (~u & 0xffffu)));
}
static __device__ __forceinline__ void merge2(unsigned& a1, unsigned& a2, unsigned b1, unsigned b2) {
  const unsigned lo = min(a1, b1);
  a1 = max(a1, b1);
  a2 = max(lo, max(a2, b2));
}
static __device__ __forceinline__ float sqdev16(const u32x4 (&v)[2], float mb) {
  f32x2 q2 = {0.f, 0.f};
#pragma unroll
  for (int i = 0; i < 8; ++i) {
    const f32x2 e = (f32x2){lo_bf(v[i >> 2][i & 3]), hi_bf(v[i >> 2][i & 3])} - mb;
    q2 += e * e;
  }
  return q2.x + q2.y;
}

constexpr int G1_NST = 4, G1_NKT = 16, G1_STAGE = 32768;
constexpr int G2_NKT = 16, G2_MRING = 98304;
constexpr int n_off(int k) { return (k % 3) * 32768; }
constexpr int m_off(int k) { return ((k + 2) % 3) * 16384; }
constexpr int k_ord(int k) { return k < 2 ? k : (k < 9 ? 2 * (k - 1) : 2 * (k - 8) + 1); }
constexpr int LDS_DUMMY = G2_MRING + 49152;
constexpr int LDS_GU = LDS_DUMMY + 4096;
constexpr int LDS_FIN = LDS_GU + 4096;
constexpr int LDS_MB = LDS_FIN + 4096;
constexpr int LDS_SEL = LDS_MB + 1536;
constexpr int LDS_SIG = LDS_SEL + 64;
constexpr int LDS_KEY = LDS_SIG + 32;
constexpr int LDS_BYTES = LDS_KEY + 128;
constexpr int LDS_ACC = n_off(G2_NKT - 3);

constexpr int tpos[16] = {0, 0, 0, 0, 106, 110, 115, 2, 2, 3, 4, 5, 0, 0, 0, 0};
constexpr int twho[16] = {0, 0, 0, 0, 7, 7, 7, 0, 1, 2, 6, 5, 0, 0, 0, 0};
constexpr int dn_cnt(int q) { return q == 5 ? 3 : q == 8 ? 4 : 0; }
constexpr int dn_first(int q) { return q <= 5 ? 0 : q <= 8 ? 3 : 7; }

static __device__ __forceinline__ void code_touch(unsigned& sink, unsigned off) {
  asm volatile("s_getpc_b64 s[40:41]\n\tk_main_rt:\n\ts_min_u32 %1, %1, k_main_end-k_main_rt-196\n\t"
               "s_load_dword %0, s[40:41], %1 offset:0\n\ts_load_dword %0, s[40:41], %1 offset:64\n\ts_load_dword %0, s[40:41], %1 offset:128\n\ts_load_dword %0, s[40:41], %1 offset:192"
               : "+s"(sink), "+s"(off) :: "s40", "s41", "scc", "memory");
}
static __device__ __forceinline__ void touch1(__amdgpu_buffer_rsrc_t r, int voff, unsigned mb, int soff) { TOUCH(r, voff, mb, LDS_DUMMY, soff); }
typedef const __attribute__((address_space(1))) u16* gptr;
typedef __attribute__((ext_vector_type(8))) u64 u64x8;
extern "C" __global__ __launch_bounds__(512, 2) void k_main(const u16* x, unsigned x2, unsigned x3, const u16* wd_re, unsigned r2, unsigned r3,
                                                 const u16* wd_im, unsigned i2, unsigned i3, const u16* w_r,
                                                 char* wsp, const u16* ws_re, const u16* ws_im,
                                                 const u16* w_gate, const u16* w_up, const u16* w_down, u16* out) {
  __shared__ __attribute__((aligned(16))) char smem[LDS_BYTES];
  u64x8 ka;
  u32x4 rm, rn;
  unsigned kz;
  asm volatile("s_load_dwordx4 %0, %4, 0\n\ts_load_dwordx16 %2, %4, 48\n\ts_andn2_b32 %3, 4, %5\n\ts_lshl_b32 %3, %3, 2\n\ts_load_dwordx4 %1, %4, %3 offset:16"
               : "=&s"(rm), "=&s"(rn), "=&s"(ka), "=&s"(kz) : "s"(__builtin_amdgcn_kernarg_segment_ptr()), "s"(blockIdx.x) : "scc", "memory");
  SB();
  const int tid = threadIdx.x, lane = tid & 63;
  const int wid = __builtin_amdgcn_readfirstlane(tid >> 6);
  const int g = blockIdx.x ^ 6, xcd = g & 7, j = g >> 3;
  if (wid < 4) {
    unsigned co = ((j * 4 + wid) * 4 + (lane & 3)) * 128;
    asm volatile(".subsection 1\nk_main_end:\n.subsection 0\n\tv_min_u32 %0, k_main_end-k_main-132, %0\n\ts_getpc_b64 s[40:41]\n\ts_mov_b32 m0, %1\n\ts_mov_b64 exec, 15\n\t"
                 "global_load_lds_dword %0, s[40:41]\n\ts_mov_b64 exec, -1"
                 : "+v"(co) : "i"(LDS_DUMMY + 768) : "s40", "s41", "memory");
  }
  const int pw = wid - 4;
  const int row0 = (wid & 3) * 8 + (lane >> 3);
  const int cm = ((lane & 7) ^ (((row0 >> 1) & 1) | ((wid & 3) << 1))) * 16;
  int vm = row0 * (D_ * 2) + cm;
  asm volatile("" : "+v"(vm));
  const int kq = xcd >> 1;
  const int tt = (xcd & 1) * 4 + (j >> 3);
  const int et = j & 7;
  const int z = kq >> 1, st = (kq & 1) * 8 + tt;
  unsigned o_m = st * 128 * D_ * 2, o_n = et * 128 * D_ * 2, mb0 = lds_addr(smem) + pw * 1024;
  asm volatile("" : "+s"(o_m), "+s"(o_n), "+s"(mb0));
#define G1_X(kt, so, q) DMA_(rm, vm, mb0, (so) + (q) * 4096, o_m, (kt) * 128 + (((q) & 1) * 64 + ((q) >> 1) * 32) * D_ * 2, "")
#define G1_W(kt, so, q) DMA_(rn, vm, mb0, (so) + 16384 + (q) * 4096, o_n, (kt) * 128 + (q) * 32 * D_ * 2, "")
#define G1_STAGE_TILE(kt, so, W) do { SB(); G1_X(kt, so, 0); W(0); G1_X(kt, so, 1); W(1); G1_X(kt, so, 2); W(2); G1_X(kt, so, 3); W(3); \
    G1_W(kt, so, 0); W(4); G1_W(kt, so, 1); W(5); G1_W(kt, so, 2); W(6); G1_W(kt, so, 3); W(7); SB(); } while (0)
#define G1_STAGE_A(kt, so, W) do { SB(); G1_X(kt, so, 0); W(0); G1_X(kt, so, 2); W(1); \
    G1_W(kt, so, 0); W(2); G1_W(kt, so, 1); W(3); G1_W(kt, so, 2); W(4); G1_W(kt, so, 3); W(5); W(6); W(7); SB(); } while (0)
#define G1_STAGE_B(kt, so) do { SB(); G1_X(kt, so, 1); G1_X(kt, so, 3); SB(); } while (0)
#define NOW(i)
  asm volatile("s_waitcnt lgkmcnt(0)" : "+s"(ka), "+s"(rm), "+s"(rn) :: "memory");
  int lrow, drow, l16;
  unsigned o_l, o_d;
  auto rows = [&](int i) __attribute__((always_inline)) {
    int g_ = g, l_ = lane;
    if (i == 0) { asm volatile("" : "+s"(g_)); drow = (g_ & ~7) + pw * 2; lrow = drow + (g_ & 3) * 512 + (g_ & 4) * 64; asm volatile("" : "+s"(drow), "+s"(lrow)); }
    if (i == 1) { asm volatile("" : "+s"(g_)); drow += (g_ & 3) * 256; o_l = lrow * D_ * 2; o_d = drow * D_ * 2; asm volatile("" : "+s"(drow), "+s"(o_l), "+s"(o_d)); }
    if (i == 2) { asm volatile("" : "+v"(l_)); l16 = l_ * 16; asm volatile("" : "+v"(l16)); }
  };
  if (__builtin_expect(wid >= 4, 1)) {
    G1_STAGE_TILE(0, 0, rows);
    __builtin_amdgcn_s_setprio(3);
  }
  x = (const u16*)(gptr)((u64)rm[0] | (u64)rm[1] << 32);
  const u16* wdz = (const u16*)(gptr)((u64)rn[0] | (u64)rn[1] << 32);
  w_r = (const u16*)(gptr)ka[0]; wsp = (char*)(gptr)ka[1]; ws_re = (const u16*)(gptr)ka[2]; ws_im = (const u16*)(gptr)ka[3];
  w_gate = (const u16*)(gptr)ka[4]; w_up = (const u16*)(gptr)ka[5]; w_down = (const u16*)(gptr)ka[6]; out = (u16*)(gptr)ka[7];
  int* flags = (int*)(wsp + WS_FLAGS);
  u64* stat = (u64*)(flags + FL_ST);
  const int fr = lane & 15, fq = lane >> 4;
  const int ha = wid >> 2;
  int trow, xo, po;
  const char *prs, *prc;
  auto epi_setup = [&]() __attribute__((always_inline)) {
    int t_ = tt, l_ = lane;
    asm volatile("" : "+s"(t_));
    asm volatile("" : "+v"(l_));
    trow = t_ * 256 + ((wid >> 1) & 1) * 128 + kq * 32 + ha * 16;
    xo = ((trow + (l_ & 15)) * D_ + et * 128 + (wid & 1) * 64 + (l_ >> 4) * 8) * 2;
    prs = wsp + WS_SLAB + (size_t)((t_ * 8 + et) * 64 + (wid & 3)) * MSG;
    prc = (const char*)(flags + FL_X + ((t_ * 8 + et) * 64 + (wid & 3)) * 32);
    asm volatile("" : "+s"(trow), "+s"(prs), "+s"(prc));
    po = l_ < 3 ? (kq * 4 + ((kq + 1 + l_) & 3)) * 512 : (int)0x80000000;
    asm volatile("" : "+v"(xo), "+v"(po));
  };
#define rs mk_rsrc(prs)
#define rc mk_rsrc(prc)
  lds_vi sig = (lds_vi)(smem + LDS_SIG + (wid & 3) * 4);
  unsigned tag;
  u32x4 xq[2];
  u32x2 sq;
  f32x4 ya[4];
  unsigned key[2];
  float nf[16], la0, la1;
#define PINV(a) asm volatile("" : "+v"(a))
  auto nf_pre = [&]() __attribute__((always_inline)) {
    if (__builtin_expect(!__all(sq.y == tag), 0)) sq = granule_wait(stat + (xo >> 11), tag);
    const float ri = __builtin_amdgcn_rcpf(hi_bf(sq.x));
    la0 = -(lo_bf(sq.x) * ri); la1 = 1.f + ri;
    PINV(la0); PINV(la1);
  };
  auto nf_step = [&](int e) __attribute__((always_inline)) {
    const int u = e >> 1;
    nf[e] = (e & 1) ? hi_bf(xq[u >> 2][u & 3]) : lo_bf(xq[u >> 2][u & 3]);
    PINV(nf[e]);
    nf[e] = __builtin_fmaf(nf[e], la1, la0);
    PINV(nf[e]);
  };
#define MSG_IDX(m) (kq * 4 + ((kq + 1 + (m)) & 3))
  unsigned seen, fx;

  if (__builtin_expect(wid >= 4, 1)) {
    constexpr int O_ST = (FL_ST - FL_AB) * 4, O_CE = (FL_CE - FL_AB) * 4, O_KEY = (FL_KEY - FL_AB) * 4, O_EP = (FL_EPOCH - FL_AB) * 4;
    const __amdgpu_buffer_rsrc_t rf = mk_rsrc(wsp + WS_FLAGS + FL_AB * 4);
    u32x4 lv[2], lv2[2], lw[2], ld[2], ld2[2];
    unsigned epv;
    {
      const char* xrow = (const char*)x + o_l;
      const char* wdrow = (const char*)wdz + o_d;
      lv[0] = ld16_s<0>(xrow, l16); lv[1] = ld16_s<1024>(xrow, l16);
      lv2[0] = ld16_s<2048>(xrow, l16); lv2[1] = ld16_s<3072>(xrow, l16);
      ld[0] = ld16_s<0>(wdrow, l16); ld[1] = ld16_s<1024>(wdrow, l16);
      ld2[0] = ld16_s<2048>(wdrow, l16); ld2[1] = ld16_s<3072>(wdrow, l16);
      lw[0] = ld16_s<0>(w_r, l16); lw[1] = ld16_s<1024>(w_r, l16);
      asm volatile("buffer_load_dword %0, off, %1, %2 sc1" : "=v"(epv) : "s"(rf), "s"(O_EP) : "memory");
    }
    SB();
    const unsigned long long did = llvm_dispatch_id();
    const unsigned dmix = (unsigned)did * 0x85EBCA6Bu + (unsigned)(did >> 32) * 0xC2B2AE35u + 0x27D4EB2Fu;
    const bool moe = et != 7;
    unsigned epoch;
    const int ci = lane & 15, rq = lane >> 4, tk = (lane >> 3) & 1;
    int mat, cg, dq, d0;
    u32x4 wv[16];
    const u16 *pgu, *pn2;
    const char *pm2, *spb;
#define rw __builtin_amdgcn_make_buffer_rsrc((void*)pgu, 0, moe ? 0x7fffffff : 0, 0x00020000)
#define rn2 mk_rsrc(pn2)
#define rm2 mk_rsrc(pm2)
    int vw, vn2, vm2;
    int ord2;
    auto k_blk = [&](int k) __attribute__((always_inline)) { return k >= 9 ? (ord2 >> (4 * (k - 9))) & 7 : tt ^ (k_ord(k) >> 1); };
#define KROT(kk_) (k_blk(kk_) * 256 + (k_ord(kk_) & 1) * 128)
    int tv2;
    unsigned sd = 0;
    auto touches = [&](auto P) __attribute__((always_inline)) {
      constexpr int p = decltype(P)::value;
      int e = et;
      asm volatile("" : "+s"(e));
#define TCH(k) if constexpr (tpos[k] == p) { if (e == twho[k]) { const int kb = KROT(k); touch1(rn2, tv2, mb0, kb); } }
      TCH(0) TCH(1) TCH(2) TCH(3) TCH(4) TCH(5) TCH(6) TCH(7) TCH(8) TCH(9) TCH(10) TCH(11) TCH(12) TCH(13) TCH(14) TCH(15)
    };
    unsigned mq0, mq1, mq2, mq3;
    const int cg8 = g & 7, rgb = g >> 3;
    const __amdgpu_buffer_rsrc_t rdn = mk_rsrc(w_down);
    const __amdgpu_buffer_rsrc_t rgu = mk_rsrc(wsp + WS_GU);
    int vdn, vgu;
    u32x4 dv[7], gv[4];
    int abo, abq[3];
    unsigned lma;
    unsigned sel[2][4];
    float g0, g1;
    u32x4 kk[4];
    unsigned k1 = 0, k2 = 0;
    const int ko_ = O_KEY + pw * 4096;
#define MM(a, b, c) __builtin_amdgcn_mfma_f32_16x16x32_bf16(__builtin_bit_cast(bf16x8, a), __builtin_bit_cast(bf16x8, b), c, 0, 0, 0)
#define PIN1(a) asm volatile("" : "+v"(a))
#define PIN2(a, b) asm volatile("" : "+v"(a), "+v"(b))
#define PIN3(a, b, c) asm volatile("" : "+v"(a), "+v"(b), "+v"(c))
    {
      int gro, go;
      u32x2 gr;
      u32x4 oh[5];
      f32x4 sa4, gw[2], gx[2];
      float su, gs, ta, tb, sx, mbv;
      unsigned dk[4], kl;
      const int hb = lane >> 5;
#pragma unroll
      for (int l = 0; l < WSN; ++l) asm volatile("" : "=v"(wv[l]));
      auto diag = [&](const f32x4& m) __attribute__((always_inline)) {
        return __uint_as_float((__float_as_uint(m[0]) & dk[0]) | (__float_as_uint(m[1]) & dk[1]) | (__float_as_uint(m[2]) & dk[2]) | (__float_as_uint(m[3]) & dk[3]));
      };
      auto setup = [&](auto JB, auto GP) __attribute__((always_inline)) {
        constexpr int jb = decltype(JB)::value, gp = decltype(GP)::value;
        SB();
        {
          int ln = lane, jj = j, p_ = pw;
          launder(ln, jj, p_);
          const int c_ = ln & 15, r_ = ln >> 4, e_ = jj & 7, t_ = (xcd & 1) * 4 + (jj >> 3);
          if constexpr (jb == 1 && gp == 0) {
            pn2 = ((kq >> 1) ? ws_im : ws_re) + (long)t_ * 256 * S_ + (kq & 1) * 1024;
            spb = (const char*)(pn2 + (e_ * 4 + p_) * 8 * S_);
            pins(pn2, spb);
          }
          if constexpr (jb == 1 && gp >= 1 && gp < 6) {
            const unsigned o = c_ == gp - 1 ? 0x3f803f80u : 0u;
            oh[gp - 1] = (u32x4){o, o, o, o};
            PIN1(oh[gp - 1]);
          }
          if constexpr (jb == 1 && gp == 6) {
            const int gm = moe ? (jj - (jj >> 3)) * 8 + xcd : 0;
            mat = gm / 112; cg = (gm % 112) >> 2; dq = gm & 3; d0 = dq * 256 + p_ * 64;
            pin4(mat, cg, dq, d0);
          }
          if constexpr (jb == 2 && gp == 0) {
            pgu = mat ? w_up : w_gate;
            pm2 = wsp + WS_ABT + ((long)e_ * 128 * 4096 + kq * 1024) * 2;
            pins(pgu, pm2);
            vw = ((d0 + r_ * 16) * F_ + cg * 128 + c_ * 8) * 2;
            PIN1(vw);
          }
          if constexpr (jb == 2 && (gp == 1 || gp == 2)) {
            dk[gp * 2 - 2] = c_ == r_ * 4 + gp * 2 - 2 ? ~0u : 0u; dk[gp * 2 - 1] = c_ == r_ * 4 + gp * 2 - 1 ? ~0u : 0u;
            PIN2(dk[gp * 2 - 2], dk[gp * 2 - 1]);
          }
          if constexpr (jb == 2 && gp == 3) { kl = lrow + (ln >> 5); go = (ln & 30) ? (int)0x80000000 : ((ln & 1) ? O_KEY : O_ST) + (int)kl * 8; PIN2(kl, go); }
          if constexpr (jb == 2 && gp == 4) { kl = 0xffff - kl; go = (ln & ~1) == 2 ? O_CE + (z * D_ + drow + (ln & 1)) * 8 : go; PIN2(kl, go); }
          if constexpr (jb == 2 && gp == 5) {
            gro = (p_ < 2 ? O_ST + (st * 128 + p_ * 64) * 8 : O_CE + (z * D_ + e_ * 128 + (p_ - 2) * 64) * 8) + ln * 8;
            tv2 = (p_ * 64 + ln) * 4096;
            lma = lds_addr(smem + LDS_MB) + (p_ < 2 ? p_ * 256 : 512 + p_ * 256) + ln * 4;
            PIN3(gro, tv2, lma);
          }
          if constexpr (jb == 2 && gp == 6) {
            const u64 lo = (t_ & 2) ? ((t_ & 4) ? 0x0150234601503247ull : 0x0514670205147603ull) : ((t_ & 4) ? 0x0123067401532076ull : 0x0567423005176432ull);
            ord2 = (int)(lo >> ((t_ & 1) * 32));
            pins(ord2, p_);
          }
          if constexpr (jb == 7 && gp == 0) {
            const int r0 = p_ * 8 + (ln >> 3);
            vn2 = r0 * (S_ * 2) + ((ln & 7) ^ ((r0 >> 1) & 7)) * 16; vm2 = r0 * 8192 + (vm & 127);
            PIN2(vn2, vm2);
          }
          if constexpr (jb == 7 && gp == 1) { vdn = jj * 112 + p_ * 28 + r_ * 7; vgu = (((ln >> 3) & 1) * 4 * F_ + vdn + min(ln & 7, 6)) * 16; PIN2(vdn, vgu); }
          if constexpr (jb == 7 && gp == 2) { vdn = (vdn * D_ + cg8 * 128 + c_ * 8) * 2; PIN1(vdn); }
          if constexpr (jb == 7 && gp == 3) {
            abo = (((kq * 8 + e_) * 4 + p_) * 16 + c_) * AB_STRIDE * 4;
            abq[2] = ln >= 16 || (ln & 7) == t_ ? (int)0x80000000 : abo;
            PIN2(abo, abq[2]);
          }
          if constexpr (jb == 7 && gp == 4) {
            abq[1] = ln >= 8 ? (int)0x80000000 : abq[2];
            abq[0] = ((ln ^ t_) & 4) ? (int)0x80000000 : abq[1];
            PIN2(abq[0], abq[1]);
          }
          if constexpr (jb == 7 && gp == 5) {
            const unsigned mb = lds_addr(smem) + p_ * 1024;
            mq0 = mb + ((3 - kq) & 3) * 4096; mq1 = mb + ((4 - kq) & 3) * 4096; mq2 = mb + ((5 - kq) & 3) * 4096; mq3 = mb + ((6 - kq) & 3) * 4096;
            pin4(mq0, mq1, mq2, mq3);
          }
          if constexpr (jb == 8 && gp < 4) {
#pragma unroll
            for (int h = 0; h < 2; ++h) {
              const unsigned hv = h ? 0x0302u : 0x0100u;
              sel[h][gp] = ((ln & 7) >> 1) == gp ? ((ln & 1) ? (hv << 16) | 0x0c0cu : 0x0c0c0000u | hv) : 0x0c0c0c0cu;
            }
            PIN2(sel[0][gp], sel[1][gp]);
          }
        }
        SB();
      };
#define WS1(i) setup(IC<1>{}, IC<i>{})
#define WS2(i) setup(IC<2>{}, IC<i>{})
#define WS3(i) do { setup(IC<7>{}, IC<i>{}); if (i >= 4) setup(IC<8>{}, IC<(i) & 3>{}); } while (0)
      G1_STAGE_TILE(1, G1_STAGE, WS1);
      WAIT_VM(19); BAR();
      G1_STAGE_TILE(2, 2 * G1_STAGE, WS2);
      G1_STAGE_TILE(3, 3 * G1_STAGE, WS3);
      auto work1 = [&](auto JB, auto GP) __attribute__((always_inline)) {
        constexpr int jb = decltype(JB)::value, gp = decltype(GP)::value;
        SB();
        if constexpr (jb == 1) {
          const f32x4 z4 = {0.f, 0.f, 0.f, 0.f};
          if constexpr (gp == 0) { sa4 = MM(lv[0], oh[0], z4); gw[0] = MM(lv[0], lw[0], z4); gx[0] = MM(lv[0], lv[0], z4); PIN3(sa4, gw[0], gx[0]); }
          if constexpr (gp == 1) { sa4 = MM(lv2[0], oh[1], sa4); gw[1] = MM(lv2[0], lw[0], z4); gx[1] = MM(lv2[0], lv2[0], z4); PIN3(sa4, gw[1], gx[1]); }
          if constexpr (gp == 2) { sa4 = MM(lv[1], oh[0], sa4); gw[0] = MM(lv[1], lw[1], gw[0]); gx[0] = MM(lv[1], lv[1], gx[0]); PIN3(sa4, gw[0], gx[0]); }
          if constexpr (gp == 3) { sa4 = MM(lv2[1], oh[1], sa4); gw[1] = MM(lv2[1], lw[1], gw[1]); gx[1] = MM(lv2[1], lv2[1], gx[1]); PIN3(sa4, gw[1], gx[1]); }
          if constexpr (gp == 4) { sa4 = MM(ld[0], oh[2], sa4); sa4 = MM(ld[1], oh[2], sa4); PIN1(sa4); }
          if constexpr (gp == 5) { sa4 = MM(ld2[0], oh[3], sa4); sa4 = MM(ld2[1], oh[3], sa4); PIN1(sa4); }
          if constexpr (gp == 6) { sa4 = MM(lw[0], oh[4], sa4); sa4 = MM(lw[1], oh[4], sa4); PIN1(sa4);
            epoch = __builtin_amdgcn_readfirstlane(epv); tag = ((epoch + 1u) * 0x9E3779B1u ^ dmix) | 1u; }
        }
        if constexpr (jb == 2) {
          if constexpr (gp == 0) { su = (sa4[0] + sa4[1]) + (sa4[2] + sa4[3]); ta = diag(gw[0]); PIN2(su, ta); }
          if constexpr (gp == 1) { su = fold32(su, su); tb = diag(gw[1]); PIN2(su, tb); }
          if constexpr (gp == 2) { su = fold16(su, su); gs = fold32(ta, tb); PIN2(su, gs); }
          if constexpr (gp == 3) { ta = diag(gx[0]); tb = diag(gx[1]); PIN2(ta, tb); }
          if constexpr (gp == 4) { gs = fold16(gs, fold32(ta, tb)); PIN1(gs); }
          if constexpr (gp == 5) { gs += dpp_f<0xB1>(gs); gs += dpp_f<0x4E>(gs); PIN1(gs); }
          if constexpr (gp == 6) { gs += dpp_f<0x141>(gs); gs += dpp_f<0x140>(gs); PIN1(gs); }
        }
        if constexpr (jb == 3) {
          if constexpr (gp == 0) {
            const float s0 = rdl(su, 0), s1 = rdl(su, 1), q0 = rdl(gs, 16), q1 = rdl(gs, 48);
            sx = hb ? s1 : s0; ta = hb ? q1 : q0; PIN2(sx, ta);
          }
          if constexpr (gp == 1) {
            const float mean = sx * (1.0f / D_);
            mbv = rbf(mean);
            ta -= sx * mean;
            if (__builtin_expect(__any(!(sx * mean <= 16.f * ta)), 0)) {
              float q = fold32(sqdev16(lv, rdl(mbv, 0)), sqdev16(lv2, rdl(mbv, 32)));
              q = row_sum(fold16(q, q));
              const float dm = mean - mbv;
              ta = fmaxf(q - (float)D_ * dm * dm, 0.f);
            }
            PIN2(mbv, ta);
          }
          if constexpr (gp == 2) { ta = rbf(rbf(ta * (1.0f / D_)) + 1e-5f); gs -= mbv * rdl(su, 4); PIN2(ta, gs); }
          if constexpr (gp == 3) { ta = rbf(__builtin_amdgcn_sqrtf(ta)); tb = __builtin_amdgcn_rcpf(ta); PIN2(ta, tb); }
          if constexpr (gp == 4) {
            const unsigned rb = __float_as_uint(rbf(gs * tb));
            kl |= (rb ^ ((unsigned)((int)rb >> 31) | 0x80000000u)) & 0xffff0000u; PIN1(kl);
          }
          if constexpr (gp == 5) { kl = (lane & 2) ? __float_as_uint(su) : (lane & 1) ? kl : (__float_as_uint(mbv) >> 16) | __float_as_uint(ta); PIN1(kl); }
        }
        if constexpr (jb == ST_JB + 3) {
          if constexpr (gp == 0) { if (!__all(gr.y == tag)) gr = granule_wait((const u64*)((const char*)(flags + FL_AB) + gro), tag); PIN1(gr); }
          if constexpr (gp == 1) { g1 = (z ? -1.0f : 1.0f) / hi_bf(gr.x); PIN1(g1); }
          if constexpr (gp == 2) { g0 = pw < 2 ? -(lo_bf(gr.x) * g1) : __uint_as_float(gr.x); PIN1(g0); }
        }
        if constexpr (gp < 7 && (jb - 1) * 7 + gp < 64) {
          constexpr int n = (jb - 1) * 7 + gp;
          sld<((n >> 1) & 7) * 4096 + (n & 1) * 64>(sd, spb, KROT(n >> 4));
        }
        SB();
      };
      auto step1 = [&](auto JB) __attribute__((always_inline)) {
        constexpr int jb = decltype(JB)::value;
        if constexpr (jb == 1) PAGE(1);
        if constexpr (jb == 7) PAGE(2);
        constexpr int n16 = jb == 12 ? 12 : jb == 11 ? 14 : 16;
        constexpr auto wl = [](int q) { return (q == 3 ? 1 : 0) + (q == ST_JB ? 1 : 0); };
        if constexpr (jb == 1) {
          asm volatile("s_waitcnt vmcnt(%11)" : "+v"(lv[0]), "+v"(lv[1]), "+v"(lw[0]), "+v"(lw[1]), "+v"(ld[0]), "+v"(ld[1]),
                       "+v"(lv2[0]), "+v"(lv2[1]), "+v"(ld2[0]), "+v"(ld2[1]), "+v"(epv) : "i"(16) : "memory");
        } else if constexpr (jb == ST_JB + 3) {
          asm volatile("s_waitcnt vmcnt(%1)" : "+v"(gr) : "i"(n16 + wl(jb - 2) + wl(jb - 1)) : "memory");
        } else wait_vm<n16 + wl(jb - 3) + wl(jb - 2) + wl(jb - 1)>();
        BAR();
        if constexpr (jb == 2) { if (pw == 0 && lane < 16) ((int*)(smem + LDS_SEL))[lane < 8 ? lane : lane + 8] = 0; }
        if constexpr (jb == 4) { if (pw == 0 && lane == 0) ((unsigned*)(smem + LDS_SEL))[5] = tag; }
#define W1(i) work1(JB, IC<i>{})
        if constexpr (jb < 10) G1_STAGE_TILE(jb - 1 + G1_NST, ((jb - 1) % G1_NST) * G1_STAGE, W1);
        else G1_STAGE_A(jb - 1 + G1_NST, ((jb - 1) % G1_NST) * G1_STAGE, W1);
        if constexpr (jb == 12) { G1_STAGE_B(13, G1_STAGE); G1_STAGE_B(14, 2 * G1_STAGE); }
        if constexpr (jb == 3) __builtin_amdgcn_raw_buffer_store_b64((u32x2){kl, tag}, rf, go, 0, 16);
        if constexpr (jb == ST_JB) {
          asm volatile("buffer_load_dwordx2 %0, %1, %2, 0 offen sc1" : "=v"(gr) : "v"(gro), "s"(rf) : "memory");
        }
        touches(IC<100 + jb>{});
        if constexpr (jb <= 7) {
          if (et == jb - 1) {
#pragma unroll
            for (int l = 0; l < WSN; ++l) bld16_nt_io(wv[l], rw, vw, l * F_ * 2);
          }
        }
        SB();
      };
      static_assert(ST_JB + 3 <= 12 && ST_JB >= 6, "schedule");
      step1(IC<1>{}); step1(IC<2>{}); step1(IC<3>{}); step1(IC<4>{}); step1(IC<5>{}); step1(IC<6>{}); step1(IC<7>{}); step1(IC<8>{}); step1(IC<9>{}); step1(IC<10>{}); step1(IC<11>{}); step1(IC<12>{});
    }
#define G2_STAGE_N(kk_, W) do { const int kb = KROT(kk_); constexpr int so = n_off(kk_); SB(); \
    DMA_(rn2, vn2, mq0, so, kb, 0, ""); W(0); DMA_(rn2, vn2, mq1, so, kb, 32 * S_ * 2, ""); W(1); \
    DMA_(rn2, vn2, mq2, so, kb, 64 * S_ * 2, ""); W(2); DMA_(rn2, vn2, mq3, so, kb, 96 * S_ * 2, ""); W(3); \
    DMA_(rn2, vn2, mq0, so + 16384, kb, 128 * S_ * 2, ""); W(4); DMA_(rn2, vn2, mq1, so + 16384, kb, 160 * S_ * 2, ""); W(5); \
    DMA_(rn2, vn2, mq2, so + 16384, kb, 192 * S_ * 2, ""); W(6); DMA_(rn2, vn2, mq3, so + 16384, kb, 224 * S_ * 2, ""); W(7); SB(); } while (0)
#define G2_STAGE_NH(kk_, h) do { const int kb = KROT(kk_); constexpr int so = n_off(kk_) + (h) * 16384; SB(); \
    DMA_(rn2, vn2, mq0, so, kb, (h) * 128 * S_ * 2, ""); DMA_(rn2, vn2, mq1, so, kb, ((h) * 128 + 32) * S_ * 2, ""); \
    DMA_(rn2, vn2, mq2, so, kb, ((h) * 128 + 64) * S_ * 2, ""); DMA_(rn2, vn2, mq3, so, kb, ((h) * 128 + 96) * S_ * 2, ""); SB(); } while (0)
#define G2_STAGE_M(kk_, W) do { const int kb = KROT(kk_); constexpr int so = G2_MRING + m_off(kk_); SB(); \
    DMA_(rm2, vm2, mb0, so, kb, 0, " sc1"); W(8); DMA_(rm2, vm2, mb0, so + 4096, kb, 32 * 8192, " sc1"); W(9); \
    DMA_(rm2, vm2, mb0, so + 8192, kb, 64 * 8192, " sc1"); W(10); DMA_(rm2, vm2, mb0, so + 12288, kb, 96 * 8192, " sc1"); W(11); SB(); } while (0)
    WAIT_VM(16); BAR();
    asm volatile("ds_write_b32 %0, %1" :: "v"(lma), "v"(g0) : "memory");
    if (pw < 2) asm volatile("ds_write_b32 %0, %1 offset:512" :: "v"(lma), "v"(g1) : "memory");
    u32x4 tv[4];
    unsigned ready = 0;
    auto need = [&](int k) __attribute__((always_inline)) {
      const int b = k_blk(k) + (k_ord(k) & 1) * 8;
#pragma unroll 1
      for (int it = 0; !((ready >> b) & 1) && it < SPIN_MAX; ++it) {
        ready |= (unsigned)__ballot(__builtin_amdgcn_raw_buffer_load_b32(rf, abq[k < 5 ? 0 : k < 9 ? 1 : 2], 0, 16) == tag);
        asm volatile("" ::: "memory");
      }
    };
    unsigned ksel, bad = 0;
    int isel;
    unsigned xw;
    auto bvec = [&](unsigned src, const unsigned (&sl)[4]) __attribute__((always_inline)) {
      return (u32x4){__builtin_amdgcn_perm(0, src, sl[0]), __builtin_amdgcn_perm(0, src, sl[1]),
                     __builtin_amdgcn_perm(0, src, sl[2]), __builtin_amdgcn_perm(0, src, sl[3])};
    };
#define BC8(v, p) (unsigned)__builtin_amdgcn_update_dpp(__builtin_amdgcn_update_dpp(0, (int)(v), 0x150 + (p), 0xf, 0x3, false), (int)(v), 0x158 + (p), 0xf, 0xc, false)
    float* lg = (float*)(smem + LDS_GU);
    f32x4 macc = {0.f, 0.f, 0.f, 0.f};
    u32x2 sv;
    unsigned hp, gbad;
    float s0, s1;
    auto work2 = [&](auto JB, auto GP) __attribute__((always_inline)) {
      constexpr int jb = decltype(JB)::value, gp = decltype(GP)::value;
      SB();
      if constexpr (jb == -1 && gp < 4) {
        constexpr int c = gp == 0 ? 0xB1 : gp == 1 ? 0x4E : gp == 2 ? 0x141 : 0x140;
        merge2(k1, k2, dpp_u<c>(k1), dpp_u<c>(k2));
        PIN2(k1, k2);
      }
      if constexpr (jb == -1 && gp == 4) {
        ksel = tk ? k2 : k1;
        isel = (0xffff - (int)(ksel & 0xffffu)) & (S_ - 1);
        k1 = (isel * D_ + d0 + rq * 16 + (lane & 7) * 2) * 2; k2 = O_ST + isel * 8;
        PIN1(ksel);
        xw = gld4_s(x, k1);
        sv = bld8_sc1(rf, k2);
      }
      if constexpr (jb == 0 && gp == 1) wait_xs<3 + 1 + 2>(xw, sv);
      if constexpr (jb == 0) {
        if constexpr (gp == 1) { if (!__all(sv.y == tag)) sv = granule_wait(stat + isel, tag); s0 = __builtin_amdgcn_rcpf(hi_bf(sv.x)); PIN1(s0); }
        if constexpr (gp == 2) { s1 = ln_elem(lo_bf(xw), lo_bf(sv.x), hi_bf(sv.x), s0); PIN1(s1); }
        if constexpr (gp == 3) { xw = pk2(s1, ln_elem(hi_bf(xw), lo_bf(sv.x), hi_bf(sv.x), s0)); PIN1(xw); }
      }
      if constexpr (jb == DG && gp < 7) { PIN1(dv[gp]); macc = MM(dv[gp], bvec(BC8(hp, gp), sel[0]), macc); PIN1(macc); }
      if constexpr ((jb == 0 && gp >= 4) || jb == 1) {
        constexpr int l = jb == 0 ? gp - 4 : gp + 4;
        if constexpr (!(l & 1)) hp = BC8(xw, l >> 1);
        PIN1(wv[l]);
        macc = MM(wv[l], bvec(hp, sel[l & 1]), macc);
        PIN2(macc, hp);
      }
      if constexpr (jb == GV_JB + 2) {
        if constexpr (gp == 0) { gbad = (gv[0].y ^ tag) | (gv[0].w ^ tag) | (gv[1].y ^ tag) | (gv[1].w ^ tag); PIN1(gbad); }
        if constexpr (gp == 1) { gbad |= (gv[2].y ^ tag) | (gv[2].w ^ tag) | (gv[3].y ^ tag) | (gv[3].w ^ tag); PIN1(gbad); }
        if constexpr (gp == 2) {
          s0 = ((__uint_as_float(gv[0].x) + __uint_as_float(gv[1].x)) + __uint_as_float(gv[2].x)) + __uint_as_float(gv[3].x);
          s1 = ((__uint_as_float(gv[0].z) + __uint_as_float(gv[1].z)) + __uint_as_float(gv[2].z)) + __uint_as_float(gv[3].z);
          PIN2(s0, s1);
        }
        if constexpr (gp == 3) {
#pragma unroll 1
          for (int it = 0; !__all(gbad == 0) && it < SPIN_MAX; ++it) {
            u32x4 q[4];
            gbad = 0;
#pragma unroll
            for (int i = 0; i < 4; ++i) { q[i] = __builtin_amdgcn_raw_buffer_load_b128(rgu, vgu, i * F_ * 16, 16); gbad |= (q[i].y ^ tag) | (q[i].w ^ tag); }
            s0 = ((__uint_as_float(q[0].x) + __uint_as_float(q[1].x)) + __uint_as_float(q[2].x)) + __uint_as_float(q[3].x);
            s1 = ((__uint_as_float(q[0].z) + __uint_as_float(q[1].z)) + __uint_as_float(q[2].z)) + __uint_as_float(q[3].z);
            asm volatile("" ::: "memory");
          }
          const unsigned gp2 = pk2(s0, s1);
          const unsigned up2 = dpp_u<0x128>(gp2);
          s0 = tk ? hi_bf(up2) : lo_bf(gp2); s1 = tk ? hi_bf(gp2) : lo_bf(up2);
          PIN2(s0, s1);
        }
        if constexpr (gp == 4) { s0 = rbf(s0 * __builtin_amdgcn_rcpf(1.f + __expf(-s0))); PIN1(s0); }
        if constexpr (gp == 5) { hp = pk2(s0 * s1, 0.f); PIN1(hp); }
      }
      if constexpr (jb >= 3 && jb <= 10 && gp >= 4) {
        constexpr int n = (jb - 3) * 8 + gp - 4;
        sld<((n >> 1) & 7) * 4096 + (n & 1) * 64>(sd, spb, KROT(12 + (n >> 4)));
      }
      if constexpr (jb == 10 && gp == 2) epi_setup();
      if constexpr (jb == 10 && gp == 4) code_touch(sd, (j * 4 + pw) * 256);
      SB();
    };
#define W2M(i) work2(IC<-1>{}, IC<i>{})
#define W20(i) work2(IC<0>{}, IC<i>{})
#define W21(i) work2(IC<1>{}, IC<i>{})
    unsigned fs;
    {
      G1_STAGE_B(15, 3 * G1_STAGE);
      G2_STAGE_NH(0, 0);
      lgkm0(sd);
      WAIT_VM(16); BAR();
      G2_STAGE_NH(0, 1);
      WAIT_VM(10); BAR();
#pragma unroll
      for (int i = 0; i < 4; ++i) kk[i] = bld16_sc1(rf, l16, ko_ + i * 1024);
#pragma unroll
      for (int l = WSN; l < 16; ++l) wv[l] = bld16_nt(rw, vw, l * F_ * 2);
      asm volatile("s_waitcnt vmcnt(%4)" : "+v"(kk[0]), "+v"(kk[1]), "+v"(kk[2]), "+v"(kk[3]) : "i"(16 - WSN) : "memory");
      touches(IC<115>{});
#pragma unroll
      for (int i = 0; i < 4; ++i) {
        k2 = max(k2, min(k1, kk[i].x)); k1 = max(k1, kk[i].x);
        k2 = max(k2, min(k1, kk[i].z)); k1 = max(k1, kk[i].z);
        bad |= (kk[i].y ^ tag) | (kk[i].w ^ tag);
      }
#pragma unroll 1
      for (int it = 0; !__all(bad == 0) && it < SPIN_MAX; ++it) {
        k1 = 0; k2 = 0; bad = 0;
#pragma unroll
        for (int i = 0; i < 4; ++i) {
          const u32x4 q = __builtin_amdgcn_raw_buffer_load_b128(rf, l16, ko_ + i * 1024, 16);
          k2 = max(k2, min(k1, q.x)); k1 = max(k1, q.x);
          k2 = max(k2, min(k1, q.z)); k1 = max(k1, q.z);
          bad |= (q.y ^ tag) | (q.w ^ tag);
        }
        asm volatile("" ::: "memory");
      }
      merge2(k1, k2, dpp_u<0xB1>(k1), dpp_u<0xB1>(k2));
      merge2(k1, k2, dpp_u<0x4E>(k1), dpp_u<0x4E>(k2));
      merge2(k1, k2, dpp_u<0x141>(k1), dpp_u<0x141>(k2));
      merge2(k1, k2, dpp_u<0x140>(k1), dpp_u<0x140>(k2));
      if ((lane & 15) == 0) *(u32x2*)(smem + LDS_KEY + (pw * 4 + rq) * 8) = (u32x2){k1, k2};
      PAGE(3);
      BAR_LGKM();
#pragma unroll
      for (int jj = 0; jj < 4; ++jj) tv[jj] = *(const u32x4*)(smem + G2_MRING + m_off(0) + (jj * 4 + pw) * 1024 + lane * 16);
      { const u32x2 kp = *(const u32x2*)(smem + LDS_KEY + ci * 8); k1 = kp.x; k2 = kp.y; }
#pragma unroll
      for (int jj = 0; jj < 4; ++jj) __builtin_amdgcn_raw_buffer_store_b128(tv[jj], rm2, vm2 + jj * 32 * 8192 + 2 * tt * 128, 0, 16);
      G2_STAGE_N(1, W2M);
      WAIT_VM(10);
      if (lane == tt) __builtin_amdgcn_raw_buffer_store_b32(tag, rf, abo, 0, 16);
      G2_STAGE_N(2, W20);
      __builtin_amdgcn_s_sleep(2);
      WAIT_VM(9);
      fs = bld4_sc1(rf, abq[0]);
    }
    BAR();
#pragma unroll
    for (int jj = 0; jj < 4; ++jj) tv[jj] = *(const u32x4*)(smem + G2_MRING + m_off(1) + (jj * 4 + pw) * 1024 + lane * 16);
    G2_STAGE_N(3, NOW);
    asm volatile("s_waitcnt vmcnt(8)" : "+v"(fs) :: "memory");
    ready = (unsigned)__ballot(fs == tag);
    need(2);
    G2_STAGE_M(2, NOW);
    need(3);
    G2_STAGE_M(3, NOW);
    fs = bld4_sc1(rf, abq[1]);
    touches(IC<1>{});
    W21(0); W21(1); W21(2); W21(3); W21(4); W21(5); W21(6); W21(7); W21(8); W21(9); W21(10); W21(11);
    asm volatile("" : "+v"(tv[0]), "+v"(tv[1]), "+v"(tv[2]), "+v"(tv[3]));
    auto step2 = [&](auto JB) __attribute__((always_inline)) {
      constexpr int jb = decltype(JB)::value;
      if constexpr (jb == 3) PAGE(4);
      if constexpr (jb == 9) PAGE(5);
      constexpr auto must = [](int q) { return (q == 2 ? 4 : 0) + ((FSM >> q) & 1) + ((q == GV_JB || q == GV_JB - 1) ? 2 : 0); };
      if constexpr (jb == 2) wait_vm<4 + must(1)>();
      else wait_vm<dn_cnt(jb - 2) + (jb == 5 ? 1 : 0) + 12 + must(jb - 1) + dn_cnt(jb - 1)>();
      if constexpr (jb == 3) { if (pw == 0 && (lane & ~8) == 0) ((unsigned*)(smem + LDS_SEL))[tk] = ksel; }
      if constexpr ((jb == 2 || jb == DG + 1)) {
        *(f32x4*)(lg + pw * 256 + lane * 4) = macc;
        macc = (f32x4){0.f, 0.f, 0.f, 0.f};
        lgkm0(sd);
      }
      BAR();
      float p0[4], p1[4];
      const int col = (pw & 1) * 64 + lane;
      if constexpr (jb == 3) {
        if (g == 0 && pw == 0 && lane == 0) __hip_atomic_store((unsigned*)flags + FL_EPOCH, epoch + 1u, __ATOMIC_RELAXED, __HIP_MEMORY_SCOPE_AGENT);
      }
      if constexpr (jb == DG + 1) {
        const float* p = lg + (pw & 1) * 32 + (lane >> 4) * 64 + ((2 * lane) & 7) * 4 + ((lane >> 2) & 3);
#pragma unroll
        for (int i = 0; i < 4; ++i) { p0[i] = p[i * 256]; p1[i] = p[i * 256 + 4]; }
      }
      if constexpr (jb == 2) {
        const float* p = lg + (col >> 5) * 64 + (col & 7) * 4 + ((col >> 3) & 3);
#pragma unroll
        for (int i = 0; i < 4; ++i) { p0[i] = p[i * 256]; p1[i] = p[i * 256 + 32]; }
      }
      if constexpr (jb == 4) {
        if (lane == 8 + tt) __builtin_amdgcn_raw_buffer_store_b32(tag, rf, abo, 0, 16);
      }
      if constexpr ((FSM >> (jb - 2)) & 1) {
        asm volatile("" : "+v"(fs));
        ready |= (unsigned)__ballot(fs == tag);
      }
      if constexpr (jb == GV_JB + 2) asm volatile("" : "+v"(gv[0]), "+v"(gv[1]), "+v"(gv[2]), "+v"(gv[3]));
#define W2(i) work2(JB, IC<i>{})
      if constexpr (jb < 13) {
        G2_STAGE_N(jb + 2, W2);
        need(jb + 2);
        G2_STAGE_M(jb + 2, W2);
      } else {
#define W14(i) do { if (i == B14P - 1) { wait_vm<B14P>(); BAR(); } } while (0)
#define W14M(i) W14(i - 8)
        need(15);
        G2_STAGE_M(15, W14M);
#define LASTQ(q) do { const int kb = KROT(15) + ((q + kq + 1) & 3) * 32 * S_ * 2; \
        DMA_(rn2, vn2, mb0, n_off(15) + q * 4096, kb, 0, ""); W14(4 + 2 * q); DMA_(rn2, vn2, mb0, n_off(15) + 16384 + q * 4096, kb, 128 * S_ * 2, ""); W14(5 + 2 * q); } while (0)
        LASTQ(0); LASTQ(1); LASTQ(2); LASTQ(3);
        SB();
      }
      if constexpr (jb == 2) {
#pragma unroll
        for (int jj = 0; jj < 4; ++jj) __builtin_amdgcn_raw_buffer_store_b128(tv[jj], rm2, vm2 + jj * 32 * 8192 + (2 * tt + 1) * 128, 0, 16);
      }
      if constexpr ((FSM >> jb) & 1) fs = bld4_sc1(rf, abq[jb < 5 ? 1 : 2]);
      if constexpr ((jb == GV_JB || jb == GV_JB - 1)) {
#pragma unroll
        for (int i = (jb == GV_JB ? 2 : 0); i < (jb == GV_JB ? 4 : 2); ++i) gv[i] = bld16_sc1(rgu, vgu, i * F_ * 16);
      }
      touches(JB);
#pragma unroll
      for (int i = dn_first(jb); i < dn_first(jb) + dn_cnt(jb); ++i) dv[i] = bld16_nt(rdn, vdn, i * D_ * 2);
      if constexpr (jb == 2) asm volatile("" : "+v"(p0[0]), "+v"(p0[1]), "+v"(p0[2]), "+v"(p0[3]), "+v"(p1[0]), "+v"(p1[1]), "+v"(p1[2]), "+v"(p1[3]));
      if constexpr (jb == DG + 1) {
        if (pw < 2)
          __builtin_amdgcn_raw_buffer_store_b128((u32x4){__float_as_uint((p0[0] + p0[1]) + (p0[2] + p0[3])), tag, __float_as_uint((p1[0] + p1[1]) + (p1[2] + p1[3])), tag},
                                                 mk_rsrc(wsp + WS_DN), (((pw * 8 + cg8) * 32 + rgb) * 64 + lane) * 16, 0, 16);
      }
      if constexpr (jb == 2) {
        if (moe && pw < 2)
          __builtin_amdgcn_raw_buffer_store_b128((u32x4){__float_as_uint((p0[0] + p0[1]) + (p0[2] + p0[3])), tag, __float_as_uint((p1[0] + p1[1]) + (p1[2] + p1[3])), tag},
                                                 rgu, ((mat * 4 + dq) * F_ + cg * 128 + col) * 16, 0, 16);
      }
      SB();
    };
    static_assert(DG + 1 <= 12 && DG >= GV_JB + 3 && dn_first(DG - 2) == 7, "schedule");
    step2(IC<2>{}); step2(IC<3>{}); step2(IC<4>{}); step2(IC<5>{}); step2(IC<6>{}); step2(IC<7>{}); step2(IC<8>{}); step2(IC<9>{}); step2(IC<10>{}); step2(IC<11>{}); step2(IC<12>{}); step2(IC<13>{});
    lgkm0(sd);
    PAGE(7);
    WAIT_VM(4); BAR();
    xq[0] = ld16_s<0>(x, xo); xq[1] = ld16_s<64>(x, xo); sq = ld8_s_sc1(stat, xo >> 11 << 3);
    WAIT_VM(3); BAR();
    asm volatile("s_waitcnt vmcnt(0)" : "+v"(xq[0]), "+v"(xq[1]), "+v"(sq) :: "memory");
    key[0] = __builtin_amdgcn_readlane(ksel, 0); key[1] = __builtin_amdgcn_readlane(ksel, 8);
    nf_pre();
#pragma unroll
    for (int n = 0; n < 16; ++n) nf_step(n);
#pragma unroll 1
    for (int c = 0; c < 2; ++c) {
      const unsigned kc = __builtin_amdgcn_readlane(ksel, c * 8);
      const int ic = (0xffff - (int)(kc & 0xffffu)) & (S_ - 1);
      if ((ic >> 8) != tt || ((ic >> 5) & 3) != kq) continue;
      const __amdgpu_buffer_rsrc_t rd = mk_rsrc(wsp + WS_DN + (size_t)(c * 8 + et) * 32768);
#pragma unroll 1
      for (int it = 0; it < SPIN_MAX; ++it) {
        if (__all(__builtin_amdgcn_raw_buffer_load_b32(rd, (lane & 31) * 1024 + 1020, 0, 16) == tag)) break;
        __builtin_amdgcn_s_sleep(2);
        asm volatile("" ::: "memory");
      }
      float s0, s1;
      unsigned bad = 1;
#pragma unroll 1
      for (int it = 0; !__all(bad == 0) && it < SPIN_MAX; ++it) {
        bad = 0; s0 = 0.f; s1 = 0.f;
#pragma unroll
        for (int i = 0; i < 8; ++i) {
          const u32x4 q = __builtin_amdgcn_raw_buffer_load_b128(rd, rq * 1024 + pw * 256 + ci * 16, i * 4096, 16);
          bad |= (q.y ^ tag) | (q.w ^ tag); s0 += __uint_as_float(q.x); s1 += __uint_as_float(q.z);
        }
        asm volatile("" ::: "memory");
      }
      s0 = fold32(s0, s1);
      s0 = fold16(s0, s0);
      const float sg = rbf(1.f / (1.f + __expf(-key_score(kc))));
      ((float*)(smem + LDS_FIN))[c * 128 + pw * 32 + ci * 2 + (lane >> 5)] = rbf(rbf(s0) * sg);
      asm volatile("s_waitcnt lgkmcnt(0)" ::: "memory");
      if (lane == 0) asm volatile("ds_add_u32 %0, %1" :: "v"(lds_addr(smem + LDS_SEL + 24 + c * 4)), "v"(1) : "memory");
    }
    asm volatile("v_mov_b32 %0, 0" : "=v"(fx), "=v"(ya[0]), "=v"(ya[1]), "=v"(ya[2]), "=v"(ya[3]));
  } else {
  {
    const int q = j * 4 + wid;
    constexpr int Q1 = WS_MB, Q2 = Q1 + 8, Q3 = Q2 + 8, Q4 = Q3 + 7, Q5 = Q4 + 7, Q6 = Q5 + 7, Q7 = Q6 + 4, Q8 = Q7 + 4, QC = Q8 + 1;
    static_assert(QC <= 128, "warm-up slots");
    unsigned dmy = 0;
    if (q < QC) {
      const char* base; int b0;
      if (q < Q1) { base = wsp; b0 = 0; }
      else if (q < Q2) { base = (const char*)ws_re; b0 = Q1; }
      else if (q < Q3) { base = (const char*)ws_im; b0 = Q2; }
      else if (q < Q4) { base = (const char*)w_gate; b0 = Q3; }
      else if (q < Q5) { base = (const char*)w_up; b0 = Q4; }
      else if (q < Q6) { base = (const char*)w_down; b0 = Q5; }
      else if (q < Q7) { base = (const char*)out; b0 = Q6; }
      else if (q < Q8) { base = (const char*)x; b0 = Q7; }
      else { base = (const char*)w_r; b0 = Q8; }
      base += (size_t)(q - b0) << 20;
      asm volatile("s_load_dword %0, %1, 0" : "+s"(dmy) : "s"(base) : "memory");
    }
    {
      const int w = g * 4 + wid;
      constexpr int R = (int)(WS_FLAGS - WS_DN);
      static_assert(FL_X == 0 && FL_ST == 6144 * 32 && FL_EPOCH == 6528 * 32, "");
      const int o = lane < 6 ? R + (lane * 1024 + w) * 128 : lane > 6 || w >= 896 ? (int)0x80000000 : w < 512 ? w * 1024 + 896 : R + (6144 - 512 + w) * 128;
      asm volatile("s_mov_b32 m0, %0\n\ts_nop 0\n\tbuffer_load_dword %1, %2, 0 offen sc1 lds"
                   :: "s"(lds_addr(smem + LDS_DUMMY) + wid * 1024 + 512), "v"(o), "s"(mk_rsrc(wsp + WS_DN)) : "memory");
    }
    WAIT_VM(1);
    if (wid == 0) { CALL_PG(1); CALL_PG(8); CALL_PG(5); }
    else if (wid == 1) { CALL_PG(2); CALL_PG(9); CALL_PG(6); }
    else if (wid == 2) { CALL_PG(3); CALL_PG(10); CALL_PG(7); }
    else { CALL_PG(4); CALL_PG(11); }
    asm volatile("s_barrier\n\ts_waitcnt lgkmcnt(0)" : "+s"(dmy) :: "memory");
  }
  PAGE(8);
  epi_setup();
  const int wr = wid >> 1, wc = wid & 1;
  const int sw = (fr >> 1) & 7;
  const int ko0 = ((0 + fq) ^ sw) * 16, ko1 = ((4 + fq) ^ sw) * 16;
  const int mrow = (fr >> 2) * 8 + (fr & 3);
  bf16x8 fm[2][4], fn[2][4];
  {
    const char* pm = smem + (wr * 64 + mrow) * 128;
    const char* pn = smem + 16384 + (wc * 64 + mrow) * 128;
    f32x4 acc[4][4];
#pragma unroll
    for (int i = 0; i < 4; ++i)
#pragma unroll
      for (int jn = 0; jn < 4; ++jn)
#pragma unroll
        for (int q = 0; q < 4; ++q) acc[i][jn][q] = 0.f;
#define TO(i) (((i) >> 1) * 4096 + ((i) & 1) * 512)
#define LDSP(T, a) ((__attribute__((address_space(3))) T*)(size_t)(a))
#define SLOTS(n) _Pragma("unroll") for (int q = 0; q < (n); ++q)
#define MFA(c, a, b) asm volatile("v_mfma_f32_16x16x32_bf16 %0, %1, %2, %0" : "+v"(c) : "v"(a), "v"(b))
#define MFA2(c, a, b, a2, b2) asm volatile("v_mfma_f32_16x16x32_bf16 %0, %1, %2, %0\n\tv_mfma_f32_16x16x32_bf16 %0, %3, %4, %0" : "+v"(c) : "v"(a), "v"(b), "v"(a2), "v"(b2))
    unsigned lm[2][2], ln[2][2];
#pragma unroll
    for (int u = 0; u < 2; ++u)
#pragma unroll
      for (int kk = 0; kk < 2; ++kk) {
        lm[u][kk] = lds_addr(pm) + u * 2 * G1_STAGE + (kk ? ko1 : ko0);
        ln[u][kk] = lds_addr(pn) + u * 2 * G1_STAGE + (kk ? ko1 : ko0);
        asm volatile("" : "+v"(lm[u][kk]), "+v"(ln[u][kk]));
      }
    auto ldm = [&](int stg, int i, int kk) __attribute__((always_inline)) { fm[kk][i] = *LDSP(const bf16x8, lm[stg >> 1][kk] + (stg & 1) * G1_STAGE + TO(i)); };
    auto ldn = [&](int stg, int jn, int kk) __attribute__((always_inline)) { fn[kk][jn] = *LDSP(const bf16x8, ln[stg >> 1][kk] + (stg & 1) * G1_STAGE + TO(jn)); };
    auto quad = [&](int a, int c, int d, int s) __attribute__((always_inline)) {
      const int i = (s < 2 || s >= 6) ? a : a + 1, jn = s < 4 ? c : d, kk = (s == 1 || s == 2 || s == 5 || s == 6);
      MFA(acc[i][jn], fm[kk][i], fn[kk][jn]);
    };
    auto quad2 = [&](int a, int c, int d, int s) __attribute__((always_inline)) {
      const int i = (s < 2 || s >= 6) ? a : a + 1, jn = s < 4 ? c : d, kk = (s == 2 || s == 6);
      MFA2(acc[i][jn], fm[kk][i], fn[kk][jn], fm[1 - kk][i], fn[1 - kk][jn]);
    };
    auto half1 = [&](int stg) __attribute__((always_inline)) {
      const int ja = (stg & 1) * 2, jb = 2 - ja;
      SLOTS(8) {
        quad(0, ja, ja + 1, q);
        if (q < 4) ldn(stg, jb + (q >> 1), q & 1); else ldm(stg, 2 + ((q - 4) >> 1), q == 5 || q == 6);
        SB();
      }
      SLOTS(4) { quad2(0, jb, jb + 1, 2 * q); SB(); }
    };
    auto half2 = [&](int stg) __attribute__((always_inline)) {
      const int ja = (stg & 1) * 2, jb = 2 - ja, nx = (stg + 1) & 3;
      SLOTS(8) {
        quad(2, jb + 1, jb, q);
        if (!(q & 1)) ldm(nx, q >> 2, q == 2 || q == 4);
        SB();
      }
      SLOTS(8) {
        quad(2, ja, ja + 1, q);
        if (!(q & 1)) ldn(nx, jb + (q >> 2), (q >> 1) & 1);
        SB();
      }
    };
    SLOTS(16) asm volatile("" : "+v"(acc[q >> 2][q & 3]));
    SB();
    SLOTS(4) { ldm(0, q >> 1, q & 1); ldn(0, q >> 1, q & 1); }
    SB();
#pragma unroll 1
    for (int k4 = 0; k4 < G1_NKT - 4; k4 += 4) {
      half1(0); BAR_LGKM();
      SB(); half2(0);
      half1(1); BAR_LGKM();
      SB(); half2(1);
      half1(2); BAR_LGKM();
      SB(); half2(2);
      half1(3); BAR_LGKM();
      SB(); half2(3);
    }
    bf16x8 gm[2][4], gn[2][8];
    unsigned am[2][2], an[2][2];
#pragma unroll
    for (int u = 0; u < 2; ++u)
#pragma unroll
      for (int kk = 0; kk < 2; ++kk) {
        am[u][kk] = lds_addr(pm) + (u ? 3 : 1) * G1_STAGE + (kk ? ko1 : ko0);
        an[u][kk] = lds_addr(pn) + (u ? 3 : 1) * G1_STAGE + (kk ? ko1 : ko0);
        asm volatile("" : "+v"(am[u][kk]), "+v"(an[u][kk]));
      }
    unsigned al = lds_addr(smem + LDS_MB) + (wr * 32 + fq * 8) * 4, ac = lds_addr(smem + LDS_MB) + (256 + wc * 64 + mrow) * 4;
    unsigned ae = lds_addr(smem + G2_MRING) + (wc * 64 + mrow) * 128 + (((wr * 4 + fq) ^ sw) * 16);
    asm volatile("" : "+v"(al), "+v"(ac), "+v"(ae));
    auto rdg = [&](int set, int t, int h, int r) __attribute__((always_inline)) {
      const int u = t == 15, o = t == 14 ? G1_STAGE : 0;
      if (r == 0 || r == 2 || r == 4 || r == 5) {
        const int i = r >= 4, kk = r == 2 || r == 4;
        gm[set][kk * 2 + i] = *LDSP(const bf16x8, am[u][kk] + o + h * 4096 + i * 512);
      } else {
        const int jn = r < 4 ? 0 : (r - 4) >> 1, kk = r < 4 ? r >> 1 : r & 1;
        gn[set][kk * 4 + jn] = *LDSP(const bf16x8, an[u][kk] + o + TO(jn));
      }
    };
    auto mmg = [&](int set, int h, int q) __attribute__((always_inline)) {
      const int jn = q >> 2, r = q & 3, i = (r >> 1) ^ (jn & 1), kk = r == 1 || r == 2;
      MFA(acc[h * 2 + i][jn], gm[set][kk * 2 + i], gn[set][kk * 4 + jn]);
    };
    f32x4 rsv[2], mrv[2];
    float cev[4], tc[32];
    auto rds = [&](int h, int r) __attribute__((always_inline)) {
      if (r < 2) mrv[r] = *LDSP(const f32x4, al + h * 256 + r * 16); else rsv[r - 2] = *LDSP(const f32x4, al + 512 + h * 256 + (r - 2) * 16);
    };
    auto prep = [&](int n) __attribute__((always_inline)) {
      tc[n] = llvm_fmul_legacy(mrv[(n >> 2) & 1][n & 3], cev[n >> 3]);
      asm volatile("" : "+v"(tc[n]));
    };
    u32x4 ep;
    auto epi = [&](int h, int q) __attribute__((always_inline)) {
      const int jn = q >> 1, i = q & 1;
      asm volatile("v_fmac_f32 %0, %4, %8\n\tv_fmac_f32 %1, %5, %9\n\tv_fmac_f32 %2, %6, %10\n\tv_fmac_f32 %3, %7, %11"
                   : "+v"(tc[q * 4]), "+v"(tc[q * 4 + 1]), "+v"(tc[q * 4 + 2]), "+v"(tc[q * 4 + 3])
                   : "v"(acc[h * 2 + i][jn][0]), "v"(acc[h * 2 + i][jn][1]), "v"(acc[h * 2 + i][jn][2]), "v"(acc[h * 2 + i][jn][3]),
                     "v"(rsv[i][0]), "v"(rsv[i][1]), "v"(rsv[i][2]), "v"(rsv[i][3]));
      ep[i * 2] = pk2(tc[q * 4], tc[q * 4 + 1]); ep[i * 2 + 1] = pk2(tc[q * 4 + 2], tc[q * 4 + 3]);
      if (i) *LDSP(u32x4, ae + m_off(h) + TO(jn)) = ep;
    };
    SB();
    half1(0);
    BAR_LGKM();
    SB();
    SLOTS(16) {
      if (q < 8) quad(2, 3, 2, q); else quad(2, 0, 1, q - 8);
      if (q < 12) rdg(0, 13, 0, q);
      SB();
    }
    BAR();
    SB();
    SLOTS(16) {
      mmg(0, 0, q);
      if (q < 12) rdg(1, 14, 0, q);
      else { rds(0, q - 12); cev[q - 12] = *LDSP(const float, ac + (((q - 12) >> 1) * 32 + ((q - 12) & 1) * 4) * 4); }
      SB();
    }
    PAGE(9);
    BAR();
    SB();
    SLOTS(16) { mmg(1, 0, q); if ((q & 3) < 3) rdg(0, 15, 0, (q >> 2) * 3 + (q & 3)); if (q >= 8) { prep(2 * q - 16); prep(2 * q - 15); } SB(); }
    SLOTS(16) { mmg(0, 0, q); if ((q & 3) < 3) rdg(1, 13, 1, (q >> 2) * 3 + (q & 3)); prep(16 + q); SB(); if (q >= 9 && (q & 1)) { epi(0, (q - 9) >> 1); SB(); } }
    SLOTS(8) { mmg(1, 1, q); if (q < 6) { rdg(0, 14, 1, 2 * q); rdg(0, 14, 1, 2 * q + 1); } SB(); if (q & 1) { epi(0, 4 + (q >> 1)); SB(); } }
    BAR_LGKM();
    SB();
    SLOTS(8) { mmg(1, 1, 8 + q); if (q < 4) rdg(1, 15, 1, q == 0 ? 1 : q == 1 ? 3 : q + 4); else rds(1, q - 4); SB(); }
    SLOTS(16) { mmg(0, 1, q); if (q < 8) rdg(1, 15, 1, q == 0 ? 0 : q == 1 ? 2 : q < 4 ? q + 2 : q + 4); prep(q); SB(); }
    asm volatile("s_waitcnt lgkmcnt(0)" ::: "memory");
    if (lane == 0) asm volatile("ds_add_u32 %0, %1" :: "v"(lds_addr(smem + LDS_SEL + 12)), "v"(1) : "memory");
    SB();
    SLOTS(16) {
      mmg(1, 1, q);
      if (q < 4) fn[0][q] = *(const bf16x8*)(smem + (wr * 128 + fr) * 128 + (q >> 1) * 2048 + ((q & 1) ? ko1 : ko0));
      else if (q < 8) fm[q & 1][(q - 4) >> 1] = *(const bf16x8*)(smem + G2_MRING + m_off(0) + (wc * 64 + mrow) * 128 + TO((q - 4) >> 1) + ((q & 1) ? ko1 : ko0));
      prep(16 + q);
      SB();
    }
    {
      lds_vi rel = (lds_vi)(smem + LDS_SEL + 12);
      int it = 0;
      while (rel[0] != 4 && it < SPIN_MAX * 8) ++it;
      asm volatile("" ::: "memory");
    }
    asm volatile("s_nop 7");
    SB();
    SLOTS(8) { epi(1, q); SB(); }
  }

  const char* pn = smem + (wr * 128 + fr) * 128;
  const char* pm = smem + G2_MRING + (wc * 64 + mrow) * 128;
  f32x4 acc[8][4];
  auto mm = [&](bool imaj, int set, int q2, int s, int mode) __attribute__((always_inline)) {
    int i, jj, kk;
    if (imaj) { i = s >> 2; const int r = s & 3; jj = (r >> 1) ^ (i & 1); kk = (r == 1 || r == 2); }
    else { jj = s >> 3; const int r = s & 7; i = jj ? 3 - (r >> 1) : (r >> 1); kk = ((r + 1) >> 1) & 1; }
    if (mode == 1 && !(s & 1)) asm volatile("v_mfma_f32_16x16x32_bf16 %0, %1, %2, 0" : "=&v"(acc[q2 * 2 + jj][i]) : "v"(fm[kk][i]), "v"(fn[set][jj * 2 + kk]));
    else if (mode) asm volatile("v_mfma_f32_16x16x32_bf16 %0, %1, %2, %0" : "+v"(acc[q2 * 2 + jj][i]) : "v"(fm[kk][i]), "v"(fn[set][jj * 2 + kk]));
    else acc[q2 * 2 + jj][i] = __builtin_amdgcn_mfma_f32_16x16x32_bf16(fm[kk][i], fn[set][jj * 2 + kk], acc[q2 * 2 + jj][i], 0, 0, 0);
  };
  unsigned bn[2], bm[2], an[2], am[2];
#pragma unroll
  for (int kk = 0; kk < 2; ++kk) {
    bn[kk] = lds_addr(pn) + (kk ? ko1 : ko0); bm[kk] = lds_addr(pm) + (kk ? ko1 : ko0);
    asm volatile("" : "+v"(bn[kk]), "+v"(bm[kk]));
    an[kk] = bn[kk]; am[kk] = bm[kk] + m_off(0);
  }
#define RDN(jt, kk, o) (*LDSP(const bf16x8, an[kk] + (o) + (jt) * 2048))
#define RDM(i, kk, o) (*LDSP(const bf16x8, am[kk] + (o) + TO(i)))
  PAGE(10);
  tag = __builtin_amdgcn_readfirstlane(((const unsigned*)(smem + LDS_SEL))[5]);
  {
    int son = 0;
    auto tile = [&](int kt, int zero) __attribute__((always_inline)) {
      SLOTS(16) {
        mm(true, 0, 0, q, zero);
        if (!(q & 1)) {
          if (q < 8) fm[(q >> 1) & 1][2 + (q >> 2)] = RDM(2 + (q >> 2), (q >> 1) & 1, 0);
          else { const int r = (q - 8) >> 1; fn[1][r] = RDN(2 + (r >> 1), r & 1, 0); }
        }
        SB();
      }
      SLOTS(16) {
        mm(false, 1, 1, q, zero);
        if (!(q & 1) && q < 8) fn[0][q >> 1] = RDN(4 + (q >> 2), (q >> 1) & 1, 0);
        SB();
      }
      const int som = son >> 1;
      son = son == 65536 ? 0 : son + 32768;
      SLOTS(16) {
        mm(false, 0, 2, q, zero);
        if (!(q & 1) && q < 8) fn[1][q >> 1] = RDN(6 + (q >> 2), (q >> 1) & 1, 0);
        if (q == 9 || q == 11) { an[q == 11] = bn[q == 11] + son; asm volatile("" : "+v"(an[q == 11])); }
        if (q == 13 || q == 15) { am[q == 15] = bm[q == 15] + som; asm volatile("" : "+v"(am[q == 15])); }
        SB();
      }
      BAR_LGKM();
      SB();
      SLOTS(16) {
        mm(true, 1, 3, q, zero);
        if (!(q & 1)) {
          if (q < 8) fn[0][q >> 1] = RDN(q >> 2, (q >> 1) & 1, 0);
          else fm[(q >> 1) & 1][(q - 8) >> 2] = RDM((q - 8) >> 2, (q >> 1) & 1, 0);
        }
        SB();
      }
    };
    SB();
    tile(0, 1);
#pragma unroll 1
    for (int kt = 1; kt < G2_NKT - 3; ++kt) tile(kt, 0);
  }
  {
    constexpr int NG = 12;
    static constexpr int g_t[NG] = {0, 0, 1, 1, 2, 2, 2, 1, 0, 0, 1, 2};
    static constexpr int g_q[NG] = {0, 1, 0, 1, 0, 1, 2, 2, 2, 3, 3, 3};
    constexpr int G14 = 1, G15 = 3, GP0 = 5, GP1 = 6;
    constexpr auto chp = [](int g) { return g == 0 || g_t[g] != g_t[g - 1]; };
    constexpr auto chn = [](int g) { return g + 1 < NG && g_t[g + 1] != g_t[g]; };
    static_assert(n_off(13) == 32768 && n_off(14) == 65536 && n_off(15) == 0, "");
    unsigned an2[2];
#pragma unroll
    for (int kk = 0; kk < 2; ++kk) {
      an[kk] = bn[kk]; an2[kk] = bn[kk] + 65536; am[kk] = bm[kk];
      asm volatile("" : "+v"(an[kk]), "+v"(an2[kk]), "+v"(am[kk]));
    }
    int vo;
    u32x4 pk;
    u32x2 kv;
    constexpr int NF0 = 152, F0 = 160, F1 = 172, F2 = 188;
    auto publish = [&](int d, int h) __attribute__((always_inline)) {
      if (h == 0) { vo = lane; PINV(vo); vo = vo * 16 + (((d + kq + 1) & 3) * 4 + kq) * 4 * MSG; PINV(vo); }
      const f32x4 v = acc[2 * d + (h >> 2)][h & 3];
      pk[(h & 1) * 2] = pk2(v[0], v[1]); pk[(h & 1) * 2 + 1] = pk2(v[2], v[3]);
      if (h & 1) __builtin_amdgcn_raw_buffer_store_b128(pk, rs, vo + (h >> 1) * 1024, 0, 16);
    };
    int fo = lane == 0 ? kq * 512 : (int)0x80000000;
#define MSG_FLAG(d) __builtin_amdgcn_raw_buffer_store_b32(tag, rc, fo + (((d) + kq + 1) & 3) * 2048, 0, 16)
    auto rdn = [&](int g, int r) __attribute__((always_inline)) {
      const int t = g_t[g], jt = g_q[g] * 2 + (r >> 1);
      fn[g & 1][r] = *LDSP(const bf16x8, (t == 1 ? an2[r & 1] : an[r & 1]) + (t == 0 ? 32768 : 0) + jt * 2048);
    };
    auto rdm = [&](int t, int i, int kk) __attribute__((always_inline)) { fm[kk][i] = *LDSP(const bf16x8, am[kk] + m_off(13 + t) + TO(i)); };
    auto group = [&](auto GI) __attribute__((always_inline)) {
      constexpr int gi = decltype(GI)::value;
      if (gi == G14) { BAR();
        SB(); }
      if (gi == 5) { BAR(); SB(); }
      if (gi == G15) {
        PAGE(11);
        BAR();
        SB();
        xq[0] = ld16_s<0>(x, xo); xq[1] = ld16_s<64>(x, xo);
        sq = ld8_s_sc1(stat, xo >> 11 << 3);
        kv = *(const u32x2*)(smem + LDS_SEL);
        SB();
      }
#pragma unroll
      for (int q = 0; q < 16; ++q) {
        const int n = gi * 16 + q;
        if (n == NF0) {
          wait_xq<12>(xq, sq);
          key[0] = __builtin_amdgcn_readfirstlane(kv.x); key[1] = __builtin_amdgcn_readfirstlane(kv.y);
          nf_pre();
          SB();
        }
        if (n == 112) {
        }
        if (n == 152) {
        }
        if (n == F0) {
          WAIT_VM(8); MSG_FLAG(0); SB(); }
        if (n == F1) { WAIT_VM(5); MSG_FLAG(1); SB(); }
        if (n == F2) {
          WAIT_VM(2);
          MSG_FLAG(2); fx = bld4_sc1(rc, po); SB(); }
        mm(chp(gi) || chn(gi), gi & 1, g_q[gi], q, 2);
        constexpr bool ni = gi + 1 < NG && (chp(gi + 1 < NG ? gi + 1 : gi) || chn(gi + 1 < NG ? gi + 1 : gi));
        auto nr = [](int r) { return ni && r >= 2 ? 5 - r : r; };
        if constexpr (chp(gi) && chn(gi)) {
          if (q < 4) rdm(g_t[gi], 2 + (q >> 1), q & 1); else if (q < 8) rdn(gi + 1, nr(q - 4)); else if (q < 12) rdm(g_t[gi + 1], (q - 8) >> 1, q & 1);
        } else if constexpr (chp(gi)) {
          if (q < 4) rdm(g_t[gi], 2 + (q >> 1), q & 1);
          else if (gi + 1 < NG && q < 12 && !(q & 1)) rdn(gi + 1, nr((q - 4) >> 1));
        } else if constexpr (chn(gi)) {
          if (!(q & 1)) { if (q < 8) rdn(gi + 1, nr(q >> 1)); else rdm(g_t[gi + 1], (q - 8) >> 2, (q >> 1) & 1); }
        } else {
          if (!(q & 1) && q < 8) rdn(gi + 1, nr(q >> 1));
        }
        if (gi == GP0 && q < 8) publish(0, q);
        if (gi == GP1 && q < 8) publish(1, q);
        if (gi == 9 && q < 8) publish(2, q);
        if (n >= NF0 && n < NF0 + 16) nf_step(n - NF0);
        SB();
      }
    };
#define GR4(i) group(IC<i>{}); group(IC<i + 1>{}); group(IC<i + 2>{}); group(IC<i + 3>{});
    GR4(0) GR4(4) GR4(8)
    asm volatile("s_nop 7");
    SB();
  }
#pragma unroll
  for (int i = 0; i < 4; ++i) *(f32x4*)(smem + LDS_ACC + wr * 16384 + wc * 4096 + i * 1024 + lane * 16) = acc[7][i];
#pragma unroll
  for (int i = 0; i < 4; ++i) ya[i] = acc[6][i];
  asm volatile("s_waitcnt lgkmcnt(0)" ::: "memory");
  if (lane == 0) sig[0] = 2;
  }

  PAGE(6);
  const unsigned rel0 = 0xffff - (key[0] & 0xffffu) - trow, rel1 = 0xffff - (key[1] & 0xffffu) - trow;
  if (min(rel0, rel1) < 16) {
#pragma unroll 1
    for (int c = 0; c < 2; ++c) {
      const unsigned rel = c ? rel1 : rel0;
      if (rel >= 16) continue;
      lds_vi cnt = (lds_vi)(smem + LDS_SEL + 24 + c * 4);
      int it = 0;
      while (cnt[0] != 4 && it < SPIN_MAX * 8) { __builtin_amdgcn_s_sleep(1); ++it; }
      asm volatile("" ::: "memory");
      if (rel == fr) {
        const float* fp = (const float*)(smem + LDS_FIN + c * 512) + (wid & 1) * 64 + fq * 8;
#pragma unroll
        for (int u4 = 0; u4 < 4; ++u4) {
          const f32x4 v = *(const f32x4*)(fp + (u4 >> 1) * 32 + (u4 & 1) * 4);
#pragma unroll
          for (int k = 0; k < 4; ++k) nf[u4 * 4 + k] += v[k];
        }
      }
    }
  }
  u32x4 pp[3][2];
  unsigned e_lo = 0x00003f80u, e_hi = 0x3f800000u;
  asm volatile("" : "+v"(e_lo), "+v"(e_hi));
  __amdgpu_buffer_rsrc_t rs_ = rs, rc_ = rc, ro_ = mk_rsrc(out);
  int mo[3] = {MSG_IDX(0) * 4 * MSG, MSG_IDX(1) * 4 * MSG, MSG_IDX(2) * 4 * MSG}, mv = lane * 16 + ha * 2048, mv2 = mv + 1024;
  asm volatile("" : "+s"(rs_), "+s"(rc_), "+s"(ro_), "+s"(mo[0]), "+s"(mo[1]), "+s"(mo[2]));
  unsigned as_ = lds_addr((const void*)smem) + LDS_SIG + (wid & 3) * 4, aa_ = lds_addr(smem + LDS_ACC) + (wid & 2) * 8192 + (wid & 1) * 4096 + lane * 16;
  int xs = ((trow + (lane >> 3)) * D_ + et * 128 + (wid & 1) * 64) * 2 + (lane & 7) * 16, xs2 = xs + 8 * D_ * 2;
  const unsigned mt = lds_addr(smem) + (wid & 2) * 8192 + (ha ? LDS_ACC + (wid & 1) * 4096 : n_off(14) + (wid & 1) * 2048);
  unsigned aw_ = mt + fr * 128 + ((fq ^ (fr & 7)) * 16), aw2 = aw_ ^ 64, ar_ = mt + (lane >> 3) * 128 + (((lane ^ (lane >> 3)) & 7) * 16);
  asm volatile("" : "+v"(mv), "+v"(mv2), "+v"(as_), "+v"(aa_), "+v"(xs), "+v"(xs2), "+v"(aw_), "+v"(aw2), "+v"(ar_));
  {
    unsigned sv_, it_ = SPIN_MAX * 8;
    asm volatile("s_cmp_eq_u32 %9, 0\n\ts_cbranch_scc1 2f\n"
                 "1:\n\tds_read_b32 %0, %10\n\ts_waitcnt lgkmcnt(0)\n\tv_cmp_eq_u32 vcc, 2, %0\n\ts_cbranch_vccnz 3f\n\ts_sleep 1\n\ts_sub_u32 %1, %1, 1\n\ts_cbranch_scc0 1b\n"
                 "3:\n\tds_read_b128 %4, %11\n\tds_read_b128 %5, %11 offset:1024\n\tds_read_b128 %6, %11 offset:2048\n\tds_read_b128 %7, %11 offset:3072\n"
                 "2:\n\ts_waitcnt vmcnt(0)\n\tv_cmp_eq_u32 vcc, %8, %3\n\ts_mov_b32 %2, vcc_lo"
                 : "=&v"(sv_), "+s"(it_), "=&s"(seen), "+v"(fx), "+v"(ya[0]), "+v"(ya[1]), "+v"(ya[2]), "+v"(ya[3])
                 : "s"(tag), "s"(ha), "v"(as_), "v"(aa_) : "vcc", "scc", "memory");
  }
#define MSG_POLL(m) do { \
    if (!((seen >> (m)) & 1)) { \
      unsigned pv_, it_ = SPIN_MAX; \
      asm volatile("1:\n\tbuffer_load_dword %0, %3, %4, 0 offen sc1\n\ts_waitcnt vmcnt(0)\n\tv_cmp_eq_u32 vcc, %5, %0\n\ts_mov_b32 %1, vcc_lo\n\t" \
                   "s_bitcmp1_b32 %1, %6\n\ts_cbranch_scc1 2f\n\ts_sub_u32 %2, %2, 1\n\ts_cbranch_scc1 2f\n\ts_sleep 1\n\ts_branch 1b\n2:" \
                   : "=&v"(pv_), "+s"(seen), "+s"(it_) : "v"(po), "s"(rc_), "s"(tag), "i"(m) : "vcc", "scc", "memory"); \
    } \
    pp[m][0] = __builtin_amdgcn_raw_buffer_load_b128(rs_, mv, mo[m], 16); \
    pp[m][1] = __builtin_amdgcn_raw_buffer_load_b128(rs_, mv2, mo[m], 16); } while (0)
#define MSG_ADD(m) do { \
    _Pragma("unroll") for (int u = 0; u < 8; ++u) { \
      ya[u >> 1][(u & 1) * 2] = add_bf(pp[m][u >> 2][u & 3], e_lo, ya[u >> 1][(u & 1) * 2]); ya[u >> 1][(u & 1) * 2 + 1] = add_bf(pp[m][u >> 2][u & 3], e_hi, ya[u >> 1][(u & 1) * 2 + 1]); } } while (0)
  MSG_POLL(2); MSG_POLL(1); MSG_POLL(0);
  asm volatile("s_waitcnt lgkmcnt(0)" : "+v"(ya[0]), "+v"(ya[1]), "+v"(ya[2]), "+v"(ya[3]) :: "memory");
  SB();
#pragma unroll
  for (int e = 0; e < 16; ++e) { float t = ya[e >> 2][e & 3] + nf[e]; PINV(t); ya[e >> 2][e & 3] = t; }
  SB();
  MSG_ADD(2); MSG_ADD(1);
  asm volatile("" : "+v"(ya[0]), "+v"(ya[1]), "+v"(ya[2]), "+v"(ya[3]));
  SB();
  MSG_ADD(0);
  {
    unsigned ov[8];
    SB();
#pragma unroll
    for (int u = 0; u < 8; ++u) ov[u] = pk2(ya[u >> 1][(u & 1) * 2], ya[u >> 1][(u & 1) * 2 + 1]);
    SB();
    *LDSP(u32x4, aw_) = (u32x4){ov[0], ov[1], ov[2], ov[3]};
    *LDSP(u32x4, aw2) = (u32x4){ov[4], ov[5], ov[6], ov[7]};
    asm volatile("" ::: "memory");
    __builtin_amdgcn_raw_buffer_store_b128(*LDSP(const u32x4, ar_), ro_, xs, 0, 16);
    __builtin_amdgcn_raw_buffer_store_b128(*LDSP(const u32x4, ar_ + 1024), ro_, xs2, 0, 16);
  }
}

extern "C" void kernel_launch(void* const* d_in, const int* in_sizes, int n_in, void* d_out, int out_size,
                              void* d_ws, size_t ws_size, hipStream_t stream) {
  const u16* x = (const u16*)d_in[0];
  const u16* ws_re = (const u16*)d_in[1];
  const u16* ws_im = (const u16*)d_in[2];
  const u16* wd_re = (const u16*)d_in[3];
  const u16* wd_im = (const u16*)d_in[4];
  const u16* w_r = (const u16*)d_in[6];
  const u16* w_gate = (const u16*)d_in[7];
  const u16* w_up = (const u16*)d_in[8];
  const u16* w_down = (const u16*)d_in[9];
  k_main<<<256, 512, 0, stream>>>(x, 0x7fffffffu, 0x00020000u, wd_re, 0x7fffffffu, 0x00020000u, wd_im, 0x7fffffffu, 0x00020000u, w_r, (char*)d_ws,
                                  ws_re, ws_im, w_gate, w_up, w_down, (u16*)d_out);
}
```

```cpp
#include <hip/hip_runtime.h>
#include <hip/hip_bf16.h>
#include <stdint.h>
#include <type_traits>

extern "C" __device__ unsigned long long llvm_dispatch_id(void) __asm("llvm.amdgcn.dispatch.id");
extern "C" __device__ float llvm_fmul_legacy(float, float) __asm("llvm.amdgcn.fmul.legacy");
typedef unsigned short u16;
typedef unsigned long long u64;
typedef __attribute__((ext_vector_type(8))) short bf16x8;
typedef __attribute__((ext_vector_type(4))) unsigned u32x4;
typedef __attribute__((ext_vector_type(2))) unsigned u32x2;
typedef __attribute__((ext_vector_type(4))) float f32x4;
typedef __attribute__((ext_vector_type(2))) float f32x2;
typedef __attribute__((ext_vector_type(2))) __bf16 bf16x2;
typedef __attribute__((address_space(3))) void* lds_vp;
typedef volatile __attribute__((address_space(3))) int* lds_vi;

constexpr int S_ = 2048, D_ = 1024, F_ = 3584;
template <int N> using IC = std::integral_constant<int, N>;

static __device__ __forceinline__ float bf2f(u16 u) { return __uint_as_float((unsigned)u << 16); }
static __device__ __forceinline__ u16 f2bf(float f) {
  __hip_bfloat16 b = __float2bfloat16(f);
  return *reinterpret_cast<u16*>(&b);
}
static __device__ __forceinline__ float rbf(float f) { return bf2f(f2bf(f)); }
static __device__ __forceinline__ float lo_bf(unsigned u) { return __uint_as_float(u << 16); }
static __device__ __forceinline__ float hi_bf(unsigned u) { return __uint_as_float(u & 0xffff0000u); }
static __device__ __forceinline__ unsigned pk2(float a, float b) {
  const bf16x2 v = __builtin_convertvector((f32x2){a, b}, bf16x2);
  return __builtin_bit_cast(unsigned, v);
}
static __device__ __forceinline__ float add_bf(unsigned u, unsigned e, float c) {
  return __builtin_amdgcn_fdot2_f32_bf16(__builtin_bit_cast(bf16x2, u), __builtin_bit_cast(bf16x2, e), c, false);
}
static __device__ __forceinline__ float ln_elem(float x, float mb, float sd, float ri) {
  const float d = rbf(x - mb);
  const float q0 = d * ri;
  return rbf(__builtin_fmaf(__builtin_fmaf(-q0, sd, d), ri, q0));
}
template <int CTRL> static __device__ __forceinline__ float dpp_f(float v) {
  return __int_as_float(__builtin_amdgcn_update_dpp(0, __float_as_int(v), CTRL, 0xf, 0xf, true));
}
template <int CTRL> static __device__ __forceinline__ unsigned dpp_u(unsigned v) {
  return (unsigned)__builtin_amdgcn_update_dpp(0, (int)v, CTRL, 0xf, 0xf, true);
}
static __device__ __forceinline__ float row_sum(float v) {
  v += dpp_f<0xB1>(v);
  v += dpp_f<0x4E>(v);
  v += dpp_f<0x141>(v);
  v += dpp_f<0x140>(v);
  return v;
}
static __device__ __forceinline__ float fold32(float a, float b) {
  const auto r = __builtin_amdgcn_permlane32_swap(__float_as_uint(a), __float_as_uint(b), false, false);
  return __uint_as_float(r[0]) + __uint_as_float(r[1]);
}
static __device__ __forceinline__ float fold16(float a, float b) {
  const auto r = __builtin_amdgcn_permlane16_swap(__float_as_uint(a), __float_as_uint(b), false, false);
  return __uint_as_float(r[0]) + __uint_as_float(r[1]);
}
static __device__ __forceinline__ float rdl(float v, int l) { return __int_as_float(__builtin_amdgcn_readlane(__float_as_int(v), l)); }

#define ST_JB 9
#define WSN 10
#define FSM 0xa2
#define GV_JB 7
#define DG 11
#define B14P 9
#define AB_STRIDE 32
#define SPIN_MAX (1 << 17)

static __device__ __forceinline__ unsigned lds_addr(const void* p) { return (unsigned)(size_t)(lds_vp)p; }

constexpr int MSG = 4096;
constexpr size_t WS_ABT   = 0;
constexpr size_t WS_SLAB  = WS_ABT + (size_t)D_ * 4096 * 2;
constexpr size_t WS_GU    = WS_SLAB + (size_t)64 * 64 * MSG;
constexpr size_t WS_DN    = WS_GU + (size_t)8 * F_ * 16;
constexpr size_t WS_FLAGS = WS_DN + (size_t)2 * 8 * 32 * 64 * 16;
constexpr int FL_X = 0;
constexpr int FL_AB = 64 * 64 * 32;
constexpr int FL_ST = FL_AB + 2048 * AB_STRIDE;
constexpr int FL_CE = FL_ST + 4096;
constexpr int FL_KEY = FL_CE + 4096;
constexpr int FL_EPOCH = FL_KEY + 4096;
constexpr int WS_MB = (int)((WS_FLAGS + (FL_EPOCH + 1) * 4 + 1048575) >> 20);

#define WAIT_VM(n) asm volatile("s_waitcnt vmcnt(" #n ")" ::: "memory")
template <int N> static __device__ __forceinline__ void wait_vm() { asm volatile("s_waitcnt vmcnt(%0)" :: "i"(N) : "memory"); }
#define BAR_LGKM() asm volatile("s_waitcnt lgkmcnt(0)\n\ts_barrier" ::: "memory")
#define BAR() asm volatile("s_barrier" ::: "memory")
#define SB() __builtin_amdgcn_sched_barrier(0)
#define PAGE(n) asm volatile("s_branch 2f\n\t.p2align 12\nk_pg" #n ":\n\ts_setpc_b64 s[42:43]\n2:")
#define CALL_PG(n) asm volatile("s_getpc_b64 s[40:41]\n3:\n\ts_mov_b32 s42, k_pg" #n "-3b\n\ts_ashr_i32 s43, s42, 31\n\ts_add_u32 s40, s40, s42\n\ts_addc_u32 s41, s41, s43\n\t" \
                                "s_swappc_b64 s[42:43], s[40:41]" ::: "s40", "s41", "s42", "s43", "scc", "memory")

static __device__ __forceinline__ __amdgpu_buffer_rsrc_t mk_rsrc(const void* p) {
  return __builtin_amdgcn_make_buffer_rsrc((void*)p, 0, 0x7fffffff, 0x00020000);
}
#define DMA_(rsrc, voff, mbase, MI, sbase, SI, pol) do { unsigned _t; \
    asm volatile("s_add_i32 m0, %2, %3\n\ts_add_i32 %0, %5, %6\n\tbuffer_load_dwordx4 %1, %4, %0 offen" pol " lds" \
                 : "=&s"(_t) : "v"(voff), "s"(mbase), "i"(MI), "s"(rsrc), "s"(sbase), "i"(SI) : "memory", "scc"); } while (0)
#define TOUCH(rsrc, voff, mbase, MI, soff) \
    asm volatile("s_add_i32 m0, %1, %2\n\ts_nop 0\n\tbuffer_load_dword %0, %3, %4 offen lds" \
                 :: "v"(voff), "s"(mbase), "i"(MI), "s"(rsrc), "s"(soff) : "memory", "scc")
template <int OFF> static __device__ __forceinline__ u32x4 ld16_s(const void* p, int voff) {
  u32x4 v;
  asm volatile("global_load_dwordx4 %0, %1, %2 offset:%3" : "=v"(v) : "v"(voff), "s"(p), "i"(OFF) : "memory");
  return v;
}
template <int OFF> static __device__ __forceinline__ void sld(unsigned& sink, const void* p, int soff) {
  asm volatile("s_load_dword %0, %1, %2 offset:%3" : "+s"(sink) : "s"(p), "s"(soff), "i"(OFF) : "memory");
}
static __device__ __forceinline__ unsigned gld4_s(const void* p, unsigned voff) {
  unsigned v;
  asm volatile("global_load_dword %0, %1, %2" : "=v"(v) : "v"(voff), "s"(p) : "memory");
  return v;
}
static __device__ __forceinline__ u32x2 bld8_sc1(__amdgpu_buffer_rsrc_t r, unsigned voff) {
  u32x2 v;
  asm volatile("buffer_load_dwordx2 %0, %1, %2, 0 offen sc1" : "=v"(v) : "v"(voff), "s"(r) : "memory");
  return v;
}
template <int N> static __device__ __forceinline__ void wait_xs(unsigned& a, u32x2& b) { asm volatile("s_waitcnt vmcnt(%2)" : "+v"(a), "+v"(b) : "i"(N) : "memory"); }
template <int N> static __device__ __forceinline__ void wait_xq(u32x4 (&a)[2], u32x2& b) { asm volatile("s_waitcnt vmcnt(%3)" : "+v"(a[0]), "+v"(a[1]), "+v"(b) : "i"(N) : "memory"); }
static __device__ __forceinline__ u32x2 ld8_s_sc1(const void* p, int voff) {
  u32x2 v;
  asm volatile("global_load_dwordx2 %0, %1, %2 sc1" : "=v"(v) : "v"(voff), "s"(p) : "memory");
  return v;
}
static __device__ __forceinline__ void lgkm0(unsigned& sink) { asm volatile("s_waitcnt lgkmcnt(0)" : "+s"(sink) :: "memory"); }
template <typename T> static __device__ __forceinline__ void pin4(T& a, T& b, T& c, T& d) { asm volatile("" : "+s"(a), "+s"(b), "+s"(c), "+s"(d)); }
template <typename T, typename U> static __device__ __forceinline__ void pins(T& a, U& b) { asm volatile("" : "+s"(a), "+s"(b)); }
static __device__ __forceinline__ void launder(int& v, int& a, int& b) { asm volatile("" : "+v"(v)); asm volatile("" : "+s"(a), "+s"(b)); }
static __device__ __forceinline__ unsigned bld4_sc1(__amdgpu_buffer_rsrc_t r, int voff) {
  unsigned v;
  asm volatile("buffer_load_dword %0, %1, %2, 0 offen sc1" : "=v"(v) : "v"(voff), "s"(r) : "memory");
  return v;
}
static __device__ __forceinline__ u32x4 bld16_nt(__amdgpu_buffer_rsrc_t r, int voff, int soff) {
  u32x4 v;
  asm volatile("buffer_load_dwordx4 %0, %1, %2, %3 offen nt" : "=v"(v) : "v"(voff), "s"(r), "s"(soff) : "memory");
  return v;
}
static __device__ __forceinline__ void bld16_nt_io(u32x4& v, __amdgpu_buffer_rsrc_t r, int voff, int soff) {
  asm volatile("buffer_load_dwordx4 %0, %1, %2, %3 offen nt" : "+v"(v) : "v"(voff), "s"(r), "s"(soff) : "memory");
}
static __device__ __forceinline__ u32x4 bld16_sc1(__amdgpu_buffer_rsrc_t r, int voff, int soff) {
  u32x4 v;
  asm volatile("buffer_load_dwordx4 %0, %1, %2, %3 offen sc1" : "=v"(v) : "v"(voff), "s"(r), "s"(soff) : "memory");
  return v;
}
static __device__ __forceinline__ u32x2 granule_wait(const u64* p, unsigned tag) {
  u64 v = 0;
#pragma unroll 1
  for (int it = 0; it < SPIN_MAX; ++it) {
    v = __hip_atomic_load(p, __ATOMIC_RELAXED, __HIP_MEMORY_SCOPE_AGENT);
    if (__all((unsigned)(v >> 32) == tag)) break;
    __builtin_amdgcn_s_sleep(2);
  }
  return (u32x2){(unsigned)v, (unsigned)(v >> 32)};
}
static __device__ __forceinline__ float key_score(unsigned key) {
  const unsigned u = key >> 16;
  return bf2f((u16)((u & 0x8000u) ? (u & 0x7fffu) : (~u & 0xffffu)));
}
static __device__ __forceinline__ void merge2(unsigned& a1, unsigned& a2, unsigned b1, unsigned b2) {
  const unsigned lo = min(a1, b1);
  a1 = max(a1, b1);
  a2 = max(lo, max(a2, b2));
}
static __device__ __forceinline__ float sqdev16(const u32x4 (&v)[2], float mb) {
  f32x2 q2 = {0.f, 0.f};
#pragma unroll
  for (int i = 0; i < 8; ++i) {
    const f32x2 e = (f32x2){lo_bf(v[i >> 2][i & 3]), hi_bf(v[i >> 2][i & 3])} - mb;
    q2 += e * e;
  }
  return q2.x + q2.y;
}

constexpr int G1_NST = 4, G1_NKT = 16, G1_STAGE = 32768;
constexpr int G2_NKT = 16, G2_MRING = 98304;
constexpr int n_off(int k) { return (k % 3) * 32768; }
constexpr int m_off(int k) { return ((k + 2) % 3) * 16384; }
constexpr int k_ord(int k) { return k < 2 ? k : (k < 9 ? 2 * (k - 1) : 2 * (k - 8) + 1); }
constexpr int LDS_DUMMY = G2_MRING + 49152;
constexpr int LDS_GU = LDS_DUMMY + 4096;
constexpr int LDS_FIN = LDS_GU + 4096;
constexpr int LDS_MB = LDS_FIN + 4096;
constexpr int LDS_SEL = LDS_MB + 1536;
constexpr int LDS_SIG = LDS_SEL + 64;
constexpr int LDS_KEY = LDS_SIG + 32;
constexpr int LDS_BYTES = LDS_KEY + 128;
constexpr int LDS_ACC = n_off(G2_NKT - 3);

constexpr int tpos[16] = {0, 0, 0, 0, 106, 110, 115, 2, 2, 3, 4, 5, 0, 0, 0, 0};
constexpr int twho[16] = {0, 0, 0, 0, 7, 7, 7, 0, 1, 2, 6, 5, 0, 0, 0, 0};
constexpr int dn_cnt(int q) { return q == 5 ? 3 : q == 8 ? 4 : 0; }
constexpr int dn_first(int q) { return q <= 5 ? 0 : q <= 8 ? 3 : 7; }

static __device__ __forceinline__ void code_touch(unsigned& sink, unsigned off) {
  asm volatile("s_getpc_b64 s[40:41]\n\tk_main_rt:\n\ts_min_u32 %1, %1, k_main_end-k_main_rt-196\n\t"
               "s_load_dword %0, s[40:41], %1 offset:0\n\ts_load_dword %0, s[40:41], %1 offset:64\n\ts_load_dword %0, s[40:41], %1 offset:128\n\ts_load_dword %0, s[40:41], %1 offset:192"
               : "+s"(sink), "+s"(off) :: "s40", "s41", "scc", "memory");
}
static __device__ __forceinline__ void touch1(__amdgpu_buffer_rsrc_t r, int voff, unsigned mb, int soff) { TOUCH(r, voff, mb, LDS_DUMMY, soff); }
typedef const __attribute__((address_space(1))) u16* gptr;
typedef __attribute__((ext_vector_type(8))) u64 u64x8;
extern "C" __global__ __launch_bounds__(512, 2) void k_main(const u16* x, unsigned x2, unsigned x3, const u16* wd_re, unsigned r2, unsigned r3,
                                                 const u16* wd_im, unsigned i2, unsigned i3, const u16* w_r,
                                                 char* wsp, const u16* ws_re, const u16* ws_im,
                                                 const u16* w_gate, const u16* w_up, const u16* w_down, u16* out) {
  __shared__ __attribute__((aligned(16))) char smem[LDS_BYTES];
  u64x8 ka;
  u32x4 rm, rn;
  unsigned kz;
  asm volatile("s_load_dwordx4 %0, %4, 0\n\ts_load_dwordx16 %2, %4, 48\n\ts_andn2_b32 %3, 4, %5\n\ts_lshl_b32 %3, %3, 2\n\ts_load_dwordx4 %1, %4, %3 offset:16"
               : "=&s"(rm), "=&s"(rn), "=&s"(ka), "=&s"(kz) : "s"(__builtin_amdgcn_kernarg_segment_ptr()), "s"(blockIdx.x) : "scc", "memory");
  SB();
  const int tid = threadIdx.x, lane = tid & 63;
  const int wid = __builtin_amdgcn_readfirstlane(tid >> 6);
  const int g = blockIdx.x ^ 6, xcd = g & 7, j = g >> 3;
  if (wid < 4) {
    unsigned co = ((j * 4 + wid) * 4 + (lane & 3)) * 128;
    asm volatile(".subsection 1\nk_main_end:\n.subsection 0\n\tv_min_u32 %0, k_main_end-k_main-132, %0\n\ts_getpc_b64 s[40:41]\n\ts_mov_b32 m0, %1\n\ts_mov_b64 exec, 15\n\t"
                 "global_load_lds_dword %0, s[40:41]\n\ts_mov_b64 exec, -1"
                 : "+v"(co) : "i"(LDS_DUMMY + 768) : "s40", "s41", "memory");
  }
  const int pw = wid - 4;
  const int row0 = (wid & 3) * 8 + (lane >> 3);
  const int cm = ((lane & 7) ^ (((row0 >> 1) & 1) | ((wid & 3) << 1))) * 16;
  int vm = row0 * (D_ * 2) + cm;
  asm volatile("" : "+v"(vm));
  const int kq = xcd >> 1;
  const int tt = (xcd & 1) * 4 + (j >> 3);
  const int et = j & 7;
  const int z = kq >> 1, st = (kq & 1) * 8 + tt;
  unsigned o_m = st * 128 * D_ * 2, o_n = et * 128 * D_ * 2, mb0 = lds_addr(smem) + pw * 1024;
  asm volatile("" : "+s"(o_m), "+s"(o_n), "+s"(mb0));
#define G1_X(kt, so, q) DMA_(rm, vm, mb0, (so) + (q) * 4096, o_m, (kt) * 128 + (((q) & 1) * 64 + ((q) >> 1) * 32) * D_ * 2, "")
#define G1_W(kt, so, q) DMA_(rn, vm, mb0, (so) + 16384 + (q) * 4096, o_n, (kt) * 128 + (q) * 32 * D_ * 2, "")
#define G1_STAGE_TILE(kt, so, W) do { SB(); G1_X(kt, so, 0); W(0); G1_X(kt, so, 1); W(1); G1_X(kt, so, 2); W(2); G1_X(kt, so, 3); W(3); \
    G1_W(kt, so, 0); W(4); G1_W(kt, so, 1); W(5); G1_W(kt, so, 2); W(6); G1_W(kt, so, 3); W(7); SB(); } while (0)
#define G1_STAGE_A(kt, so, W) do { SB(); G1_X(kt, so, 0); W(0); G1_X(kt, so, 2); W(1); \
    G1_W(kt, so, 0); W(2); G1_W(kt, so, 1); W(3); G1_W(kt, so, 2); W(4); G1_W(kt, so, 3); W(5); W(6); W(7); SB(); } while (0)
#define G1_STAGE_B(kt, so) do { SB(); G1_X(kt, so, 1); G1_X(kt, so, 3); SB(); } while (0)
#define NOW(i)
  asm volatile("s_waitcnt lgkmcnt(0)" : "+s"(ka), "+s"(rm), "+s"(rn) :: "memory");
  int lrow, drow, l16;
  unsigned o_l, o_d;
  auto rows = [&](int i) __attribute__((always_inline)) {
    int g_ = g, l_ = lane;
    if (i == 0) { asm volatile("" : "+s"(g_)); drow = (g_ & ~7) + pw * 2; lrow = drow + (g_ & 3) * 512 + (g_ & 4) * 64; asm volatile("" : "+s"(drow), "+s"(lrow)); }
    if (i == 1) { asm volatile("" : "+s"(g_)); drow += (g_ & 3) * 256; o_l = lrow * D_ * 2; o_d = drow * D_ * 2; asm volatile("" : "+s"(drow), "+s"(o_l), "+s"(o_d)); }
    if (i == 2) { asm volatile("" : "+v"(l_)); l16 = l_ * 16; asm volatile("" : "+v"(l16)); }
  };
  if (__builtin_expect(wid >= 4, 1)) {
    G1_STAGE_TILE(0, 0, rows);
    __builtin_amdgcn_s_setprio(3);
  }
  x = (const u16*)(gptr)((u64)rm[0] | (u64)rm[1] << 32);
  const u16* wdz = (const u16*)(gptr)((u64)rn[0] | (u64)rn[1] << 32);
  w_r = (const u16*)(gptr)ka[0]; wsp = (char*)(gptr)ka[1]; ws_re = (const u16*)(gptr)ka[2]; ws_im = (const u16*)(gptr)ka[3];
  w_gate = (const u16*)(gptr)ka[4]; w_up = (const u16*)(gptr)ka[5]; w_down = (const u16*)(gptr)ka[6]; out = (u16*)(gptr)ka[7];
  int* flags = (int*)(wsp + WS_FLAGS);
  u64* stat = (u64*)(flags + FL_ST);
  const int fr = lane & 15, fq = lane >> 4;
  const int ha = wid >> 2;
  int trow, xo, po;
  const char *prs, *prc;
  auto epi_setup = [&]() __attribute__((always_inline)) {
    int t_ = tt, l_ = lane;
    asm volatile("" : "+s"(t_));
    asm volatile("" : "+v"(l_));
    trow = t_ * 256 + ((wid >> 1) & 1) * 128 + kq * 32 + ha * 16;
    xo = ((trow + (l_ & 15)) * D_ + et * 128 + (wid & 1) * 64 + (l_ >> 4) * 8) * 2;
    prs = wsp + WS_SLAB + (size_t)((t_ * 8 + et) * 64 + (wid & 3)) * MSG;
    prc = (const char*)(flags + FL_X + ((t_ * 8 + et) * 64 + (wid & 3)) * 32);
    asm volatile("" : "+s"(trow), "+s"(prs), "+s"(prc));
    po = l_ < 3 ? (kq * 4 + ((kq + 1 + l_) & 3)) * 512 : (int)0x80000000;
    asm volatile("" : "+v"(xo), "+v"(po));
  };
#define rs mk_rsrc(prs)
#define rc mk_rsrc(prc)
  lds_vi sig = (lds_vi)(smem + LDS_SIG + (wid & 3) * 4);
  unsigned tag;
  u32x4 xq[2];
  u32x2 sq;
  f32x4 ya[4];
  unsigned key[2];
  float nf[16], la0, la1;
#define PINV(a) asm volatile("" : "+v"(a))
  auto nf_pre = [&]() __attribute__((always_inline)) {
    if (__builtin_expect(!__all(sq.y == tag), 0)) sq = granule_wait(stat + (xo >> 11), tag);
    const float ri = __builtin_amdgcn_rcpf(hi_bf(sq.x));
    la0 = -(lo_bf(sq.x) * ri); la1 = 1.f + ri;
    PINV(la0); PINV(la1);
  };
  auto nf_step = [&](int e) __attribute__((always_inline)) {
    const int u = e >> 1;
    nf[e] = (e & 1) ? hi_bf(xq[u >> 2][u & 3]) : lo_bf(xq[u >> 2][u & 3]);
    PINV(nf[e]);
    nf[e] = __builtin_fmaf(nf[e], la1, la0);
    PINV(nf[e]);
  };
#define MSG_IDX(m) (kq * 4 + ((kq + 1 + (m)) & 3))
  unsigned seen, fx;

  if (__builtin_expect(wid >= 4, 1)) {
    constexpr int O_ST = (FL_ST - FL_AB) * 4, O_CE = (FL_CE - FL_AB) * 4, O_KEY = (FL_KEY - FL_AB) * 4, O_EP = (FL_EPOCH - FL_AB) * 4;
    const __amdgpu_buffer_rsrc_t rf = mk_rsrc(wsp + WS_FLAGS + FL_AB * 4);
    u32x4 lv[2], lv2[2], lw[2], ld[2], ld2[2];
    unsigned epv;
    {
      const char* xrow = (const char*)x + o_l;
      const char* wdrow = (const char*)wdz + o_d;
      lv[0] = ld16_s<0>(xrow, l16); lv[1] = ld16_s<1024>(xrow, l16);
      lv2[0] = ld16_s<2048>(xrow, l16); lv2[1] = ld16_s<3072>(xrow, l16);
      ld[0] = ld16_s<0>(wdrow, l16); ld[1] = ld16_s<1024>(wdrow, l16);
      ld2[0] = ld16_s<2048>(wdrow, l16); ld2[1] = ld16_s<3072>(wdrow, l16);
      lw[0] = ld16_s<0>(w_r, l16); lw[1] = ld16_s<1024>(w_r, l16);
      asm volatile("buffer_load_dword %0, off, %1, %2 sc1" : "=v"(epv) : "s"(rf), "s"(O_EP) : "memory");
    }
    SB();
    const unsigned long long did = llvm_dispatch_id();
    const unsigned dmix = (unsigned)did * 0x85EBCA6Bu + (unsigned)(did >> 32) * 0xC2B2AE35u + 0x27D4EB2Fu;
    const bool moe = et != 7;
    unsigned epoch;
    const int ci = lane & 15, rq = lane >> 4, tk = (lane >> 3) & 1;
    int mat, cg, dq, d0;
    u32x4 wv[16];
    const u16 *pgu, *pn2;
    const char *pm2, *spb;
#define rw __builtin_amdgcn_make_buffer_rsrc((void*)pgu, 0, moe ? 0x7fffffff : 0, 0x00020000)
#define rn2 mk_rsrc(pn2)
#define rm2 mk_rsrc(pm2)
    int vw, vn2, vm2;
    int ord2;
    auto k_blk = [&](int k) __attribute__((always_inline)) { return k >= 9 ? (ord2 >> (4 * (k - 9))) & 7 : tt ^ (k_ord(k) >> 1); };
#define KROT(kk_) (k_blk(kk_) * 256 + (k_ord(kk_) & 1) * 128)
    int tv2;
    unsigned sd = 0;
    auto touches = [&](auto P) __attribute__((always_inline)) {
      constexpr int p = decltype(P)::value;
      int e = et;
      asm volatile("" : "+s"(e));
#define TCH(k) if constexpr (tpos[k] == p) { if (e == twho[k]) { const int kb = KROT(k); touch1(rn2, tv2, mb0, kb); } }
      TCH(0) TCH(1) TCH(2) TCH(3) TCH(4) TCH(5) TCH(6) TCH(7) TCH(8) TCH(9) TCH(10) TCH(11) TCH(12) TCH(13) TCH(14) TCH(15)
    };
    unsigned mq0, mq1, mq2, mq3;
    const int cg8 = g & 7, rgb = g >> 3;
    const __amdgpu_buffer_rsrc_t rdn = mk_rsrc(w_down);
    const __amdgpu_buffer_rsrc_t rgu = mk_rsrc(wsp + WS_GU);
    int vdn, vgu;
    u32x4 dv[7], gv[4];
    int abo, abq[3];
    unsigned lma;
    unsigned sel[2][4];
    float g0, g1;
    u32x4 kk[4];
    unsigned k1 = 0, k2 = 0;
    const int ko_ = O_KEY + pw * 4096;
#define MM(a, b, c) __builtin_amdgcn_mfma_f32_16x16x32_bf16(__builtin_bit_cast(bf16x8, a), __builtin_bit_cast(bf16x8, b), c, 0, 0, 0)
#define PIN1(a) asm volatile("" : "+v"(a))
#define PIN2(a, b) asm volatile("" : "+v"(a), "+v"(b))
#define PIN3(a, b, c) asm volatile("" : "+v"(a), "+v"(b), "+v"(c))
    {
      int gro, go;
      u32x2 gr;
      u32x4 oh[5];
      f32x4 sa4, gw[2], gx[2];
      float su, gs, ta, tb, sx, mbv;
      unsigned dk[4], kl;
      const int hb = lane >> 5;
#pragma unroll
      for (int l = 0; l < WSN; ++l) asm volatile("" : "=v"(wv[l]));
      auto diag = [&](const f32x4& m) __attribute__((always_inline)) {
        return __uint_as_float((__float_as_uint(m[0]) & dk[0]) | (__float_as_uint(m[1]) & dk[1]) | (__float_as_uint(m[2]) & dk[2]) | (__float_as_uint(m[3]) & dk[3]));
      };
      auto setup = [&](auto JB, auto GP) __attribute__((always_inline)) {
        constexpr int jb = decltype(JB)::value, gp = decltype(GP)::value;
        SB();
        {
          int ln = lane, jj = j, p_ = pw;
          launder(ln, jj, p_);
          const int c_ = ln & 15, r_ = ln >> 4, e_ = jj & 7, t_ = (xcd & 1) * 4 + (jj >> 3);
          if constexpr (jb == 1 && gp == 0) {
            pn2 = ((kq >> 1) ? ws_im : ws_re) + (long)t_ * 256 * S_ + (kq & 1) * 1024;
            spb = (const char*)(pn2 + (e_ * 4 + p_) * 8 * S_);
            pins(pn2, spb);
          }
          if constexpr (jb == 1 && gp >= 1 && gp < 6) {
            const unsigned o = c_ == gp - 1 ? 0x3f803f80u : 0u;
            oh[gp - 1] = (u32x4){o, o, o, o};
            PIN1(oh[gp - 1]);
          }
          if constexpr (jb == 1 && gp == 6) {
            const int gm = moe ? (jj - (jj >> 3)) * 8 + xcd : 0;
            mat = gm / 112; cg = (gm % 112) >> 2; dq = gm & 3; d0 = dq * 256 + p_ * 64;
            pin4(mat, cg, dq, d0);
          }
          if constexpr (jb == 2 && gp == 0) {
            pgu = mat ? w_up : w_gate;
            pm2 = wsp + WS_ABT + ((long)e_ * 128 * 4096 + kq * 1024) * 2;
            pins(pgu, pm2);
            vw = ((d0 + r_ * 16) * F_ + cg * 128 + c_ * 8) * 2;
            PIN1(vw);
          }
          if constexpr (jb == 2 && (gp == 1 || gp == 2)) {
            dk[gp * 2 - 2] = c_ == r_ * 4 + gp * 2 - 2 ? ~0u : 0u; dk[gp * 2 - 1] = c_ == r_ * 4 + gp * 2 - 1 ? ~0u : 0u;
            PIN2(dk[gp * 2 - 2], dk[gp * 2 - 1]);
          }
          if constexpr (jb == 2 && gp == 3) { kl = lrow + (ln >> 5); go = (ln & 30) ? (int)0x80000000 : ((ln & 1) ? O_KEY : O_ST) + (int)kl * 8; PIN2(kl, go); }
          if constexpr (jb == 2 && gp == 4) { kl = 0xffff - kl; go = (ln & ~1) == 2 ? O_CE + (z * D_ + drow + (ln & 1)) * 8 : go; PIN2(kl, go); }
          if constexpr (jb == 2 && gp == 5) {
            gro = (p_ < 2 ? O_ST + (st * 128 + p_ * 64) * 8 : O_CE + (z * D_ + e_ * 128 + (p_ - 2) * 64) * 8) + ln * 8;
            tv2 = (p_ * 64 + ln) * 4096;
            lma = lds_addr(smem + LDS_MB) + (p_ < 2 ? p_ * 256 : 512 + p_ * 256) + ln * 4;
            PIN3(gro, tv2, lma);
          }
          if constexpr (jb == 2 && gp == 6) {
            const u64 lo = (t_ & 2) ? ((t_ & 4) ? 0x0150234601503247ull : 0x0514670205147603ull) : ((t_ & 4) ? 0x0123067401532076ull : 0x0567423005176432ull);
            ord2 = (int)(lo >> ((t_ & 1) * 32));
            pins(ord2, p_);
          }
          if constexpr (jb == 7 && gp == 0) {
            const int r0 = p_ * 8 + (ln >> 3);
            vn2 = r0 * (S_ * 2) + ((ln & 7) ^ ((r0 >> 1) & 7)) * 16; vm2 = r0 * 8192 + (vm & 127);
            PIN2(vn2, vm2);
          }
          if constexpr (jb == 7 && gp == 1) { vdn = jj * 112 + p_ * 28 + r_ * 7; vgu = (((ln >> 3) & 1) * 4 * F_ + vdn + min(ln & 7, 6)) * 16; PIN2(vdn, vgu); }
          if constexpr (jb == 7 && gp == 2) { vdn = (vdn * D_ + cg8 * 128 + c_ * 8) * 2; PIN1(vdn); }
          if constexpr (jb == 7 && gp == 3) {
            abo = (((kq * 8 + e_) * 4 + p_) * 16 + c_) * AB_STRIDE * 4;
            abq[2] = ln >= 16 || (ln & 7) == t_ ? (int)0x80000000 : abo;
            PIN2(abo, abq[2]);
          }
          if constexpr (jb == 7 && gp == 4) {
            abq[1] = ln >= 8 ? (int)0x80000000 : abq[2];
            abq[0] = ((ln ^ t_) & 4) ? (int)0x80000000 : abq[1];
            PIN2(abq[0], abq[1]);
          }
          if constexpr (jb == 7 && gp == 5) {
            const unsigned mb = lds_addr(smem) + p_ * 1024;
            mq0 = mb + ((3 - kq) & 3) * 4096; mq1 = mb + ((4 - kq) & 3) * 4096; mq2 = mb + ((5 - kq) & 3) * 4096; mq3 = mb + ((6 - kq) & 3) * 4096;
            pin4(mq0, mq1, mq2, mq3);
          }
          if constexpr (jb == 8 && gp < 4) {
#pragma unroll
            for (int h = 0; h < 2; ++h) {
              const unsigned hv = h ? 0x0302u : 0x0100u;
              sel[h][gp] = ((ln & 7) >> 1) == gp ? ((ln & 1) ? (hv << 16) | 0x0c0cu : 0x0c0c0000u | hv) : 0x0c0c0c0cu;
            }
            PIN2(sel[0][gp], sel[1][gp]);
          }
        }
        SB();
      };
#define WS1(i) setup(IC<1>{}, IC<i>{})
#define WS2(i) setup(IC<2>{}, IC<i>{})
#define WS3(i) do { setup(IC<7>{}, IC<i>{}); if (i >= 4) setup(IC<8>{}, IC<(i) & 3>{}); } while (0)
      G1_STAGE_TILE(1, G1_STAGE, WS1);
      WAIT_VM(19); BAR();
      G1_STAGE_TILE(2, 2 * G1_STAGE, WS2);
      G1_STAGE_TILE(3, 3 * G1_STAGE, WS3);
      auto work1 = [&](auto JB, auto GP) __attribute__((always_inline)) {
        constexpr int jb = decltype(JB)::value, gp = decltype(GP)::value;
        SB();
        if constexpr (jb == 1) {
          const f32x4 z4 = {0.f, 0.f, 0.f, 0.f};
          if constexpr (gp == 0) { sa4 = MM(lv[0], oh[0], z4); gw[0] = MM(lv[0], lw[0], z4); gx[0] = MM(lv[0], lv[0], z4); PIN3(sa4, gw[0], gx[0]); }
          if constexpr (gp == 1) { sa4 = MM(lv2[0], oh[1], sa4); gw[1] = MM(lv2[0], lw[0], z4); gx[1] = MM(lv2[0], lv2[0], z4); PIN3(sa4, gw[1], gx[1]); }
          if constexpr (gp == 2) { sa4 = MM(lv[1], oh[0], sa4); gw[0] = MM(lv[1], lw[1], gw[0]); gx[0] = MM(lv[1], lv[1], gx[0]); PIN3(sa4, gw[0], gx[0]); }
          if constexpr (gp == 3) { sa4 = MM(lv2[1], oh[1], sa4); gw[1] = MM(lv2[1], lw[1], gw[1]); gx[1] = MM(lv2[1], lv2[1], gx[1]); PIN3(sa4, gw[1], gx[1]); }
          if constexpr (gp == 4) { sa4 = MM(ld[0], oh[2], sa4); sa4 = MM(ld[1], oh[2], sa4); PIN1(sa4); }
          if constexpr (gp == 5) { sa4 = MM(ld2[0], oh[3], sa4); sa4 = MM(ld2[1], oh[3], sa4); PIN1(sa4); }
          if constexpr (gp == 6) { sa4 = MM(lw[0], oh[4], sa4); sa4 = MM(lw[1], oh[4], sa4); PIN1(sa4);
            epoch = __builtin_amdgcn_readfirstlane(epv); tag = ((epoch + 1u) * 0x9E3779B1u ^ dmix) | 1u; }
        }
        if constexpr (jb == 2) {
          if constexpr (gp == 0) { su = (sa4[0] + sa4[1]) + (sa4[2] + sa4[3]); ta = diag(gw[0]); PIN2(su, ta); }
          if constexpr (gp == 1) { su = fold32(su, su); tb = diag(gw[1]); PIN2(su, tb); }
          if constexpr (gp == 2) { su = fold16(su, su); gs = fold32(ta, tb); PIN2(su, gs); }
          if constexpr (gp == 3) { ta = diag(gx[0]); tb = diag(gx[1]); PIN2(ta, tb); }
          if constexpr (gp == 4) { gs = fold16(gs, fold32(ta, tb)); PIN1(gs); }
          if constexpr (gp == 5) { gs += dpp_f<0xB1>(gs); gs += dpp_f<0x4E>(gs); PIN1(gs); }
          if constexpr (gp == 6) { gs += dpp_f<0x141>(gs); gs += dpp_f<0x140>(gs); PIN1(gs); }
        }
        if constexpr (jb == 3) {
          if constexpr (gp == 0) {
            const float s0 = rdl(su, 0), s1 = rdl(su, 1), q0 = rdl(gs, 16), q1 = rdl(gs, 48);
            sx = hb ? s1 : s0; ta = hb ? q1 : q0; PIN2(sx, ta);
          }
          if constexpr (gp == 1) {
            const float mean = sx * (1.0f / D_);
            mbv = rbf(mean);
            ta -= sx * mean;
            if (__builtin_expect(__any(!(sx * mean <= 16.f * ta)), 0)) {
              float q = fold32(sqdev16(lv, rdl(mbv, 0)), sqdev16(lv2, rdl(mbv, 32)));
              q = row_sum(fold16(q, q));
              const float dm = mean - mbv;
              ta = fmaxf(q - (float)D_ * dm * dm, 0.f);
            }
            PIN2(mbv, ta);
          }
          if constexpr (gp == 2) { ta = rbf(rbf(ta * (1.0f / D_)) + 1e-5f); gs -= mbv * rdl(su, 4); PIN2(ta, gs); }
          if constexpr (gp == 3) { ta = rbf(__builtin_amdgcn_sqrtf(ta)); tb = __builtin_amdgcn_rcpf(ta); PIN2(ta, tb); }
          if constexpr (gp == 4) {
            const unsigned rb = __float_as_uint(rbf(gs * tb));
            kl |= (rb ^ ((unsigned)((int)rb >> 31) | 0x80000000u)) & 0xffff0000u; PIN1(kl);
          }
          if constexpr (gp == 5) { kl = (lane & 2) ? __float_as_uint(su) : (lane & 1) ? kl : (__float_as_uint(mbv) >> 16) | __float_as_uint(ta); PIN1(kl); }
        }
        if constexpr (jb == ST_JB + 3) {
          if constexpr (gp == 0) { if (!__all(gr.y == tag)) gr = granule_wait((const u64*)((const char*)(flags + FL_AB) + gro), tag); PIN1(gr); }
          if constexpr (gp == 1) { g1 = __builtin_amdgcn_rcpf(__uint_as_float((gr.x & 0xffff0000u) ^ (z << 31))); PIN1(g1); }
          if constexpr (gp == 2) { g0 = pw < 2 ? -(lo_bf(gr.x) * g1) : __uint_as_float(gr.x); PIN1(g0); }
        }
        if constexpr (gp < 7 && (jb - 1) * 7 + gp < 64) {
          constexpr int n = (jb - 1) * 7 + gp;
          sld<((n >> 1) & 7) * 4096 + (n & 1) * 64>(sd, spb, KROT(n >> 4));
        }
        SB();
      };
      auto step1 = [&](auto JB) __attribute__((always_inline)) {
        constexpr int jb = decltype(JB)::value;
        if constexpr (jb == 1) PAGE(1);
        if constexpr (jb == 7) PAGE(2);
        constexpr int n16 = jb == 12 ? 12 : jb == 11 ? 14 : 16;
        constexpr auto wl = [](int q) { return (q == 3 ? 1 : 0) + (q == ST_JB ? 1 : 0); };
        if constexpr (jb == 1) {
          asm volatile("s_waitcnt vmcnt(%11)" : "+v"(lv[0]), "+v"(lv[1]), "+v"(lw[0]), "+v"(lw[1]), "+v"(ld[0]), "+v"(ld[1]),
                       "+v"(lv2[0]), "+v"(lv2[1]), "+v"(ld2[0]), "+v"(ld2[1]), "+v"(epv) : "i"(16) : "memory");
        } else if constexpr (jb == ST_JB + 3) {
          asm volatile("s_waitcnt vmcnt(%1)" : "+v"(gr) : "i"(n16 + wl(jb - 2) + wl(jb - 1)) : "memory");
        } else wait_vm<n16 + wl(jb - 3) + wl(jb - 2) + wl(jb - 1)>();
        BAR();
#define W1(i) work1(JB, IC<i>{})
        if constexpr (jb < 10) G1_STAGE_TILE(jb - 1 + G1_NST, ((jb - 1) % G1_NST) * G1_STAGE, W1);
        else G1_STAGE_A(jb - 1 + G1_NST, ((jb - 1) % G1_NST) * G1_STAGE, W1);
        if constexpr (jb == 12) { G1_STAGE_B(13, G1_STAGE); G1_STAGE_B(14, 2 * G1_STAGE); }
        if constexpr (jb == 3) __builtin_amdgcn_raw_buffer_store_b64((u32x2){kl, tag}, rf, go, 0, 16);
        if constexpr (jb == ST_JB) {
          asm volatile("buffer_load_dwordx2 %0, %1, %2, 0 offen sc1" : "=v"(gr) : "v"(gro), "s"(rf) : "memory");
        }
        touches(IC<100 + jb>{});
        if constexpr (jb <= 7) {
          if (et == jb - 1) {
#pragma unroll
            for (int l = 0; l < WSN; ++l) bld16_nt_io(wv[l], rw, vw, l * F_ * 2);
          }
        }
        SB();
      };
      static_assert(ST_JB + 3 <= 12 && ST_JB >= 6, "schedule");
      step1(IC<1>{}); step1(IC<2>{}); step1(IC<3>{}); step1(IC<4>{}); step1(IC<5>{}); step1(IC<6>{}); step1(IC<7>{}); step1(IC<8>{}); step1(IC<9>{}); step1(IC<10>{}); step1(IC<11>{}); step1(IC<12>{});
    }
#define G2_STAGE_N(kk_, W) do { const int kb = KROT(kk_); constexpr int so = n_off(kk_); SB(); \
    DMA_(rn2, vn2, mq0, so, kb, 0, ""); W(0); DMA_(rn2, vn2, mq1, so, kb, 32 * S_ * 2, ""); W(1); \
    DMA_(rn2, vn2, mq2, so, kb, 64 * S_ * 2, ""); W(2); DMA_(rn2, vn2, mq3, so, kb, 96 * S_ * 2, ""); W(3); \
    DMA_(rn2, vn2, mq0, so + 16384, kb, 128 * S_ * 2, ""); W(4); DMA_(rn2, vn2, mq1, so + 16384, kb, 160 * S_ * 2, ""); W(5); \
    DMA_(rn2, vn2, mq2, so + 16384, kb, 192 * S_ * 2, ""); W(6); DMA_(rn2, vn2, mq3, so + 16384, kb, 224 * S_ * 2, ""); W(7); SB(); } while (0)
#define G2_STAGE_NH(kk_, h) do { const int kb = KROT(kk_); constexpr int so = n_off(kk_) + (h) * 16384; SB(); \
    DMA_(rn2, vn2, mq0, so, kb, (h) * 128 * S_ * 2, ""); DMA_(rn2, vn2, mq1, so, kb, ((h) * 128 + 32) * S_ * 2, ""); \
    DMA_(rn2, vn2, mq2, so, kb, ((h) * 128 + 64) * S_ * 2, ""); DMA_(rn2, vn2, mq3, so, kb, ((h) * 128 + 96) * S_ * 2, ""); SB(); } while (0)
#define G2_STAGE_M(kk_, W) do { const int kb = KROT(kk_); constexpr int so = G2_MRING + m_off(kk_); SB(); \
    DMA_(rm2, vm2, mb0, so, kb, 0, " sc1"); W(8); DMA_(rm2, vm2, mb0, so + 4096, kb, 32 * 8192, " sc1"); W(9); \
    DMA_(rm2, vm2, mb0, so + 8192, kb, 64 * 8192, " sc1"); W(10); DMA_(rm2, vm2, mb0, so + 12288, kb, 96 * 8192, " sc1"); W(11); SB(); } while (0)
    WAIT_VM(16); BAR();
    asm volatile("ds_write_b32 %0, %1" :: "v"(lma), "v"(g0) : "memory");
    if (pw < 2) asm volatile("ds_write_b32 %0, %1 offset:512" :: "v"(lma), "v"(g1) : "memory");
    u32x4 tv[4];
    unsigned ready = 0;
    auto need = [&](int k) __attribute__((always_inline)) {
      const int b = k_blk(k) + (k_ord(k) & 1) * 8;
#pragma unroll 1
      for (int it = 0; !((ready >> b) & 1) && it < SPIN_MAX; ++it) {
        ready |= (unsigned)__ballot(__builtin_amdgcn_raw_buffer_load_b32(rf, abq[k < 5 ? 0 : k < 9 ? 1 : 2], 0, 16) == tag);
        asm volatile("" ::: "memory");
      }
    };
    unsigned ksel, bad = 0;
    int isel;
    unsigned xw;
    auto bvec = [&](unsigned src, const unsigned (&sl)[4]) __attribute__((always_inline)) {
      return (u32x4){__builtin_amdgcn_perm(0, src, sl[0]), __builtin_amdgcn_perm(0, src, sl[1]),
                     __builtin_amdgcn_perm(0, src, sl[2]), __builtin_amdgcn_perm(0, src, sl[3])};
    };
#define BC8(v, p) (unsigned)__builtin_amdgcn_update_dpp(__builtin_amdgcn_update_dpp(0, (int)(v), 0x150 + (p), 0xf, 0x3, false), (int)(v), 0x158 + (p), 0xf, 0xc, false)
    float* lg = (float*)(smem + LDS_GU);
    f32x4 macc = {0.f, 0.f, 0.f, 0.f};
    u32x2 sv;
    unsigned hp, gbad;
    float s0, s1;
    auto work2 = [&](auto JB, auto GP) __attribute__((always_inline)) {
      constexpr int jb = decltype(JB)::value, gp = decltype(GP)::value;
      SB();
      if constexpr (jb == -1 && gp < 4) {
        constexpr int c = gp == 0 ? 0xB1 : gp == 1 ? 0x4E : gp == 2 ? 0x141 : 0x140;
        merge2(k1, k2, dpp_u<c>(k1), dpp_u<c>(k2));
        PIN2(k1, k2);
      }
      if constexpr (jb == -1 && gp == 4) {
        ksel = tk ? k2 : k1;
        isel = (0xffff - (int)(ksel & 0xffffu)) & (S_ - 1);
        k1 = (isel * D_ + d0 + rq * 16 + (lane & 7) * 2) * 2; k2 = O_ST + isel * 8;
        PIN1(ksel);
        xw = gld4_s(x, k1);
        sv = bld8_sc1(rf, k2);
      }
      if constexpr (jb == 0 && gp == 1) wait_xs<3 + 1 + 2>(xw, sv);
      if constexpr (jb == 0) {
        if constexpr (gp == 1) { if (!__all(sv.y == tag)) sv = granule_wait(stat + isel, tag); s0 = __builtin_amdgcn_rcpf(hi_bf(sv.x)); PIN1(s0); }
        if constexpr (gp == 2) { s1 = ln_elem(lo_bf(xw), lo_bf(sv.x), hi_bf(sv.x), s0); PIN1(s1); }
        if constexpr (gp == 3) { xw = pk2(s1, ln_elem(hi_bf(xw), lo_bf(sv.x), hi_bf(sv.x), s0)); PIN1(xw); }
      }
      if constexpr (jb == DG && gp < 7) { PIN1(dv[gp]); macc = MM(dv[gp], bvec(BC8(hp, gp), sel[0]), macc); PIN1(macc); }
      if constexpr ((jb == 0 && gp >= 4) || jb == 1) {
        constexpr int l = jb == 0 ? gp - 4 : gp + 4;
        if constexpr (!(l & 1)) hp = BC8(xw, l >> 1);
        PIN1(wv[l]);
        macc = MM(wv[l], bvec(hp, sel[l & 1]), macc);
        PIN2(macc, hp);
      }
      if constexpr (jb == GV_JB + 2) {
        if constexpr (gp == 0) { gbad = (gv[0].y ^ tag) | (gv[0].w ^ tag) | (gv[1].y ^ tag) | (gv[1].w ^ tag); PIN1(gbad); }
        if constexpr (gp == 1) { gbad |= (gv[2].y ^ tag) | (gv[2].w ^ tag) | (gv[3].y ^ tag) | (gv[3].w ^ tag); PIN1(gbad); }
        if constexpr (gp == 2) {
          s0 = ((__uint_as_float(gv[0].x) + __uint_as_float(gv[1].x)) + __uint_as_float(gv[2].x)) + __uint_as_float(gv[3].x);
          s1 = ((__uint_as_float(gv[0].z) + __uint_as_float(gv[1].z)) + __uint_as_float(gv[2].z)) + __uint_as_float(gv[3].z);
          PIN2(s0, s1);
        }
        if constexpr (gp == 3) {
#pragma unroll 1
          for (int it = 0; !__all(gbad == 0) && it < SPIN_MAX; ++it) {
            u32x4 q[4];
            gbad = 0;
#pragma unroll
            for (int i = 0; i < 4; ++i) { q[i] = __builtin_amdgcn_raw_buffer_load_b128(rgu, vgu, i * F_ * 16, 16); gbad |= (q[i].y ^ tag) | (q[i].w ^ tag); }
            s0 = ((__uint_as_float(q[0].x) + __uint_as_float(q[1].x)) + __uint_as_float(q[2].x)) + __uint_as_float(q[3].x);
            s1 = ((__uint_as_float(q[0].z) + __uint_as_float(q[1].z)) + __uint_as_float(q[2].z)) + __uint_as_float(q[3].z);
            asm volatile("" ::: "memory");
          }
          const unsigned gp2 = pk2(s0, s1);
          const unsigned up2 = dpp_u<0x128>(gp2);
          s0 = tk ? hi_bf(up2) : lo_bf(gp2); s1 = tk ? hi_bf(gp2) : lo_bf(up2);
          PIN2(s0, s1);
        }
        if constexpr (gp == 4) { s0 = rbf(s0 * __builtin_amdgcn_rcpf(1.f + __expf(-s0))); PIN1(s0); }
        if constexpr (gp == 5) { hp = pk2(s0 * s1, 0.f); PIN1(hp); }
      }
      if constexpr (jb >= 3 && jb <= 10 && gp >= 4) {
        constexpr int n = (jb - 3) * 8 + gp - 4;
        sld<((n >> 1) & 7) * 4096 + (n & 1) * 64>(sd, spb, KROT(12 + (n >> 4)));
      }
      if constexpr (jb == 10 && gp == 2) epi_setup();
      if constexpr (jb == 10 && gp == 4) code_touch(sd, (j * 4 + pw) * 256);
      SB();
    };
#define W2M(i) work2(IC<-1>{}, IC<i>{})
#define W20(i) work2(IC<0>{}, IC<i>{})
#define W21(i) work2(IC<1>{}, IC<i>{})
    unsigned fs;
    {
      G1_STAGE_B(15, 3 * G1_STAGE);
      G2_STAGE_NH(0, 0);
      lgkm0(sd);
      WAIT_VM(16); BAR();
      G2_STAGE_NH(0, 1);
      WAIT_VM(10); BAR();
#pragma unroll
      for (int i = 0; i < 4; ++i) kk[i] = bld16_sc1(rf, l16, ko_ + i * 1024);
#pragma unroll
      for (int l = WSN; l < 16; ++l) wv[l] = bld16_nt(rw, vw, l * F_ * 2);
      asm volatile("s_waitcnt vmcnt(%4)" : "+v"(kk[0]), "+v"(kk[1]), "+v"(kk[2]), "+v"(kk[3]) : "i"(16 - WSN) : "memory");
      touches(IC<115>{});
#pragma unroll
      for (int i = 0; i < 4; ++i) {
        k2 = max(k2, min(k1, kk[i].x)); k1 = max(k1, kk[i].x);
        k2 = max(k2, min(k1, kk[i].z)); k1 = max(k1, kk[i].z);
        bad |= (kk[i].y ^ tag) | (kk[i].w ^ tag);
      }
#pragma unroll 1
      for (int it = 0; !__all(bad == 0) && it < SPIN_MAX; ++it) {
        k1 = 0; k2 = 0; bad = 0;
#pragma unroll
        for (int i = 0; i < 4; ++i) {
          const u32x4 q = __builtin_amdgcn_raw_buffer_load_b128(rf, l16, ko_ + i * 1024, 16);
          k2 = max(k2, min(k1, q.x)); k1 = max(k1, q.x);
          k2 = max(k2, min(k1, q.z)); k1 = max(k1, q.z);
          bad |= (q.y ^ tag) | (q.w ^ tag);
        }
        asm volatile("" ::: "memory");
      }
      merge2(k1, k2, dpp_u<0xB1>(k1), dpp_u<0xB1>(k2));
      merge2(k1, k2, dpp_u<0x4E>(k1), dpp_u<0x4E>(k2));
      merge2(k1, k2, dpp_u<0x141>(k1), dpp_u<0x141>(k2));
      merge2(k1, k2, dpp_u<0x140>(k1), dpp_u<0x140>(k2));
      if ((lane & 15) == 0) { *(u32x2*)(smem + LDS_KEY + (pw * 4 + rq) * 8) = (u32x2){k1, k2}; ((unsigned*)(smem + LDS_SEL))[5] = tag; }
      PAGE(3);
      BAR_LGKM();
#pragma unroll
      for (int jj = 0; jj < 4; ++jj) tv[jj] = *(const u32x4*)(smem + G2_MRING + m_off(0) + (jj * 4 + pw) * 1024 + lane * 16);
      { const u32x2 kp = *(const u32x2*)(smem + LDS_KEY + ci * 8); k1 = kp.x; k2 = kp.y; }
#pragma unroll
      for (int jj = 0; jj < 4; ++jj) __builtin_amdgcn_raw_buffer_store_b128(tv[jj], rm2, vm2 + jj * 32 * 8192 + 2 * tt * 128, 0, 16);
      G2_STAGE_N(1, W2M);
      WAIT_VM(10);
      if (lane == tt) __builtin_amdgcn_raw_buffer_store_b32(tag, rf, abo, 0, 16);
      G2_STAGE_N(2, W20);
      __builtin_amdgcn_s_sleep(2);
      WAIT_VM(9);
      fs = bld4_sc1(rf, abq[0]);
    }
    BAR();
#pragma unroll
    for (int jj = 0; jj < 4; ++jj) tv[jj] = *(const u32x4*)(smem + G2_MRING + m_off(1) + (jj * 4 + pw) * 1024 + lane * 16);
    G2_STAGE_N(3, NOW);
    asm volatile("s_waitcnt vmcnt(8)" : "+v"(fs) :: "memory");
    ready = (unsigned)__ballot(fs == tag);
    need(2);
    G2_STAGE_M(2, NOW);
    need(3);
    G2_STAGE_M(3, NOW);
    fs = bld4_sc1(rf, abq[1]);
    touches(IC<1>{});
    W21(0); W21(1); W21(2); W21(3); W21(4); W21(5); W21(6); W21(7); W21(8); W21(9); W21(10); W21(11);
    asm volatile("" : "+v"(tv[0]), "+v"(tv[1]), "+v"(tv[2]), "+v"(tv[3]));
    auto step2 = [&](auto JB) __attribute__((always_inline)) {
      constexpr int jb = decltype(JB)::value;
      if constexpr (jb == 3) PAGE(4);
      if constexpr (jb == 9) PAGE(5);
      constexpr auto must = [](int q) { return (q == 2 ? 4 : 0) + ((FSM >> q) & 1) + ((q == GV_JB || q == GV_JB - 1) ? 2 : 0); };
      if constexpr (jb == 2) wait_vm<4 + must(1)>();
      else wait_vm<dn_cnt(jb - 2) + (jb == 5 ? 1 : 0) + 12 + must(jb - 1) + dn_cnt(jb - 1)>();
      if constexpr (jb == 3) { if (pw == 0 && (lane & ~8) == 0) ((unsigned*)(smem + LDS_SEL))[tk] = ksel; }
      if constexpr ((jb == 2 || jb == DG + 1)) {
        *(f32x4*)(lg + pw * 256 + lane * 4) = macc;
        macc = (f32x4){0.f, 0.f, 0.f, 0.f};
        lgkm0(sd);
      }
      BAR();
      float p0[4], p1[4];
      const int col = (pw & 1) * 64 + lane;
      if constexpr (jb == 3) {
        if (g == 0 && pw == 0 && lane == 0) __hip_atomic_store((unsigned*)flags + FL_EPOCH, epoch + 1u, __ATOMIC_RELAXED, __HIP_MEMORY_SCOPE_AGENT);
      }
      if constexpr (jb == DG + 1) {
        const float* p = lg + (pw & 1) * 32 + (lane >> 4) * 64 + ((2 * lane) & 7) * 4 + ((lane >> 2) & 3);
#pragma unroll
        for (int i = 0; i < 4; ++i) { p0[i] = p[i * 256]; p1[i] = p[i * 256 + 4]; }
      }
      if constexpr (jb == 2) {
        const float* p = lg + (col >> 5) * 64 + (col & 7) * 4 + ((col >> 3) & 3);
#pragma unroll
        for (int i = 0; i < 4; ++i) { p0[i] = p[i * 256]; p1[i] = p[i * 256 + 32]; }
      }
      if constexpr (jb == 4) {
        if (lane == 8 + tt) __builtin_amdgcn_raw_buffer_store_b32(tag, rf, abo, 0, 16);
      }
      if constexpr ((FSM >> (jb - 2)) & 1) {
        asm volatile("" : "+v"(fs));
        ready |= (unsigned)__ballot(fs == tag);
      }
      if constexpr (jb == GV_JB + 2) asm volatile("" : "+v"(gv[0]), "+v"(gv[1]), "+v"(gv[2]), "+v"(gv[3]));
#define W2(i) work2(JB, IC<i>{})
      if constexpr (jb < 13) {
        G2_STAGE_N(jb + 2, W2);
        need(jb + 2);
        G2_STAGE_M(jb + 2, W2);
      } else {
#define W14(i) do { if (i == B14P - 1) { wait_vm<B14P>(); BAR(); } } while (0)
#define W14M(i) W14(i - 8)
        need(15);
        G2_STAGE_M(15, W14M);
#define LASTQ(q) do { const int kb = KROT(15) + ((q + kq + 1) & 3) * 32 * S_ * 2; \
        DMA_(rn2, vn2, mb0, n_off(15) + q * 4096, kb, 0, ""); W14(4 + 2 * q); DMA_(rn2, vn2, mb0, n_off(15) + 16384 + q * 4096, kb, 128 * S_ * 2, ""); W14(5 + 2 * q); } while (0)
        LASTQ(0); LASTQ(1); LASTQ(2); LASTQ(3);
        SB();
      }
      if constexpr (jb == 2) {
#pragma unroll
        for (int jj = 0; jj < 4; ++jj) __builtin_amdgcn_raw_buffer_store_b128(tv[jj], rm2, vm2 + jj * 32 * 8192 + (2 * tt + 1) * 128, 0, 16);
      }
      if constexpr ((FSM >> jb) & 1) fs = bld4_sc1(rf, abq[jb < 5 ? 1 : 2]);
      if constexpr ((jb == GV_JB || jb == GV_JB - 1)) {
#pragma unroll
        for (int i = (jb == GV_JB ? 2 : 0); i < (jb == GV_JB ? 4 : 2); ++i) gv[i] = bld16_sc1(rgu, vgu, i * F_ * 16);
      }
      touches(JB);
#pragma unroll
      for (int i = dn_first(jb); i < dn_first(jb) + dn_cnt(jb); ++i) dv[i] = bld16_nt(rdn, vdn, i * D_ * 2);
      if constexpr (jb == 2) asm volatile("" : "+v"(p0[0]), "+v"(p0[1]), "+v"(p0[2]), "+v"(p0[3]), "+v"(p1[0]), "+v"(p1[1]), "+v"(p1[2]), "+v"(p1[3]));
      if constexpr (jb == DG + 1) {
        if (pw < 2)
          __builtin_amdgcn_raw_buffer_store_b128((u32x4){__float_as_uint((p0[0] + p0[1]) + (p0[2] + p0[3])), tag, __float_as_uint((p1[0] + p1[1]) + (p1[2] + p1[3])), tag},
                                                 mk_rsrc(wsp + WS_DN), (((pw * 8 + cg8) * 32 + rgb) * 64 + lane) * 16, 0, 16);
      }
      if constexpr (jb == 2) {
        if (moe && pw < 2)
          __builtin_amdgcn_raw_buffer_store_b128((u32x4){__float_as_uint((p0[0] + p0[1]) + (p0[2] + p0[3])), tag, __float_as_uint((p1[0] + p1[1]) + (p1[2] + p1[3])), tag},
                                                 rgu, ((mat * 4 + dq) * F_ + cg * 128 + col) * 16, 0, 16);
      }
      SB();
    };
    static_assert(DG + 1 <= 12 && DG >= GV_JB + 3 && dn_first(DG - 2) == 7, "schedule");
    step2(IC<2>{}); step2(IC<3>{}); step2(IC<4>{}); step2(IC<5>{}); step2(IC<6>{}); step2(IC<7>{}); step2(IC<8>{}); step2(IC<9>{}); step2(IC<10>{}); step2(IC<11>{}); step2(IC<12>{}); step2(IC<13>{});
    lgkm0(sd);
    PAGE(7);
    WAIT_VM(4); BAR();
    xq[0] = ld16_s<0>(x, xo); xq[1] = ld16_s<64>(x, xo); sq = ld8_s_sc1(stat, xo >> 11 << 3);
    WAIT_VM(3); BAR();
    asm volatile("s_waitcnt vmcnt(0)" : "+v"(xq[0]), "+v"(xq[1]), "+v"(sq) :: "memory");
    key[0] = __builtin_amdgcn_readlane(ksel, 0); key[1] = __builtin_amdgcn_readlane(ksel, 8);
    nf_pre();
#pragma unroll
    for (int n = 0; n < 16; ++n) nf_step(n);
#pragma unroll 1
    for (int c = 0; c < 2; ++c) {
      const unsigned kc = __builtin_amdgcn_readlane(ksel, c * 8);
      const int ic = (0xffff - (int)(kc & 0xffffu)) & (S_ - 1);
      if ((ic >> 8) != tt || ((ic >> 5) & 3) != kq) continue;
      const __amdgpu_buffer_rsrc_t rd = mk_rsrc(wsp + WS_DN + (size_t)(c * 8 + et) * 32768);
#pragma unroll 1
      for (int it = 0; it < SPIN_MAX; ++it) {
        if (__all(__builtin_amdgcn_raw_buffer_load_b32(rd, (lane & 31) * 1024 + 1020, 0, 16) == tag)) break;
        __builtin_amdgcn_s_sleep(2);
        asm volatile("" ::: "memory");
      }
      float s0, s1;
      unsigned bad = 1;
#pragma unroll 1
      for (int it = 0; !__all(bad == 0) && it < SPIN_MAX; ++it) {
        bad = 0; s0 = 0.f; s1 = 0.f;
#pragma unroll
        for (int i = 0; i < 8; ++i) {
          const u32x4 q = __builtin_amdgcn_raw_buffer_load_b128(rd, rq * 1024 + pw * 256 + ci * 16, i * 4096, 16);
          bad |= (q.y ^ tag) | (q.w ^ tag); s0 += __uint_as_float(q.x); s1 += __uint_as_float(q.z);
        }
        asm volatile("" ::: "memory");
      }
      s0 = fold32(s0, s1);
      s0 = fold16(s0, s0);
      const float sg = rbf(__builtin_amdgcn_rcpf(1.f + __expf(-key_score(kc))));
      ((float*)(smem + LDS_FIN))[c * 128 + pw * 32 + ci * 2 + (lane >> 5)] = rbf(rbf(s0) * sg);
      asm volatile("s_waitcnt lgkmcnt(0)" ::: "memory");
      if (lane == 0) asm volatile("ds_add_u32 %0, %1" :: "v"(lds_addr(smem + LDS_SEL + 24 + c * 4)), "v"(1) : "memory");
    }
    asm volatile("v_mov_b32 %0, 0" : "=v"(fx), "=v"(ya[0]), "=v"(ya[1]), "=v"(ya[2]), "=v"(ya[3]));
  } else {
  {
    const int q = j * 4 + wid;
    constexpr int Q1 = WS_MB, Q2 = Q1 + 8, Q3 = Q2 + 8, Q4 = Q3 + 7, Q5 = Q4 + 7, Q6 = Q5 + 7, Q7 = Q6 + 4, Q8 = Q7 + 4, QC = Q8 + 1;
    static_assert(QC <= 128, "warm-up slots");
    unsigned dmy = 0;
    if (q < QC) {
      const char* base; int b0;
      if (q < Q1) { base = wsp; b0 = 0; }
      else if (q < Q2) { base = (const char*)ws_re; b0 = Q1; }
      else if (q < Q3) { base = (const char*)ws_im; b0 = Q2; }
      else if (q < Q4) { base = (const char*)w_gate; b0 = Q3; }
      else if (q < Q5) { base = (const char*)w_up; b0 = Q4; }
      else if (q < Q6) { base = (const char*)w_down; b0 = Q5; }
      else if (q < Q7) { base = (const char*)out; b0 = Q6; }
      else if (q < Q8) { base = (const char*)x; b0 = Q7; }
      else { base = (const char*)w_r; b0 = Q8; }
      base += (size_t)(q - b0) << 20;
      asm volatile("s_load_dword %0, %1, 0" : "+s"(dmy) : "s"(base) : "memory");
    }
    {
      const int w = g * 4 + wid;
      constexpr int R = (int)(WS_FLAGS - WS_DN);
      static_assert(FL_X == 0 && FL_ST == 6144 * 32 && FL_EPOCH == 6528 * 32, "");
      const int o = lane < 6 ? R + (lane * 1024 + w) * 128 : lane > 6 || w >= 896 ? (int)0x80000000 : w < 512 ? w * 1024 + 896 : R + (6144 - 512 + w) * 128;
      asm volatile("s_mov_b32 m0, %0\n\ts_nop 0\n\tbuffer_load_dword %1, %2, 0 offen sc1 lds"
                   :: "s"(lds_addr(smem + LDS_DUMMY) + wid * 1024 + 512), "v"(o), "s"(mk_rsrc(wsp + WS_DN)) : "memory");
    }
    if (tid < 24) ((int*)(smem + LDS_SEL))[tid] = 0;
    WAIT_VM(1);
    if (wid == 0) { CALL_PG(1); CALL_PG(8); CALL_PG(5); }
    else if (wid == 1) { CALL_PG(2); CALL_PG(9); CALL_PG(6); }
    else if (wid == 2) { CALL_PG(3); CALL_PG(10); CALL_PG(7); }
    else { CALL_PG(4); CALL_PG(11); }
    asm volatile("s_barrier\n\ts_waitcnt lgkmcnt(0)" : "+s"(dmy) :: "memory");
  }
  PAGE(8);
  epi_setup();
  const int wr = wid >> 1, wc = wid & 1;
  const int sw = (fr >> 1) & 7;
  const int ko0 = ((0 + fq) ^ sw) * 16, ko1 = ((4 + fq) ^ sw) * 16;
  const int mrow = (fr >> 2) * 8 + (fr & 3);
  bf16x8 fm[2][4], fn[2][4];
  {
    const char* pm = smem + (wr * 64 + mrow) * 128;
    const char* pn = smem + 16384 + (wc * 64 + mrow) * 128;
    f32x4 acc[4][4];
#pragma unroll
    for (int i = 0; i < 4; ++i)
#pragma unroll
      for (int jn = 0; jn < 4; ++jn)
#pragma unroll
        for (int q = 0; q < 4; ++q) acc[i][jn][q] = 0.f;
#define TO(i) (((i) >> 1) * 4096 + ((i) & 1) * 512)
#define LDSP(T, a) ((__attribute__((address_space(3))) T*)(size_t)(a))
#define SLOTS(n) _Pragma("unroll") for (int q = 0; q < (n); ++q)
#define MFA(c, a, b) asm volatile("v_mfma_f32_16x16x32_bf16 %0, %1, %2, %0" : "+v"(c) : "v"(a), "v"(b))
#define MFA2(c, a, b, a2, b2) asm volatile("v_mfma_f32_16x16x32_bf16 %0, %1, %2, %0\n\tv_mfma_f32_16x16x32_bf16 %0, %3, %4, %0" : "+v"(c) : "v"(a), "v"(b), "v"(a2), "v"(b2))
    unsigned lm[2][2], ln[2][2];
#pragma unroll
    for (int u = 0; u < 2; ++u)
#pragma unroll
      for (int kk = 0; kk < 2; ++kk) {
        lm[u][kk] = lds_addr(pm) + u * 2 * G1_STAGE + (kk ? ko1 : ko0);
        ln[u][kk] = lds_addr(pn) + u * 2 * G1_STAGE + (kk ? ko1 : ko0);
        asm volatile("" : "+v"(lm[u][kk]), "+v"(ln[u][kk]));
      }
    auto ldm = [&](int stg, int i, int kk) __attribute__((always_inline)) { fm[kk][i] = *LDSP(const bf16x8, lm[stg >> 1][kk] + (stg & 1) * G1_STAGE + TO(i)); };
    auto ldn = [&](int stg, int jn, int kk) __attribute__((always_inline)) { fn[kk][jn] = *LDSP(const bf16x8, ln[stg >> 1][kk] + (stg & 1) * G1_STAGE + TO(jn)); };
    auto quad = [&](int a, int c, int d, int s) __attribute__((always_inline)) {
      const int i = (s < 2 || s >= 6) ? a : a + 1, jn = s < 4 ? c : d, kk = (s == 1 || s == 2 || s == 5 || s == 6);
      MFA(acc[i][jn], fm[kk][i], fn[kk][jn]);
    };
    auto quad2 = [&](int a, int c, int d, int s) __attribute__((always_inline)) {
      const int i = (s < 2 || s >= 6) ? a : a + 1, jn = s < 4 ? c : d, kk = (s == 2 || s == 6);
      MFA2(acc[i][jn], fm[kk][i], fn[kk][jn], fm[1 - kk][i], fn[1 - kk][jn]);
    };
    auto half1 = [&](int stg) __attribute__((always_inline)) {
      const int ja = (stg & 1) * 2, jb = 2 - ja;
      SLOTS(8) {
        quad(0, ja, ja + 1, q);
        if (q < 4) ldn(stg, jb + (q >> 1), q & 1); else ldm(stg, 2 + ((q - 4) >> 1), q == 5 || q == 6);
        SB();
      }
      SLOTS(4) { quad2(0, jb, jb + 1, 2 * q); SB(); }
    };
    auto half2 = [&](int stg) __attribute__((always_inline)) {
      const int ja = (stg & 1) * 2, jb = 2 - ja, nx = (stg + 1) & 3;
      SLOTS(8) {
        quad(2, jb + 1, jb, q);
        if (!(q & 1)) ldm(nx, q >> 2, q == 2 || q == 4);
        SB();
      }
      SLOTS(8) {
        quad(2, ja, ja + 1, q);
        if (!(q & 1)) ldn(nx, jb + (q >> 2), (q >> 1) & 1);
        SB();
      }
    };
    SLOTS(16) asm volatile("" : "+v"(acc[q >> 2][q & 3]));
    SB();
    SLOTS(4) { ldm(0, q >> 1, q & 1); ldn(0, q >> 1, q & 1); }
    SB();
#pragma unroll 1
    for (int k4 = 0; k4 < G1_NKT - 4; k4 += 4) {
      half1(0); BAR_LGKM();
      SB(); half2(0);
      half1(1); BAR_LGKM();
      SB(); half2(1);
      half1(2); BAR_LGKM();
      SB(); half2(2);
      half1(3); BAR_LGKM();
      SB(); half2(3);
    }
    bf16x8 gm[2][4], gn[2][8];
    unsigned am[2][2], an[2][2];
#pragma unroll
    for (int u = 0; u < 2; ++u)
#pragma unroll
      for (int kk = 0; kk < 2; ++kk) {
        am[u][kk] = lds_addr(pm) + (u ? 3 : 1) * G1_STAGE + (kk ? ko1 : ko0);
        an[u][kk] = lds_addr(pn) + (u ? 3 : 1) * G1_STAGE + (kk ? ko1 : ko0);
        asm volatile("" : "+v"(am[u][kk]), "+v"(an[u][kk]));
      }
    unsigned al = lds_addr(smem + LDS_MB) + (wr * 32 + fq * 8) * 4, ac = lds_addr(smem + LDS_MB) + (256 + wc * 64 + mrow) * 4;
    unsigned ae = lds_addr(smem + G2_MRING) + (wc * 64 + mrow) * 128 + (((wr * 4 + fq) ^ sw) * 16);
    asm volatile("" : "+v"(al), "+v"(ac), "+v"(ae));
    auto rdg = [&](int set, int t, int h, int r) __attribute__((always_inline)) {
      const int u = t == 15, o = t == 14 ? G1_STAGE : 0;
      if (r == 0 || r == 2 || r == 4 || r == 5) {
        const int i = r >= 4, kk = r == 2 || r == 4;
        gm[set][kk * 2 + i] = *LDSP(const bf16x8, am[u][kk] + o + h * 4096 + i * 512);
      } else {
        const int jn = r < 4 ? 0 : (r - 4) >> 1, kk = r < 4 ? r >> 1 : r & 1;
        gn[set][kk * 4 + jn] = *LDSP(const bf16x8, an[u][kk] + o + TO(jn));
      }
    };
    auto mmg = [&](int set, int h, int q) __attribute__((always_inline)) {
      const int jn = q >> 2, r = q & 3, i = (r >> 1) ^ (jn & 1), kk = r == 1 || r == 2;
      MFA(acc[h * 2 + i][jn], gm[set][kk * 2 + i], gn[set][kk * 4 + jn]);
    };
    f32x4 rsv[2], mrv[2];
    float cev[4], tc[32];
    auto rds = [&](int h, int r) __attribute__((always_inline)) {
      if (r < 2) mrv[r] = *LDSP(const f32x4, al + h * 256 + r * 16); else rsv[r - 2] = *LDSP(const f32x4, al + 512 + h * 256 + (r - 2) * 16);
    };
    auto prep = [&](int n) __attribute__((always_inline)) {
      tc[n] = llvm_fmul_legacy(mrv[(n >> 2) & 1][n & 3], cev[n >> 3]);
      asm volatile("" : "+v"(tc[n]));
    };
    u32x4 ep;
    auto epi = [&](int h, int q) __attribute__((always_inline)) {
      const int jn = q >> 1, i = q & 1;
      asm volatile("v_fmac_f32 %0, %4, %8\n\tv_fmac_f32 %1, %5, %9\n\tv_fmac_f32 %2, %6, %10\n\tv_fmac_f32 %3, %7, %11"
                   : "+v"(tc[q * 4]), "+v"(tc[q * 4 + 1]), "+v"(tc[q * 4 + 2]), "+v"(tc[q * 4 + 3])
                   : "v"(acc[h * 2 + i][jn][0]), "v"(acc[h * 2 + i][jn][1]), "v"(acc[h * 2 + i][jn][2]), "v"(acc[h * 2 + i][jn][3]),
                     "v"(rsv[i][0]), "v"(rsv[i][1]), "v"(rsv[i][2]), "v"(rsv[i][3]));
      ep[i * 2] = pk2(tc[q * 4], tc[q * 4 + 1]); ep[i * 2 + 1] = pk2(tc[q * 4 + 2], tc[q * 4 + 3]);
      if (i) *LDSP(u32x4, ae + m_off(h) + TO(jn)) = ep;
    };
    SB();
    half1(0);
    BAR_LGKM();
    SB();
    SLOTS(16) {
      if (q < 8) quad(2, 3, 2, q); else quad(2, 0, 1, q - 8);
      if (q < 12) rdg(0, 13, 0, q);
      SB();
    }
    BAR();
    SB();
    SLOTS(16) {
      mmg(0, 0, q);
      if (q < 12) rdg(1, 14, 0, q);
      else { rds(0, q - 12); cev[q - 12] = *LDSP(const float, ac + (((q - 12) >> 1) * 32 + ((q - 12) & 1) * 4) * 4); }
      SB();
    }
    PAGE(9);
    BAR();
    SB();
    SLOTS(16) { mmg(1, 0, q); if ((q & 3) < 3) rdg(0, 15, 0, (q >> 2) * 3 + (q & 3)); if (q >= 8) { prep(2 * q - 16); prep(2 * q - 15); } SB(); }
    SLOTS(16) { mmg(0, 0, q); if ((q & 3) < 3) rdg(1, 13, 1, (q >> 2) * 3 + (q & 3)); prep(16 + q); SB(); if (q >= 9 && (q & 1)) { epi(0, (q - 9) >> 1); SB(); } }
    SLOTS(8) { mmg(1, 1, q); if (q < 6) { rdg(0, 14, 1, 2 * q); rdg(0, 14, 1, 2 * q + 1); } SB(); if (q & 1) { epi(0, 4 + (q >> 1)); SB(); } }
    BAR_LGKM();
    SB();
    SLOTS(8) { mmg(1, 1, 8 + q); if (q < 4) rdg(1, 15, 1, q == 0 ? 1 : q == 1 ? 3 : q + 4); else rds(1, q - 4); SB(); }
    SLOTS(16) { mmg(0, 1, q); if (q < 8) rdg(1, 15, 1, q == 0 ? 0 : q == 1 ? 2 : q < 4 ? q + 2 : q + 4); prep(q); SB(); }
    asm volatile("s_waitcnt lgkmcnt(0)" ::: "memory");
    if (lane == 0) asm volatile("ds_add_u32 %0, %1" :: "v"(lds_addr(smem + LDS_SEL + 12)), "v"(1) : "memory");
    SB();
    SLOTS(16) {
      mmg(1, 1, q);
      if (q < 4) fn[0][q] = *(const bf16x8*)(smem + (wr * 128 + fr) * 128 + (q >> 1) * 2048 + ((q & 1) ? ko1 : ko0));
      else if (q < 8) fm[q & 1][(q - 4) >> 1] = *(const bf16x8*)(smem + G2_MRING + m_off(0) + (wc * 64 + mrow) * 128 + TO((q - 4) >> 1) + ((q & 1) ? ko1 : ko0));
      prep(16 + q);
      SB();
    }
    {
      lds_vi rel = (lds_vi)(smem + LDS_SEL + 12);
      int it = 0;
      while (rel[0] != 4 && it < SPIN_MAX * 8) ++it;
      asm volatile("" ::: "memory");
    }
    asm volatile("s_nop 7");
    SB();
    SLOTS(8) { epi(1, q); SB(); }
  }

  const char* pn = smem + (wr * 128 + fr) * 128;
  const char* pm = smem + G2_MRING + (wc * 64 + mrow) * 128;
  f32x4 acc[8][4];
  auto mm = [&](bool imaj, int set, int q2, int s, int mode) __attribute__((always_inline)) {
    int i, jj, kk;
    if (imaj) { i = s >> 2; const int r = s & 3; jj = (r >> 1) ^ (i & 1); kk = (r == 1 || r == 2); }
    else { jj = s >> 3; const int r = s & 7; i = jj ? 3 - (r >> 1) : (r >> 1); kk = ((r + 1) >> 1) & 1; }
    if (mode == 1 && !(s & 1)) asm volatile("v_mfma_f32_16x16x32_bf16 %0, %1, %2, 0" : "=&v"(acc[q2 * 2 + jj][i]) : "v"(fm[kk][i]), "v"(fn[set][jj * 2 + kk]));
    else if (mode) asm volatile("v_mfma_f32_16x16x32_bf16 %0, %1, %2, %0" : "+v"(acc[q2 * 2 + jj][i]) : "v"(fm[kk][i]), "v"(fn[set][jj * 2 + kk]));
    else acc[q2 * 2 + jj][i] = __builtin_amdgcn_mfma_f32_16x16x32_bf16(fm[kk][i], fn[set][jj * 2 + kk], acc[q2 * 2 + jj][i], 0, 0, 0);
  };
  unsigned bn[2], bm[2], an[2], am[2];
#pragma unroll
  for (int kk = 0; kk < 2; ++kk) {
    bn[kk] = lds_addr(pn) + (kk ? ko1 : ko0); bm[kk] = lds_addr(pm) + (kk ? ko1 : ko0);
    asm volatile("" : "+v"(bn[kk]), "+v"(bm[kk]));
    an[kk] = bn[kk]; am[kk] = bm[kk] + m_off(0);
  }
#define RDN(jt, kk, o) (*LDSP(const bf16x8, an[kk] + (o) + (jt) * 2048))
#define RDM(i, kk, o) (*LDSP(const bf16x8, am[kk] + (o) + TO(i)))
  PAGE(10);
  tag = __builtin_amdgcn_readfirstlane(((const unsigned*)(smem + LDS_SEL))[5]);
  {
    int son = 0;
    auto tile = [&](int kt, int zero) __attribute__((always_inline)) {
      SLOTS(16) {
        mm(true, 0, 0, q, zero);
        if (!(q & 1)) {
          if (q < 8) fm[(q >> 1) & 1][2 + (q >> 2)] = RDM(2 + (q >> 2), (q >> 1) & 1, 0);
          else { const int r = (q - 8) >> 1; fn[1][r] = RDN(2 + (r >> 1), r & 1, 0); }
        }
        SB();
      }
      SLOTS(16) {
        mm(false, 1, 1, q, zero);
        if (!(q & 1) && q < 8) fn[0][q >> 1] = RDN(4 + (q >> 2), (q >> 1) & 1, 0);
        SB();
      }
      const int som = son >> 1;
      son = son == 65536 ? 0 : son + 32768;
      SLOTS(16) {
        mm(false, 0, 2, q, zero);
        if (!(q & 1) && q < 8) fn[1][q >> 1] = RDN(6 + (q >> 2), (q >> 1) & 1, 0);
        if (q == 9 || q == 11) { an[q == 11] = bn[q == 11] + son; asm volatile("" : "+v"(an[q == 11])); }
        if (q == 13 || q == 15) { am[q == 15] = bm[q == 15] + som; asm volatile("" : "+v"(am[q == 15])); }
        SB();
      }
      BAR_LGKM();
      SB();
      SLOTS(16) {
        mm(true, 1, 3, q, zero);
        if (!(q & 1)) {
          if (q < 8) fn[0][q >> 1] = RDN(q >> 2, (q >> 1) & 1, 0);
          else fm[(q >> 1) & 1][(q - 8) >> 2] = RDM((q - 8) >> 2, (q >> 1) & 1, 0);
        }
        SB();
      }
    };
    SB();
    tile(0, 1);
#pragma unroll 1
    for (int kt = 1; kt < G2_NKT - 3; ++kt) tile(kt, 0);
  }
  {
    constexpr int NG = 12;
    static constexpr int g_t[NG] = {0, 0, 1, 1, 2, 2, 2, 1, 0, 0, 1, 2};
    static constexpr int g_q[NG] = {0, 1, 0, 1, 0, 1, 2, 2, 2, 3, 3, 3};
    constexpr int G14 = 1, G15 = 3, GP0 = 5, GP1 = 6;
    constexpr auto chp = [](int g) { return g == 0 || g_t[g] != g_t[g - 1]; };
    constexpr auto chn = [](int g) { return g + 1 < NG && g_t[g + 1] != g_t[g]; };
    static_assert(n_off(13) == 32768 && n_off(14) == 65536 && n_off(15) == 0, "");
    unsigned an2[2];
#pragma unroll
    for (int kk = 0; kk < 2; ++kk) {
      an[kk] = bn[kk]; an2[kk] = bn[kk] + 65536; am[kk] = bm[kk];
      asm volatile("" : "+v"(an[kk]), "+v"(an2[kk]), "+v"(am[kk]));
    }
    int vo;
    u32x4 pk;
    u32x2 kv;
    constexpr int NF0 = 152, F0 = 160, F1 = 172, F2 = 188;
    auto publish = [&](int d, int h) __attribute__((always_inline)) {
      if (h == 0) { vo = lane; PINV(vo); vo = vo * 16 + (((d + kq + 1) & 3) * 4 + kq) * 4 * MSG; PINV(vo); }
      const f32x4 v = acc[2 * d + (h >> 2)][h & 3];
      pk[(h & 1) * 2] = pk2(v[0], v[1]); pk[(h & 1) * 2 + 1] = pk2(v[2], v[3]);
      if (h & 1) __builtin_amdgcn_raw_buffer_store_b128(pk, rs, vo + (h >> 1) * 1024, 0, 16);
    };
    int fo = lane == 0 ? kq * 512 : (int)0x80000000;
#define MSG_FLAG(d) __builtin_amdgcn_raw_buffer_store_b32(tag, rc, fo + (((d) + kq + 1) & 3) * 2048, 0, 16)
    auto rdn = [&](int g, int r) __attribute__((always_inline)) {
      const int t = g_t[g], jt = g_q[g] * 2 + (r >> 1);
      fn[g & 1][r] = *LDSP(const bf16x8, (t == 1 ? an2[r & 1] : an[r & 1]) + (t == 0 ? 32768 : 0) + jt * 2048);
    };
    auto rdm = [&](int t, int i, int kk) __attribute__((always_inline)) { fm[kk][i] = *LDSP(const bf16x8, am[kk] + m_off(13 + t) + TO(i)); };
    auto group = [&](auto GI) __attribute__((always_inline)) {
      constexpr int gi = decltype(GI)::value;
      if (gi == G14) { BAR();
        SB(); }
      if (gi == 5) { BAR(); SB(); }
      if (gi == G15) {
        PAGE(11);
        BAR();
        SB();
        xq[0] = ld16_s<0>(x, xo); xq[1] = ld16_s<64>(x, xo);
        sq = ld8_s_sc1(stat, xo >> 11 << 3);
        kv = *(const u32x2*)(smem + LDS_SEL);
        SB();
      }
#pragma unroll
      for (int q = 0; q < 16; ++q) {
        const int n = gi * 16 + q;
        if (n == NF0) {
          wait_xq<12>(xq, sq);
          key[0] = __builtin_amdgcn_readfirstlane(kv.x); key[1] = __builtin_amdgcn_readfirstlane(kv.y);
          nf_pre();
          SB();
        }
        if (n == 112) {
        }
        if (n == 152) {
        }
        if (n == F0) {
          WAIT_VM(8); MSG_FLAG(0); SB(); }
        if (n == F1) { WAIT_VM(5); MSG_FLAG(1); SB(); }
        if (n == F2) {
          WAIT_VM(2);
          MSG_FLAG(2); fx = bld4_sc1(rc, po); SB(); }
        mm(chp(gi) || chn(gi), gi & 1, g_q[gi], q, 2);
        constexpr bool ni = gi + 1 < NG && (chp(gi + 1 < NG ? gi + 1 : gi) || chn(gi + 1 < NG ? gi + 1 : gi));
        auto nr = [](int r) { return ni && r >= 2 ? 5 - r : r; };
        if constexpr (chp(gi) && chn(gi)) {
          if (q < 4) rdm(g_t[gi], 2 + (q >> 1), q & 1); else if (q < 8) rdn(gi + 1, nr(q - 4)); else if (q < 12) rdm(g_t[gi + 1], (q - 8) >> 1, q & 1);
        } else if constexpr (chp(gi)) {
          if (q < 4) rdm(g_t[gi], 2 + (q >> 1), q & 1);
          else if (gi + 1 < NG && q < 12 && !(q & 1)) rdn(gi + 1, nr((q - 4) >> 1));
        } else if constexpr (chn(gi)) {
          if (!(q & 1)) { if (q < 8) rdn(gi + 1, nr(q >> 1)); else rdm(g_t[gi + 1], (q - 8) >> 2, (q >> 1) & 1); }
        } else {
          if (!(q & 1) && q < 8) rdn(gi + 1, nr(q >> 1));
        }
        if (gi == GP0 && q < 8) publish(0, q);
        if (gi == GP1 && q < 8) publish(1, q);
        if (gi == 9 && q < 8) publish(2, q);
        if (n >= NF0 && n < NF0 + 16) nf_step(n - NF0);
        SB();
      }
    };
#define GR4(i) group(IC<i>{}); group(IC<i + 1>{}); group(IC<i + 2>{}); group(IC<i + 3>{});
    GR4(0) GR4(4) GR4(8)
    asm volatile("s_nop 7");
    SB();
  }
#pragma unroll
  for (int i = 0; i < 4; ++i) *(f32x4*)(smem + LDS_ACC + wr * 16384 + wc * 4096 + i * 1024 + lane * 16) = acc[7][i];
#pragma unroll
  for (int i = 0; i < 4; ++i) ya[i] = acc[6][i];
  asm volatile("s_waitcnt lgkmcnt(0)" ::: "memory");
  if (lane == 0) sig[0] = 2;
  }

  PAGE(6);
  const unsigned rel0 = 0xffff - (key[0] & 0xffffu) - trow, rel1 = 0xffff - (key[1] & 0xffffu) - trow;
  if (min(rel0, rel1) < 16) {
#pragma unroll 1
    for (int c = 0; c < 2; ++c) {
      const unsigned rel = c ? rel1 : rel0;
      if (rel >= 16) continue;
      lds_vi cnt = (lds_vi)(smem + LDS_SEL + 24 + c * 4);
      int it = 0;
      while (cnt[0] != 4 && it < SPIN_MAX * 8) { __builtin_amdgcn_s_sleep(1); ++it; }
      asm volatile("" ::: "memory");
      if (rel == fr) {
        const float* fp = (const float*)(smem + LDS_FIN + c * 512) + (wid & 1) * 64 + fq * 8;
#pragma unroll
        for (int u4 = 0; u4 < 4; ++u4) {
          const f32x4 v = *(const f32x4*)(fp + (u4 >> 1) * 32 + (u4 & 1) * 4);
#pragma unroll
          for (int k = 0; k < 4; ++k) nf[u4 * 4 + k] += v[k];
        }
      }
    }
  }
  u32x4 pp[3][2];
  unsigned e_lo = 0x00003f80u, e_hi = 0x3f800000u;
  asm volatile("" : "+v"(e_lo), "+v"(e_hi));
  __amdgpu_buffer_rsrc_t rs_ = rs, rc_ = rc, ro_ = mk_rsrc(out);
  int mo[3] = {MSG_IDX(0) * 4 * MSG, MSG_IDX(1) * 4 * MSG, MSG_IDX(2) * 4 * MSG}, mv = lane * 16 + ha * 2048, mv2 = mv + 1024;
  asm volatile("" : "+s"(rs_), "+s"(rc_), "+s"(ro_), "+s"(mo[0]), "+s"(mo[1]), "+s"(mo[2]));
  unsigned as_ = lds_addr((const void*)smem) + LDS_SIG + (wid & 3) * 4, aa_ = lds_addr(smem + LDS_ACC) + (wid & 2) * 8192 + (wid & 1) * 4096 + lane * 16;
  int xs = ((trow + (lane >> 3)) * D_ + et * 128 + (wid & 1) * 64) * 2 + (lane & 7) * 16, xs2 = xs + 8 * D_ * 2;
  const unsigned mt = lds_addr(smem) + (wid & 2) * 8192 + (ha ? LDS_ACC + (wid & 1) * 4096 : n_off(14) + (wid & 1) * 2048);
  unsigned aw_ = mt + fr * 128 + ((fq ^ (fr & 7)) * 16), aw2 = aw_ ^ 64, ar_ = mt + (lane >> 3) * 128 + (((lane ^ (lane >> 3)) & 7) * 16);
  asm volatile("" : "+v"(mv), "+v"(mv2), "+v"(as_), "+v"(aa_), "+v"(xs), "+v"(xs2), "+v"(aw_), "+v"(aw2), "+v"(ar_));
  {
    unsigned sv_, it_ = SPIN_MAX * 8;
    asm volatile("s_cmp_eq_u32 %9, 0\n\ts_cbranch_scc1 2f\n"
                 "1:\n\tds_read_b32 %0, %10\n\ts_waitcnt lgkmcnt(0)\n\tv_cmp_eq_u32 vcc, 2, %0\n\ts_cbranch_vccnz 3f\n\ts_sleep 1\n\ts_sub_u32 %1, %1, 1\n\ts_cbranch_scc0 1b\n"
                 "3:\n\tds_read_b128 %4, %11\n\tds_read_b128 %5, %11 offset:1024\n\tds_read_b128 %6, %11 offset:2048\n\tds_read_b128 %7, %11 offset:3072\n"
                 "2:\n\ts_waitcnt vmcnt(0)\n\tv_cmp_eq_u32 vcc, %8, %3\n\ts_mov_b32 %2, vcc_lo"
                 : "=&v"(sv_), "+s"(it_), "=&s"(seen), "+v"(fx), "+v"(ya[0]), "+v"(ya[1]), "+v"(ya[2]), "+v"(ya[3])
                 : "s"(tag), "s"(ha), "v"(as_), "v"(aa_) : "vcc", "scc", "memory");
  }
#define MSG_POLL(m) do { \
    if (!((seen >> (m)) & 1)) { \
      unsigned pv_, it_ = SPIN_MAX; \
      asm volatile("1:\n\tbuffer_load_dword %0, %3, %4, 0 offen sc1\n\ts_waitcnt vmcnt(0)\n\tv_cmp_eq_u32 vcc, %5, %0\n\ts_mov_b32 %1, vcc_lo\n\t" \
                   "s_bitcmp1_b32 %1, %6\n\ts_cbranch_scc1 2f\n\ts_sub_u32 %2, %2, 1\n\ts_cbranch_scc1 2f\n\ts_sleep 1\n\ts_branch 1b\n2:" \
                   : "=&v"(pv_), "+s"(seen), "+s"(it_) : "v"(po), "s"(rc_), "s"(tag), "i"(m) : "vcc", "scc", "memory"); \
    } \
    pp[m][0] = __builtin_amdgcn_raw_buffer_load_b128(rs_, mv, mo[m], 16); \
    pp[m][1] = __builtin_amdgcn_raw_buffer_load_b128(rs_, mv2, mo[m], 16); } while (0)
#define MSG_ADD(m) do { \
    _Pragma("unroll") for (int u = 0; u < 8; ++u) { \
      ya[u >> 1][(u & 1) * 2] = add_bf(pp[m][u >> 2][u & 3], e_lo, ya[u >> 1][(u & 1) * 2]); ya[u >> 1][(u & 1) * 2 + 1] = add_bf(pp[m][u >> 2][u & 3], e_hi, ya[u >> 1][(u & 1) * 2 + 1]); } } while (0)
  MSG_POLL(2); MSG_POLL(1); MSG_POLL(0);
  asm volatile("s_waitcnt lgkmcnt(0)" : "+v"(ya[0]), "+v"(ya[1]), "+v"(ya[2]), "+v"(ya[3]) :: "memory");
  SB();
#pragma unroll
  for (int e = 0; e < 16; ++e) { float t = ya[e >> 2][e & 3] + nf[e]; PINV(t); ya[e >> 2][e & 3] = t; }
  SB();
  MSG_ADD(2); MSG_ADD(1);
  asm volatile("" : "+v"(ya[0]), "+v"(ya[1]), "+v"(ya[2]), "+v"(ya[3]));
  SB();
  MSG_ADD(0);
  {
    unsigned ov[8];
    SB();
#pragma unroll
    for (int u = 0; u < 8; ++u) ov[u] = pk2(ya[u >> 1][(u & 1) * 2], ya[u >> 1][(u & 1) * 2 + 1]);
    SB();
    *LDSP(u32x4, aw_) = (u32x4){ov[0], ov[1], ov[2], ov[3]};
    *LDSP(u32x4, aw2) = (u32x4){ov[4], ov[5], ov[6], ov[7]};
    asm volatile("" ::: "memory");
    __builtin_amdgcn_raw_buffer_store_b128(*LDSP(const u32x4, ar_), ro_, xs, 0, 16);
    __builtin_amdgcn_raw_buffer_store_b128(*LDSP(const u32x4, ar_ + 1024), ro_, xs2, 0, 16);
  }
}

extern "C" void kernel_launch(void* const* d_in, const int* in_sizes, int n_in, void* d_out, int out_size,
                              void* d_ws, size_t ws_size, hipStream_t stream) {
  const u16* x = (const u16*)d_in[0];
  const u16* ws_re = (const u16*)d_in[1];
  const u16* ws_im = (const u16*)d_in[2];
  const u16* wd_re = (const u16*)d_in[3];
  const u16* wd_im = (const u16*)d_in[4];
  const u16* w_r = (const u16*)d_in[6];
  const u16* w_gate = (const u16*)d_in[7];
  const u16* w_up = (const u16*)d_in[8];
  const u16* w_down = (const u16*)d_in[9];
  k_main<<<256, 512, 0, stream>>>(x, 0x7fffffffu, 0x00020000u, wd_re, 0x7fffffffu, 0x00020000u, wd_im, 0x7fffffffu, 0x00020000u, w_r, (char*)d_ws,
                                  ws_re, ws_im, w_gate, w_up, w_down, (u16*)d_out);
}
```

```cpp
#include <hip/hip_runtime.h>
#include <hip/hip_bf16.h>
#include <stdint.h>
#include <type_traits>

extern "C" __device__ unsigned long long llvm_dispatch_id(void) __asm("llvm.amdgcn.dispatch.id");
extern "C" __device__ float llvm_fmul_legacy(float, float) __asm("llvm.amdgcn.fmul.legacy");
typedef unsigned short u16;
typedef unsigned long long u64;
typedef __attribute__((ext_vector_type(8))) short bf16x8;
typedef __attribute__((ext_vector_type(4))) unsigned u32x4;
typedef __attribute__((ext_vector_type(2))) unsigned u32x2;
typedef __attribute__((ext_vector_type(4))) float f32x4;
typedef __attribute__((ext_vector_type(2))) float f32x2;
typedef __attribute__((ext_vector_type(2))) __bf16 bf16x2;
typedef __attribute__((address_space(3))) void* lds_vp;
typedef volatile __attribute__((address_space(3))) int* lds_vi;

constexpr int S_ = 2048, D_ = 1024, F_ = 3584;
template <int N> using IC = std::integral_constant<int, N>;

static __device__ __forceinline__ float bf2f(u16 u) { return __uint_as_float((unsigned)u << 16); }
static __device__ __forceinline__ u16 f2bf(float f) {
  __hip_bfloat16 b = __float2bfloat16(f);
  return *reinterpret_cast<u16*>(&b);
}
static __device__ __forceinline__ float rbf(float f) { return bf2f(f2bf(f)); }
static __device__ __forceinline__ float lo_bf(unsigned u) { return __uint_as_float(u << 16); }
static __device__ __forceinline__ float hi_bf(unsigned u) { return __uint_as_float(u & 0xffff0000u); }
static __device__ __forceinline__ unsigned pk2(float a, float b) {
  const bf16x2 v = __builtin_convertvector((f32x2){a, b}, bf16x2);
  return __builtin_bit_cast(unsigned, v);
}
static __device__ __forceinline__ float add_bf(unsigned u, unsigned e, float c) {
  return __builtin_amdgcn_fdot2_f32_bf16(__builtin_bit_cast(bf16x2, u), __builtin_bit_cast(bf16x2, e), c, false);
}
static __device__ __forceinline__ float ln_elem(float x, float mb, float sd, float ri) {
  const float d = rbf(x - mb);
  const float q0 = d * ri;
  return rbf(__builtin_fmaf(__builtin_fmaf(-q0, sd, d), ri, q0));
}
template <int CTRL> static __device__ __forceinline__ float dpp_f(float v) {
  return __int_as_float(__builtin_amdgcn_update_dpp(0, __float_as_int(v), CTRL, 0xf, 0xf, true));
}
template <int CTRL> static __device__ __forceinline__ unsigned dpp_u(unsigned v) {
  return (unsigned)__builtin_amdgcn_update_dpp(0, (int)v, CTRL, 0xf, 0xf, true);
}
static __device__ __forceinline__ float row_sum(float v) {
  v += dpp_f<0xB1>(v);
  v += dpp_f<0x4E>(v);
  v += dpp_f<0x141>(v);
  v += dpp_f<0x140>(v);
  return v;
}
static __device__ __forceinline__ float fold32(float a, float b) {
  const auto r = __builtin_amdgcn_permlane32_swap(__float_as_uint(a), __float_as_uint(b), false, false);
  return __uint_as_float(r[0]) + __uint_as_float(r[1]);
}
static __device__ __forceinline__ float fold16(float a, float b) {
  const auto r = __builtin_amdgcn_permlane16_swap(__float_as_uint(a), __float_as_uint(b), false, false);
  return __uint_as_float(r[0]) + __uint_as_float(r[1]);
}
static __device__ __forceinline__ float rdl(float v, int l) { return __int_as_float(__builtin_amdgcn_readlane(__float_as_int(v), l)); }

#define ST_JB 9
#define WSN 10
#define BST 0x25543321u
#define FSM 0xa2
#define GV_JB 7
#define DG 11
#define B14P 9
#define AB_STRIDE 32
#define SPIN_MAX (1 << 17)

static __device__ __forceinline__ unsigned lds_addr(const void* p) { return (unsigned)(size_t)(lds_vp)p; }

constexpr int MSG = 4096;
constexpr size_t WS_ABT   = 0;
constexpr size_t WS_SLAB  = WS_ABT + (size_t)D_ * 4096 * 2;
constexpr size_t WS_GU    = WS_SLAB + (size_t)64 * 64 * MSG;
constexpr size_t WS_DN    = WS_GU + (size_t)8 * F_ * 16;
constexpr size_t WS_FLAGS = WS_DN + (size_t)2 * 8 * 32 * 64 * 16;
constexpr int FL_X = 0;
constexpr int FL_AB = 64 * 64 * 32;
constexpr int FL_ST = FL_AB + 2048 * AB_STRIDE;
constexpr int FL_CE = FL_ST + 4096;
constexpr int FL_KEY = FL_CE + 4096;
constexpr int FL_EPOCH = FL_KEY + 4096;
constexpr int WS_MB = (int)((WS_FLAGS + (FL_EPOCH + 1) * 4 + 1048575) >> 20);

#define WAIT_VM(n) asm volatile("s_waitcnt vmcnt(" #n ")" ::: "memory")
template <int N> static __device__ __forceinline__ void wait_vm() { asm volatile("s_waitcnt vmcnt(%0)" :: "i"(N) : "memory"); }
#define BAR_LGKM() asm volatile("s_waitcnt lgkmcnt(0)\n\ts_barrier" ::: "memory")
#define BAR() asm volatile("s_barrier" ::: "memory")
#define SB() __builtin_amdgcn_sched_barrier(0)
#define PAGE(n) asm volatile("s_branch 2f\n\t.p2align 12\nk_pg" #n ":\n\ts_setpc_b64 s[42:43]\n2:")
#define CALL_PG(n) asm volatile("s_getpc_b64 s[40:41]\n3:\n\ts_mov_b32 s42, k_pg" #n "-3b\n\ts_ashr_i32 s43, s42, 31\n\ts_add_u32 s40, s40, s42\n\ts_addc_u32 s41, s41, s43\n\t" \
                                "s_swappc_b64 s[42:43], s[40:41]" ::: "s40", "s41", "s42", "s43", "scc", "memory")

static __device__ __forceinline__ __amdgpu_buffer_rsrc_t mk_rsrc(const void* p) {
  return __builtin_amdgcn_make_buffer_rsrc((void*)p, 0, 0x7fffffff, 0x00020000);
}
#define DMA_(rsrc, voff, mbase, MI, sbase, SI, pol) do { unsigned _t; \
    asm volatile("s_add_i32 m0, %2, %3\n\ts_add_i32 %0, %5, %6\n\tbuffer_load_dwordx4 %1, %4, %0 offen" pol " lds" \
                 : "=&s"(_t) : "v"(voff), "s"(mbase), "i"(MI), "s"(rsrc), "s"(sbase), "i"(SI) : "memory", "scc"); } while (0)
#define TOUCH(rsrc, voff, mbase, MI, soff) \
    asm volatile("s_add_i32 m0, %1, %2\n\ts_nop 0\n\tbuffer_load_dword %0, %3, %4 offen lds" \
                 :: "v"(voff), "s"(mbase), "i"(MI), "s"(rsrc), "s"(soff) : "memory", "scc")
template <int OFF> static __device__ __forceinline__ u32x4 ld16_s(const void* p, int voff) {
  u32x4 v;
  asm volatile("global_load_dwordx4 %0, %1, %2 offset:%3" : "=v"(v) : "v"(voff), "s"(p), "i"(OFF) : "memory");
  return v;
}
template <int OFF> static __device__ __forceinline__ void sld(unsigned& sink, const void* p, int soff) {
  asm volatile("s_load_dword %0, %1, %2 offset:%3" : "+s"(sink) : "s"(p), "s"(soff), "i"(OFF) : "memory");
}
static __device__ __forceinline__ unsigned gld4_s(const void* p, unsigned voff) {
  unsigned v;
  asm volatile("global_load_dword %0, %1, %2" : "=v"(v) : "v"(voff), "s"(p) : "memory");
  return v;
}
static __device__ __forceinline__ u32x2 bld8_sc1(__amdgpu_buffer_rsrc_t r, unsigned voff) {
  u32x2 v;
  asm volatile("buffer_load_dwordx2 %0, %1, %2, 0 offen sc1" : "=v"(v) : "v"(voff), "s"(r) : "memory");
  return v;
}
template <int N> static __device__ __forceinline__ void wait_xs(unsigned& a, u32x2& b) { asm volatile("s_waitcnt vmcnt(%2)" : "+v"(a), "+v"(b) : "i"(N) : "memory"); }
template <int N> static __device__ __forceinline__ void wait_xq(u32x4 (&a)[2], u32x2& b) { asm volatile("s_waitcnt vmcnt(%3)" : "+v"(a[0]), "+v"(a[1]), "+v"(b) : "i"(N) : "memory"); }
static __device__ __forceinline__ u32x2 ld8_s_sc1(const void* p, int voff) {
  u32x2 v;
  asm volatile("global_load_dwordx2 %0, %1, %2 sc1" : "=v"(v) : "v"(voff), "s"(p) : "memory");
  return v;
}
static __device__ __forceinline__ void lgkm0(unsigned& sink) { asm volatile("s_waitcnt lgkmcnt(0)" : "+s"(sink) :: "memory"); }
template <typename T> static __device__ __forceinline__ void pin4(T& a, T& b, T& c, T& d) { asm volatile("" : "+s"(a), "+s"(b), "+s"(c), "+s"(d)); }
template <typename T, typename U> static __device__ __forceinline__ void pins(T& a, U& b) { asm volatile("" : "+s"(a), "+s"(b)); }
static __device__ __forceinline__ void launder(int& v, int& a, int& b) { asm volatile("" : "+v"(v)); asm volatile("" : "+s"(a), "+s"(b)); }
static __device__ __forceinline__ unsigned bld4_sc1(__amdgpu_buffer_rsrc_t r, int voff) {
  unsigned v;
  asm volatile("buffer_load_dword %0, %1, %2, 0 offen sc1" : "=v"(v) : "v"(voff), "s"(r) : "memory");
  return v;
}
static __device__ __forceinline__ u32x4 bld16_nt(__amdgpu_buffer_rsrc_t r, int voff, int soff) {
  u32x4 v;
  asm volatile("buffer_load_dwordx4 %0, %1, %2, %3 offen nt" : "=v"(v) : "v"(voff), "s"(r), "s"(soff) : "memory");
  return v;
}
static __device__ __forceinline__ void bld16_nt_io(u32x4& v, __amdgpu_buffer_rsrc_t r, int voff, int soff) {
  asm volatile("buffer_load_dwordx4 %0, %1, %2, %3 offen nt" : "+v"(v) : "v"(voff), "s"(r), "s"(soff) : "memory");
}
static __device__ __forceinline__ u32x4 bld16_sc1(__amdgpu_buffer_rsrc_t r, int voff, int soff) {
  u32x4 v;
  asm volatile("buffer_load_dwordx4 %0, %1, %2, %3 offen sc1" : "=v"(v) : "v"(voff), "s"(r), "s"(soff) : "memory");
  return v;
}
static __device__ __forceinline__ u32x2 granule_wait(const u64* p, unsigned tag) {
  u64 v = 0;
#pragma unroll 1
  for (int it = 0; it < SPIN_MAX; ++it) {
    v = __hip_atomic_load(p, __ATOMIC_RELAXED, __HIP_MEMORY_SCOPE_AGENT);
    if (__all((unsigned)(v >> 32) == tag)) break;
    __builtin_amdgcn_s_sleep(2);
  }
  return (u32x2){(unsigned)v, (unsigned)(v >> 32)};
}
static __device__ __forceinline__ float key_score(unsigned key) {
  const unsigned u = key >> 16;
  return bf2f((u16)((u & 0x8000u) ? (u & 0x7fffu) : (~u & 0xffffu)));
}
static __device__ __forceinline__ void merge2(unsigned& a1, unsigned& a2, unsigned b1, unsigned b2) {
  const unsigned lo = min(a1, b1);
  a1 = max(a1, b1);
  a2 = max(lo, max(a2, b2));
}
static __device__ __forceinline__ float sqdev16(const u32x4 (&v)[2], float mb) {
  f32x2 q2 = {0.f, 0.f};
#pragma unroll
  for (int i = 0; i < 8; ++i) {
    const f32x2 e = (f32x2){lo_bf(v[i >> 2][i & 3]), hi_bf(v[i >> 2][i & 3])} - mb;
    q2 += e * e;
  }
  return q2.x + q2.y;
}

constexpr int G1_NST = 4, G1_NKT = 16, G1_STAGE = 32768;
constexpr int G2_NKT = 16, G2_MRING = 98304;
constexpr int n_off(int k) { return (k % 3) * 32768; }
constexpr int m_off(int k) { return ((k + 2) % 3) * 16384; }
constexpr int k_ord(int k) { return k < 2 ? k : (k < 9 ? 2 * (k - 1) : 2 * (k - 8) + 1); }
constexpr int LDS_DUMMY = G2_MRING + 49152;
constexpr int LDS_GU = LDS_DUMMY + 4096;
constexpr int LDS_FIN = LDS_GU + 4096;
constexpr int LDS_MB = LDS_FIN + 4096;
constexpr int LDS_SEL = LDS_MB + 1536;
constexpr int LDS_SIG = LDS_SEL + 64;
constexpr int LDS_KEY = LDS_SIG + 32;
constexpr int LDS_BYTES = LDS_KEY + 128;
constexpr int LDS_ACC = n_off(G2_NKT - 3);

constexpr int tpos[16] = {0, 0, 0, 0, 100, 110, 115, 2, 2, 3, 4, 5, 0, 0, 0, 0};
constexpr int twho[16] = {0, 0, 0, 0, 7, 7, 7, 0, 1, 2, 3, 4, 0, 0, 0, 0};
constexpr int dn_cnt(int q) { return q == 5 ? 3 : q == 8 ? 4 : 0; }
constexpr int dn_first(int q) { return q <= 5 ? 0 : q <= 8 ? 3 : 7; }

static __device__ __forceinline__ void code_touch(unsigned& sink, unsigned off) {
  asm volatile("s_getpc_b64 s[40:41]\n\tk_main_rt:\n\ts_min_u32 %1, %1, k_main_end-k_main_rt-196\n\t"
               "s_load_dword %0, s[40:41], %1 offset:0\n\ts_load_dword %0, s[40:41], %1 offset:64\n\ts_load_dword %0, s[40:41], %1 offset:128\n\ts_load_dword %0, s[40:41], %1 offset:192"
               : "+s"(sink), "+s"(off) :: "s40", "s41", "scc", "memory");
}
static __device__ __forceinline__ void touch1(__amdgpu_buffer_rsrc_t r, int voff, unsigned mb, int soff) { TOUCH(r, voff, mb, LDS_DUMMY, soff); }
typedef const __attribute__((address_space(1))) u16* gptr;
typedef __attribute__((ext_vector_type(8))) u64 u64x8;
extern "C" __global__ __launch_bounds__(512, 2) void k_main(const u16* x, unsigned x2, unsigned x3, const u16* wd_re, unsigned r2, unsigned r3,
                                                 const u16* wd_im, unsigned i2, unsigned i3, const u16* w_r,
                                                 char* wsp, const u16* ws_re, const u16* ws_im,
                                                 const u16* w_gate, const u16* w_up, const u16* w_down, u16* out) {
  __shared__ __attribute__((aligned(16))) char smem[LDS_BYTES];
  u64x8 ka;
  u32x4 rm, rn;
  unsigned kz;
  asm volatile("s_load_dwordx4 %0, %4, 0\n\ts_load_dwordx16 %2, %4, 48\n\ts_andn2_b32 %3, 4, %5\n\ts_lshl_b32 %3, %3, 2\n\ts_load_dwordx4 %1, %4, %3 offset:16"
               : "=&s"(rm), "=&s"(rn), "=&s"(ka), "=&s"(kz) : "s"(__builtin_amdgcn_kernarg_segment_ptr()), "s"(blockIdx.x) : "scc", "memory");
  SB();
  const int tid = threadIdx.x, lane = tid & 63;
  const int wid = __builtin_amdgcn_readfirstlane(tid >> 6);
  const int g = blockIdx.x ^ 6, xcd = g & 7, j = g >> 3;
  if (wid < 4) {
    unsigned co = ((j * 4 + wid) * 4 + (lane & 3)) * 128;
    asm volatile(".subsection 1\nk_main_end:\n.subsection 0\n\tv_min_u32 %0, k_main_end-k_main-132, %0\n\ts_getpc_b64 s[40:41]\n\ts_mov_b32 m0, %1\n\ts_mov_b64 exec, 15\n\t"
                 "global_load_lds_dword %0, s[40:41]\n\ts_mov_b64 exec, -1"
                 : "+v"(co) : "i"(LDS_DUMMY + 768) : "s40", "s41", "memory");
  }
  const int pw = wid - 4;
  const int row0 = (wid & 3) * 8 + (lane >> 3);
  const int cm = ((lane & 7) ^ (((row0 >> 1) & 1) | ((wid & 3) << 1))) * 16;
  int vm = row0 * (D_ * 2) + cm;
  asm volatile("" : "+v"(vm));
  const int kq = xcd >> 1;
  const int tt = (xcd & 1) * 4 + (j >> 3);
  const int et = j & 7;
  const int z = kq >> 1, st = (kq & 1) * 8 + tt;
  unsigned o_m = st * 128 * D_ * 2, o_n = et * 128 * D_ * 2, mb0 = lds_addr(smem) + pw * 1024;
  asm volatile("" : "+s"(o_m), "+s"(o_n), "+s"(mb0));
#define G1_X(kt, so, q) DMA_(rm, vm, mb0, (so) + (q) * 4096, o_m, (kt) * 128 + (((q) & 1) * 64 + ((q) >> 1) * 32) * D_ * 2, "")
#define G1_W(kt, so, q) DMA_(rn, vm, mb0, (so) + 16384 + (q) * 4096, o_n, (kt) * 128 + (q) * 32 * D_ * 2, "")
#define G1_STAGE_TILE(kt, so, W) do { SB(); G1_X(kt, so, 0); W(0); G1_X(kt, so, 1); W(1); G1_X(kt, so, 2); W(2); G1_X(kt, so, 3); W(3); \
    G1_W(kt, so, 0); W(4); G1_W(kt, so, 1); W(5); G1_W(kt, so, 2); W(6); G1_W(kt, so, 3); W(7); SB(); } while (0)
#define G1_STAGE_A(kt, so, W) do { SB(); G1_X(kt, so, 0); W(0); G1_X(kt, so, 2); W(1); \
    G1_W(kt, so, 0); W(2); G1_W(kt, so, 1); W(3); G1_W(kt, so, 2); W(4); G1_W(kt, so, 3); W(5); W(6); W(7); SB(); } while (0)
#define G1_STAGE_B(kt, so) do { SB(); G1_X(kt, so, 1); G1_X(kt, so, 3); SB(); } while (0)
#define NOW(i)
  asm volatile("s_waitcnt lgkmcnt(0)" : "+s"(ka), "+s"(rm), "+s"(rn) :: "memory");
  int lrow, drow, l16;
  unsigned o_l, o_d;
  auto rows = [&](int i) __attribute__((always_inline)) {
    int g_ = g, l_ = lane;
    if (i == 0) { asm volatile("" : "+s"(g_)); drow = (g_ & ~7) + pw * 2; lrow = drow + (g_ & 3) * 512 + (g_ & 4) * 64; asm volatile("" : "+s"(drow), "+s"(lrow)); }
    if (i == 1) { asm volatile("" : "+s"(g_)); drow += (g_ & 3) * 256; o_l = lrow * D_ * 2; o_d = drow * D_ * 2; asm volatile("" : "+s"(drow), "+s"(o_l), "+s"(o_d)); }
    if (i == 2) { asm volatile("" : "+v"(l_)); l16 = l_ * 16; asm volatile("" : "+v"(l16)); }
  };
  if (__builtin_expect(wid >= 4, 1)) {
    G1_STAGE_TILE(0, 0, rows);
    __builtin_amdgcn_s_setprio(3);
  }
  x = (const u16*)(gptr)((u64)rm[0] | (u64)rm[1] << 32);
  const u16* wdz = (const u16*)(gptr)((u64)rn[0] | (u64)rn[1] << 32);
  w_r = (const u16*)(gptr)ka[0]; wsp = (char*)(gptr)ka[1]; ws_re = (const u16*)(gptr)ka[2]; ws_im = (const u16*)(gptr)ka[3];
  w_gate = (const u16*)(gptr)ka[4]; w_up = (const u16*)(gptr)ka[5]; w_down = (const u16*)(gptr)ka[6]; out = (u16*)(gptr)ka[7];
  int* flags = (int*)(wsp + WS_FLAGS);
  u64* stat = (u64*)(flags + FL_ST);
  const int fr = lane & 15, fq = lane >> 4;
  const int ha = wid >> 2;
  int trow, xo, po;
  const char *prs, *prc;
  auto epi_setup = [&]() __attribute__((always_inline)) {
    int t_ = tt, l_ = lane;
    asm volatile("" : "+s"(t_));
    asm volatile("" : "+v"(l_));
    trow = t_ * 256 + ((wid >> 1) & 1) * 128 + kq * 32 + ha * 16;
    xo = ((trow + (l_ & 15)) * D_ + et * 128 + (wid & 1) * 64 + (l_ >> 4) * 8) * 2;
    prs = wsp + WS_SLAB + (size_t)((t_ * 8 + et) * 64 + (wid & 3)) * MSG;
    prc = (const char*)(flags + FL_X + ((t_ * 8 + et) * 64 + (wid & 3)) * 32);
    asm volatile("" : "+s"(trow), "+s"(prs), "+s"(prc));
    po = l_ < 3 ? (kq * 4 + ((kq + 1 + l_) & 3)) * 512 : (int)0x80000000;
    asm volatile("" : "+v"(xo), "+v"(po));
  };
#define rs mk_rsrc(prs)
#define rc mk_rsrc(prc)
  lds_vi sig = (lds_vi)(smem + LDS_SIG + (wid & 3) * 4);
  unsigned tag;
  u32x4 xq[2];
  u32x2 sq;
  f32x4 ya[4];
  unsigned key[2];
  float nf[16], la0, la1;
#define PINV(a) asm volatile("" : "+v"(a))
  auto nf_pre = [&]() __attribute__((always_inline)) {
    if (__builtin_expect(!__all(sq.y == tag), 0)) sq = granule_wait(stat + (xo >> 11), tag);
    const float ri = __builtin_amdgcn_rcpf(hi_bf(sq.x));
    la0 = -(lo_bf(sq.x) * ri); la1 = 1.f + ri;
    PINV(la0); PINV(la1);
  };
  auto nf_step = [&](int e) __attribute__((always_inline)) {
    const int u = e >> 1;
    nf[e] = (e & 1) ? hi_bf(xq[u >> 2][u & 3]) : lo_bf(xq[u >> 2][u & 3]);
    PINV(nf[e]);
    nf[e] = __builtin_fmaf(nf[e], la1, la0);
    PINV(nf[e]);
  };
#define MSG_IDX(m) (kq * 4 + ((kq + 1 + (m)) & 3))
  unsigned seen, fx;

  if (__builtin_expect(wid >= 4, 1)) {
    __builtin_amdgcn_s_waitcnt(0xc07f);
    constexpr int O_ST = (FL_ST - FL_AB) * 4, O_CE = (FL_CE - FL_AB) * 4, O_KEY = (FL_KEY - FL_AB) * 4, O_EP = (FL_EPOCH - FL_AB) * 4;
    const __amdgpu_buffer_rsrc_t rf = mk_rsrc(wsp + WS_FLAGS + FL_AB * 4);
    u32x4 lv[2], lv2[2], lw[2], ld[2], ld2[2];
    unsigned epv;
    {
      const char* xrow = (const char*)x + o_l;
      const char* wdrow = (const char*)wdz + o_d;
      lv[0] = ld16_s<0>(xrow, l16); lv[1] = ld16_s<1024>(xrow, l16);
      lv2[0] = ld16_s<2048>(xrow, l16); lv2[1] = ld16_s<3072>(xrow, l16);
      ld[0] = ld16_s<0>(wdrow, l16); ld[1] = ld16_s<1024>(wdrow, l16);
      ld2[0] = ld16_s<2048>(wdrow, l16); ld2[1] = ld16_s<3072>(wdrow, l16);
      lw[0] = ld16_s<0>(w_r, l16); lw[1] = ld16_s<1024>(w_r, l16);
      asm volatile("buffer_load_dword %0, off, %1, %2 sc1" : "=v"(epv) : "s"(rf), "s"(O_EP) : "memory");
    }
    SB();
    const unsigned long long did = llvm_dispatch_id();
    const unsigned dmix = (unsigned)did * 0x85EBCA6Bu + (unsigned)(did >> 32) * 0xC2B2AE35u + 0x27D4EB2Fu;
    const bool moe = et != 7;
    unsigned epoch;
    const int ci = lane & 15, rq = lane >> 4, tk = (lane >> 3) & 1;
    int mat, cg, dq, d0;
    u32x4 wv[16];
    const u16 *pgu, *pn2;
    const char *pm2, *spb;
#define rw __builtin_amdgcn_make_buffer_rsrc((void*)pgu, 0, moe ? 0x7fffffff : 0, 0x00020000)
#define rn2 mk_rsrc(pn2)
#define rm2 mk_rsrc(pm2)
    int vw, vn2, vm2;
    int ord2;
    auto k_blk = [&](int k) __attribute__((always_inline)) { return k >= 9 ? (ord2 >> (4 * (k - 9))) & 7 : tt ^ (k_ord(k) >> 1); };
#define KROT(kk_) (k_blk(kk_) * 256 + (k_ord(kk_) & 1) * 128)
    int tv2;
    unsigned sd = 0;
    auto touches = [&](auto P) __attribute__((always_inline)) {
      constexpr int p = decltype(P)::value;
      int e = et;
      asm volatile("" : "+s"(e));
#define TCH(k) if constexpr (tpos[k] == p) { if (e == twho[k]) { const int kb = KROT(k); touch1(rn2, tv2, mb0, kb); } }
      TCH(0) TCH(1) TCH(2) TCH(3) TCH(4) TCH(5) TCH(6) TCH(7) TCH(8) TCH(9) TCH(10) TCH(11) TCH(12) TCH(13) TCH(14) TCH(15)
    };
    unsigned mq0, mq1, mq2, mq3;
    const int cg8 = g & 7, rgb = g >> 3;
    const __amdgpu_buffer_rsrc_t rdn = mk_rsrc(w_down);
    const __amdgpu_buffer_rsrc_t rgu = mk_rsrc(wsp + WS_GU);
    int vdn, vgu;
    u32x4 dv[7], gv[4];
    int abo, abq[3];
    unsigned lma;
    unsigned sel[2][4];
    float g0, g1;
    u32x4 kk[4];
    unsigned k1 = 0, k2 = 0;
    const int ko_ = O_KEY + pw * 4096;
#define MM(a, b, c) __builtin_amdgcn_mfma_f32_16x16x32_bf16(__builtin_bit_cast(bf16x8, a), __builtin_bit_cast(bf16x8, b), c, 0, 0, 0)
#define PIN1(a) asm volatile("" : "+v"(a))
#define PIN2(a, b) asm volatile("" : "+v"(a), "+v"(b))
#define PIN3(a, b, c) asm volatile("" : "+v"(a), "+v"(b), "+v"(c))
    {
      int gro, go;
      u32x2 gr;
      u32x4 oh[5];
      unsigned dk[4], kl;
#pragma unroll
      for (int l = 0; l < WSN; ++l) asm volatile("" : "=v"(wv[l]));
      auto diag = [&](const f32x4& m) __attribute__((always_inline)) {
        return __uint_as_float((__float_as_uint(m[0]) & dk[0]) | (__float_as_uint(m[1]) & dk[1]) | (__float_as_uint(m[2]) & dk[2]) | (__float_as_uint(m[3]) & dk[3]));
      };
      auto setup = [&](auto JB, auto GP) __attribute__((always_inline)) {
        constexpr int jb = decltype(JB)::value, gp = decltype(GP)::value;
        SB();
        {
          int ln = lane, jj = j, p_ = pw;
          launder(ln, jj, p_);
          const int c_ = ln & 15, r_ = ln >> 4, e_ = jj & 7, t_ = (xcd & 1) * 4 + (jj >> 3);
          if constexpr (jb == 1 && gp == 0) {
            pn2 = ((kq >> 1) ? ws_im : ws_re) + (long)t_ * 256 * S_ + (kq & 1) * 1024;
            spb = (const char*)(pn2 + (e_ * 4 + p_) * 8 * S_);
            pins(pn2, spb);
          }
          if constexpr (jb == 1 && gp >= 1 && gp < 6) {
            const unsigned o = c_ == gp - 1 ? 0x3f803f80u : 0u;
            oh[gp - 1] = (u32x4){o, o, o, o};
            PIN1(oh[gp - 1]);
          }
          if constexpr (jb == 1 && gp == 6) {
            const int gm = moe ? (jj - (jj >> 3)) * 8 + xcd : 0;
            mat = gm / 112; cg = (gm % 112) >> 2; dq = gm & 3; d0 = dq * 256 + p_ * 64;
            pin4(mat, cg, dq, d0);
          }
          if constexpr (jb == 2 && gp == 0) {
            pgu = mat ? w_up : w_gate;
            pm2 = wsp + WS_ABT + ((long)e_ * 128 * 4096 + kq * 1024) * 2;
            pins(pgu, pm2);
            vw = ((d0 + r_ * 16) * F_ + cg * 128 + c_ * 8) * 2;
            PIN1(vw);
          }
          if constexpr (jb == 2 && (gp == 1 || gp == 2)) {
            dk[gp * 2 - 2] = c_ == r_ * 4 + gp * 2 - 2 ? ~0u : 0u; dk[gp * 2 - 1] = c_ == r_ * 4 + gp * 2 - 1 ? ~0u : 0u;
            PIN2(dk[gp * 2 - 2], dk[gp * 2 - 1]);
          }
          if constexpr (jb == 2 && gp == 3) { kl = lrow + (ln >> 5); go = (ln & 30) ? (int)0x80000000 : ((ln & 1) ? O_KEY : O_ST) + (int)kl * 8; PIN2(kl, go); }
          if constexpr (jb == 2 && gp == 4) { kl = 0xffff - kl; go = (ln & ~1) == 2 ? O_CE + (z * D_ + drow + (ln & 1)) * 8 : go; PIN2(kl, go); }
          if constexpr (jb == 2 && gp == 5) {
            gro = (p_ < 2 ? O_ST + (st * 128 + p_ * 64) * 8 : O_CE + (z * D_ + e_ * 128 + (p_ - 2) * 64) * 8) + ln * 8;
            tv2 = (p_ * 64 + ln) * 4096;
            lma = lds_addr(smem + LDS_MB) + (p_ < 2 ? p_ * 256 : 512 + p_ * 256) + ln * 4;
            PIN3(gro, tv2, lma);
          }
          if constexpr (jb == 2 && gp == 6) {
            const u64 lo = (t_ & 2) ? ((t_ & 4) ? 0x0150234601503247ull : 0x0514670205147603ull) : ((t_ & 4) ? 0x0123067401532076ull : 0x0567423005176432ull);
            ord2 = (int)(lo >> ((t_ & 1) * 32));
            pins(ord2, p_);
          }
          if constexpr (jb == 7 && gp == 0) {
            const int r0 = p_ * 8 + (ln >> 3);
            vn2 = r0 * (S_ * 2) + ((ln & 7) ^ ((r0 >> 1) & 7)) * 16; vm2 = r0 * 8192 + (vm & 127);
            PIN2(vn2, vm2);
          }
          if constexpr (jb == 7 && gp == 1) { vdn = jj * 112 + p_ * 28 + r_ * 7; vgu = (((ln >> 3) & 1) * 4 * F_ + vdn + min(ln & 7, 6)) * 16; PIN2(vdn, vgu); }
          if constexpr (jb == 7 && gp == 2) { vdn = (vdn * D_ + cg8 * 128 + c_ * 8) * 2; PIN1(vdn); }
          if constexpr (jb == 7 && gp == 3) {
            abo = (((kq * 8 + e_) * 4 + p_) * 16 + c_) * AB_STRIDE * 4;
            abq[2] = ln >= 16 || (ln & 7) == t_ ? (int)0x80000000 : abo;
            PIN2(abo, abq[2]);
          }
          if constexpr (jb == 7 && gp == 4) {
            abq[1] = ln >= 8 ? (int)0x80000000 : abq[2];
            abq[0] = ((ln ^ t_) & 4) ? (int)0x80000000 : abq[1];
            PIN2(abq[0], abq[1]);
          }
          if constexpr (jb == 7 && gp == 5) {
            const unsigned mb = lds_addr(smem) + p_ * 1024;
            mq0 = mb + ((3 - kq) & 3) * 4096; mq1 = mb + ((4 - kq) & 3) * 4096; mq2 = mb + ((5 - kq) & 3) * 4096; mq3 = mb + ((6 - kq) & 3) * 4096;
            pin4(mq0, mq1, mq2, mq3);
          }
          if constexpr (jb == 8 && gp < 4) {
#pragma unroll
            for (int h = 0; h < 2; ++h) {
              const unsigned hv = h ? 0x0302u : 0x0100u;
              sel[h][gp] = ((ln & 7) >> 1) == gp ? ((ln & 1) ? (hv << 16) | 0x0c0cu : 0x0c0c0000u | hv) : 0x0c0c0c0cu;
            }
            PIN2(sel[0][gp], sel[1][gp]);
          }
        }
        SB();
      };
#define WS1(i) setup(IC<1>{}, IC<i>{})
#define WS2(i) setup(IC<2>{}, IC<i>{})
#define WS3(i) do { setup(IC<7>{}, IC<i>{}); if (i >= 4) setup(IC<8>{}, IC<(i) & 3>{}); } while (0)
      G1_STAGE_TILE(1, G1_STAGE, WS1);
      WAIT_VM(19); BAR();
      G1_STAGE_TILE(2, 2 * G1_STAGE, WS2);
      G1_STAGE_TILE(3, 3 * G1_STAGE, WS3);
      auto work1 = [&](auto JB, auto GP) __attribute__((always_inline)) {
        constexpr int jb = decltype(JB)::value, gp = decltype(GP)::value;
        SB();
        if constexpr (jb == ST_JB + 3) {
          if constexpr (gp == 0) { if (!__all(gr.y == tag)) gr = granule_wait((const u64*)((const char*)(flags + FL_AB) + gro), tag); PIN1(gr); }
          if constexpr (gp == 1) { g1 = __builtin_amdgcn_rcpf(__uint_as_float((gr.x & 0xffff0000u) ^ (z << 31))); PIN1(g1); }
          if constexpr (gp == 2) { g0 = pw < 2 ? -(lo_bf(gr.x) * g1) : __uint_as_float(gr.x); PIN1(g0); }
        }
        SB();
      };
      auto step1 = [&](auto JB) __attribute__((always_inline)) {
        constexpr int jb = decltype(JB)::value;
        constexpr int n16 = jb == 12 ? 12 : jb == 11 ? 14 : 16;
        constexpr auto wl = [](int q) { return q == ST_JB ? 1 : 0; };
        if constexpr (jb == ST_JB + 3) {
          asm volatile("s_waitcnt vmcnt(%1)" : "+v"(gr) : "i"(n16 + wl(jb - 2) + wl(jb - 1)) : "memory");
        } else wait_vm<n16 + wl(jb - 3) + wl(jb - 2) + wl(jb - 1)>();
        BAR();
#define W1(i) work1(JB, IC<i>{})
        if constexpr (jb < 10) G1_STAGE_TILE(jb - 1 + G1_NST, ((jb - 1) % G1_NST) * G1_STAGE, W1);
        else G1_STAGE_A(jb - 1 + G1_NST, ((jb - 1) % G1_NST) * G1_STAGE, W1);
        if constexpr (jb == 12) { G1_STAGE_B(13, G1_STAGE); G1_STAGE_B(14, 2 * G1_STAGE); }
        if constexpr (jb == ST_JB) {
          asm volatile("buffer_load_dwordx2 %0, %1, %2, 0 offen sc1" : "=v"(gr) : "v"(gro), "s"(rf) : "memory");
        }
        touches(IC<100 + jb>{});
        SB();
      };
      static_assert(ST_JB == 9, "schedule");
      {
        PAGE(1);
        const int bs = (BST >> (4 * et)) & 15;
        unsigned rm_ = o_m + 512, rn_ = o_n + 512 - 128, ln_ = mb0;
        int jb = 0, sn = tt * 256;
        auto stepr = [&]() __attribute__((always_inline)) {
          const int so = sn;
          const unsigned rl = ln_;
          WAIT_VM(16); BAR();
          SB();
          DMA_(rm, vm, rl, 0, rm_, 0, ""); sld<0>(sd, spb, so); DMA_(rm, vm, rl, 4096, rm_, 64 * D_ * 2, ""); sld<64>(sd, spb, so);
          DMA_(rm, vm, rl, 8192, rm_, 32 * D_ * 2, ""); sld<4096>(sd, spb, so); DMA_(rm, vm, rl, 12288, rm_, 96 * D_ * 2, ""); sld<4096 + 64>(sd, spb, so);
          rm_ += 128; rn_ += 128; ++jb;
          sn = (((0x4210 >> ((jb >> 1) * 4)) & 7) ^ (2 * tt)) * 128 + (jb & 1) * 16384; ln_ = mb0 + (jb & 3) * G1_STAGE;
          asm volatile("" : "+s"(rm_), "+s"(jb), "+s"(rn_), "+s"(sn), "+s"(ln_));
          DMA_(rn, vm, rl, 16384, rn_, 0, ""); sld<8192>(sd, spb, so); DMA_(rn, vm, rl, 16384 + 4096, rn_, 32 * D_ * 2, ""); sld<8192 + 64>(sd, spb, so);
          DMA_(rn, vm, rl, 16384 + 8192, rn_, 64 * D_ * 2, ""); sld<12288>(sd, spb, so); sld<12288 + 64>(sd, spb, so); DMA_(rn, vm, rl, 16384 + 12288, rn_, 96 * D_ * 2, "");
          SB();
        };
#pragma unroll 1
        do stepr(); while (jb < bs);
        if (moe) {
#pragma unroll
          for (int l = 0; l < WSN; ++l) bld16_nt_io(wv[l], rw, vw, l * F_ * 2);
        }
        touches(IC<100>{});
        asm volatile("" : "+v"(lv[0]), "+v"(lv[1]), "+v"(lw[0]), "+v"(lw[1]), "+v"(ld[0]), "+v"(ld[1]), "+v"(lv2[0]), "+v"(lv2[1]), "+v"(ld2[0]), "+v"(ld2[1]), "+v"(epv));
        SB();
        epoch = __builtin_amdgcn_readfirstlane(epv); tag = ((epoch + 1u) * 0x9E3779B1u ^ dmix) | 1u;
        const f32x4 z4 = {0.f, 0.f, 0.f, 0.f};
        f32x4 gw[2], gx[2];
        gw[0] = MM(lv[1], lw[1], MM(lv[0], lw[0], z4)); gw[1] = MM(lv2[1], lw[1], MM(lv2[0], lw[0], z4));
        gx[0] = MM(lv[1], lv[1], MM(lv[0], lv[0], z4)); gx[1] = MM(lv2[1], lv2[1], MM(lv2[0], lv2[0], z4));
        SB();
        f32x4 sa4 = MM(lv[0], oh[0], z4);
        sa4 = MM(lv2[0], oh[1], sa4); sa4 = MM(lv[1], oh[0], sa4); sa4 = MM(lv2[1], oh[1], sa4);
        sa4 = MM(ld[0], oh[2], sa4); sa4 = MM(ld[1], oh[2], sa4); sa4 = MM(ld2[0], oh[3], sa4); sa4 = MM(ld2[1], oh[3], sa4);
        sa4 = MM(lw[0], oh[4], sa4); sa4 = MM(lw[1], oh[4], sa4);
        float gs = row_sum(fold16(fold32(diag(gw[0]), diag(gw[1])), fold32(diag(gx[0]), diag(gx[1]))));
        float su = (sa4[0] + sa4[1]) + (sa4[2] + sa4[3]);
        su = fold32(su, su); su = fold16(su, su);
        const float sx = __int_as_float(__builtin_amdgcn_update_dpp(__builtin_amdgcn_update_dpp(0, __float_as_int(su), 0x150, 0xf, 0xf, true), __float_as_int(su), 0x151, 0xc, 0xf, false));
        const float mean = sx * (1.0f / D_), mbv = rbf(mean);
        float ta = __uint_as_float(__builtin_amdgcn_permlane16_swap(__float_as_uint(gs), __float_as_uint(gs), false, false)[1]) - sx * mean;
        if (__builtin_expect(__any(!(sx * mean <= 16.f * ta)), 0)) {
          float q = fold32(sqdev16(lv, rdl(mbv, 0)), sqdev16(lv2, rdl(mbv, 32)));
          q = row_sum(fold16(q, q));
          const float dm = mean - mbv;
          ta = fmaxf(q - (float)D_ * dm * dm, 0.f);
        }
        ta = rbf(__builtin_amdgcn_sqrtf(rbf(rbf(ta * (1.0f / D_)) + 1e-5f)));
        gs -= mbv * rdl(su, 4);
        const unsigned rb = __float_as_uint(rbf(gs * __builtin_amdgcn_rcpf(ta)));
        kl |= (rb ^ ((unsigned)((int)rb >> 31) | 0x80000000u)) & 0xffff0000u;
        unsigned k0 = (__float_as_uint(mbv) >> 16) | __float_as_uint(ta);
        PIN2(kl, k0);
        kl = (lane & 1) ? kl : k0;
        PIN1(kl);
        kl = (lane & 2) ? __float_as_uint(su) : kl;
        __builtin_amdgcn_raw_buffer_store_b64((u32x2){kl, tag}, rf, go, 0, 16);
#pragma unroll 1
        while (jb < 8) stepr();
      }
      step1(IC<9>{}); step1(IC<10>{}); step1(IC<11>{}); step1(IC<12>{});
    }
#define G2_STAGE_N(kk_, W) do { const int kb = KROT(kk_); constexpr int so = n_off(kk_); SB(); \
    DMA_(rn2, vn2, mq0, so, kb, 0, ""); W(0); DMA_(rn2, vn2, mq1, so, kb, 32 * S_ * 2, ""); W(1); \
    DMA_(rn2, vn2, mq2, so, kb, 64 * S_ * 2, ""); W(2); DMA_(rn2, vn2, mq3, so, kb, 96 * S_ * 2, ""); W(3); \
    DMA_(rn2, vn2, mq0, so + 16384, kb, 128 * S_ * 2, ""); W(4); DMA_(rn2, vn2, mq1, so + 16384, kb, 160 * S_ * 2, ""); W(5); \
    DMA_(rn2, vn2, mq2, so + 16384, kb, 192 * S_ * 2, ""); W(6); DMA_(rn2, vn2, mq3, so + 16384, kb, 224 * S_ * 2, ""); W(7); SB(); } while (0)
#define G2_STAGE_NH(kk_, h) do { const int kb = KROT(kk_); constexpr int so = n_off(kk_) + (h) * 16384; SB(); \
    DMA_(rn2, vn2, mq0, so, kb, (h) * 128 * S_ * 2, ""); DMA_(rn2, vn2, mq1, so, kb, ((h) * 128 + 32) * S_ * 2, ""); \
    DMA_(rn2, vn2, mq2, so, kb, ((h) * 128 + 64) * S_ * 2, ""); DMA_(rn2, vn2, mq3, so, kb, ((h) * 128 + 96) * S_ * 2, ""); SB(); } while (0)
#define G2_STAGE_M(kk_, W) do { const int kb = KROT(kk_); constexpr int so = G2_MRING + m_off(kk_); SB(); \
    DMA_(rm2, vm2, mb0, so, kb, 0, " sc1"); W(8); DMA_(rm2, vm2, mb0, so + 4096, kb, 32 * 8192, " sc1"); W(9); \
    DMA_(rm2, vm2, mb0, so + 8192, kb, 64 * 8192, " sc1"); W(10); DMA_(rm2, vm2, mb0, so + 12288, kb, 96 * 8192, " sc1"); W(11); SB(); } while (0)
    WAIT_VM(16); BAR();
    asm volatile("ds_write_b32 %0, %1" :: "v"(lma), "v"(g0) : "memory");
    if (pw < 2) asm volatile("ds_write_b32 %0, %1 offset:512" :: "v"(lma), "v"(g1) : "memory");
    u32x4 tv[4];
    unsigned ready = 0;
    auto need = [&](int k) __attribute__((always_inline)) {
      const int b = k_blk(k) + (k_ord(k) & 1) * 8;
#pragma unroll 1
      for (int it = 0; !((ready >> b) & 1) && it < SPIN_MAX; ++it) {
        ready |= (unsigned)__ballot(__builtin_amdgcn_raw_buffer_load_b32(rf, abq[k < 5 ? 0 : k < 9 ? 1 : 2], 0, 16) == tag);
        asm volatile("" ::: "memory");
      }
    };
    unsigned ksel, bad = 0;
    int isel;
    unsigned xw;
    auto bvec = [&](unsigned src, const unsigned (&sl)[4]) __attribute__((always_inline)) {
      return (u32x4){__builtin_amdgcn_perm(0, src, sl[0]), __builtin_amdgcn_perm(0, src, sl[1]),
                     __builtin_amdgcn_perm(0, src, sl[2]), __builtin_amdgcn_perm(0, src, sl[3])};
    };
#define BC8(v, p) (unsigned)__builtin_amdgcn_update_dpp(__builtin_amdgcn_update_dpp(0, (int)(v), 0x150 + (p), 0xf, 0x3, false), (int)(v), 0x158 + (p), 0xf, 0xc, false)
    float* lg = (float*)(smem + LDS_GU);
    f32x4 macc = {0.f, 0.f, 0.f, 0.f};
    u32x2 sv;
    unsigned hp, gbad;
    float s0, s1;
    auto work2 = [&](auto JB, auto GP) __attribute__((always_inline)) {
      constexpr int jb = decltype(JB)::value, gp = decltype(GP)::value;
      SB();
      if constexpr (jb == -1 && gp < 4) {
        constexpr int c = gp == 0 ? 0xB1 : gp == 1 ? 0x4E : gp == 2 ? 0x141 : 0x140;
        merge2(k1, k2, dpp_u<c>(k1), dpp_u<c>(k2));
        PIN2(k1, k2);
      }
      if constexpr (jb == -1 && gp == 4) {
        ksel = tk ? k2 : k1;
        isel = (0xffff - (int)(ksel & 0xffffu)) & (S_ - 1);
        k1 = (isel * D_ + d0 + rq * 16 + (lane & 7) * 2) * 2; k2 = O_ST + isel * 8;
        PIN1(ksel);
        xw = gld4_s(x, k1);
        sv = bld8_sc1(rf, k2);
      }
      if constexpr (jb == 0 && gp == 1) wait_xs<3 + 1 + 2>(xw, sv);
      if constexpr (jb == 0) {
        if constexpr (gp == 1) { if (!__all(sv.y == tag)) sv = granule_wait(stat + isel, tag); s0 = __builtin_amdgcn_rcpf(hi_bf(sv.x)); PIN1(s0); }
        if constexpr (gp == 2) { s1 = ln_elem(lo_bf(xw), lo_bf(sv.x), hi_bf(sv.x), s0); PIN1(s1); }
        if constexpr (gp == 3) { xw = pk2(s1, ln_elem(hi_bf(xw), lo_bf(sv.x), hi_bf(sv.x), s0)); PIN1(xw); }
      }
      if constexpr (jb == DG && gp < 7) { PIN1(dv[gp]); macc = MM(dv[gp], bvec(BC8(hp, gp), sel[0]), macc); PIN1(macc); }
      if constexpr ((jb == 0 && gp >= 4) || jb == 1) {
        constexpr int l = jb == 0 ? gp - 4 : gp + 4;
        if constexpr (!(l & 1)) hp = BC8(xw, l >> 1);
        PIN1(wv[l]);
        macc = MM(wv[l], bvec(hp, sel[l & 1]), macc);
        PIN2(macc, hp);
      }
      if constexpr (jb == GV_JB + 2) {
        if constexpr (gp == 0) { gbad = (gv[0].y ^ tag) | (gv[0].w ^ tag) | (gv[1].y ^ tag) | (gv[1].w ^ tag); PIN1(gbad); }
        if constexpr (gp == 1) { gbad |= (gv[2].y ^ tag) | (gv[2].w ^ tag) | (gv[3].y ^ tag) | (gv[3].w ^ tag); PIN1(gbad); }
        if constexpr (gp == 2) {
          s0 = ((__uint_as_float(gv[0].x) + __uint_as_float(gv[1].x)) + __uint_as_float(gv[2].x)) + __uint_as_float(gv[3].x);
          s1 = ((__uint_as_float(gv[0].z) + __uint_as_float(gv[1].z)) + __uint_as_float(gv[2].z)) + __uint_as_float(gv[3].z);
          PIN2(s0, s1);
        }
        if constexpr (gp == 3) {
#pragma unroll 1
          for (int it = 0; !__all(gbad == 0) && it < SPIN_MAX; ++it) {
            u32x4 q[4];
            gbad = 0;
#pragma unroll
            for (int i = 0; i < 4; ++i) { q[i] = __builtin_amdgcn_raw_buffer_load_b128(rgu, vgu, i * F_ * 16, 16); gbad |= (q[i].y ^ tag) | (q[i].w ^ tag); }
            s0 = ((__uint_as_float(q[0].x) + __uint_as_float(q[1].x)) + __uint_as_float(q[2].x)) + __uint_as_float(q[3].x);
            s1 = ((__uint_as_float(q[0].z) + __uint_as_float(q[1].z)) + __uint_as_float(q[2].z)) + __uint_as_float(q[3].z);
            asm volatile("" ::: "memory");
          }
          const unsigned gp2 = pk2(s0, s1);
          const unsigned up2 = dpp_u<0x128>(gp2);
          s0 = tk ? hi_bf(up2) : lo_bf(gp2); s1 = tk ? hi_bf(gp2) : lo_bf(up2);
          PIN2(s0, s1);
        }
        if constexpr (gp == 4) { s0 = rbf(s0 * __builtin_amdgcn_rcpf(1.f + __expf(-s0))); PIN1(s0); }
        if constexpr (gp == 5) { hp = pk2(s0 * s1, 0.f); PIN1(hp); }
      }
      if constexpr (jb >= 3 && jb <= 10 && gp >= 4) {
        constexpr int n = (jb - 3) * 8 + gp - 4;
        sld<((n >> 1) & 7) * 4096 + (n & 1) * 64>(sd, spb, KROT(12 + (n >> 4)));
      }
      if constexpr (jb == 10 && gp == 2) epi_setup();
      if constexpr (jb == 10 && gp == 4) code_touch(sd, (j * 4 + pw) * 256);
      SB();
    };
#define W2M(i) work2(IC<-1>{}, IC<i>{})
#define W20(i) work2(IC<0>{}, IC<i>{})
#define W21(i) work2(IC<1>{}, IC<i>{})
    unsigned fs;
    {
      G1_STAGE_B(15, 3 * G1_STAGE);
      G2_STAGE_NH(0, 0);
      lgkm0(sd);
      WAIT_VM(16); BAR();
      G2_STAGE_NH(0, 1);
      WAIT_VM(10); BAR();
#pragma unroll
      for (int i = 0; i < 4; ++i) kk[i] = bld16_sc1(rf, l16, ko_ + i * 1024);
#pragma unroll
      for (int l = WSN; l < 16; ++l) wv[l] = bld16_nt(rw, vw, l * F_ * 2);
      asm volatile("s_waitcnt vmcnt(%4)" : "+v"(kk[0]), "+v"(kk[1]), "+v"(kk[2]), "+v"(kk[3]) : "i"(16 - WSN) : "memory");
      touches(IC<115>{});
#pragma unroll
      for (int i = 0; i < 4; ++i) {
        k2 = max(k2, min(k1, kk[i].x)); k1 = max(k1, kk[i].x);
        k2 = max(k2, min(k1, kk[i].z)); k1 = max(k1, kk[i].z);
        bad |= (kk[i].y ^ tag) | (kk[i].w ^ tag);
      }
#pragma unroll 1
      for (int it = 0; !__all(bad == 0) && it < SPIN_MAX; ++it) {
        k1 = 0; k2 = 0; bad = 0;
#pragma unroll
        for (int i = 0; i < 4; ++i) {
          const u32x4 q = __builtin_amdgcn_raw_buffer_load_b128(rf, l16, ko_ + i * 1024, 16);
          k2 = max(k2, min(k1, q.x)); k1 = max(k1, q.x);
          k2 = max(k2, min(k1, q.z)); k1 = max(k1, q.z);
          bad |= (q.y ^ tag) | (q.w ^ tag);
        }
        asm volatile("" ::: "memory");
      }
      merge2(k1, k2, dpp_u<0xB1>(k1), dpp_u<0xB1>(k2));
      merge2(k1, k2, dpp_u<0x4E>(k1), dpp_u<0x4E>(k2));
      merge2(k1, k2, dpp_u<0x141>(k1), dpp_u<0x141>(k2));
      merge2(k1, k2, dpp_u<0x140>(k1), dpp_u<0x140>(k2));
      if ((lane & 15) == 0) { *(u32x2*)(smem + LDS_KEY + (pw * 4 + rq) * 8) = (u32x2){k1, k2}; ((unsigned*)(smem + LDS_SEL))[5] = tag; }
      PAGE(3);
      BAR_LGKM();
#pragma unroll
      for (int jj = 0; jj < 4; ++jj) tv[jj] = *(const u32x4*)(smem + G2_MRING + m_off(0) + (jj * 4 + pw) * 1024 + lane * 16);
      { const u32x2 kp = *(const u32x2*)(smem + LDS_KEY + ci * 8); k1 = kp.x; k2 = kp.y; }
#pragma unroll
      for (int jj = 0; jj < 4; ++jj) __builtin_amdgcn_raw_buffer_store_b128(tv[jj], rm2, vm2 + jj * 32 * 8192 + 2 * tt * 128, 0, 16);
      G2_STAGE_N(1, W2M);
      WAIT_VM(10);
      if (lane == tt) __builtin_amdgcn_raw_buffer_store_b32(tag, rf, abo, 0, 16);
      G2_STAGE_N(2, W20);
      __builtin_amdgcn_s_sleep(2);
      WAIT_VM(9);
      fs = bld4_sc1(rf, abq[0]);
    }
    BAR();
#pragma unroll
    for (int jj = 0; jj < 4; ++jj) tv[jj] = *(const u32x4*)(smem + G2_MRING + m_off(1) + (jj * 4 + pw) * 1024 + lane * 16);
    G2_STAGE_N(3, NOW);
    asm volatile("s_waitcnt vmcnt(8)" : "+v"(fs) :: "memory");
    ready = (unsigned)__ballot(fs == tag);
    need(2);
    G2_STAGE_M(2, NOW);
    need(3);
    G2_STAGE_M(3, NOW);
    fs = bld4_sc1(rf, abq[1]);
    touches(IC<1>{});
    W21(0); W21(1); W21(2); W21(3); W21(4); W21(5); W21(6); W21(7); W21(8); W21(9); W21(10); W21(11);
    asm volatile("" : "+v"(tv[0]), "+v"(tv[1]), "+v"(tv[2]), "+v"(tv[3]));
    auto step2 = [&](auto JB) __attribute__((always_inline)) {
      constexpr int jb = decltype(JB)::value;
      if constexpr (jb == 3) PAGE(4);
      if constexpr (jb == 9) PAGE(5);
      constexpr auto must = [](int q) { return (q == 2 ? 4 : 0) + ((FSM >> q) & 1) + ((q == GV_JB || q == GV_JB - 1) ? 2 : 0); };
      if constexpr (jb == 2) wait_vm<4 + must(1)>();
      else wait_vm<dn_cnt(jb - 2) + (jb == 5 ? 1 : 0) + 12 + must(jb - 1) + dn_cnt(jb - 1)>();
      if constexpr (jb == 3) { if (pw == 0 && (lane & ~8) == 0) ((unsigned*)(smem + LDS_SEL))[tk] = ksel; }
      if constexpr ((jb == 2 || jb == DG + 1)) {
        *(f32x4*)(lg + pw * 256 + lane * 4) = macc;
        macc = (f32x4){0.f, 0.f, 0.f, 0.f};
        lgkm0(sd);
      }
      BAR();
      float p0[4], p1[4];
      const int col = (pw & 1) * 64 + lane;
      if constexpr (jb == 3) {
        if (g == 0 && pw == 0 && lane == 0) __hip_atomic_store((unsigned*)flags + FL_EPOCH, epoch + 1u, __ATOMIC_RELAXED, __HIP_MEMORY_SCOPE_AGENT);
      }
      if constexpr (jb == DG + 1) {
        const float* p = lg + (pw & 1) * 32 + (lane >> 4) * 64 + ((2 * lane) & 7) * 4 + ((lane >> 2) & 3);
#pragma unroll
        for (int i = 0; i < 4; ++i) { p0[i] = p[i * 256]; p1[i] = p[i * 256 + 4]; }
      }
      if constexpr (jb == 2) {
        const float* p = lg + (col >> 5) * 64 + (col & 7) * 4 + ((col >> 3) & 3);
#pragma unroll
        for (int i = 0; i < 4; ++i) { p0[i] = p[i * 256]; p1[i] = p[i * 256 + 32]; }
      }
      if constexpr (jb == 4) {
        if (lane == 8 + tt) __builtin_amdgcn_raw_buffer_store_b32(tag, rf, abo, 0, 16);
      }
      if constexpr ((FSM >> (jb - 2)) & 1) {
        asm volatile("" : "+v"(fs));
        ready |= (unsigned)__ballot(fs == tag);
      }
      if constexpr (jb == GV_JB + 2) asm volatile("" : "+v"(gv[0]), "+v"(gv[1]), "+v"(gv[2]), "+v"(gv[3]));
#define W2(i) work2(JB, IC<i>{})
      if constexpr (jb < 13) {
        G2_STAGE_N(jb + 2, W2);
        need(jb + 2);
        G2_STAGE_M(jb + 2, W2);
      } else {
#define W14(i) do { if (i == B14P - 1) { wait_vm<B14P>(); BAR(); } } while (0)
#define W14M(i) W14(i - 8)
        need(15);
        G2_STAGE_M(15, W14M);
#define LASTQ(q) do { const int kb = KROT(15) + ((q + kq + 1) & 3) * 32 * S_ * 2; \
        DMA_(rn2, vn2, mb0, n_off(15) + q * 4096, kb, 0, ""); W14(4 + 2 * q); DMA_(rn2, vn2, mb0, n_off(15) + 16384 + q * 4096, kb, 128 * S_ * 2, ""); W14(5 + 2 * q); } while (0)
        LASTQ(0); LASTQ(1); LASTQ(2); LASTQ(3);
        SB();
      }
      if constexpr (jb == 2) {
#pragma unroll
        for (int jj = 0; jj < 4; ++jj) __builtin_amdgcn_raw_buffer_store_b128(tv[jj], rm2, vm2 + jj * 32 * 8192 + (2 * tt + 1) * 128, 0, 16);
      }
      if constexpr ((FSM >> jb) & 1) fs = bld4_sc1(rf, abq[jb < 5 ? 1 : 2]);
      if constexpr ((jb == GV_JB || jb == GV_JB - 1)) {
#pragma unroll
        for (int i = (jb == GV_JB ? 2 : 0); i < (jb == GV_JB ? 4 : 2); ++i) gv[i] = bld16_sc1(rgu, vgu, i * F_ * 16);
      }
      touches(JB);
#pragma unroll
      for (int i = dn_first(jb); i < dn_first(jb) + dn_cnt(jb); ++i) dv[i] = bld16_nt(rdn, vdn, i * D_ * 2);
      if constexpr (jb == 2) asm volatile("" : "+v"(p0[0]), "+v"(p0[1]), "+v"(p0[2]), "+v"(p0[3]), "+v"(p1[0]), "+v"(p1[1]), "+v"(p1[2]), "+v"(p1[3]));
      if constexpr (jb == DG + 1) {
        if (pw < 2)
          __builtin_amdgcn_raw_buffer_store_b128((u32x4){__float_as_uint((p0[0] + p0[1]) + (p0[2] + p0[3])), tag, __float_as_uint((p1[0] + p1[1]) + (p1[2] + p1[3])), tag},
                                                 mk_rsrc(wsp + WS_DN), (((pw * 8 + cg8) * 32 + rgb) * 64 + lane) * 16, 0, 16);
      }
      if constexpr (jb == 2) {
        if (moe && pw < 2)
          __builtin_amdgcn_raw_buffer_store_b128((u32x4){__float_as_uint((p0[0] + p0[1]) + (p0[2] + p0[3])), tag, __float_as_uint((p1[0] + p1[1]) + (p1[2] + p1[3])), tag},
                                                 rgu, ((mat * 4 + dq) * F_ + cg * 128 + col) * 16, 0, 16);
      }
      SB();
    };
    static_assert(DG + 1 <= 12 && DG >= GV_JB + 3 && dn_first(DG - 2) == 7, "schedule");
    step2(IC<2>{}); step2(IC<3>{}); step2(IC<4>{}); step2(IC<5>{}); step2(IC<6>{}); step2(IC<7>{}); step2(IC<8>{}); step2(IC<9>{}); step2(IC<10>{}); step2(IC<11>{}); step2(IC<12>{}); step2(IC<13>{});
    lgkm0(sd);
    PAGE(7);
    WAIT_VM(4); BAR();
    xq[0] = ld16_s<0>(x, xo); xq[1] = ld16_s<64>(x, xo); sq = ld8_s_sc1(stat, xo >> 11 << 3);
    WAIT_VM(3); BAR();
    asm volatile("s_waitcnt vmcnt(0)" : "+v"(xq[0]), "+v"(xq[1]), "+v"(sq) :: "memory");
    key[0] = __builtin_amdgcn_readlane(ksel, 0); key[1] = __builtin_amdgcn_readlane(ksel, 8);
    nf_pre();
#pragma unroll
    for (int n = 0; n < 16; ++n) nf_step(n);
#pragma unroll 1
    for (int c = 0; c < 2; ++c) {
      const unsigned kc = __builtin_amdgcn_readlane(ksel, c * 8);
      const int ic = (0xffff - (int)(kc & 0xffffu)) & (S_ - 1);
      if ((ic >> 8) != tt || ((ic >> 5) & 3) != kq) continue;
      const __amdgpu_buffer_rsrc_t rd = mk_rsrc(wsp + WS_DN + (size_t)(c * 8 + et) * 32768);
#pragma unroll 1
      for (int it = 0; it < SPIN_MAX; ++it) {
        if (__all(__builtin_amdgcn_raw_buffer_load_b32(rd, (lane & 31) * 1024 + 1020, 0, 16) == tag)) break;
        __builtin_amdgcn_s_sleep(2);
        asm volatile("" ::: "memory");
      }
      float s0, s1;
      unsigned bad = 1;
#pragma unroll 1
      for (int it = 0; !__all(bad == 0) && it < SPIN_MAX; ++it) {
        bad = 0; s0 = 0.f; s1 = 0.f;
#pragma unroll
        for (int i = 0; i < 8; ++i) {
          const u32x4 q = __builtin_amdgcn_raw_buffer_load_b128(rd, rq * 1024 + pw * 256 + ci * 16, i * 4096, 16);
          bad |= (q.y ^ tag) | (q.w ^ tag); s0 += __uint_as_float(q.x); s1 += __uint_as_float(q.z);
        }
        asm volatile("" ::: "memory");
      }
      s0 = fold32(s0, s1);
      s0 = fold16(s0, s0);
      const float sg = rbf(__builtin_amdgcn_rcpf(1.f + __expf(-key_score(kc))));
      ((float*)(smem + LDS_FIN))[c * 128 + pw * 32 + ci * 2 + (lane >> 5)] = rbf(rbf(s0) * sg);
      asm volatile("s_waitcnt lgkmcnt(0)" ::: "memory");
      if (lane == 0) asm volatile("ds_add_u32 %0, %1" :: "v"(lds_addr(smem + LDS_SEL + 24 + c * 4)), "v"(1) : "memory");
    }
    asm volatile("v_mov_b32 %0, 0" : "=v"(fx), "=v"(ya[0]), "=v"(ya[1]), "=v"(ya[2]), "=v"(ya[3]));
  } else {
  {
    const int q = j * 4 + wid;
    constexpr int Q1 = WS_MB, Q2 = Q1 + 8, Q3 = Q2 + 8, Q4 = Q3 + 7, Q5 = Q4 + 7, Q6 = Q5 + 7, Q7 = Q6 + 4, Q8 = Q7 + 4, QC = Q8 + 1;
    static_assert(QC <= 128, "warm-up slots");
    unsigned dmy = 0;
    if (q < QC) {
      const char* base; int b0;
      if (q < Q1) { base = wsp; b0 = 0; }
      else if (q < Q2) { base = (const char*)ws_re; b0 = Q1; }
      else if (q < Q3) { base = (const char*)ws_im; b0 = Q2; }
      else if (q < Q4) { base = (const char*)w_gate; b0 = Q3; }
      else if (q < Q5) { base = (const char*)w_up; b0 = Q4; }
      else if (q < Q6) { base = (const char*)w_down; b0 = Q5; }
      else if (q < Q7) { base = (const char*)out; b0 = Q6; }
      else if (q < Q8) { base = (const char*)x; b0 = Q7; }
      else { base = (const char*)w_r; b0 = Q8; }
      base += (size_t)(q - b0) << 20;
      asm volatile("s_load_dword %0, %1, 0" : "+s"(dmy) : "s"(base) : "memory");
    }
    {
      const int w = g * 4 + wid;
      constexpr int R = (int)(WS_FLAGS - WS_DN);
      static_assert(FL_X == 0 && FL_ST == 6144 * 32 && FL_EPOCH == 6528 * 32, "");
      const int o = lane < 6 ? R + (lane * 1024 + w) * 128 : lane > 6 || w >= 896 ? (int)0x80000000 : w < 512 ? w * 1024 + 896 : R + (6144 - 512 + w) * 128;
      asm volatile("s_mov_b32 m0, %0\n\ts_nop 0\n\tbuffer_load_dword %1, %2, 0 offen sc1 lds"
                   :: "s"(lds_addr(smem + LDS_DUMMY) + wid * 1024 + 512), "v"(o), "s"(mk_rsrc(wsp + WS_DN)) : "memory");
    }
    if (tid < 24) ((int*)(smem + LDS_SEL))[tid] = 0;
    WAIT_VM(1);
    if (wid == 0) { CALL_PG(1); CALL_PG(8); CALL_PG(5); }
    else if (wid == 1) { CALL_PG(9); CALL_PG(6); }
    else if (wid == 2) { CALL_PG(3); CALL_PG(10); CALL_PG(7); }
    else { CALL_PG(4); CALL_PG(11); }
    asm volatile("s_barrier\n\ts_waitcnt lgkmcnt(0)" : "+s"(dmy) :: "memory");
  }
  PAGE(8);
  epi_setup();
  const int wr = wid >> 1, wc = wid & 1;
  const int sw = (fr >> 1) & 7;
  const int ko0 = ((0 + fq) ^ sw) * 16, ko1 = ((4 + fq) ^ sw) * 16;
  const int mrow = (fr >> 2) * 8 + (fr & 3);
  bf16x8 fm[2][4], fn[2][4];
  {
    const char* pm = smem + (wr * 64 + mrow) * 128;
    const char* pn = smem + 16384 + (wc * 64 + mrow) * 128;
    f32x4 acc[4][4];
#pragma unroll
    for (int i = 0; i < 4; ++i)
#pragma unroll
      for (int jn = 0; jn < 4; ++jn)
#pragma unroll
        for (int q = 0; q < 4; ++q) acc[i][jn][q] = 0.f;
#define TO(i) (((i) >> 1) * 4096 + ((i) & 1) * 512)
#define LDSP(T, a) ((__attribute__((address_space(3))) T*)(size_t)(a))
#define SLOTS(n) _Pragma("unroll") for (int q = 0; q < (n); ++q)
#define MFA(c, a, b) asm volatile("v_mfma_f32_16x16x32_bf16 %0, %1, %2, %0" : "+v"(c) : "v"(a), "v"(b))
#define MFA2(c, a, b, a2, b2) asm volatile("v_mfma_f32_16x16x32_bf16 %0, %1, %2, %0\n\tv_mfma_f32_16x16x32_bf16 %0, %3, %4, %0" : "+v"(c) : "v"(a), "v"(b), "v"(a2), "v"(b2))
    unsigned lm[2][2], ln[2][2];
#pragma unroll
    for (int u = 0; u < 2; ++u)
#pragma unroll
      for (int kk = 0; kk < 2; ++kk) {
        lm[u][kk] = lds_addr(pm) + u * 2 * G1_STAGE + (kk ? ko1 : ko0);
        ln[u][kk] = lds_addr(pn) + u * 2 * G1_STAGE + (kk ? ko1 : ko0);
        asm volatile("" : "+v"(lm[u][kk]), "+v"(ln[u][kk]));
      }
    auto ldm = [&](int stg, int i, int kk) __attribute__((always_inline)) { fm[kk][i] = *LDSP(const bf16x8, lm[stg >> 1][kk] + (stg & 1) * G1_STAGE + TO(i)); };
    auto ldn = [&](int stg, int jn, int kk) __attribute__((always_inline)) { fn[kk][jn] = *LDSP(const bf16x8, ln[stg >> 1][kk] + (stg & 1) * G1_STAGE + TO(jn)); };
    auto quad = [&](int a, int c, int d, int s) __attribute__((always_inline)) {
      const int i = (s < 2 || s >= 6) ? a : a + 1, jn = s < 4 ? c : d, kk = (s == 1 || s == 2 || s == 5 || s == 6);
      MFA(acc[i][jn], fm[kk][i], fn[kk][jn]);
    };
    auto quad2 = [&](int a, int c, int d, int s) __attribute__((always_inline)) {
      const int i = (s < 2 || s >= 6) ? a : a + 1, jn = s < 4 ? c : d, kk = (s == 2 || s == 6);
      MFA2(acc[i][jn], fm[kk][i], fn[kk][jn], fm[1 - kk][i], fn[1 - kk][jn]);
    };
    auto half1 = [&](int stg) __attribute__((always_inline)) {
      const int ja = (stg & 1) * 2, jb = 2 - ja;
      SLOTS(8) {
        quad(0, ja, ja + 1, q);
        if (q < 4) ldn(stg, jb + (q >> 1), q & 1); else ldm(stg, 2 + ((q - 4) >> 1), q == 5 || q == 6);
        SB();
      }
      SLOTS(4) { quad2(0, jb, jb + 1, 2 * q); SB(); }
    };
    auto half2 = [&](int stg) __attribute__((always_inline)) {
      const int ja = (stg & 1) * 2, jb = 2 - ja, nx = (stg + 1) & 3;
      SLOTS(8) {
        quad(2, jb + 1, jb, q);
        if (!(q & 1)) ldm(nx, q >> 2, q == 2 || q == 4);
        SB();
      }
      SLOTS(8) {
        quad(2, ja, ja + 1, q);
        if (!(q & 1)) ldn(nx, jb + (q >> 2), (q >> 1) & 1);
        SB();
      }
    };
    SLOTS(16) asm volatile("" : "+v"(acc[q >> 2][q & 3]));
    SB();
    SLOTS(4) { ldm(0, q >> 1, q & 1); ldn(0, q >> 1, q & 1); }
    SB();
#pragma unroll 1
    for (int k4 = 0; k4 < G1_NKT - 4; k4 += 4) {
      half1(0); BAR_LGKM();
      SB(); half2(0);
      half1(1); BAR_LGKM();
      SB(); half2(1);
      half1(2); BAR_LGKM();
      SB(); half2(2);
      half1(3); BAR_LGKM();
      SB(); half2(3);
    }
    bf16x8 gm[2][4], gn[2][8];
    unsigned am[2][2], an[2][2];
#pragma unroll
    for (int u = 0; u < 2; ++u)
#pragma unroll
      for (int kk = 0; kk < 2; ++kk) {
        am[u][kk] = lds_addr(pm) + (u ? 3 : 1) * G1_STAGE + (kk ? ko1 : ko0);
        an[u][kk] = lds_addr(pn) + (u ? 3 : 1) * G1_STAGE + (kk ? ko1 : ko0);
        asm volatile("" : "+v"(am[u][kk]), "+v"(an[u][kk]));
      }
    unsigned al = lds_addr(smem + LDS_MB) + (wr * 32 + fq * 8) * 4, ac = lds_addr(smem + LDS_MB) + (256 + wc * 64 + mrow) * 4;
    unsigned ae = lds_addr(smem + G2_MRING) + (wc * 64 + mrow) * 128 + (((wr * 4 + fq) ^ sw) * 16);
    asm volatile("" : "+v"(al), "+v"(ac), "+v"(ae));
    auto rdg = [&](int set, int t, int h, int r) __attribute__((always_inline)) {
      const int u = t == 15, o = t == 14 ? G1_STAGE : 0;
      if (r == 0 || r == 2 || r == 4 || r == 5) {
        const int i = r >= 4, kk = r == 2 || r == 4;
        gm[set][kk * 2 + i] = *LDSP(const bf16x8, am[u][kk] + o + h * 4096 + i * 512);
      } else {
        const int jn = r < 4 ? 0 : (r - 4) >> 1, kk = r < 4 ? r >> 1 : r & 1;
        gn[set][kk * 4 + jn] = *LDSP(const bf16x8, an[u][kk] + o + TO(jn));
      }
    };
    auto mmg = [&](int set, int h, int q) __attribute__((always_inline)) {
      const int jn = q >> 2, r = q & 3, i = (r >> 1) ^ (jn & 1), kk = r == 1 || r == 2;
      MFA(acc[h * 2 + i][jn], gm[set][kk * 2 + i], gn[set][kk * 4 + jn]);
    };
    f32x4 rsv[2], mrv[2];
    float cev[4], tc[32];
    auto rds = [&](int h, int r) __attribute__((always_inline)) {
      if (r < 2) mrv[r] = *LDSP(const f32x4, al + h * 256 + r * 16); else rsv[r - 2] = *LDSP(const f32x4, al + 512 + h * 256 + (r - 2) * 16);
    };
    auto prep = [&](int n) __attribute__((always_inline)) {
      tc[n] = llvm_fmul_legacy(mrv[(n >> 2) & 1][n & 3], cev[n >> 3]);
      asm volatile("" : "+v"(tc[n]));
    };
    u32x4 ep;
    auto epi = [&](int h, int q) __attribute__((always_inline)) {
      const int jn = q >> 1, i = q & 1;
      asm volatile("v_fmac_f32 %0, %4, %8\n\tv_fmac_f32 %1, %5, %9\n\tv_fmac_f32 %2, %6, %10\n\tv_fmac_f32 %3, %7, %11"
                   : "+v"(tc[q * 4]), "+v"(tc[q * 4 + 1]), "+v"(tc[q * 4 + 2]), "+v"(tc[q * 4 + 3])
                   : "v"(acc[h * 2 + i][jn][0]), "v"(acc[h * 2 + i][jn][1]), "v"(acc[h * 2 + i][jn][2]), "v"(acc[h * 2 + i][jn][3]),
                     "v"(rsv[i][0]), "v"(rsv[i][1]), "v"(rsv[i][2]), "v"(rsv[i][3]));
      ep[i * 2] = pk2(tc[q * 4], tc[q * 4 + 1]); ep[i * 2 + 1] = pk2(tc[q * 4 + 2], tc[q * 4 + 3]);
      if (i) *LDSP(u32x4, ae + m_off(h) + TO(jn)) = ep;
    };
    SB();
    half1(0);
    BAR_LGKM();
    SB();
    SLOTS(16) {
      if (q < 8) quad(2, 3, 2, q); else quad(2, 0, 1, q - 8);
      if (q < 12) rdg(0, 13, 0, q);
      SB();
    }
    BAR();
    SB();
    SLOTS(16) {
      mmg(0, 0, q);
      if (q < 12) rdg(1, 14, 0, q);
      else { rds(0, q - 12); cev[q - 12] = *LDSP(const float, ac + (((q - 12) >> 1) * 32 + ((q - 12) & 1) * 4) * 4); }
      SB();
    }
    PAGE(9);
    BAR();
    SB();
    SLOTS(16) { mmg(1, 0, q); if ((q & 3) < 3) rdg(0, 15, 0, (q >> 2) * 3 + (q & 3)); if (q >= 8) { prep(2 * q - 16); prep(2 * q - 15); } SB(); }
    SLOTS(16) { mmg(0, 0, q); if ((q & 3) < 3) rdg(1, 13, 1, (q >> 2) * 3 + (q & 3)); prep(16 + q); SB(); if (q >= 9 && (q & 1)) { epi(0, (q - 9) >> 1); SB(); } }
    SLOTS(8) { mmg(1, 1, q); if (q < 6) { rdg(0, 14, 1, 2 * q); rdg(0, 14, 1, 2 * q + 1); } SB(); if (q & 1) { epi(0, 4 + (q >> 1)); SB(); } }
    BAR_LGKM();
    SB();
    SLOTS(8) { mmg(1, 1, 8 + q); if (q < 4) rdg(1, 15, 1, q == 0 ? 1 : q == 1 ? 3 : q + 4); else rds(1, q - 4); SB(); }
    SLOTS(16) { mmg(0, 1, q); if (q < 8) rdg(1, 15, 1, q == 0 ? 0 : q == 1 ? 2 : q < 4 ? q + 2 : q + 4); prep(q); SB(); }
    asm volatile("s_waitcnt lgkmcnt(0)" ::: "memory");
    if (lane == 0) asm volatile("ds_add_u32 %0, %1" :: "v"(lds_addr(smem + LDS_SEL + 12)), "v"(1) : "memory");
    SB();
    SLOTS(16) {
      mmg(1, 1, q);
      if (q < 4) fn[0][q] = *(const bf16x8*)(smem + (wr * 128 + fr) * 128 + (q >> 1) * 2048 + ((q & 1) ? ko1 : ko0));
      else if (q < 8) fm[q & 1][(q - 4) >> 1] = *(const bf16x8*)(smem + G2_MRING + m_off(0) + (wc * 64 + mrow) * 128 + TO((q - 4) >> 1) + ((q & 1) ? ko1 : ko0));
      prep(16 + q);
      SB();
    }
    {
      lds_vi rel = (lds_vi)(smem + LDS_SEL + 12);
      int it = 0;
      while (rel[0] != 4 && it < SPIN_MAX * 8) ++it;
      asm volatile("" ::: "memory");
    }
    asm volatile("s_nop 7");
    SB();
    SLOTS(8) { epi(1, q); SB(); }
  }

  const char* pn = smem + (wr * 128 + fr) * 128;
  const char* pm = smem + G2_MRING + (wc * 64 + mrow) * 128;
  f32x4 acc[8][4];
  auto mm = [&](bool imaj, int set, int q2, int s, int mode) __attribute__((always_inline)) {
    int i, jj, kk;
    if (imaj) { i = s >> 2; const int r = s & 3; jj = (r >> 1) ^ (i & 1); kk = (r == 1 || r == 2); }
    else { jj = s >> 3; const int r = s & 7; i = jj ? 3 - (r >> 1) : (r >> 1); kk = ((r + 1) >> 1) & 1; }
    if (mode == 1 && !(s & 1)) asm volatile("v_mfma_f32_16x16x32_bf16 %0, %1, %2, 0" : "=&v"(acc[q2 * 2 + jj][i]) : "v"(fm[kk][i]), "v"(fn[set][jj * 2 + kk]));
    else if (mode) asm volatile("v_mfma_f32_16x16x32_bf16 %0, %1, %2, %0" : "+v"(acc[q2 * 2 + jj][i]) : "v"(fm[kk][i]), "v"(fn[set][jj * 2 + kk]));
    else acc[q2 * 2 + jj][i] = __builtin_amdgcn_mfma_f32_16x16x32_bf16(fm[kk][i], fn[set][jj * 2 + kk], acc[q2 * 2 + jj][i], 0, 0, 0);
  };
  unsigned bn[2], bm[2], an[2], am[2];
#pragma unroll
  for (int kk = 0; kk < 2; ++kk) {
    bn[kk] = lds_addr(pn) + (kk ? ko1 : ko0); bm[kk] = lds_addr(pm) + (kk ? ko1 : ko0);
    asm volatile("" : "+v"(bn[kk]), "+v"(bm[kk]));
    an[kk] = bn[kk]; am[kk] = bm[kk] + m_off(0);
  }
#define RDN(jt, kk, o) (*LDSP(const bf16x8, an[kk] + (o) + (jt) * 2048))
#define RDM(i, kk, o) (*LDSP(const bf16x8, am[kk] + (o) + TO(i)))
  PAGE(10);
  tag = __builtin_amdgcn_readfirstlane(((const unsigned*)(smem + LDS_SEL))[5]);
  {
    int son = 0;
    auto tile = [&](int kt, int zero) __attribute__((always_inline)) {
      SLOTS(16) {
        mm(true, 0, 0, q, zero);
        if (!(q & 1)) {
          if (q < 8) fm[(q >> 1) & 1][2 + (q >> 2)] = RDM(2 + (q >> 2), (q >> 1) & 1, 0);
          else { const int r = (q - 8) >> 1; fn[1][r] = RDN(2 + (r >> 1), r & 1, 0); }
        }
        SB();
      }
      SLOTS(16) {
        mm(false, 1, 1, q, zero);
        if (!(q & 1) && q < 8) fn[0][q >> 1] = RDN(4 + (q >> 2), (q >> 1) & 1, 0);
        SB();
      }
      const int som = son >> 1;
      son = son == 65536 ? 0 : son + 32768;
      SLOTS(16) {
        mm(false, 0, 2, q, zero);
        if (!(q & 1) && q < 8) fn[1][q >> 1] = RDN(6 + (q >> 2), (q >> 1) & 1, 0);
        if (q == 9 || q == 11) { an[q == 11] = bn[q == 11] + son; asm volatile("" : "+v"(an[q == 11])); }
        if (q == 13 || q == 15) { am[q == 15] = bm[q == 15] + som; asm volatile("" : "+v"(am[q == 15])); }
        SB();
      }
      BAR_LGKM();
      SB();
      SLOTS(16) {
        mm(true, 1, 3, q, zero);
        if (!(q & 1)) {
          if (q < 8) fn[0][q >> 1] = RDN(q >> 2, (q >> 1) & 1, 0);
          else fm[(q >> 1) & 1][(q - 8) >> 2] = RDM((q - 8) >> 2, (q >> 1) & 1, 0);
        }
        SB();
      }
    };
    SB();
    tile(0, 1);
#pragma unroll 1
    for (int kt = 1; kt < G2_NKT - 3; ++kt) tile(kt, 0);
  }
  {
    constexpr int NG = 12;
    static constexpr int g_t[NG] = {0, 0, 1, 1, 2, 2, 2, 1, 0, 0, 1, 2};
    static constexpr int g_q[NG] = {0, 1, 0, 1, 0, 1, 2, 2, 2, 3, 3, 3};
    constexpr int G14 = 1, G15 = 3, GP0 = 5, GP1 = 6;
    constexpr auto chp = [](int g) { return g == 0 || g_t[g] != g_t[g - 1]; };
    constexpr auto chn = [](int g) { return g + 1 < NG && g_t[g + 1] != g_t[g]; };
    static_assert(n_off(13) == 32768 && n_off(14) == 65536 && n_off(15) == 0, "");
    unsigned an2[2];
#pragma unroll
    for (int kk = 0; kk < 2; ++kk) {
      an[kk] = bn[kk]; an2[kk] = bn[kk] + 65536; am[kk] = bm[kk];
      asm volatile("" : "+v"(an[kk]), "+v"(an2[kk]), "+v"(am[kk]));
    }
    int vo;
    u32x4 pk;
    u32x2 kv;
    constexpr int NF0 = 152, F0 = 160, F1 = 172, F2 = 188;
    auto publish = [&](int d, int h) __attribute__((always_inline)) {
      if (h == 0) { vo = lane; PINV(vo); vo = vo * 16 + (((d + kq + 1) & 3) * 4 + kq) * 4 * MSG; PINV(vo); }
      const f32x4 v = acc[2 * d + (h >> 2)][h & 3];
      pk[(h & 1) * 2] = pk2(v[0], v[1]); pk[(h & 1) * 2 + 1] = pk2(v[2], v[3]);
      if (h & 1) __builtin_amdgcn_raw_buffer_store_b128(pk, rs, vo + (h >> 1) * 1024, 0, 16);
    };
    int fo = lane == 0 ? kq * 512 : (int)0x80000000;
#define MSG_FLAG(d) __builtin_amdgcn_raw_buffer_store_b32(tag, rc, fo + (((d) + kq + 1) & 3) * 2048, 0, 16)
    auto rdn = [&](int g, int r) __attribute__((always_inline)) {
      const int t = g_t[g], jt = g_q[g] * 2 + (r >> 1);
      fn[g & 1][r] = *LDSP(const bf16x8, (t == 1 ? an2[r & 1] : an[r & 1]) + (t == 0 ? 32768 : 0) + jt * 2048);
    };
    auto rdm = [&](int t, int i, int kk) __attribute__((always_inline)) { fm[kk][i] = *LDSP(const bf16x8, am[kk] + m_off(13 + t) + TO(i)); };
    auto group = [&](auto GI) __attribute__((always_inline)) {
      constexpr int gi = decltype(GI)::value;
      if (gi == G14) { BAR();
        SB(); }
      if (gi == 5) { BAR(); SB(); }
      if (gi == G15) {
        PAGE(11);
        BAR();
        SB();
        xq[0] = ld16_s<0>(x, xo); xq[1] = ld16_s<64>(x, xo);
        sq = ld8_s_sc1(stat, xo >> 11 << 3);
        kv = *(const u32x2*)(smem + LDS_SEL);
        SB();
      }
#pragma unroll
      for (int q = 0; q < 16; ++q) {
        const int n = gi * 16 + q;
        if (n == NF0) {
          wait_xq<12>(xq, sq);
          key[0] = __builtin_amdgcn_readfirstlane(kv.x); key[1] = __builtin_amdgcn_readfirstlane(kv.y);
          nf_pre();
          SB();
        }
        if (n == 112) {
        }
        if (n == 152) {
        }
        if (n == F0) {
          WAIT_VM(8); MSG_FLAG(0); SB(); }
        if (n == F1) { WAIT_VM(5); MSG_FLAG(1); SB(); }
        if (n == F2) {
          WAIT_VM(2);
          MSG_FLAG(2); fx = bld4_sc1(rc, po); SB(); }
        mm(chp(gi) || chn(gi), gi & 1, g_q[gi], q, 2);
        constexpr bool ni = gi + 1 < NG && (chp(gi + 1 < NG ? gi + 1 : gi) || chn(gi + 1 < NG ? gi + 1 : gi));
        auto nr = [](int r) { return ni && r >= 2 ? 5 - r : r; };
        if constexpr (chp(gi) && chn(gi)) {
          if (q < 4) rdm(g_t[gi], 2 + (q >> 1), q & 1); else if (q < 8) rdn(gi + 1, nr(q - 4)); else if (q < 12) rdm(g_t[gi + 1], (q - 8) >> 1, q & 1);
        } else if constexpr (chp(gi)) {
          if (q < 4) rdm(g_t[gi], 2 + (q >> 1), q & 1);
          else if (gi + 1 < NG && q < 12 && !(q & 1)) rdn(gi + 1, nr((q - 4) >> 1));
        } else if constexpr (chn(gi)) {
          if (!(q & 1)) { if (q < 8) rdn(gi + 1, nr(q >> 1)); else rdm(g_t[gi + 1], (q - 8) >> 2, (q >> 1) & 1); }
        } else {
          if (!(q & 1) && q < 8) rdn(gi + 1, nr(q >> 1));
        }
        if (gi == GP0 && q < 8) publish(0, q);
        if (gi == GP1 && q < 8) publish(1, q);
        if (gi == 9 && q < 8) publish(2, q);
        if (n >= NF0 && n < NF0 + 16) nf_step(n - NF0);
        SB();
      }
    };
#define GR4(i) group(IC<i>{}); group(IC<i + 1>{}); group(IC<i + 2>{}); group(IC<i + 3>{});
    GR4(0) GR4(4) GR4(8)
    asm volatile("s_nop 7");
    SB();
  }
#pragma unroll
  for (int i = 0; i < 4; ++i) *(f32x4*)(smem + LDS_ACC + wr * 16384 + wc * 4096 + i * 1024 + lane * 16) = acc[7][i];
#pragma unroll
  for (int i = 0; i < 4; ++i) ya[i] = acc[6][i];
  asm volatile("s_waitcnt lgkmcnt(0)" ::: "memory");
  if (lane == 0) sig[0] = 2;
  }

  PAGE(6);
  const unsigned rel0 = 0xffff - (key[0] & 0xffffu) - trow, rel1 = 0xffff - (key[1] & 0xffffu) - trow;
  if (min(rel0, rel1) < 16) {
#pragma unroll 1
    for (int c = 0; c < 2; ++c) {
      const unsigned rel = c ? rel1 : rel0;
      if (rel >= 16) continue;
      lds_vi cnt = (lds_vi)(smem + LDS_SEL + 24 + c * 4);
      int it = 0;
      while (cnt[0] != 4 && it < SPIN_MAX * 8) { __builtin_amdgcn_s_sleep(1); ++it; }
      asm volatile("" ::: "memory");
      if (rel == fr) {
        const float* fp = (const float*)(smem + LDS_FIN + c * 512) + (wid & 1) * 64 + fq * 8;
#pragma unroll
        for (int u4 = 0; u4 < 4; ++u4) {
          const f32x4 v = *(const f32x4*)(fp + (u4 >> 1) * 32 + (u4 & 1) * 4);
#pragma unroll
          for (int k = 0; k < 4; ++k) nf[u4 * 4 + k] += v[k];
        }
      }
    }
  }
  u32x4 pp[3][2];
  unsigned e_lo = 0x00003f80u, e_hi = 0x3f800000u;
  asm volatile("" : "+v"(e_lo), "+v"(e_hi));
  __amdgpu_buffer_rsrc_t rs_ = rs, rc_ = rc, ro_ = mk_rsrc(out);
  int mo[3] = {MSG_IDX(0) * 4 * MSG, MSG_IDX(1) * 4 * MSG, MSG_IDX(2) * 4 * MSG}, mv = lane * 16 + ha * 2048, mv2 = mv + 1024;
  asm volatile("" : "+s"(rs_), "+s"(rc_), "+s"(ro_), "+s"(mo[0]), "+s"(mo[1]), "+s"(mo[2]));
  unsigned as_ = lds_addr((const void*)smem) + LDS_SIG + (wid & 3) * 4, aa_ = lds_addr(smem + LDS_ACC) + (wid & 2) * 8192 + (wid & 1) * 4096 + lane * 16;
  int xs = ((trow + (lane >> 3)) * D_ + et * 128 + (wid & 1) * 64) * 2 + (lane & 7) * 16, xs2 = xs + 8 * D_ * 2;
  const unsigned mt = lds_addr(smem) + (wid & 2) * 8192 + (ha ? LDS_ACC + (wid & 1) * 4096 : n_off(14) + (wid & 1) * 2048);
  unsigned aw_ = mt + fr * 128 + ((fq ^ (fr & 7)) * 16), aw2 = aw_ ^ 64, ar_ = mt + (lane >> 3) * 128 + (((lane ^ (lane >> 3)) & 7) * 16);
  asm volatile("" : "+v"(mv), "+v"(mv2), "+v"(as_), "+v"(aa_), "+v"(xs), "+v"(xs2), "+v"(aw_), "+v"(aw2), "+v"(ar_));
  {
    unsigned sv_, it_ = SPIN_MAX * 8;
    asm volatile("s_cmp_eq_u32 %9, 0\n\ts_cbranch_scc1 2f\n"
                 "1:\n\tds_read_b32 %0, %10\n\ts_waitcnt lgkmcnt(0)\n\tv_cmp_eq_u32 vcc, 2, %0\n\ts_cbranch_vccnz 3f\n\ts_sleep 1\n\ts_sub_u32 %1, %1, 1\n\ts_cbranch_scc0 1b\n"
                 "3:\n\tds_read_b128 %4, %11\n\tds_read_b128 %5, %11 offset:1024\n\tds_read_b128 %6, %11 offset:2048\n\tds_read_b128 %7, %11 offset:3072\n"
                 "2:\n\ts_waitcnt vmcnt(0)\n\tv_cmp_eq_u32 vcc, %8, %3\n\ts_mov_b32 %2, vcc_lo"
                 : "=&v"(sv_), "+s"(it_), "=&s"(seen), "+v"(fx), "+v"(ya[0]), "+v"(ya[1]), "+v"(ya[2]), "+v"(ya[3])
                 : "s"(tag), "s"(ha), "v"(as_), "v"(aa_) : "vcc", "scc", "memory");
  }
#define MSG_POLL(m) do { \
    if (!((seen >> (m)) & 1)) { \
      unsigned pv_, it_ = SPIN_MAX; \
      asm volatile("1:\n\tbuffer_load_dword %0, %3, %4, 0 offen sc1\n\ts_waitcnt vmcnt(0)\n\tv_cmp_eq_u32 vcc, %5, %0\n\ts_mov_b32 %1, vcc_lo\n\t" \
                   "s_bitcmp1_b32 %1, %6\n\ts_cbranch_scc1 2f\n\ts_sub_u32 %2, %2, 1\n\ts_cbranch_scc1 2f\n\ts_sleep 1\n\ts_branch 1b\n2:" \
                   : "=&v"(pv_), "+s"(seen), "+s"(it_) : "v"(po), "s"(rc_), "s"(tag), "i"(m) : "vcc", "scc", "memory"); \
    } \
    pp[m][0] = __builtin_amdgcn_raw_buffer_load_b128(rs_, mv, mo[m], 16); \
    pp[m][1] = __builtin_amdgcn_raw_buffer_load_b128(rs_, mv2, mo[m], 16); } while (0)
#define MSG_ADD(m) do { \
    _Pragma("unroll") for (int u = 0; u < 8; ++u) { \
      ya[u >> 1][(u & 1) * 2] = add_bf(pp[m][u >> 2][u & 3], e_lo, ya[u >> 1][(u & 1) * 2]); ya[u >> 1][(u & 1) * 2 + 1] = add_bf(pp[m][u >> 2][u & 3], e_hi, ya[u >> 1][(u & 1) * 2 + 1]); } } while (0)
  MSG_POLL(2); MSG_POLL(1); MSG_POLL(0);
  asm volatile("s_waitcnt lgkmcnt(0)" : "+v"(ya[0]), "+v"(ya[1]), "+v"(ya[2]), "+v"(ya[3]) :: "memory");
  SB();
#pragma unroll
  for (int e = 0; e < 16; ++e) { float t = ya[e >> 2][e & 3] + nf[e]; PINV(t); ya[e >> 2][e & 3] = t; }
  SB();
  MSG_ADD(2); MSG_ADD(1);
  asm volatile("" : "+v"(ya[0]), "+v"(ya[1]), "+v"(ya[2]), "+v"(ya[3]));
  SB();
  MSG_ADD(0);
  {
    unsigned ov[8];
    SB();
#pragma unroll
    for (int u = 0; u < 8; ++u) ov[u] = pk2(ya[u >> 1][(u & 1) * 2], ya[u >> 1][(u & 1) * 2 + 1]);
    SB();
    *LDSP(u32x4, aw_) = (u32x4){ov[0], ov[1], ov[2], ov[3]};
    *LDSP(u32x4, aw2) = (u32x4){ov[4], ov[5], ov[6], ov[7]};
    asm volatile("" ::: "memory");
    __builtin_amdgcn_raw_buffer_store_b128(*LDSP(const u32x4, ar_), ro_, xs, 0, 16);
    __builtin_amdgcn_raw_buffer_store_b128(*LDSP(const u32x4, ar_ + 1024), ro_, xs2, 0, 16);
  }
}

extern "C" void kernel_launch(void* const* d_in, const int* in_sizes, int n_in, void* d_out, int out_size,
                              void* d_ws, size_t ws_size, hipStream_t stream) {
  const u16* x = (const u16*)d_in[0];
  const u16* ws_re = (const u16*)d_in[1];
  const u16* ws_im = (const u16*)d_in[2];
  const u16* wd_re = (const u16*)d_in[3];
  const u16* wd_im = (const u16*)d_in[4];
  const u16* w_r = (const u16*)d_in[6];
  const u16* w_gate = (const u16*)d_in[7];
  const u16* w_up = (const u16*)d_in[8];
  const u16* w_down = (const u16*)d_in[9];
  k_main<<<256, 512, 0, stream>>>(x, 0x7fffffffu, 0x00020000u, wd_re, 0x7fffffffu, 0x00020000u, wd_im, 0x7fffffffu, 0x00020000u, w_r, (char*)d_ws,
                                  ws_re, ws_im, w_gate, w_up, w_down, (u16*)d_out);
}
```

```cpp
#include <hip/hip_runtime.h>
#include <hip/hip_bf16.h>
#include <stdint.h>
#include <type_traits>

extern "C" __device__ unsigned long long llvm_dispatch_id(void) __asm("llvm.amdgcn.dispatch.id");
extern "C" __device__ float llvm_fmul_legacy(float, float) __asm("llvm.amdgcn.fmul.legacy");
typedef unsigned short u16;
typedef unsigned long long u64;
typedef __attribute__((ext_vector_type(8))) short bf16x8;
typedef __attribute__((ext_vector_type(4))) unsigned u32x4;
typedef __attribute__((ext_vector_type(2))) unsigned u32x2;
typedef __attribute__((ext_vector_type(4))) float f32x4;
typedef __attribute__((ext_vector_type(2))) float f32x2;
typedef __attribute__((ext_vector_type(2))) __bf16 bf16x2;
typedef __attribute__((address_space(3))) void* lds_vp;
typedef volatile __attribute__((address_space(3))) int* lds_vi;

constexpr int S_ = 2048, D_ = 1024, F_ = 3584;
template <int N> using IC = std::integral_constant<int, N>;

static __device__ __forceinline__ float bf2f(u16 u) { return __uint_as_float((unsigned)u << 16); }
static __device__ __forceinline__ u16 f2bf(float f) {
  __hip_bfloat16 b = __float2bfloat16(f);
  return *reinterpret_cast<u16*>(&b);
}
static __device__ __forceinline__ float rbf(float f) { return bf2f(f2bf(f)); }
static __device__ __forceinline__ float lo_bf(unsigned u) { return __uint_as_float(u << 16); }
static __device__ __forceinline__ float hi_bf(unsigned u) { return __uint_as_float(u & 0xffff0000u); }
static __device__ __forceinline__ unsigned pk2(float a, float b) {
  const bf16x2 v = __builtin_convertvector((f32x2){a, b}, bf16x2);
  return __builtin_bit_cast(unsigned, v);
}
static __device__ __forceinline__ float add_bf(unsigned u, unsigned e, float c) {
  return __builtin_amdgcn_fdot2_f32_bf16(__builtin_bit_cast(bf16x2, u), __builtin_bit_cast(bf16x2, e), c, false);
}
static __device__ __forceinline__ float ln_elem(float x, float mb, float sd, float ri) {
  const float d = rbf(x - mb);
  const float q0 = d * ri;
  return rbf(__builtin_fmaf(__builtin_fmaf(-q0, sd, d), ri, q0));
}
template <int CTRL> static __device__ __forceinline__ float dpp_f(float v) {
  return __int_as_float(__builtin_amdgcn_update_dpp(0, __float_as_int(v), CTRL, 0xf, 0xf, true));
}
template <int CTRL> static __device__ __forceinline__ unsigned dpp_u(unsigned v) {
  return (unsigned)__builtin_amdgcn_update_dpp(0, (int)v, CTRL, 0xf, 0xf, true);
}
static __device__ __forceinline__ float row_sum(float v) {
  v += dpp_f<0xB1>(v);
  v += dpp_f<0x4E>(v);
  v += dpp_f<0x141>(v);
  v += dpp_f<0x140>(v);
  return v;
}
static __device__ __forceinline__ float fold32(float a, float b) {
  const auto r = __builtin_amdgcn_permlane32_swap(__float_as_uint(a), __float_as_uint(b), false, false);
  return __uint_as_float(r[0]) + __uint_as_float(r[1]);
}
static __device__ __forceinline__ float fold16(float a, float b) {
  const auto r = __builtin_amdgcn_permlane16_swap(__float_as_uint(a), __float_as_uint(b), false, false);
  return __uint_as_float(r[0]) + __uint_as_float(r[1]);
}
static __device__ __forceinline__ float rdl(float v, int l) { return __int_as_float(__builtin_amdgcn_readlane(__float_as_int(v), l)); }

#define ST_JB 9
#define WSN 10
#define BST 0x25543321u
#define FSM 0xa2
#define GV_JB 8
#define DG 11
#define B14P 9
#define AB_STRIDE 32
#define SPIN_MAX (1 << 17)

static __device__ __forceinline__ unsigned lds_addr(const void* p) { return (unsigned)(size_t)(lds_vp)p; }

constexpr int MSG = 4096;
constexpr size_t WS_ABT   = 0;
constexpr size_t WS_SLAB  = WS_ABT + (size_t)D_ * 4096 * 2;
constexpr size_t WS_GU    = WS_SLAB + (size_t)64 * 64 * MSG;
constexpr size_t WS_DN    = WS_GU + (size_t)8 * F_ * 16;
constexpr size_t WS_FLAGS = WS_DN + (size_t)2 * 8 * 32 * 64 * 16;
constexpr int FL_X = 0;
constexpr int FL_AB = 64 * 64 * 32;
constexpr int FL_ST = FL_AB + 2048 * AB_STRIDE;
constexpr int FL_CE = FL_ST + 4096;
constexpr int FL_KEY = FL_CE + 4096;
constexpr int FL_EPOCH = FL_KEY + 4096;
constexpr int WS_MB = (int)((WS_FLAGS + (FL_EPOCH + 1) * 4 + 1048575) >> 20);

#define WAIT_VM(n) asm volatile("s_waitcnt vmcnt(" #n ")" ::: "memory")
template <int N> static __device__ __forceinline__ void wait_vm() { asm volatile("s_waitcnt vmcnt(%0)" :: "i"(N) : "memory"); }
#define BAR_LGKM() asm volatile("s_waitcnt lgkmcnt(0)\n\ts_barrier" ::: "memory")
#define BAR() asm volatile("s_barrier" ::: "memory")
#define SB() __builtin_amdgcn_sched_barrier(0)
#define PAGE(n) asm volatile("s_branch 2f\n\t.p2align 12\nk_pg" #n ":\n\ts_setpc_b64 s[42:43]\n2:")
#define CALL_PG(n) asm volatile("s_getpc_b64 s[40:41]\n3:\n\ts_mov_b32 s42, k_pg" #n "-3b\n\ts_ashr_i32 s43, s42, 31\n\ts_add_u32 s40, s40, s42\n\ts_addc_u32 s41, s41, s43\n\t" \
                                "s_swappc_b64 s[42:43], s[40:41]" ::: "s40", "s41", "s42", "s43", "scc", "memory")

static __device__ __forceinline__ __amdgpu_buffer_rsrc_t mk_rsrc(const void* p) {
  return __builtin_amdgcn_make_buffer_rsrc((void*)p, 0, 0x7fffffff, 0x00020000);
}
#define DMA_(rsrc, voff, mbase, MI, sbase, SI, pol) do { unsigned _t; \
    asm volatile("s_add_i32 m0, %2, %3\n\ts_add_i32 %0, %5, %6\n\tbuffer_load_dwordx4 %1, %4, %0 offen" pol " lds" \
                 : "=&s"(_t) : "v"(voff), "s"(mbase), "i"(MI), "s"(rsrc), "s"(sbase), "i"(SI) : "memory", "scc"); } while (0)
#define TOUCH(rsrc, voff, mbase, MI, soff) \
    asm volatile("s_add_i32 m0, %1, %2\n\ts_nop 0\n\tbuffer_load_dword %0, %3, %4 offen lds" \
                 :: "v"(voff), "s"(mbase), "i"(MI), "s"(rsrc), "s"(soff) : "memory", "scc")
template <int OFF> static __device__ __forceinline__ u32x4 ld16_s(const void* p, int voff) {
  u32x4 v;
  asm volatile("global_load_dwordx4 %0, %1, %2 offset:%3" : "=v"(v) : "v"(voff), "s"(p), "i"(OFF) : "memory");
  return v;
}
template <int OFF> static __device__ __forceinline__ void sld(unsigned& sink, const void* p, int soff) {
  asm volatile("s_load_dword %0, %1, %2 offset:%3" : "+s"(sink) : "s"(p), "s"(soff), "i"(OFF) : "memory");
}
static __device__ __forceinline__ unsigned gld4_s(const void* p, unsigned voff) {
  unsigned v;
  asm volatile("global_load_dword %0, %1, %2" : "=v"(v) : "v"(voff), "s"(p) : "memory");
  return v;
}
static __device__ __forceinline__ u32x2 bld8_sc1(__amdgpu_buffer_rsrc_t r, unsigned voff) {
  u32x2 v;
  asm volatile("buffer_load_dwordx2 %0, %1, %2, 0 offen sc1" : "=v"(v) : "v"(voff), "s"(r) : "memory");
  return v;
}
template <int N> static __device__ __forceinline__ void wait_xs(unsigned& a, u32x2& b) { asm volatile("s_waitcnt vmcnt(%2)" : "+v"(a), "+v"(b) : "i"(N) : "memory"); }
template <int N> static __device__ __forceinline__ void wait_xq(u32x4 (&a)[2], u32x2& b) { asm volatile("s_waitcnt vmcnt(%3)" : "+v"(a[0]), "+v"(a[1]), "+v"(b) : "i"(N) : "memory"); }
static __device__ __forceinline__ u32x2 ld8_s_sc1(const void* p, int voff) {
  u32x2 v;
  asm volatile("global_load_dwordx2 %0, %1, %2 sc1" : "=v"(v) : "v"(voff), "s"(p) : "memory");
  return v;
}
static __device__ __forceinline__ void lgkm0(unsigned& sink) { asm volatile("s_waitcnt lgkmcnt(0)" : "+s"(sink) :: "memory"); }
template <typename T> static __device__ __forceinline__ void pin4(T& a, T& b, T& c, T& d) { asm volatile("" : "+s"(a), "+s"(b), "+s"(c), "+s"(d)); }
template <typename T, typename U> static __device__ __forceinline__ void pins(T& a, U& b) { asm volatile("" : "+s"(a), "+s"(b)); }
static __device__ __forceinline__ void launder(int& v, int& a, int& b) { asm volatile("" : "+v"(v)); asm volatile("" : "+s"(a), "+s"(b)); }
static __device__ __forceinline__ unsigned bld4_sc1(__amdgpu_buffer_rsrc_t r, int voff) {
  unsigned v;
  asm volatile("buffer_load_dword %0, %1, %2, 0 offen sc1" : "=v"(v) : "v"(voff), "s"(r) : "memory");
  return v;
}
static __device__ __forceinline__ u32x4 bld16_nt(__amdgpu_buffer_rsrc_t r, int voff, int soff) {
  u32x4 v;
  asm volatile("buffer_load_dwordx4 %0, %1, %2, %3 offen nt" : "=v"(v) : "v"(voff), "s"(r), "s"(soff) : "memory");
  return v;
}
static __device__ __forceinline__ void bld16_nt_io(u32x4& v, __amdgpu_buffer_rsrc_t r, int voff, int soff) {
  asm volatile("buffer_load_dwordx4 %0, %1, %2, %3 offen nt" : "+v"(v) : "v"(voff), "s"(r), "s"(soff) : "memory");
}
static __device__ __forceinline__ u32x4 bld16_sc1(__amdgpu_buffer_rsrc_t r, int voff, int soff) {
  u32x4 v;
  asm volatile("buffer_load_dwordx4 %0, %1, %2, %3 offen sc1" : "=v"(v) : "v"(voff), "s"(r), "s"(soff) : "memory");
  return v;
}
static __device__ __forceinline__ u32x2 granule_wait(const u64* p, unsigned tag) {
  u64 v = 0;
#pragma unroll 1
  for (int it = 0; it < SPIN_MAX; ++it) {
    v = __hip_atomic_load(p, __ATOMIC_RELAXED, __HIP_MEMORY_SCOPE_AGENT);
    if (__all((unsigned)(v >> 32) == tag)) break;
    __builtin_amdgcn_s_sleep(2);
  }
  return (u32x2){(unsigned)v, (unsigned)(v >> 32)};
}
static __device__ __forceinline__ float key_score(unsigned key) {
  const unsigned u = key >> 16;
  return bf2f((u16)((u & 0x8000u) ? (u & 0x7fffu) : (~u & 0xffffu)));
}
static __device__ __forceinline__ void merge2(unsigned& a1, unsigned& a2, unsigned b1, unsigned b2) {
  const unsigned lo = min(a1, b1);
  a1 = max(a1, b1);
  a2 = max(lo, max(a2, b2));
}
static __device__ __forceinline__ float sqdev16(const u32x4 (&v)[2], float mb) {
  f32x2 q2 = {0.f, 0.f};
#pragma unroll
  for (int i = 0; i < 8; ++i) {
    const f32x2 e = (f32x2){lo_bf(v[i >> 2][i & 3]), hi_bf(v[i >> 2][i & 3])} - mb;
    q2 += e * e;
  }
  return q2.x + q2.y;
}

constexpr int G1_NST = 4, G1_NKT = 16, G1_STAGE = 32768;
constexpr int G2_NKT = 16, G2_MRING = 98304;
constexpr int n_off(int k) { return (k % 3) * 32768; }
constexpr int m_off(int k) { return ((k + 2) % 3) * 16384; }
constexpr int k_ord(int k) { return k < 2 ? k : (k < 9 ? 2 * (k - 1) : 2 * (k - 8) + 1); }
constexpr int LDS_DUMMY = G2_MRING + 49152;
constexpr int LDS_GU = LDS_DUMMY + 4096;
constexpr int LDS_FIN = LDS_GU + 4096;
constexpr int LDS_MB = LDS_FIN + 4096;
constexpr int LDS_SEL = LDS_MB + 1536;
constexpr int LDS_SIG = LDS_SEL + 64;
constexpr int LDS_KEY = LDS_SIG + 32;
constexpr int LDS_BYTES = LDS_KEY + 128;
constexpr int LDS_ACC = n_off(G2_NKT - 3);

constexpr int tpos[16] = {0, 0, 0, 0, 100, 110, 115, 2, 2, 3, 4, 5, 0, 0, 0, 0};
constexpr int twho[16] = {0, 0, 0, 0, 7, 7, 7, 0, 1, 2, 3, 4, 0, 0, 0, 0};
constexpr int dn_cnt(int q) { return q == 5 ? 3 : q == 8 ? 4 : 0; }
constexpr int dn_first(int q) { return q <= 5 ? 0 : q <= 8 ? 3 : 7; }

static __device__ __forceinline__ void code_touch(unsigned& sink, unsigned off) {
  asm volatile("s_getpc_b64 s[40:41]\n\tk_main_rt:\n\ts_min_u32 %1, %1, k_main_end-k_main_rt-196\n\t"
               "s_load_dword %0, s[40:41], %1 offset:0\n\ts_load_dword %0, s[40:41], %1 offset:64\n\ts_load_dword %0, s[40:41], %1 offset:128\n\ts_load_dword %0, s[40:41], %1 offset:192"
               : "+s"(sink), "+s"(off) :: "s40", "s41", "scc", "memory");
}
static __device__ __forceinline__ void touch1(__amdgpu_buffer_rsrc_t r, int voff, unsigned mb, int soff) { TOUCH(r, voff, mb, LDS_DUMMY, soff); }
typedef const __attribute__((address_space(1))) u16* gptr;
typedef __attribute__((ext_vector_type(8))) u64 u64x8;
extern "C" __global__ __launch_bounds__(512, 2) void k_main(const u16* x, unsigned x2, unsigned x3, const u16* wd_re, unsigned r2, unsigned r3,
                                                 const u16* wd_im, unsigned i2, unsigned i3, const u16* w_r,
                                                 char* wsp, const u16* ws_re, const u16* ws_im,
                                                 const u16* w_gate, const u16* w_up, const u16* w_down, u16* out) {
  __shared__ __attribute__((aligned(16))) char smem[LDS_BYTES];
  u64x8 ka;
  u32x4 rm, rn;
  unsigned kz;
  asm volatile("s_load_dwordx4 %0, %4, 0\n\ts_load_dwordx16 %2, %4, 48\n\ts_andn2_b32 %3, 4, %5\n\ts_lshl_b32 %3, %3, 2\n\ts_load_dwordx4 %1, %4, %3 offset:16"
               : "=&s"(rm), "=&s"(rn), "=&s"(ka), "=&s"(kz) : "s"(__builtin_amdgcn_kernarg_segment_ptr()), "s"(blockIdx.x) : "scc", "memory");
  SB();
  const int tid = threadIdx.x, lane = tid & 63;
  const int wid = __builtin_amdgcn_readfirstlane(tid >> 6);
  const int g = blockIdx.x ^ 6, xcd = g & 7, j = g >> 3;
  if (wid < 4) {
    unsigned co = ((j * 4 + wid) * 4 + (lane & 3)) * 128;
    asm volatile(".subsection 1\nk_main_end:\n.subsection 0\n\tv_min_u32 %0, k_main_end-k_main-132, %0\n\ts_getpc_b64 s[40:41]\n\ts_mov_b32 m0, %1\n\ts_mov_b64 exec, 15\n\t"
                 "global_load_lds_dword %0, s[40:41]\n\ts_mov_b64 exec, -1"
                 : "+v"(co) : "i"(LDS_DUMMY + 768) : "s40", "s41", "memory");
  }
  const int pw = wid - 4;
  const int row0 = (wid & 3) * 8 + (lane >> 3);
  const int cm = ((lane & 7) ^ (((row0 >> 1) & 1) | ((wid & 3) << 1))) * 16;
  int vm = row0 * (D_ * 2) + cm;
  asm volatile("" : "+v"(vm));
  const int kq = xcd >> 1;
  const int tt = (xcd & 1) * 4 + (j >> 3);
  const int et = j & 7;
  const int z = kq >> 1, st = (kq & 1) * 8 + tt;
  unsigned o_m = st * 128 * D_ * 2, o_n = et * 128 * D_ * 2, mb0 = lds_addr(smem) + pw * 1024;
  asm volatile("" : "+s"(o_m), "+s"(o_n), "+s"(mb0));
#define G1_X(kt, so, q) DMA_(rm, vm, mb0, (so) + (q) * 4096, o_m, (kt) * 128 + (((q) & 1) * 64 + ((q) >> 1) * 32) * D_ * 2, "")
#define G1_W(kt, so, q) DMA_(rn, vm, mb0, (so) + 16384 + (q) * 4096, o_n, (kt) * 128 + (q) * 32 * D_ * 2, "")
#define G1_STAGE_TILE(kt, so, W) do { SB(); G1_X(kt, so, 0); W(0); G1_X(kt, so, 1); W(1); G1_X(kt, so, 2); W(2); G1_X(kt, so, 3); W(3); \
    G1_W(kt, so, 0); W(4); G1_W(kt, so, 1); W(5); G1_W(kt, so, 2); W(6); G1_W(kt, so, 3); W(7); SB(); } while (0)
#define G1_STAGE_A(kt, so, W) do { SB(); G1_X(kt, so, 0); W(0); G1_X(kt, so, 2); W(1); \
    G1_W(kt, so, 0); W(2); G1_W(kt, so, 1); W(3); G1_W(kt, so, 2); W(4); G1_W(kt, so, 3); W(5); W(6); W(7); SB(); } while (0)
#define G1_STAGE_B(kt, so) do { SB(); G1_X(kt, so, 1); G1_X(kt, so, 3); SB(); } while (0)
#define NOW(i)
  asm volatile("s_waitcnt lgkmcnt(0)" : "+s"(ka), "+s"(rm), "+s"(rn) :: "memory");
  int lrow, drow, l16;
  unsigned o_l, o_d;
  auto rows = [&](int i) __attribute__((always_inline)) {
    int g_ = g, l_ = lane;
    if (i == 0) { asm volatile("" : "+s"(g_)); drow = (g_ & ~7) + pw * 2; lrow = drow + (g_ & 3) * 512 + (g_ & 4) * 64; asm volatile("" : "+s"(drow), "+s"(lrow)); }
    if (i == 1) { asm volatile("" : "+s"(g_)); drow += (g_ & 3) * 256; o_l = lrow * D_ * 2; o_d = drow * D_ * 2; asm volatile("" : "+s"(drow), "+s"(o_l), "+s"(o_d)); }
    if (i == 2) { asm volatile("" : "+v"(l_)); l16 = l_ * 16; asm volatile("" : "+v"(l16)); }
  };
  if (__builtin_expect(wid >= 4, 1)) {
    G1_STAGE_TILE(0, 0, rows);
    __builtin_amdgcn_s_setprio(3);
  }
  x = (const u16*)(gptr)((u64)rm[0] | (u64)rm[1] << 32);
  const u16* wdz = (const u16*)(gptr)((u64)rn[0] | (u64)rn[1] << 32);
  w_r = (const u16*)(gptr)ka[0]; wsp = (char*)(gptr)ka[1]; ws_re = (const u16*)(gptr)ka[2]; ws_im = (const u16*)(gptr)ka[3];
  w_gate = (const u16*)(gptr)ka[4]; w_up = (const u16*)(gptr)ka[5]; w_down = (const u16*)(gptr)ka[6]; out = (u16*)(gptr)ka[7];
  int* flags = (int*)(wsp + WS_FLAGS);
  u64* stat = (u64*)(flags + FL_ST);
  const int fr = lane & 15, fq = lane >> 4;
  const int ha = wid >> 2;
  int trow, xo, po;
  const char *prs, *prc;
  auto epi_setup = [&]() __attribute__((always_inline)) {
    int t_ = tt, l_ = lane;
    asm volatile("" : "+s"(t_));
    asm volatile("" : "+v"(l_));
    trow = t_ * 256 + ((wid >> 1) & 1) * 128 + kq * 32 + ha * 16;
    xo = ((trow + (l_ & 15)) * D_ + et * 128 + (wid & 1) * 64 + (l_ >> 4) * 8) * 2;
    prs = wsp + WS_SLAB + (size_t)((t_ * 8 + et) * 64 + (wid & 3)) * MSG;
    prc = (const char*)(flags + FL_X + ((t_ * 8 + et) * 64 + (wid & 3)) * 32);
    asm volatile("" : "+s"(trow), "+s"(prs), "+s"(prc));
    po = l_ < 3 ? (kq * 4 + ((kq + 1 + l_) & 3)) * 512 : (int)0x80000000;
    asm volatile("" : "+v"(xo), "+v"(po));
  };
#define rs mk_rsrc(prs)
#define rc mk_rsrc(prc)
  lds_vi sig = (lds_vi)(smem + LDS_SIG + (wid & 3) * 4);
  unsigned tag;
  u32x4 xq[2];
  u32x2 sq;
  f32x4 ya[4];
  unsigned key[2];
  float nf[16], la0, la1;
#define PINV(a) asm volatile("" : "+v"(a))
  auto nf_pre = [&]() __attribute__((always_inline)) {
    if (__builtin_expect(!__all(sq.y == tag), 0)) sq = granule_wait(stat + (xo >> 11), tag);
    const float ri = __builtin_amdgcn_rcpf(hi_bf(sq.x));
    la0 = -(lo_bf(sq.x) * ri); la1 = 1.f + ri;
    PINV(la0); PINV(la1);
  };
  auto nf_step = [&](int e) __attribute__((always_inline)) {
    const int u = e >> 1;
    nf[e] = (e & 1) ? hi_bf(xq[u >> 2][u & 3]) : lo_bf(xq[u >> 2][u & 3]);
    PINV(nf[e]);
    nf[e] = __builtin_fmaf(nf[e], la1, la0);
    PINV(nf[e]);
  };
#define MSG_IDX(m) (kq * 4 + ((kq + 1 + (m)) & 3))
  unsigned seen, fx;

  if (__builtin_expect(wid >= 4, 1)) {
    __builtin_amdgcn_s_waitcnt(0xc07f);
    constexpr int O_ST = (FL_ST - FL_AB) * 4, O_CE = (FL_CE - FL_AB) * 4, O_KEY = (FL_KEY - FL_AB) * 4, O_EP = (FL_EPOCH - FL_AB) * 4;
    const __amdgpu_buffer_rsrc_t rf = mk_rsrc(wsp + WS_FLAGS + FL_AB * 4);
    u32x4 lv[2], lv2[2], lw[2], ld[2], ld2[2];
    unsigned epv;
    {
      const char* xrow = (const char*)x + o_l;
      const char* wdrow = (const char*)wdz + o_d;
      lv[0] = ld16_s<0>(xrow, l16); lv[1] = ld16_s<1024>(xrow, l16);
      lv2[0] = ld16_s<2048>(xrow, l16); lv2[1] = ld16_s<3072>(xrow, l16);
      ld[0] = ld16_s<0>(wdrow, l16); ld[1] = ld16_s<1024>(wdrow, l16);
      ld2[0] = ld16_s<2048>(wdrow, l16); ld2[1] = ld16_s<3072>(wdrow, l16);
      lw[0] = ld16_s<0>(w_r, l16); lw[1] = ld16_s<1024>(w_r, l16);
      asm volatile("buffer_load_dword %0, off, %1, %2 sc1" : "=v"(epv) : "s"(rf), "s"(O_EP) : "memory");
    }
    SB();
    const unsigned long long did = llvm_dispatch_id();
    const unsigned dmix = (unsigned)did * 0x85EBCA6Bu + (unsigned)(did >> 32) * 0xC2B2AE35u + 0x27D4EB2Fu;
    const bool moe = et != 7;
    unsigned epoch;
    const int ci = lane & 15, rq = lane >> 4, tk = (lane >> 3) & 1;
    int mat, cg, dq, d0;
    u32x4 wv[16];
    const u16 *pgu, *pn2;
    const char *pm2, *spb;
#define rw __builtin_amdgcn_make_buffer_rsrc((void*)pgu, 0, moe ? 0x7fffffff : 0, 0x00020000)
#define rn2 mk_rsrc(pn2)
#define rm2 mk_rsrc(pm2)
    int vw, vn2, vm2;
    int ord2;
    auto k_blk = [&](int k) __attribute__((always_inline)) { return k >= 9 ? (ord2 >> (4 * (k - 9))) & 7 : tt ^ (k_ord(k) >> 1); };
#define KROT(kk_) (k_blk(kk_) * 256 + (k_ord(kk_) & 1) * 128)
    int tv2;
    unsigned sd = 0;
    auto touches = [&](auto P) __attribute__((always_inline)) {
      constexpr int p = decltype(P)::value;
      int e = et;
      asm volatile("" : "+s"(e));
#define TCH(k) if constexpr (tpos[k] == p) { if (e == twho[k]) { const int kb = KROT(k); touch1(rn2, tv2, mb0, kb); } }
      TCH(0) TCH(1) TCH(2) TCH(3) TCH(4) TCH(5) TCH(6) TCH(7) TCH(8) TCH(9) TCH(10) TCH(11) TCH(12) TCH(13) TCH(14) TCH(15)
    };
    unsigned mq0, mq1, mq2, mq3;
    const int cg8 = g & 7, rgb = g >> 3;
    const __amdgpu_buffer_rsrc_t rdn = mk_rsrc(w_down);
    const __amdgpu_buffer_rsrc_t rgu = mk_rsrc(wsp + WS_GU);
    int vdn, vgu;
    u32x4 dv[7], gv[4];
    int abo, abq[3];
    unsigned lma;
    unsigned sel[4];
    float g0, g1;
    u32x4 kk[4];
    unsigned k1 = 0, k2 = 0;
    const int ko_ = O_KEY + pw * 4096;
#define MM(a, b, c) __builtin_amdgcn_mfma_f32_16x16x32_bf16(__builtin_bit_cast(bf16x8, a), __builtin_bit_cast(bf16x8, b), c, 0, 0, 0)
#define PIN1(a) asm volatile("" : "+v"(a))
#define PIN2(a, b) asm volatile("" : "+v"(a), "+v"(b))
#define PIN3(a, b, c) asm volatile("" : "+v"(a), "+v"(b), "+v"(c))
    {
      int gro, go;
      u32x2 gr;
      u32x4 oh[5];
      unsigned dk[4], kl;
#pragma unroll
      for (int l = 0; l < WSN; ++l) asm volatile("" : "=v"(wv[l]));
      auto diag = [&](const f32x4& m) __attribute__((always_inline)) {
        return __uint_as_float((__float_as_uint(m[0]) & dk[0]) | (__float_as_uint(m[1]) & dk[1]) | (__float_as_uint(m[2]) & dk[2]) | (__float_as_uint(m[3]) & dk[3]));
      };
      auto setup = [&](auto JB, auto GP) __attribute__((always_inline)) {
        constexpr int jb = decltype(JB)::value, gp = decltype(GP)::value;
        SB();
        {
          int ln = lane, jj = j, p_ = pw;
          launder(ln, jj, p_);
          const int c_ = ln & 15, r_ = ln >> 4, e_ = jj & 7, t_ = (xcd & 1) * 4 + (jj >> 3);
          if constexpr (jb == 1 && gp == 0) {
            pn2 = ((kq >> 1) ? ws_im : ws_re) + (long)t_ * 256 * S_ + (kq & 1) * 1024;
            spb = (const char*)(pn2 + (e_ * 4 + p_) * 8 * S_);
            pins(pn2, spb);
          }
          if constexpr (jb == 1 && gp >= 1 && gp < 6) {
            const unsigned o = c_ == gp - 1 ? 0x3f803f80u : 0u;
            oh[gp - 1] = (u32x4){o, o, o, o};
            PIN1(oh[gp - 1]);
          }
          if constexpr (jb == 1 && gp == 6) {
            const int gm = moe ? (jj - (jj >> 3)) * 8 + xcd : 0;
            mat = gm / 112; cg = (gm % 112) >> 2; dq = gm & 3; d0 = dq * 256 + p_ * 64;
            pin4(mat, cg, dq, d0);
          }
          if constexpr (jb == 2 && gp == 0) {
            pgu = mat ? w_up : w_gate;
            pm2 = wsp + WS_ABT + ((long)e_ * 128 * 4096 + kq * 1024) * 2;
            pins(pgu, pm2);
            vw = ((d0 + r_ * 16) * F_ + cg * 128 + c_ * 8) * 2;
            PIN1(vw);
          }
          if constexpr (jb == 2 && (gp == 1 || gp == 2)) {
            dk[gp * 2 - 2] = c_ == r_ * 4 + gp * 2 - 2 ? ~0u : 0u; dk[gp * 2 - 1] = c_ == r_ * 4 + gp * 2 - 1 ? ~0u : 0u;
            PIN2(dk[gp * 2 - 2], dk[gp * 2 - 1]);
          }
          if constexpr (jb == 2 && gp == 3) { kl = lrow + (ln >> 5); go = (ln & 30) ? (int)0x80000000 : ((ln & 1) ? O_KEY : O_ST) + (int)kl * 8; PIN2(kl, go); }
          if constexpr (jb == 2 && gp == 4) { kl = 0xffff - kl; go = (ln & ~1) == 2 ? O_CE + (z * D_ + drow + (ln & 1)) * 8 : go; PIN2(kl, go); }
          if constexpr (jb == 2 && gp == 5) {
            gro = (p_ < 2 ? O_ST + (st * 128 + p_ * 64) * 8 : O_CE + (z * D_ + e_ * 128 + (p_ - 2) * 64) * 8) + ln * 8;
            tv2 = (p_ * 64 + ln) * 4096;
            lma = lds_addr(smem + LDS_MB) + (p_ < 2 ? p_ * 256 : 512 + p_ * 256) + ln * 4;
            PIN3(gro, tv2, lma);
          }
          if constexpr (jb == 2 && gp == 6) {
            const u64 lo = (t_ & 2) ? ((t_ & 4) ? 0x0150234601503247ull : 0x0514670205147603ull) : ((t_ & 4) ? 0x0123067401532076ull : 0x0567423005176432ull);
            ord2 = (int)(lo >> ((t_ & 1) * 32));
            pins(ord2, p_);
          }
          if constexpr (jb == 7 && gp == 0) {
            const int r0 = p_ * 8 + (ln >> 3);
            vn2 = r0 * (S_ * 2) + ((ln & 7) ^ ((r0 >> 1) & 7)) * 16; vm2 = r0 * 8192 + (vm & 127);
            PIN2(vn2, vm2);
          }
          if constexpr (jb == 7 && gp == 1) { vdn = jj * 112 + p_ * 28 + r_ * 7; vgu = (((ln >> 3) & 1) * 4 * F_ + vdn + min(ln & 7, 6)) * 16; PIN2(vdn, vgu); }
          if constexpr (jb == 7 && gp == 2) { vdn = (vdn * D_ + cg8 * 128 + c_ * 8) * 2; PIN1(vdn); }
          if constexpr (jb == 7 && gp == 3) {
            abo = (((kq * 8 + e_) * 4 + p_) * 16 + c_) * AB_STRIDE * 4;
            abq[2] = ln >= 16 || (ln & 7) == t_ ? (int)0x80000000 : abo;
            PIN2(abo, abq[2]);
          }
          if constexpr (jb == 7 && gp == 4) {
            abq[1] = ln >= 8 ? (int)0x80000000 : abq[2];
            abq[0] = ((ln ^ t_) & 4) ? (int)0x80000000 : abq[1];
            PIN2(abq[0], abq[1]);
          }
          if constexpr (jb == 7 && gp == 5) {
            const unsigned mb = lds_addr(smem) + p_ * 1024;
            mq0 = mb + ((3 - kq) & 3) * 4096; mq1 = mb + ((4 - kq) & 3) * 4096; mq2 = mb + ((5 - kq) & 3) * 4096; mq3 = mb + ((6 - kq) & 3) * 4096;
            pin4(mq0, mq1, mq2, mq3);
          }
          if constexpr (jb == 8 && gp < 4) { sel[gp] = ((ln & 7) >> 1) == gp ? ((ln & 1) ? 0xffff0000u : 0xffffu) : 0u; PIN1(sel[gp]); }
        }
        SB();
      };
#define WS1(i) setup(IC<1>{}, IC<i>{})
#define WS2(i) setup(IC<2>{}, IC<i>{})
#define WS3(i) do { setup(IC<7>{}, IC<i>{}); if (i >= 4) setup(IC<8>{}, IC<(i) & 3>{}); } while (0)
      G1_STAGE_TILE(1, G1_STAGE, WS1);
      WAIT_VM(19); BAR();
      G1_STAGE_TILE(2, 2 * G1_STAGE, WS2);
      G1_STAGE_TILE(3, 3 * G1_STAGE, WS3);
      auto work1 = [&](auto JB, auto GP) __attribute__((always_inline)) {
        constexpr int jb = decltype(JB)::value, gp = decltype(GP)::value;
        SB();
        if constexpr (jb == ST_JB + 3) {
          if constexpr (gp == 0) { if (!__all(gr.y == tag)) gr = granule_wait((const u64*)((const char*)(flags + FL_AB) + gro), tag); PIN1(gr); }
          if constexpr (gp == 1) { g1 = __builtin_amdgcn_rcpf(__uint_as_float((gr.x & 0xffff0000u) ^ (z << 31))); PIN1(g1); }
          if constexpr (gp == 2) { g0 = pw < 2 ? -(lo_bf(gr.x) * g1) : __uint_as_float(gr.x); PIN1(g0); }
        }
        SB();
      };
      auto step1 = [&](auto JB) __attribute__((always_inline)) {
        constexpr int jb = decltype(JB)::value;
        constexpr int n16 = jb == 12 ? 12 : jb == 11 ? 14 : 16;
        constexpr auto wl = [](int q) { return q == ST_JB ? 1 : 0; };
        if constexpr (jb == ST_JB + 3) {
          asm volatile("s_waitcnt vmcnt(%1)" : "+v"(gr) : "i"(n16 + wl(jb - 2) + wl(jb - 1)) : "memory");
        } else wait_vm<n16 + wl(jb - 3) + wl(jb - 2) + wl(jb - 1)>();
        BAR();
#define W1(i) work1(JB, IC<i>{})
        if constexpr (jb < 10) G1_STAGE_TILE(jb - 1 + G1_NST, ((jb - 1) % G1_NST) * G1_STAGE, W1);
        else G1_STAGE_A(jb - 1 + G1_NST, ((jb - 1) % G1_NST) * G1_STAGE, W1);
        if constexpr (jb == 12) { G1_STAGE_B(13, G1_STAGE); G1_STAGE_B(14, 2 * G1_STAGE); }
        if constexpr (jb == ST_JB) {
          asm volatile("buffer_load_dwordx2 %0, %1, %2, 0 offen sc1" : "=v"(gr) : "v"(gro), "s"(rf) : "memory");
        }
        touches(IC<100 + jb>{});
        SB();
      };
      static_assert(ST_JB == 9, "schedule");
      {
        PAGE(1);
        const int bs = (BST >> (4 * et)) & 15;
        unsigned rm_ = o_m + 512, rn_ = o_n + 512 - 128, ln_ = mb0;
        int jb = 0, sn = tt * 256;
        auto stepr = [&]() __attribute__((always_inline)) {
          const int so = sn;
          const unsigned rl = ln_;
          WAIT_VM(16); BAR();
          SB();
          DMA_(rm, vm, rl, 0, rm_, 0, ""); sld<0>(sd, spb, so); DMA_(rm, vm, rl, 4096, rm_, 64 * D_ * 2, ""); sld<64>(sd, spb, so);
          DMA_(rm, vm, rl, 8192, rm_, 32 * D_ * 2, ""); sld<4096>(sd, spb, so); DMA_(rm, vm, rl, 12288, rm_, 96 * D_ * 2, ""); sld<4096 + 64>(sd, spb, so);
          rm_ += 128; rn_ += 128; ++jb;
          sn = (((0x4210 >> ((jb >> 1) * 4)) & 7) ^ (2 * tt)) * 128 + (jb & 1) * 16384; ln_ = mb0 + (jb & 3) * G1_STAGE;
          asm volatile("" : "+s"(rm_), "+s"(jb), "+s"(rn_), "+s"(sn), "+s"(ln_));
          DMA_(rn, vm, rl, 16384, rn_, 0, ""); sld<8192>(sd, spb, so); DMA_(rn, vm, rl, 16384 + 4096, rn_, 32 * D_ * 2, ""); sld<8192 + 64>(sd, spb, so);
          DMA_(rn, vm, rl, 16384 + 8192, rn_, 64 * D_ * 2, ""); sld<12288>(sd, spb, so); sld<12288 + 64>(sd, spb, so); DMA_(rn, vm, rl, 16384 + 12288, rn_, 96 * D_ * 2, "");
          SB();
        };
#pragma unroll 1
        do stepr(); while (jb < bs);
        if (moe) {
#pragma unroll
          for (int l = 0; l < WSN; ++l) bld16_nt_io(wv[l], rw, vw, l * F_ * 2);
        }
        touches(IC<100>{});
        asm volatile("" : "+v"(lv[0]), "+v"(lv[1]), "+v"(lw[0]), "+v"(lw[1]), "+v"(ld[0]), "+v"(ld[1]), "+v"(lv2[0]), "+v"(lv2[1]), "+v"(ld2[0]), "+v"(ld2[1]), "+v"(epv));
        SB();
        epoch = __builtin_amdgcn_readfirstlane(epv); tag = ((epoch + 1u) * 0x9E3779B1u ^ dmix) | 1u;
        const f32x4 z4 = {0.f, 0.f, 0.f, 0.f};
        f32x4 gw[2], gx[2];
        gw[0] = MM(lv[1], lw[1], MM(lv[0], lw[0], z4)); gw[1] = MM(lv2[1], lw[1], MM(lv2[0], lw[0], z4));
        gx[0] = MM(lv[1], lv[1], MM(lv[0], lv[0], z4)); gx[1] = MM(lv2[1], lv2[1], MM(lv2[0], lv2[0], z4));
        SB();
        f32x4 sa4 = MM(lv[0], oh[0], z4);
        sa4 = MM(lv2[0], oh[1], sa4); sa4 = MM(lv[1], oh[0], sa4); sa4 = MM(lv2[1], oh[1], sa4);
        sa4 = MM(ld[0], oh[2], sa4); sa4 = MM(ld[1], oh[2], sa4); sa4 = MM(ld2[0], oh[3], sa4); sa4 = MM(ld2[1], oh[3], sa4);
        sa4 = MM(lw[0], oh[4], sa4); sa4 = MM(lw[1], oh[4], sa4);
        float gs = row_sum(fold16(fold32(diag(gw[0]), diag(gw[1])), fold32(diag(gx[0]), diag(gx[1]))));
        float su = (sa4[0] + sa4[1]) + (sa4[2] + sa4[3]);
        su = fold32(su, su); su = fold16(su, su);
        const float sx = __int_as_float(__builtin_amdgcn_update_dpp(__builtin_amdgcn_update_dpp(0, __float_as_int(su), 0x150, 0xf, 0xf, true), __float_as_int(su), 0x151, 0xc, 0xf, false));
        const float mean = sx * (1.0f / D_), mbv = rbf(mean);
        float ta = __uint_as_float(__builtin_amdgcn_permlane16_swap(__float_as_uint(gs), __float_as_uint(gs), false, false)[1]) - sx * mean;
        if (__builtin_expect(__any(!(sx * mean <= 16.f * ta)), 0)) {
          float q = fold32(sqdev16(lv, rdl(mbv, 0)), sqdev16(lv2, rdl(mbv, 32)));
          q = row_sum(fold16(q, q));
          const float dm = mean - mbv;
          ta = fmaxf(q - (float)D_ * dm * dm, 0.f);
        }
        ta = rbf(__builtin_amdgcn_sqrtf(rbf(rbf(ta * (1.0f / D_)) + 1e-5f)));
        gs -= mbv * rdl(su, 4);
        const unsigned rb = __float_as_uint(rbf(gs * __builtin_amdgcn_rcpf(ta)));
        kl |= (rb ^ ((unsigned)((int)rb >> 31) | 0x80000000u)) & 0xffff0000u;
        unsigned k0 = (__float_as_uint(mbv) >> 16) | __float_as_uint(ta);
        PIN2(kl, k0);
        kl = (lane & 1) ? kl : k0;
        PIN1(kl);
        kl = (lane & 2) ? __float_as_uint(su) : kl;
        __builtin_amdgcn_raw_buffer_store_b64((u32x2){kl, tag}, rf, go, 0, 16);
#pragma unroll 1
        while (jb < 8) stepr();
      }
      step1(IC<9>{}); step1(IC<10>{}); step1(IC<11>{}); step1(IC<12>{});
    }
#define G2_STAGE_N(kk_, W) do { const int kb = KROT(kk_); constexpr int so = n_off(kk_); SB(); \
    DMA_(rn2, vn2, mq0, so, kb, 0, ""); W(0); DMA_(rn2, vn2, mq1, so, kb, 32 * S_ * 2, ""); W(1); \
    DMA_(rn2, vn2, mq2, so, kb, 64 * S_ * 2, ""); W(2); DMA_(rn2, vn2, mq3, so, kb, 96 * S_ * 2, ""); W(3); \
    DMA_(rn2, vn2, mq0, so + 16384, kb, 128 * S_ * 2, ""); W(4); DMA_(rn2, vn2, mq1, so + 16384, kb, 160 * S_ * 2, ""); W(5); \
    DMA_(rn2, vn2, mq2, so + 16384, kb, 192 * S_ * 2, ""); W(6); DMA_(rn2, vn2, mq3, so + 16384, kb, 224 * S_ * 2, ""); W(7); SB(); } while (0)
#define G2_STAGE_NH(kk_, h) do { const int kb = KROT(kk_); constexpr int so = n_off(kk_) + (h) * 16384; SB(); \
    DMA_(rn2, vn2, mq0, so, kb, (h) * 128 * S_ * 2, ""); DMA_(rn2, vn2, mq1, so, kb, ((h) * 128 + 32) * S_ * 2, ""); \
    DMA_(rn2, vn2, mq2, so, kb, ((h) * 128 + 64) * S_ * 2, ""); DMA_(rn2, vn2, mq3, so, kb, ((h) * 128 + 96) * S_ * 2, ""); SB(); } while (0)
#define G2_STAGE_M(kk_, W) do { const int kb = KROT(kk_); constexpr int so = G2_MRING + m_off(kk_); SB(); \
    DMA_(rm2, vm2, mb0, so, kb, 0, " sc1"); W(8); DMA_(rm2, vm2, mb0, so + 4096, kb, 32 * 8192, " sc1"); W(9); \
    DMA_(rm2, vm2, mb0, so + 8192, kb, 64 * 8192, " sc1"); W(10); DMA_(rm2, vm2, mb0, so + 12288, kb, 96 * 8192, " sc1"); W(11); SB(); } while (0)
    WAIT_VM(16); BAR();
    asm volatile("ds_write_b32 %0, %1" :: "v"(lma), "v"(g0) : "memory");
    if (pw < 2) asm volatile("ds_write_b32 %0, %1 offset:512" :: "v"(lma), "v"(g1) : "memory");
    u32x4 tv[4];
    unsigned ready = 0;
    auto need = [&](int k) __attribute__((always_inline)) {
      const int b = k_blk(k) + (k_ord(k) & 1) * 8;
#pragma unroll 1
      for (int it = 0; !((ready >> b) & 1) && it < SPIN_MAX; ++it) {
        ready |= (unsigned)__ballot(__builtin_amdgcn_raw_buffer_load_b32(rf, abq[k < 5 ? 0 : k < 9 ? 1 : 2], 0, 16) == tag);
        asm volatile("" ::: "memory");
      }
    };
    unsigned ksel, bad = 0;
    int isel;
    unsigned xw;
#define QP(p) " quad_perm:[" #p "," #p "," #p "," #p "] row_mask:0xf bank_mask:0xf\n\t"
#define GSTEP_(w, v, p) \
    asm volatile("v_and_b32_dpp v228, %2, %3" QP(p) "v_and_b32_dpp v229, %2, %4" QP(p) "v_and_b32_dpp v230, %2, %5" QP(p) "v_and_b32_dpp v231, %2, %6" QP(p) \
                 "s_nop 1\n\tv_mfma_f32_16x16x32_bf16 %0, %1, v[228:231], %0" \
                 : "+v"(macc) : "v"(w), "v"(v), "v"(sel[0]), "v"(sel[1]), "v"(sel[2]), "v"(sel[3]) : "v228", "v229", "v230", "v231")
    f32x4 macc = {0.f, 0.f, 0.f, 0.f};
    auto gstep = [&](const u32x4& w, unsigned v, int p) __attribute__((always_inline)) {
      if (p == 0) GSTEP_(w, v, 0); else if (p == 1) GSTEP_(w, v, 1); else if (p == 2) GSTEP_(w, v, 2); else GSTEP_(w, v, 3);
    };
#define QLO(v) (unsigned)__builtin_amdgcn_update_dpp((int)(v), (int)(v), 0x114, 0xf, 0xa, false)
#define QHI(v) (unsigned)__builtin_amdgcn_update_dpp((int)(v), (int)(v), 0x104, 0xf, 0x5, false)
    unsigned xa[4];
    float* lg = (float*)(smem + LDS_GU);
    u32x2 sv;
    unsigned gbad;
    float s0, s1;
    auto work2 = [&](auto JB, auto GP) __attribute__((always_inline)) {
      constexpr int jb = decltype(JB)::value, gp = decltype(GP)::value;
      SB();
      if constexpr (jb == -1 && gp < 4) {
        constexpr int c = gp == 0 ? 0xB1 : gp == 1 ? 0x4E : gp == 2 ? 0x141 : 0x140;
        merge2(k1, k2, dpp_u<c>(k1), dpp_u<c>(k2));
        PIN2(k1, k2);
      }
      if constexpr (jb == -1 && gp == 4) {
        ksel = tk ? k2 : k1;
        isel = (0xffff - (int)(ksel & 0xffffu)) & (S_ - 1);
        k1 = (isel * D_ + d0 + rq * 16 + (lane & 7) * 2) * 2; k2 = O_ST + isel * 8;
        PIN1(ksel);
        xw = gld4_s(x, k1);
        sv = bld8_sc1(rf, k2);
      }
      if constexpr (jb == 0 && gp == 1) wait_xs<3 + 1 + 2>(xw, sv);
      if constexpr (jb == 0) {
        if constexpr (gp == 1) { if (!__all(sv.y == tag)) sv = granule_wait(stat + isel, tag); s0 = __builtin_amdgcn_rcpf(hi_bf(sv.x)); PIN1(s0); }
        if constexpr (gp == 2) { s1 = ln_elem(lo_bf(xw), lo_bf(sv.x), hi_bf(sv.x), s0); PIN1(s1); }
        if constexpr (gp == 3) { s0 = ln_elem(hi_bf(xw), lo_bf(sv.x), hi_bf(sv.x), s0); PIN1(s0); }
        if constexpr (gp == 4) {
          const unsigned a = pk2(s1, s1), b = pk2(s0, s0);
          xa[0] = QLO(a); xa[1] = QLO(b); xa[2] = QHI(a); xa[3] = QHI(b);
          PIN2(xa[0], xa[1]); PIN2(xa[2], xa[3]);
        }
      }
      if constexpr (jb == DG && gp < 7) gstep(dv[gp], xa[gp >> 2], gp & 3);
      if constexpr ((jb == 0 && gp >= 5) || jb == 1 || (jb == 2 && gp == 0)) {
        constexpr int l = jb == 0 ? gp - 5 : jb == 1 ? gp + 3 : 15;
        gstep(wv[l], xa[(l >> 3) * 2 + (l & 1)], (l >> 1) & 3);
      }
      if constexpr (jb == GV_JB + 2) {
        if constexpr (gp == 0) { gbad = (gv[0].y ^ tag) | (gv[0].w ^ tag) | (gv[1].y ^ tag) | (gv[1].w ^ tag); PIN1(gbad); }
        if constexpr (gp == 1) { gbad |= (gv[2].y ^ tag) | (gv[2].w ^ tag) | (gv[3].y ^ tag) | (gv[3].w ^ tag); PIN1(gbad); }
        if constexpr (gp == 2) {
          s0 = ((__uint_as_float(gv[0].x) + __uint_as_float(gv[1].x)) + __uint_as_float(gv[2].x)) + __uint_as_float(gv[3].x);
          s1 = ((__uint_as_float(gv[0].z) + __uint_as_float(gv[1].z)) + __uint_as_float(gv[2].z)) + __uint_as_float(gv[3].z);
          PIN2(s0, s1);
        }
        if constexpr (gp == 3) {
#pragma unroll 1
          for (int it = 0; !__all(gbad == 0) && it < SPIN_MAX; ++it) {
            u32x4 q[4];
            gbad = 0;
#pragma unroll
            for (int i = 0; i < 4; ++i) { q[i] = __builtin_amdgcn_raw_buffer_load_b128(rgu, vgu, i * F_ * 16, 16); gbad |= (q[i].y ^ tag) | (q[i].w ^ tag); }
            s0 = ((__uint_as_float(q[0].x) + __uint_as_float(q[1].x)) + __uint_as_float(q[2].x)) + __uint_as_float(q[3].x);
            s1 = ((__uint_as_float(q[0].z) + __uint_as_float(q[1].z)) + __uint_as_float(q[2].z)) + __uint_as_float(q[3].z);
            asm volatile("" ::: "memory");
          }
          const unsigned gp2 = pk2(s0, s1);
          const unsigned up2 = dpp_u<0x128>(gp2);
          s0 = tk ? hi_bf(up2) : lo_bf(gp2); s1 = tk ? hi_bf(gp2) : lo_bf(up2);
          PIN2(s0, s1);
        }
        if constexpr (gp == 4) { s0 = rbf(s0 * __builtin_amdgcn_rcpf(1.f + __expf(-s0))); PIN1(s0); }
        if constexpr (gp == 5) { const unsigned h = pk2(s0 * s1, s0 * s1); xa[0] = QLO(h); xa[1] = QHI(h); PIN2(xa[0], xa[1]); }
      }
      if constexpr (jb >= 3 && jb <= 10 && gp >= 4) {
        constexpr int n = (jb - 3) * 8 + gp - 4;
        sld<((n >> 1) & 7) * 4096 + (n & 1) * 64>(sd, spb, KROT(12 + (n >> 4)));
      }
      if constexpr (jb == 10 && gp == 2) epi_setup();
      if constexpr (jb == 10 && gp == 4) code_touch(sd, (j * 4 + pw) * 256);
      SB();
    };
#define W2M(i) work2(IC<-1>{}, IC<i>{})
#define W20(i) work2(IC<0>{}, IC<i>{})
#define W21(i) work2(IC<1>{}, IC<i>{})
    unsigned fs;
    {
      G1_STAGE_B(15, 3 * G1_STAGE);
      G2_STAGE_NH(0, 0);
      lgkm0(sd);
      WAIT_VM(16); BAR();
      G2_STAGE_NH(0, 1);
      WAIT_VM(10); BAR();
#pragma unroll
      for (int i = 0; i < 4; ++i) kk[i] = bld16_sc1(rf, l16, ko_ + i * 1024);
#pragma unroll
      for (int l = WSN; l < 16; ++l) wv[l] = bld16_nt(rw, vw, l * F_ * 2);
      asm volatile("s_waitcnt vmcnt(%4)" : "+v"(kk[0]), "+v"(kk[1]), "+v"(kk[2]), "+v"(kk[3]) : "i"(16 - WSN) : "memory");
      touches(IC<115>{});
#pragma unroll
      for (int i = 0; i < 4; ++i) {
        k2 = max(k2, min(k1, kk[i].x)); k1 = max(k1, kk[i].x);
        k2 = max(k2, min(k1, kk[i].z)); k1 = max(k1, kk[i].z);
        bad |= (kk[i].y ^ tag) | (kk[i].w ^ tag);
      }
#pragma unroll 1
      for (int it = 0; !__all(bad == 0) && it < SPIN_MAX; ++it) {
        k1 = 0; k2 = 0; bad = 0;
#pragma unroll
        for (int i = 0; i < 4; ++i) {
          const u32x4 q = __builtin_amdgcn_raw_buffer_load_b128(rf, l16, ko_ + i * 1024, 16);
          k2 = max(k2, min(k1, q.x)); k1 = max(k1, q.x);
          k2 = max(k2, min(k1, q.z)); k1 = max(k1, q.z);
          bad |= (q.y ^ tag) | (q.w ^ tag);
        }
        asm volatile("" ::: "memory");
      }
      merge2(k1, k2, dpp_u<0xB1>(k1), dpp_u<0xB1>(k2));
      merge2(k1, k2, dpp_u<0x4E>(k1), dpp_u<0x4E>(k2));
      merge2(k1, k2, dpp_u<0x141>(k1), dpp_u<0x141>(k2));
      merge2(k1, k2, dpp_u<0x140>(k1), dpp_u<0x140>(k2));
      if ((lane & 15) == 0) { *(u32x2*)(smem + LDS_KEY + (pw * 4 + rq) * 8) = (u32x2){k1, k2}; ((unsigned*)(smem + LDS_SEL))[5] = tag; }
      PAGE(3);
      BAR_LGKM();
#pragma unroll
      for (int jj = 0; jj < 4; ++jj) tv[jj] = *(const u32x4*)(smem + G2_MRING + m_off(0) + (jj * 4 + pw) * 1024 + lane * 16);
      { const u32x2 kp = *(const u32x2*)(smem + LDS_KEY + ci * 8); k1 = kp.x; k2 = kp.y; }
#pragma unroll
      for (int jj = 0; jj < 4; ++jj) __builtin_amdgcn_raw_buffer_store_b128(tv[jj], rm2, vm2 + jj * 32 * 8192 + 2 * tt * 128, 0, 16);
      G2_STAGE_N(1, W2M);
      WAIT_VM(10);
      if (lane == tt) __builtin_amdgcn_raw_buffer_store_b32(tag, rf, abo, 0, 16);
      G2_STAGE_N(2, W20);
      __builtin_amdgcn_s_sleep(2);
      WAIT_VM(9);
      fs = bld4_sc1(rf, abq[0]);
    }
    BAR();
#pragma unroll
    for (int jj = 0; jj < 4; ++jj) tv[jj] = *(const u32x4*)(smem + G2_MRING + m_off(1) + (jj * 4 + pw) * 1024 + lane * 16);
    G2_STAGE_N(3, NOW);
    asm volatile("s_waitcnt vmcnt(8)" : "+v"(fs) :: "memory");
    ready = (unsigned)__ballot(fs == tag);
    need(2);
    G2_STAGE_M(2, NOW);
    need(3);
    G2_STAGE_M(3, NOW);
    fs = bld4_sc1(rf, abq[1]);
    touches(IC<1>{});
    W21(0); W21(1); W21(2); W21(3); W21(4); W21(5); W21(6); W21(7); W21(8); W21(9); W21(10); W21(11);
    asm volatile("" : "+v"(tv[0]), "+v"(tv[1]), "+v"(tv[2]), "+v"(tv[3]));
    auto step2 = [&](auto JB) __attribute__((always_inline)) {
      constexpr int jb = decltype(JB)::value;
      if constexpr (jb == 3) PAGE(4);
      if constexpr (jb == 9) PAGE(5);
      constexpr auto must = [](int q) { return (q == 2 ? 4 : 0) + ((FSM >> q) & 1) + ((q == GV_JB || q == GV_JB - 1) ? 2 : 0); };
      if constexpr (jb == 2) wait_vm<4 + must(1)>();
      else wait_vm<dn_cnt(jb - 2) + (jb == 5 ? 1 : 0) + 12 + must(jb - 1) + dn_cnt(jb - 1)>();
      if constexpr (jb == 3) { if (pw == 0 && (lane & ~8) == 0) ((unsigned*)(smem + LDS_SEL))[tk] = ksel; }
      if constexpr ((jb == 3 || jb == DG + 1)) {
        *(f32x4*)(lg + pw * 256 + lane * 4) = macc;
        macc = (f32x4){0.f, 0.f, 0.f, 0.f};
        lgkm0(sd);
      }
      BAR();
      float p0[4], p1[4];
      const int col = (pw & 1) * 64 + lane;
      if constexpr (jb == 3) {
        if (g == 0 && pw == 0 && lane == 0) __hip_atomic_store((unsigned*)flags + FL_EPOCH, epoch + 1u, __ATOMIC_RELAXED, __HIP_MEMORY_SCOPE_AGENT);
      }
      if constexpr (jb == DG + 1) {
        const float* p = lg + (pw & 1) * 32 + (lane >> 4) * 64 + ((2 * lane) & 7) * 4 + ((lane >> 2) & 3);
#pragma unroll
        for (int i = 0; i < 4; ++i) { p0[i] = p[i * 256]; p1[i] = p[i * 256 + 4]; }
      }
      if constexpr (jb == 3) {
        const float* p = lg + (col >> 5) * 64 + (col & 7) * 4 + ((col >> 3) & 3);
#pragma unroll
        for (int i = 0; i < 4; ++i) { p0[i] = p[i * 256]; p1[i] = p[i * 256 + 32]; }
      }
      if constexpr (jb == 4) {
        if (lane == 8 + tt) __builtin_amdgcn_raw_buffer_store_b32(tag, rf, abo, 0, 16);
      }
      if constexpr ((FSM >> (jb - 2)) & 1) {
        asm volatile("" : "+v"(fs));
        ready |= (unsigned)__ballot(fs == tag);
      }
      if constexpr (jb == GV_JB + 2) asm volatile("" : "+v"(gv[0]), "+v"(gv[1]), "+v"(gv[2]), "+v"(gv[3]));
#define W2(i) do { if (jb == 3 && i == 2) asm volatile("" : "+v"(p0[0]), "+v"(p0[1]), "+v"(p0[2]), "+v"(p0[3]), "+v"(p1[0]), "+v"(p1[1]), "+v"(p1[2]), "+v"(p1[3])); work2(JB, IC<i>{}); } while (0)
      if constexpr (jb < 13) {
        G2_STAGE_N(jb + 2, W2);
        need(jb + 2);
        G2_STAGE_M(jb + 2, W2);
      } else {
#define W14(i) do { if (i == B14P - 1) { wait_vm<B14P>(); BAR(); } } while (0)
#define W14M(i) W14(i - 8)
        need(15);
        G2_STAGE_M(15, W14M);
#define LASTQ(q) do { const int kb = KROT(15) + ((q + kq + 1) & 3) * 32 * S_ * 2; \
        DMA_(rn2, vn2, mb0, n_off(15) + q * 4096, kb, 0, ""); W14(4 + 2 * q); DMA_(rn2, vn2, mb0, n_off(15) + 16384 + q * 4096, kb, 128 * S_ * 2, ""); W14(5 + 2 * q); } while (0)
        LASTQ(0); LASTQ(1); LASTQ(2); LASTQ(3);
        SB();
      }
      if constexpr (jb == 2) {
#pragma unroll
        for (int jj = 0; jj < 4; ++jj) __builtin_amdgcn_raw_buffer_store_b128(tv[jj], rm2, vm2 + jj * 32 * 8192 + (2 * tt + 1) * 128, 0, 16);
      }
      if constexpr ((FSM >> jb) & 1) fs = bld4_sc1(rf, abq[jb < 5 ? 1 : 2]);
      if constexpr ((jb == GV_JB || jb == GV_JB - 1)) {
#pragma unroll
        for (int i = (jb == GV_JB ? 2 : 0); i < (jb == GV_JB ? 4 : 2); ++i) gv[i] = bld16_sc1(rgu, vgu, i * F_ * 16);
      }
      touches(JB);
#pragma unroll
      for (int i = dn_first(jb); i < dn_first(jb) + dn_cnt(jb); ++i) dv[i] = bld16_nt(rdn, vdn, i * D_ * 2);
      if constexpr (jb == DG + 1) {
        if (pw < 2)
          __builtin_amdgcn_raw_buffer_store_b128((u32x4){__float_as_uint((p0[0] + p0[1]) + (p0[2] + p0[3])), tag, __float_as_uint((p1[0] + p1[1]) + (p1[2] + p1[3])), tag},
                                                 mk_rsrc(wsp + WS_DN), (((pw * 8 + cg8) * 32 + rgb) * 64 + lane) * 16, 0, 16);
      }
      if constexpr (jb == 3) {
        if (moe && pw < 2)
          __builtin_amdgcn_raw_buffer_store_b128((u32x4){__float_as_uint((p0[0] + p0[1]) + (p0[2] + p0[3])), tag, __float_as_uint((p1[0] + p1[1]) + (p1[2] + p1[3])), tag},
                                                 rgu, ((mat * 4 + dq) * F_ + cg * 128 + col) * 16, 0, 16);
      }
      SB();
    };
    static_assert(DG + 1 <= 12 && DG >= GV_JB + 3 && dn_first(DG - 2) == 7, "schedule");
    step2(IC<2>{}); step2(IC<3>{}); step2(IC<4>{}); step2(IC<5>{}); step2(IC<6>{}); step2(IC<7>{}); step2(IC<8>{}); step2(IC<9>{}); step2(IC<10>{}); step2(IC<11>{}); step2(IC<12>{}); step2(IC<13>{});
    lgkm0(sd);
    PAGE(7);
    WAIT_VM(4); BAR();
    xq[0] = ld16_s<0>(x, xo); xq[1] = ld16_s<64>(x, xo); sq = ld8_s_sc1(stat, xo >> 11 << 3);
    WAIT_VM(3); BAR();
    asm volatile("s_waitcnt vmcnt(0)" : "+v"(xq[0]), "+v"(xq[1]), "+v"(sq) :: "memory");
    key[0] = __builtin_amdgcn_readlane(ksel, 0); key[1] = __builtin_amdgcn_readlane(ksel, 8);
    nf_pre();
#pragma unroll
    for (int n = 0; n < 16; ++n) nf_step(n);
#pragma unroll 1
    for (int c = 0; c < 2; ++c) {
      const unsigned kc = __builtin_amdgcn_readlane(ksel, c * 8);
      const int ic = (0xffff - (int)(kc & 0xffffu)) & (S_ - 1);
      if ((ic >> 8) != tt || ((ic >> 5) & 3) != kq) continue;
      const __amdgpu_buffer_rsrc_t rd = mk_rsrc(wsp + WS_DN + (size_t)(c * 8 + et) * 32768);
#pragma unroll 1
      for (int it = 0; it < SPIN_MAX; ++it) {
        if (__all(__builtin_amdgcn_raw_buffer_load_b32(rd, (lane & 31) * 1024 + 1020, 0, 16) == tag)) break;
        __builtin_amdgcn_s_sleep(2);
        asm volatile("" ::: "memory");
      }
      float s0, s1;
      unsigned bad = 1;
#pragma unroll 1
      for (int it = 0; !__all(bad == 0) && it < SPIN_MAX; ++it) {
        bad = 0; s0 = 0.f; s1 = 0.f;
#pragma unroll
        for (int i = 0; i < 8; ++i) {
          const u32x4 q = __builtin_amdgcn_raw_buffer_load_b128(rd, rq * 1024 + pw * 256 + ci * 16, i * 4096, 16);
          bad |= (q.y ^ tag) | (q.w ^ tag); s0 += __uint_as_float(q.x); s1 += __uint_as_float(q.z);
        }
        asm volatile("" ::: "memory");
      }
      s0 = fold32(s0, s1);
      s0 = fold16(s0, s0);
      const float sg = rbf(__builtin_amdgcn_rcpf(1.f + __expf(-key_score(kc))));
      ((float*)(smem + LDS_FIN))[c * 128 + pw * 32 + ci * 2 + (lane >> 5)] = rbf(rbf(s0) * sg);
      asm volatile("s_waitcnt lgkmcnt(0)" ::: "memory");
      if (lane == 0) asm volatile("ds_add_u32 %0, %1" :: "v"(lds_addr(smem + LDS_SEL + 24 + c * 4)), "v"(1) : "memory");
    }
    asm volatile("v_mov_b32 %0, 0" : "=v"(fx), "=v"(ya[0]), "=v"(ya[1]), "=v"(ya[2]), "=v"(ya[3]));
  } else {
  {
    const int q = j * 4 + wid;
    constexpr int Q1 = WS_MB, Q2 = Q1 + 8, Q3 = Q2 + 8, Q4 = Q3 + 7, Q5 = Q4 + 7, Q6 = Q5 + 7, Q7 = Q6 + 4, Q8 = Q7 + 4, QC = Q8 + 1;
    static_assert(QC <= 128, "warm-up slots");
    unsigned dmy = 0;
    if (q < QC) {
      const char* base; int b0;
      if (q < Q1) { base = wsp; b0 = 0; }
      else if (q < Q2) { base = (const char*)ws_re; b0 = Q1; }
      else if (q < Q3) { base = (const char*)ws_im; b0 = Q2; }
      else if (q < Q4) { base = (const char*)w_gate; b0 = Q3; }
      else if (q < Q5) { base = (const char*)w_up; b0 = Q4; }
      else if (q < Q6) { base = (const char*)w_down; b0 = Q5; }
      else if (q < Q7) { base = (const char*)out; b0 = Q6; }
      else if (q < Q8) { base = (const char*)x; b0 = Q7; }
      else { base = (const char*)w_r; b0 = Q8; }
      base += (size_t)(q - b0) << 20;
      asm volatile("s_load_dword %0, %1, 0" : "+s"(dmy) : "s"(base) : "memory");
    }
    {
      const int w = g * 4 + wid;
      constexpr int R = (int)(WS_FLAGS - WS_DN);
      static_assert(FL_X == 0 && FL_ST == 6144 * 32 && FL_EPOCH == 6528 * 32, "");
      const int o = lane < 6 ? R + (lane * 1024 + w) * 128 : lane > 6 || w >= 896 ? (int)0x80000000 : w < 512 ? w * 1024 + 896 : R + (6144 - 512 + w) * 128;
      asm volatile("s_mov_b32 m0, %0\n\ts_nop 0\n\tbuffer_load_dword %1, %2, 0 offen sc1 lds"
                   :: "s"(lds_addr(smem + LDS_DUMMY) + wid * 1024 + 512), "v"(o), "s"(mk_rsrc(wsp + WS_DN)) : "memory");
    }
    if (tid < 24) ((int*)(smem + LDS_SEL))[tid] = 0;
    WAIT_VM(1);
    if (wid == 0) { CALL_PG(1); CALL_PG(8); CALL_PG(5); }
    else if (wid == 1) { CALL_PG(9); CALL_PG(6); }
    else if (wid == 2) { CALL_PG(3); CALL_PG(10); CALL_PG(7); }
    else { CALL_PG(4); CALL_PG(11); }
    asm volatile("s_barrier\n\ts_waitcnt lgkmcnt(0)" : "+s"(dmy) :: "memory");
  }
  PAGE(8);
  epi_setup();
  const int wr = wid >> 1, wc = wid & 1;
  const int sw = (fr >> 1) & 7;
  const int ko0 = ((0 + fq) ^ sw) * 16, ko1 = ((4 + fq) ^ sw) * 16;
  const int mrow = (fr >> 2) * 8 + (fr & 3);
  bf16x8 fm[2][4], fn[2][4];
  {
    const char* pm = smem + (wr * 64 + mrow) * 128;
    const char* pn = smem + 16384 + (wc * 64 + mrow) * 128;
    f32x4 acc[4][4];
#pragma unroll
    for (int i = 0; i < 4; ++i)
#pragma unroll
      for (int jn = 0; jn < 4; ++jn)
#pragma unroll
        for (int q = 0; q < 4; ++q) acc[i][jn][q] = 0.f;
#define TO(i) (((i) >> 1) * 4096 + ((i) & 1) * 512)
#define LDSP(T, a) ((__attribute__((address_space(3))) T*)(size_t)(a))
#define SLOTS(n) _Pragma("unroll") for (int q = 0; q < (n); ++q)
#define MFA(c, a, b) asm volatile("v_mfma_f32_16x16x32_bf16 %0, %1, %2, %0" : "+v"(c) : "v"(a), "v"(b))
#define MFA2(c, a, b, a2, b2) asm volatile("v_mfma_f32_16x16x32_bf16 %0, %1, %2, %0\n\tv_mfma_f32_16x16x32_bf16 %0, %3, %4, %0" : "+v"(c) : "v"(a), "v"(b), "v"(a2), "v"(b2))
    unsigned lm[2][2], ln[2][2];
#pragma unroll
    for (int u = 0; u < 2; ++u)
#pragma unroll
      for (int kk = 0; kk < 2; ++kk) {
        lm[u][kk] = lds_addr(pm) + u * 2 * G1_STAGE + (kk ? ko1 : ko0);
        ln[u][kk] = lds_addr(pn) + u * 2 * G1_STAGE + (kk ? ko1 : ko0);
        asm volatile("" : "+v"(lm[u][kk]), "+v"(ln[u][kk]));
      }
    auto ldm = [&](int stg, int i, int kk) __attribute__((always_inline)) { fm[kk][i] = *LDSP(const bf16x8, lm[stg >> 1][kk] + (stg & 1) * G1_STAGE + TO(i)); };
    auto ldn = [&](int stg, int jn, int kk) __attribute__((always_inline)) { fn[kk][jn] = *LDSP(const bf16x8, ln[stg >> 1][kk] + (stg & 1) * G1_STAGE + TO(jn)); };
    auto quad = [&](int a, int c, int d, int s) __attribute__((always_inline)) {
      const int i = (s < 2 || s >= 6) ? a : a + 1, jn = s < 4 ? c : d, kk = (s == 1 || s == 2 || s == 5 || s == 6);
      MFA(acc[i][jn], fm[kk][i], fn[kk][jn]);
    };
    auto quad2 = [&](int a, int c, int d, int s) __attribute__((always_inline)) {
      const int i = (s < 2 || s >= 6) ? a : a + 1, jn = s < 4 ? c : d, kk = (s == 2 || s == 6);
      MFA2(acc[i][jn], fm[kk][i], fn[kk][jn], fm[1 - kk][i], fn[1 - kk][jn]);
    };
    auto half1 = [&](int stg) __attribute__((always_inline)) {
      const int ja = (stg & 1) * 2, jb = 2 - ja;
      SLOTS(8) {
        quad(0, ja, ja + 1, q);
        if (q < 4) ldn(stg, jb + (q >> 1), q & 1); else ldm(stg, 2 + ((q - 4) >> 1), q == 5 || q == 6);
        SB();
      }
      SLOTS(4) { quad2(0, jb, jb + 1, 2 * q); SB(); }
    };
    auto half2 = [&](int stg) __attribute__((always_inline)) {
      const int ja = (stg & 1) * 2, jb = 2 - ja, nx = (stg + 1) & 3;
      SLOTS(8) {
        quad(2, jb + 1, jb, q);
        if (!(q & 1)) ldm(nx, q >> 2, q == 2 || q == 4);
        SB();
      }
      SLOTS(8) {
        quad(2, ja, ja + 1, q);
        if (!(q & 1)) ldn(nx, jb + (q >> 2), (q >> 1) & 1);
        SB();
      }
    };
    SLOTS(16) asm volatile("" : "+v"(acc[q >> 2][q & 3]));
    SB();
    SLOTS(4) { ldm(0, q >> 1, q & 1); ldn(0, q >> 1, q & 1); }
    SB();
#pragma unroll 1
    for (int k4 = 0; k4 < G1_NKT - 4; k4 += 4) {
      half1(0); BAR_LGKM();
      SB(); half2(0);
      half1(1); BAR_LGKM();
      SB(); half2(1);
      half1(2); BAR_LGKM();
      SB(); half2(2);
      half1(3); BAR_LGKM();
      SB(); half2(3);
    }
    bf16x8 gm[2][4], gn[2][8];
    unsigned am[2][2], an[2][2];
#pragma unroll
    for (int u = 0; u < 2; ++u)
#pragma unroll
      for (int kk = 0; kk < 2; ++kk) {
        am[u][kk] = lds_addr(pm) + (u ? 3 : 1) * G1_STAGE + (kk ? ko1 : ko0);
        an[u][kk] = lds_addr(pn) + (u ? 3 : 1) * G1_STAGE + (kk ? ko1 : ko0);
        asm volatile("" : "+v"(am[u][kk]), "+v"(an[u][kk]));
      }
    unsigned al = lds_addr(smem + LDS_MB) + (wr * 32 + fq * 8) * 4, ac = lds_addr(smem + LDS_MB) + (256 + wc * 64 + mrow) * 4;
    unsigned ae = lds_addr(smem + G2_MRING) + (wc * 64 + mrow) * 128 + (((wr * 4 + fq) ^ sw) * 16);
    asm volatile("" : "+v"(al), "+v"(ac), "+v"(ae));
    auto rdg = [&](int set, int t, int h, int r) __attribute__((always_inline)) {
      const int u = t == 15, o = t == 14 ? G1_STAGE : 0;
      if (r == 0 || r == 2 || r == 4 || r == 5) {
        const int i = r >= 4, kk = r == 2 || r == 4;
        gm[set][kk * 2 + i] = *LDSP(const bf16x8, am[u][kk] + o + h * 4096 + i * 512);
      } else {
        const int jn = r < 4 ? 0 : (r - 4) >> 1, kk = r < 4 ? r >> 1 : r & 1;
        gn[set][kk * 4 + jn] = *LDSP(const bf16x8, an[u][kk] + o + TO(jn));
      }
    };
    auto mmg = [&](int set, int h, int q) __attribute__((always_inline)) {
      const int jn = q >> 2, r = q & 3, i = (r >> 1) ^ (jn & 1), kk = r == 1 || r == 2;
      MFA(acc[h * 2 + i][jn], gm[set][kk * 2 + i], gn[set][kk * 4 + jn]);
    };
    f32x4 rsv[2], mrv[2];
    float cev[4], tc[32];
    auto rds = [&](int h, int r) __attribute__((always_inline)) {
      if (r < 2) mrv[r] = *LDSP(const f32x4, al + h * 256 + r * 16); else rsv[r - 2] = *LDSP(const f32x4, al + 512 + h * 256 + (r - 2) * 16);
    };
    auto prep = [&](int n) __attribute__((always_inline)) {
      tc[n] = llvm_fmul_legacy(mrv[(n >> 2) & 1][n & 3], cev[n >> 3]);
      asm volatile("" : "+v"(tc[n]));
    };
    u32x4 ep;
    auto epi = [&](int h, int q) __attribute__((always_inline)) {
      const int jn = q >> 1, i = q & 1;
      asm volatile("v_fmac_f32 %0, %4, %8\n\tv_fmac_f32 %1, %5, %9\n\tv_fmac_f32 %2, %6, %10\n\tv_fmac_f32 %3, %7, %11"
                   : "+v"(tc[q * 4]), "+v"(tc[q * 4 + 1]), "+v"(tc[q * 4 + 2]), "+v"(tc[q * 4 + 3])
                   : "v"(acc[h * 2 + i][jn][0]), "v"(acc[h * 2 + i][jn][1]), "v"(acc[h * 2 + i][jn][2]), "v"(acc[h * 2 + i][jn][3]),
                     "v"(rsv[i][0]), "v"(rsv[i][1]), "v"(rsv[i][2]), "v"(rsv[i][3]));
      ep[i * 2] = pk2(tc[q * 4], tc[q * 4 + 1]); ep[i * 2 + 1] = pk2(tc[q * 4 + 2], tc[q * 4 + 3]);
      if (i) *LDSP(u32x4, ae + m_off(h) + TO(jn)) = ep;
    };
    SB();
    half1(0);
    BAR_LGKM();
    SB();
    SLOTS(16) {
      if (q < 8) quad(2, 3, 2, q); else quad(2, 0, 1, q - 8);
      if (q < 12) rdg(0, 13, 0, q);
      SB();
    }
    BAR();
    SB();
    SLOTS(16) {
      mmg(0, 0, q);
      if (q < 12) rdg(1, 14, 0, q);
      else { rds(0, q - 12); cev[q - 12] = *LDSP(const float, ac + (((q - 12) >> 1) * 32 + ((q - 12) & 1) * 4) * 4); }
      SB();
    }
    PAGE(9);
    BAR();
    SB();
    SLOTS(16) { mmg(1, 0, q); if ((q & 3) < 3) rdg(0, 15, 0, (q >> 2) * 3 + (q & 3)); if (q >= 8) { prep(2 * q - 16); prep(2 * q - 15); } SB(); }
    SLOTS(16) { mmg(0, 0, q); if ((q & 3) < 3) rdg(1, 13, 1, (q >> 2) * 3 + (q & 3)); prep(16 + q); SB(); if (q >= 9 && (q & 1)) { epi(0, (q - 9) >> 1); SB(); } }
    SLOTS(8) { mmg(1, 1, q); if (q < 6) { rdg(0, 14, 1, 2 * q); rdg(0, 14, 1, 2 * q + 1); } SB(); if (q & 1) { epi(0, 4 + (q >> 1)); SB(); } }
    BAR_LGKM();
    SB();
    SLOTS(8) { mmg(1, 1, 8 + q); if (q < 4) rdg(1, 15, 1, q == 0 ? 1 : q == 1 ? 3 : q + 4); else rds(1, q - 4); SB(); }
    SLOTS(16) { mmg(0, 1, q); if (q < 8) rdg(1, 15, 1, q == 0 ? 0 : q == 1 ? 2 : q < 4 ? q + 2 : q + 4); prep(q); SB(); }
    asm volatile("s_waitcnt lgkmcnt(0)" ::: "memory");
    if (lane == 0) asm volatile("ds_add_u32 %0, %1" :: "v"(lds_addr(smem + LDS_SEL + 12)), "v"(1) : "memory");
    SB();
    SLOTS(16) {
      mmg(1, 1, q);
      if (q < 4) fn[0][q] = *(const bf16x8*)(smem + (wr * 128 + fr) * 128 + (q >> 1) * 2048 + ((q & 1) ? ko1 : ko0));
      else if (q < 8) fm[q & 1][(q - 4) >> 1] = *(const bf16x8*)(smem + G2_MRING + m_off(0) + (wc * 64 + mrow) * 128 + TO((q - 4) >> 1) + ((q & 1) ? ko1 : ko0));
      prep(16 + q);
      SB();
    }
    {
      lds_vi rel = (lds_vi)(smem + LDS_SEL + 12);
      int it = 0;
      while (rel[0] != 4 && it < SPIN_MAX * 8) ++it;
      asm volatile("" ::: "memory");
    }
    asm volatile("s_nop 7");
    SB();
    SLOTS(8) { epi(1, q); SB(); }
  }

  const char* pn = smem + (wr * 128 + fr) * 128;
  const char* pm = smem + G2_MRING + (wc * 64 + mrow) * 128;
  f32x4 acc[8][4];
  auto mm = [&](bool imaj, int set, int q2, int s, int mode) __attribute__((always_inline)) {
    int i, jj, kk;
    if (imaj) { i = s >> 2; const int r = s & 3; jj = (r >> 1) ^ (i & 1); kk = (r == 1 || r == 2); }
    else { jj = s >> 3; const int r = s & 7; i = jj ? 3 - (r >> 1) : (r >> 1); kk = ((r + 1) >> 1) & 1; }
    if (mode == 1 && !(s & 1)) asm volatile("v_mfma_f32_16x16x32_bf16 %0, %1, %2, 0" : "=&v"(acc[q2 * 2 + jj][i]) : "v"(fm[kk][i]), "v"(fn[set][jj * 2 + kk]));
    else if (mode) asm volatile("v_mfma_f32_16x16x32_bf16 %0, %1, %2, %0" : "+v"(acc[q2 * 2 + jj][i]) : "v"(fm[kk][i]), "v"(fn[set][jj * 2 + kk]));
    else acc[q2 * 2 + jj][i] = __builtin_amdgcn_mfma_f32_16x16x32_bf16(fm[kk][i], fn[set][jj * 2 + kk], acc[q2 * 2 + jj][i], 0, 0, 0);
  };
  unsigned bn[2], bm[2], an[2], am[2];
#pragma unroll
  for (int kk = 0; kk < 2; ++kk) {
    bn[kk] = lds_addr(pn) + (kk ? ko1 : ko0); bm[kk] = lds_addr(pm) + (kk ? ko1 : ko0);
    asm volatile("" : "+v"(bn[kk]), "+v"(bm[kk]));
    an[kk] = bn[kk]; am[kk] = bm[kk] + m_off(0);
  }
#define RDN(jt, kk, o) (*LDSP(const bf16x8, an[kk] + (o) + (jt) * 2048))
#define RDM(i, kk, o) (*LDSP(const bf16x8, am[kk] + (o) + TO(i)))
  PAGE(10);
  tag = __builtin_amdgcn_readfirstlane(((const unsigned*)(smem + LDS_SEL))[5]);
  {
    int son = 0;
    auto tile = [&](int kt, int zero) __attribute__((always_inline)) {
      SLOTS(16) {
        mm(true, 0, 0, q, zero);
        if (!(q & 1)) {
          if (q < 8) fm[(q >> 1) & 1][2 + (q >> 2)] = RDM(2 + (q >> 2), (q >> 1) & 1, 0);
          else { const int r = (q - 8) >> 1; fn[1][r] = RDN(2 + (r >> 1), r & 1, 0); }
        }
        SB();
      }
      SLOTS(16) {
        mm(false, 1, 1, q, zero);
        if (!(q & 1) && q < 8) fn[0][q >> 1] = RDN(4 + (q >> 2), (q >> 1) & 1, 0);
        SB();
      }
      const int som = son >> 1;
      son = son == 65536 ? 0 : son + 32768;
      SLOTS(16) {
        mm(false, 0, 2, q, zero);
        if (!(q & 1) && q < 8) fn[1][q >> 1] = RDN(6 + (q >> 2), (q >> 1) & 1, 0);
        if (q == 9 || q == 11) { an[q == 11] = bn[q == 11] + son; asm volatile("" : "+v"(an[q == 11])); }
        if (q == 13 || q == 15) { am[q == 15] = bm[q == 15] + som; asm volatile("" : "+v"(am[q == 15])); }
        SB();
      }
      BAR_LGKM();
      SB();
      SLOTS(16) {
        mm(true, 1, 3, q, zero);
        if (!(q & 1)) {
          if (q < 8) fn[0][q >> 1] = RDN(q >> 2, (q >> 1) & 1, 0);
          else fm[(q >> 1) & 1][(q - 8) >> 2] = RDM((q - 8) >> 2, (q >> 1) & 1, 0);
        }
        SB();
      }
    };
    SB();
    tile(0, 1);
#pragma unroll 1
    for (int kt = 1; kt < G2_NKT - 3; ++kt) tile(kt, 0);
  }
  {
    constexpr int NG = 12;
    static constexpr int g_t[NG] = {0, 0, 1, 1, 2, 2, 2, 1, 0, 0, 1, 2};
    static constexpr int g_q[NG] = {0, 1, 0, 1, 0, 1, 2, 2, 2, 3, 3, 3};
    constexpr int G14 = 1, G15 = 3, GP0 = 5, GP1 = 6;
    constexpr auto chp = [](int g) { return g == 0 || g_t[g] != g_t[g - 1]; };
    constexpr auto chn = [](int g) { return g + 1 < NG && g_t[g + 1] != g_t[g]; };
    static_assert(n_off(13) == 32768 && n_off(14) == 65536 && n_off(15) == 0, "");
    unsigned an2[2];
#pragma unroll
    for (int kk = 0; kk < 2; ++kk) {
      an[kk] = bn[kk]; an2[kk] = bn[kk] + 65536; am[kk] = bm[kk];
      asm volatile("" : "+v"(an[kk]), "+v"(an2[kk]), "+v"(am[kk]));
    }
    int vo;
    u32x4 pk;
    u32x2 kv;
    constexpr int NF0 = 152, F0 = 160, F1 = 172, F2 = 188;
    auto publish = [&](int d, int h) __attribute__((always_inline)) {
      if (h == 0) { vo = lane; PINV(vo); vo = vo * 16 + (((d + kq + 1) & 3) * 4 + kq) * 4 * MSG; PINV(vo); }
      const f32x4 v = acc[2 * d + (h >> 2)][h & 3];
      pk[(h & 1) * 2] = pk2(v[0], v[1]); pk[(h & 1) * 2 + 1] = pk2(v[2], v[3]);
      if (h & 1) __builtin_amdgcn_raw_buffer_store_b128(pk, rs, vo + (h >> 1) * 1024, 0, 16);
    };
    int fo = lane == 0 ? kq * 512 : (int)0x80000000;
#define MSG_FLAG(d) __builtin_amdgcn_raw_buffer_store_b32(tag, rc, fo + (((d) + kq + 1) & 3) * 2048, 0, 16)
    auto rdn = [&](int g, int r) __attribute__((always_inline)) {
      const int t = g_t[g], jt = g_q[g] * 2 + (r >> 1);
      fn[g & 1][r] = *LDSP(const bf16x8, (t == 1 ? an2[r & 1] : an[r & 1]) + (t == 0 ? 32768 : 0) + jt * 2048);
    };
    auto rdm = [&](int t, int i, int kk) __attribute__((always_inline)) { fm[kk][i] = *LDSP(const bf16x8, am[kk] + m_off(13 + t) + TO(i)); };
    auto group = [&](auto GI) __attribute__((always_inline)) {
      constexpr int gi = decltype(GI)::value;
      if (gi == G14) { BAR();
        SB(); }
      if (gi == 5) { BAR(); SB(); }
      if (gi == G15) {
        PAGE(11);
        BAR();
        SB();
        xq[0] = ld16_s<0>(x, xo); xq[1] = ld16_s<64>(x, xo);
        sq = ld8_s_sc1(stat, xo >> 11 << 3);
        kv = *(const u32x2*)(smem + LDS_SEL);
        SB();
      }
#pragma unroll
      for (int q = 0; q < 16; ++q) {
        const int n = gi * 16 + q;
        if (n == NF0) {
          wait_xq<12>(xq, sq);
          key[0] = __builtin_amdgcn_readfirstlane(kv.x); key[1] = __builtin_amdgcn_readfirstlane(kv.y);
          nf_pre();
          SB();
        }
        if (n == 112) {
        }
        if (n == 152) {
        }
        if (n == F0) {
          WAIT_VM(8); MSG_FLAG(0); SB(); }
        if (n == F1) { WAIT_VM(5); MSG_FLAG(1); SB(); }
        if (n == F2) {
          WAIT_VM(2);
          MSG_FLAG(2); fx = bld4_sc1(rc, po); SB(); }
        mm(chp(gi) || chn(gi), gi & 1, g_q[gi], q, 2);
        constexpr bool ni = gi + 1 < NG && (chp(gi + 1 < NG ? gi + 1 : gi) || chn(gi + 1 < NG ? gi + 1 : gi));
        auto nr = [](int r) { return ni && r >= 2 ? 5 - r : r; };
        if constexpr (chp(gi) && chn(gi)) {
          if (q < 4) rdm(g_t[gi], 2 + (q >> 1), q & 1); else if (q < 8) rdn(gi + 1, nr(q - 4)); else if (q < 12) rdm(g_t[gi + 1], (q - 8) >> 1, q & 1);
        } else if constexpr (chp(gi)) {
          if (q < 4) rdm(g_t[gi], 2 + (q >> 1), q & 1);
          else if (gi + 1 < NG && q < 12 && !(q & 1)) rdn(gi + 1, nr((q - 4) >> 1));
        } else if constexpr (chn(gi)) {
          if (!(q & 1)) { if (q < 8) rdn(gi + 1, nr(q >> 1)); else rdm(g_t[gi + 1], (q - 8) >> 2, (q >> 1) & 1); }
        } else {
          if (!(q & 1) && q < 8) rdn(gi + 1, nr(q >> 1));
        }
        if (gi == GP0 && q < 8) publish(0, q);
        if (gi == GP1 && q < 8) publish(1, q);
        if (gi == 9 && q < 8) publish(2, q);
        if (n >= NF0 && n < NF0 + 16) nf_step(n - NF0);
        SB();
      }
    };
#define GR4(i) group(IC<i>{}); group(IC<i + 1>{}); group(IC<i + 2>{}); group(IC<i + 3>{});
    GR4(0) GR4(4) GR4(8)
    asm volatile("s_nop 7");
    SB();
  }
#pragma unroll
  for (int i = 0; i < 4; ++i) *(f32x4*)(smem + LDS_ACC + wr * 16384 + wc * 4096 + i * 1024 + lane * 16) = acc[7][i];
#pragma unroll
  for (int i = 0; i < 4; ++i) ya[i] = acc[6][i];
  asm volatile("s_waitcnt lgkmcnt(0)" ::: "memory");
  if (lane == 0) sig[0] = 2;
  }

  PAGE(6);
  const unsigned rel0 = 0xffff - (key[0] & 0xffffu) - trow, rel1 = 0xffff - (key[1] & 0xffffu) - trow;
  if (min(rel0, rel1) < 16) {
#pragma unroll 1
    for (int c = 0; c < 2; ++c) {
      const unsigned rel = c ? rel1 : rel0;
      if (rel >= 16) continue;
      lds_vi cnt = (lds_vi)(smem + LDS_SEL + 24 + c * 4);
      int it = 0;
      while (cnt[0] != 4 && it < SPIN_MAX * 8) { __builtin_amdgcn_s_sleep(1); ++it; }
      asm volatile("" ::: "memory");
      if (rel == fr) {
        const float* fp = (const float*)(smem + LDS_FIN + c * 512) + (wid & 1) * 64 + fq * 8;
#pragma unroll
        for (int u4 = 0; u4 < 4; ++u4) {
          const f32x4 v = *(const f32x4*)(fp + (u4 >> 1) * 32 + (u4 & 1) * 4);
#pragma unroll
          for (int k = 0; k < 4; ++k) nf[u4 * 4 + k] += v[k];
        }
      }
    }
  }
  u32x4 pp[3][2];
  unsigned e_lo = 0x00003f80u, e_hi = 0x3f800000u;
  asm volatile("" : "+v"(e_lo), "+v"(e_hi));
  __amdgpu_buffer_rsrc_t rs_ = rs, rc_ = rc, ro_ = mk_rsrc(out);
  int mo[3] = {MSG_IDX(0) * 4 * MSG, MSG_IDX(1) * 4 * MSG, MSG_IDX(2) * 4 * MSG}, mv = lane * 16 + ha * 2048, mv2 = mv + 1024;
  asm volatile("" : "+s"(rs_), "+s"(rc_), "+s"(ro_), "+s"(mo[0]), "+s"(mo[1]), "+s"(mo[2]));
  unsigned as_ = lds_addr((const void*)smem) + LDS_SIG + (wid & 3) * 4, aa_ = lds_addr(smem + LDS_ACC) + (wid & 2) * 8192 + (wid & 1) * 4096 + lane * 16;
  int xs = ((trow + (lane >> 3)) * D_ + et * 128 + (wid & 1) * 64) * 2 + (lane & 7) * 16, xs2 = xs + 8 * D_ * 2;
  const unsigned mt = lds_addr(smem) + (wid & 2) * 8192 + (ha ? LDS_ACC + (wid & 1) * 4096 : n_off(14) + (wid & 1) * 2048);
  unsigned aw_ = mt + fr * 128 + ((fq ^ (fr & 7)) * 16), aw2 = aw_ ^ 64, ar_ = mt + (lane >> 3) * 128 + (((lane ^ (lane >> 3)) & 7) * 16);
  asm volatile("" : "+v"(mv), "+v"(mv2), "+v"(as_), "+v"(aa_), "+v"(xs), "+v"(xs2), "+v"(aw_), "+v"(aw2), "+v"(ar_));
  {
    unsigned sv_, it_ = SPIN_MAX * 8;
    asm volatile("s_cmp_eq_u32 %9, 0\n\ts_cbranch_scc1 2f\n"
                 "1:\n\tds_read_b32 %0, %10\n\ts_waitcnt lgkmcnt(0)\n\tv_cmp_eq_u32 vcc, 2, %0\n\ts_cbranch_vccnz 3f\n\ts_sleep 1\n\ts_sub_u32 %1, %1, 1\n\ts_cbranch_scc0 1b\n"
                 "3:\n\tds_read_b128 %4, %11\n\tds_read_b128 %5, %11 offset:1024\n\tds_read_b128 %6, %11 offset:2048\n\tds_read_b128 %7, %11 offset:3072\n"
                 "2:\n\ts_waitcnt vmcnt(0)\n\tv_cmp_eq_u32 vcc, %8, %3\n\ts_mov_b32 %2, vcc_lo"
                 : "=&v"(sv_), "+s"(it_), "=&s"(seen), "+v"(fx), "+v"(ya[0]), "+v"(ya[1]), "+v"(ya[2]), "+v"(ya[3])
                 : "s"(tag), "s"(ha), "v"(as_), "v"(aa_) : "vcc", "scc", "memory");
  }
#define MSG_POLL(m) do { \
    if (!((seen >> (m)) & 1)) { \
      unsigned pv_, it_ = SPIN_MAX; \
      asm volatile("1:\n\tbuffer_load_dword %0, %3, %4, 0 offen sc1\n\ts_waitcnt vmcnt(0)\n\tv_cmp_eq_u32 vcc, %5, %0\n\ts_mov_b32 %1, vcc_lo\n\t" \
                   "s_bitcmp1_b32 %1, %6\n\ts_cbranch_scc1 2f\n\ts_sub_u32 %2, %2, 1\n\ts_cbranch_scc1 2f\n\ts_sleep 1\n\ts_branch 1b\n2:" \
                   : "=&v"(pv_), "+s"(seen), "+s"(it_) : "v"(po), "s"(rc_), "s"(tag), "i"(m) : "vcc", "scc", "memory"); \
    } \
    pp[m][0] = __builtin_amdgcn_raw_buffer_load_b128(rs_, mv, mo[m], 16); \
    pp[m][1] = __builtin_amdgcn_raw_buffer_load_b128(rs_, mv2, mo[m], 16); } while (0)
#define MSG_ADD(m) do { \
    _Pragma("unroll") for (int u = 0; u < 8; ++u) { \
      ya[u >> 1][(u & 1) * 2] = add_bf(pp[m][u >> 2][u & 3], e_lo, ya[u >> 1][(u & 1) * 2]); ya[u >> 1][(u & 1) * 2 + 1] = add_bf(pp[m][u >> 2][u & 3], e_hi, ya[u >> 1][(u & 1) * 2 + 1]); } } while (0)
  MSG_POLL(2); MSG_POLL(1); MSG_POLL(0);
  asm volatile("s_waitcnt lgkmcnt(0)" : "+v"(ya[0]), "+v"(ya[1]), "+v"(ya[2]), "+v"(ya[3]) :: "memory");
  SB();
#pragma unroll
  for (int e = 0; e < 16; ++e) { float t = ya[e >> 2][e & 3] + nf[e]; PINV(t); ya[e >> 2][e & 3] = t; }
  SB();
  MSG_ADD(2); MSG_ADD(1);
  asm volatile("" : "+v"(ya[0]), "+v"(ya[1]), "+v"(ya[2]), "+v"(ya[3]));
  SB();
  MSG_ADD(0);
  {
    unsigned ov[8];
    SB();
#pragma unroll
    for (int u = 0; u < 8; ++u) ov[u] = pk2(ya[u >> 1][(u & 1) * 2], ya[u >> 1][(u & 1) * 2 + 1]);
    SB();
    *LDSP(u32x4, aw_) = (u32x4){ov[0], ov[1], ov[2], ov[3]};
    *LDSP(u32x4, aw2) = (u32x4){ov[4], ov[5], ov[6], ov[7]};
    asm volatile("" ::: "memory");
    __builtin_amdgcn_raw_buffer_store_b128(*LDSP(const u32x4, ar_), ro_, xs, 0, 16);
    __builtin_amdgcn_raw_buffer_store_b128(*LDSP(const u32x4, ar_ + 1024), ro_, xs2, 0, 16);
  }
}

extern "C" void kernel_launch(void* const* d_in, const int* in_sizes, int n_in, void* d_out, int out_size,
                              void* d_ws, size_t ws_size, hipStream_t stream) {
  const u16* x = (const u16*)d_in[0];
  const u16* ws_re = (const u16*)d_in[1];
  const u16* ws_im = (const u16*)d_in[2];
  const u16* wd_re = (const u16*)d_in[3];
  const u16* wd_im = (const u16*)d_in[4];
  const u16* w_r = (const u16*)d_in[6];
  const u16* w_gate = (const u16*)d_in[7];
  const u16* w_up = (const u16*)d_in[8];
  const u16* w_down = (const u16*)d_in[9];
  k_main<<<256, 512, 0, stream>>>(x, 0x7fffffffu, 0x00020000u, wd_re, 0x7fffffffu, 0x00020000u, wd_im, 0x7fffffffu, 0x00020000u, w_r, (char*)d_ws,
                                  ws_re, ws_im, w_gate, w_up, w_down, (u16*)d_out);
}
```

```cpp
#include <hip/hip_runtime.h>
#include <hip/hip_bf16.h>
#include <stdint.h>
#include <type_traits>

extern "C" __device__ unsigned long long llvm_dispatch_id(void) __asm("llvm.amdgcn.dispatch.id");
extern "C" __device__ float llvm_fmul_legacy(float, float) __asm("llvm.amdgcn.fmul.legacy");
typedef unsigned short u16;
typedef unsigned long long u64;
typedef __attribute__((ext_vector_type(8))) short bf16x8;
typedef __attribute__((ext_vector_type(4))) unsigned u32x4;
typedef __attribute__((ext_vector_type(2))) unsigned u32x2;
typedef __attribute__((ext_vector_type(4))) float f32x4;
typedef __attribute__((ext_vector_type(2))) float f32x2;
typedef __attribute__((ext_vector_type(2))) __bf16 bf16x2;
typedef __attribute__((address_space(3))) void* lds_vp;
typedef volatile __attribute__((address_space(3))) int* lds_vi;

constexpr int S_ = 2048, D_ = 1024, F_ = 3584;
template <int N> using IC = std::integral_constant<int, N>;

static __device__ __forceinline__ float bf2f(u16 u) { return __uint_as_float((unsigned)u << 16); }
static __device__ __forceinline__ u16 f2bf(float f) {
  __hip_bfloat16 b = __float2bfloat16(f);
  return *reinterpret_cast<u16*>(&b);
}
static __device__ __forceinline__ float rbf(float f) { return bf2f(f2bf(f)); }
static __device__ __forceinline__ float lo_bf(unsigned u) { return __uint_as_float(u << 16); }
static __device__ __forceinline__ float hi_bf(unsigned u) { return __uint_as_float(u & 0xffff0000u); }
static __device__ __forceinline__ unsigned pk2(float a, float b) {
  const bf16x2 v = __builtin_convertvector((f32x2){a, b}, bf16x2);
  return __builtin_bit_cast(unsigned, v);
}
static __device__ __forceinline__ float add_bf(unsigned u, unsigned e, float c) {
  return __builtin_amdgcn_fdot2_f32_bf16(__builtin_bit_cast(bf16x2, u), __builtin_bit_cast(bf16x2, e), c, false);
}
static __device__ __forceinline__ float ln_elem(float x, float mb, float sd, float ri) {
  const float d = rbf(x - mb);
  const float q0 = d * ri;
  return rbf(__builtin_fmaf(__builtin_fmaf(-q0, sd, d), ri, q0));
}
template <int CTRL> static __device__ __forceinline__ float dpp_f(float v) {
  return __int_as_float(__builtin_amdgcn_update_dpp(0, __float_as_int(v), CTRL, 0xf, 0xf, true));
}
template <int CTRL> static __device__ __forceinline__ unsigned dpp_u(unsigned v) {
  return (unsigned)__builtin_amdgcn_update_dpp(0, (int)v, CTRL, 0xf, 0xf, true);
}
static __device__ __forceinline__ float row_sum(float v) {
  v += dpp_f<0xB1>(v);
  v += dpp_f<0x4E>(v);
  v += dpp_f<0x141>(v);
  v += dpp_f<0x140>(v);
  return v;
}
static __device__ __forceinline__ float fold32(float a, float b) {
  const auto r = __builtin_amdgcn_permlane32_swap(__float_as_uint(a), __float_as_uint(b), false, false);
  return __uint_as_float(r[0]) + __uint_as_float(r[1]);
}
static __device__ __forceinline__ float fold16(float a, float b) {
  const auto r = __builtin_amdgcn_permlane16_swap(__float_as_uint(a), __float_as_uint(b), false, false);
  return __uint_as_float(r[0]) + __uint_as_float(r[1]);
}
static __device__ __forceinline__ float rdl(float v, int l) { return __int_as_float(__builtin_amdgcn_readlane(__float_as_int(v), l)); }

#define ST_JB 9
#define WSN 10
#define BST 0x25543321u
#define FSM 0xa2
#define GV_JB 8
#define DG 11
#define B14P 9
#define AB_STRIDE 32
#define SPIN_MAX (1 << 17)

static __device__ __forceinline__ unsigned lds_addr(const void* p) { return (unsigned)(size_t)(lds_vp)p; }

constexpr int MSG = 4096;
constexpr size_t WS_ABT   = 0;
constexpr size_t WS_SLAB  = WS_ABT + (size_t)D_ * 4096 * 2;
constexpr size_t WS_GU    = WS_SLAB + (size_t)64 * 64 * MSG;
constexpr size_t WS_DN    = WS_GU + (size_t)8 * F_ * 16;
constexpr size_t WS_FLAGS = WS_DN + (size_t)2 * 8 * 32 * 64 * 16;
constexpr int FL_X = 0;
constexpr int FL_AB = 64 * 64 * 32;
constexpr int FL_ST = FL_AB + 2048 * AB_STRIDE;
constexpr int FL_CE = FL_ST + 4096;
constexpr int FL_KEY = FL_CE + 4096;
constexpr int FL_EPOCH = FL_KEY + 4096;
constexpr int WS_MB = (int)((WS_FLAGS + (FL_EPOCH + 1) * 4 + 1048575) >> 20);

#define WAIT_VM(n) asm volatile("s_waitcnt vmcnt(" #n ")" ::: "memory")
template <int N> static __device__ __forceinline__ void wait_vm() { asm volatile("s_waitcnt vmcnt(%0)" :: "i"(N) : "memory"); }
#define BAR_LGKM() asm volatile("s_waitcnt lgkmcnt(0)\n\ts_barrier" ::: "memory")
#define BAR() asm volatile("s_barrier" ::: "memory")
#define SB() __builtin_amdgcn_sched_barrier(0)
#define PAGE(n) asm volatile("s_branch 2f\n\t.p2align 12\nk_pg" #n ":\n\ts_setpc_b64 s[42:43]\n2:")
#define CALL_PG(n) asm volatile("s_getpc_b64 s[40:41]\n3:\n\ts_mov_b32 s42, k_pg" #n "-3b\n\ts_ashr_i32 s43, s42, 31\n\ts_add_u32 s40, s40, s42\n\ts_addc_u32 s41, s41, s43\n\t" \
                                "s_swappc_b64 s[42:43], s[40:41]" ::: "s40", "s41", "s42", "s43", "scc", "memory")

static __device__ __forceinline__ __amdgpu_buffer_rsrc_t mk_rsrc(const void* p) {
  return __builtin_amdgcn_make_buffer_rsrc((void*)p, 0, 0x7fffffff, 0x00020000);
}
#define DMA_(rsrc, voff, mbase, MI, sbase, SI, pol) do { unsigned _t; \
    asm volatile("s_add_i32 m0, %2, %3\n\ts_add_i32 %0, %5, %6\n\tbuffer_load_dwordx4 %1, %4, %0 offen" pol " lds" \
                 : "=&s"(_t) : "v"(voff), "s"(mbase), "i"(MI), "s"(rsrc), "s"(sbase), "i"(SI) : "memory", "scc"); } while (0)
#define TOUCH(rsrc, voff, mbase, MI, soff) \
    asm volatile("s_add_i32 m0, %1, %2\n\ts_nop 0\n\tbuffer_load_dword %0, %3, %4 offen lds" \
                 :: "v"(voff), "s"(mbase), "i"(MI), "s"(rsrc), "s"(soff) : "memory", "scc")
template <int OFF> static __device__ __forceinline__ u32x4 ld16_s(const void* p, int voff) {
  u32x4 v;
  asm volatile("global_load_dwordx4 %0, %1, %2 offset:%3" : "=v"(v) : "v"(voff), "s"(p), "i"(OFF) : "memory");
  return v;
}
template <int OFF> static __device__ __forceinline__ void sld(unsigned& sink, const void* p, int soff) {
  asm volatile("s_load_dword %0, %1, %2 offset:%3" : "+s"(sink) : "s"(p), "s"(soff), "i"(OFF) : "memory");
}
static __device__ __forceinline__ unsigned gld4_s(const void* p, unsigned voff) {
  unsigned v;
  asm volatile("global_load_dword %0, %1, %2" : "=v"(v) : "v"(voff), "s"(p) : "memory");
  return v;
}
static __device__ __forceinline__ u32x2 bld8_sc1(__amdgpu_buffer_rsrc_t r, unsigned voff) {
  u32x2 v;
  asm volatile("buffer_load_dwordx2 %0, %1, %2, 0 offen sc1" : "=v"(v) : "v"(voff), "s"(r) : "memory");
  return v;
}
template <int N> static __device__ __forceinline__ void wait_xs(unsigned& a, u32x2& b) { asm volatile("s_waitcnt vmcnt(%2)" : "+v"(a), "+v"(b) : "i"(N) : "memory"); }
template <int N> static __device__ __forceinline__ void wait_xq(u32x2 (&a)[4], u32x2& b) { asm volatile("s_waitcnt vmcnt(%5)" : "+v"(a[0]), "+v"(a[1]), "+v"(a[2]), "+v"(a[3]), "+v"(b) : "i"(N) : "memory"); }
template <int OFF> static __device__ __forceinline__ u32x2 ld8_s(const void* p, int voff) {
  u32x2 v;
  asm volatile("global_load_dwordx2 %0, %1, %2 offset:%3" : "=v"(v) : "v"(voff), "s"(p), "i"(OFF) : "memory");
  return v;
}
static __device__ __forceinline__ u32x2 ld8_s_sc1(const void* p, int voff) {
  u32x2 v;
  asm volatile("global_load_dwordx2 %0, %1, %2 sc1" : "=v"(v) : "v"(voff), "s"(p) : "memory");
  return v;
}
static __device__ __forceinline__ void lgkm0(unsigned& sink) { asm volatile("s_waitcnt lgkmcnt(0)" : "+s"(sink) :: "memory"); }
template <typename T> static __device__ __forceinline__ void pin4(T& a, T& b, T& c, T& d) { asm volatile("" : "+s"(a), "+s"(b), "+s"(c), "+s"(d)); }
template <typename T, typename U> static __device__ __forceinline__ void pins(T& a, U& b) { asm volatile("" : "+s"(a), "+s"(b)); }
static __device__ __forceinline__ void launder(int& v, int& a, int& b) { asm volatile("" : "+v"(v)); asm volatile("" : "+s"(a), "+s"(b)); }
static __device__ __forceinline__ unsigned bld4_sc1(__amdgpu_buffer_rsrc_t r, int voff) {
  unsigned v;
  asm volatile("buffer_load_dword %0, %1, %2, 0 offen sc1" : "=v"(v) : "v"(voff), "s"(r) : "memory");
  return v;
}
static __device__ __forceinline__ u32x4 bld16_nt(__amdgpu_buffer_rsrc_t r, int voff, int soff) {
  u32x4 v;
  asm volatile("buffer_load_dwordx4 %0, %1, %2, %3 offen nt" : "=v"(v) : "v"(voff), "s"(r), "s"(soff) : "memory");
  return v;
}
static __device__ __forceinline__ void bld16_nt_io(u32x4& v, __amdgpu_buffer_rsrc_t r, int voff, int soff) {
  asm volatile("buffer_load_dwordx4 %0, %1, %2, %3 offen nt" : "+v"(v) : "v"(voff), "s"(r), "s"(soff) : "memory");
}
static __device__ __forceinline__ u32x4 bld16_sc1(__amdgpu_buffer_rsrc_t r, int voff, int soff) {
  u32x4 v;
  asm volatile("buffer_load_dwordx4 %0, %1, %2, %3 offen sc1" : "=v"(v) : "v"(voff), "s"(r), "s"(soff) : "memory");
  return v;
}
static __device__ __forceinline__ u32x2 granule_wait(const u64* p, unsigned tag) {
  u64 v = 0;
#pragma unroll 1
  for (int it = 0; it < SPIN_MAX; ++it) {
    v = __hip_atomic_load(p, __ATOMIC_RELAXED, __HIP_MEMORY_SCOPE_AGENT);
    if (__all((unsigned)(v >> 32) == tag)) break;
    __builtin_amdgcn_s_sleep(2);
  }
  return (u32x2){(unsigned)v, (unsigned)(v >> 32)};
}
static __device__ __forceinline__ float key_score(unsigned key) {
  const unsigned u = key >> 16;
  return bf2f((u16)((u & 0x8000u) ? (u & 0x7fffu) : (~u & 0xffffu)));
}
static __device__ __forceinline__ void merge2(unsigned& a1, unsigned& a2, unsigned b1, unsigned b2) {
  const unsigned lo = min(a1, b1);
  a1 = max(a1, b1);
  a2 = max(lo, max(a2, b2));
}
static __device__ __forceinline__ float sqdev16(const u32x4 (&v)[2], float mb) {
  f32x2 q2 = {0.f, 0.f};
#pragma unroll
  for (int i = 0; i < 8; ++i) {
    const f32x2 e = (f32x2){lo_bf(v[i >> 2][i & 3]), hi_bf(v[i >> 2][i & 3])} - mb;
    q2 += e * e;
  }
  return q2.x + q2.y;
}

constexpr int G1_NST = 4, G1_NKT = 16, G1_STAGE = 32768;
constexpr int G2_NKT = 16, G2_MRING = 98304;
constexpr int n_off(int k) { return (k % 3) * 32768; }
constexpr int m_off(int k) { return ((k + 2) % 3) * 16384; }
constexpr int k_ord(int k) { return k < 2 ? k : (k < 9 ? 2 * (k - 1) : 2 * (k - 8) + 1); }
constexpr int LDS_DUMMY = G2_MRING + 49152;
constexpr int LDS_GU = LDS_DUMMY + 4096;
constexpr int LDS_FIN = LDS_GU + 4096;
constexpr int LDS_MB = LDS_FIN + 4096;
constexpr int LDS_SEL = LDS_MB + 1536;
constexpr int LDS_SIG = LDS_SEL + 64;
constexpr int LDS_KEY = LDS_SIG + 32;
constexpr int LDS_BYTES = LDS_KEY + 128;
constexpr int LDS_ACC = n_off(G2_NKT - 3);

constexpr int tpos[16] = {0, 0, 0, 0, 100, 110, 115, 2, 2, 3, 4, 5, 0, 0, 0, 0};
constexpr int twho[16] = {0, 0, 0, 0, 7, 7, 7, 0, 1, 2, 3, 4, 0, 0, 0, 0};
constexpr int dn_cnt(int q) { return q == 5 ? 3 : q == 8 ? 4 : 0; }
constexpr int dn_first(int q) { return q <= 5 ? 0 : q <= 8 ? 3 : 7; }

static __device__ __forceinline__ void code_touch(unsigned& sink, unsigned off) {
  asm volatile("s_getpc_b64 s[40:41]\n\tk_main_rt:\n\ts_min_u32 %1, %1, k_main_end-k_main_rt-196\n\t"
               "s_load_dword %0, s[40:41], %1 offset:0\n\ts_load_dword %0, s[40:41], %1 offset:64\n\ts_load_dword %0, s[40:41], %1 offset:128\n\ts_load_dword %0, s[40:41], %1 offset:192"
               : "+s"(sink), "+s"(off) :: "s40", "s41", "scc", "memory");
}
static __device__ __forceinline__ void touch1(__amdgpu_buffer_rsrc_t r, int voff, unsigned mb, int soff) { TOUCH(r, voff, mb, LDS_DUMMY, soff); }
typedef const __attribute__((address_space(1))) u16* gptr;
typedef __attribute__((ext_vector_type(8))) u64 u64x8;
extern "C" __global__ __launch_bounds__(512, 2) void k_main(const u16* x, unsigned x2, unsigned x3, const u16* wd_re, unsigned r2, unsigned r3,
                                                 const u16* wd_im, unsigned i2, unsigned i3, const u16* w_r,
                                                 char* wsp, const u16* ws_re, const u16* ws_im,
                                                 const u16* w_gate, const u16* w_up, const u16* w_down, u16* out) {
  __shared__ __attribute__((aligned(16))) char smem[LDS_BYTES];
  u64x8 ka;
  u32x4 rm, rn;
  unsigned kz;
  asm volatile("s_load_dwordx4 %0, %4, 0\n\ts_load_dwordx16 %2, %4, 48\n\ts_andn2_b32 %3, 4, %5\n\ts_lshl_b32 %3, %3, 2\n\ts_load_dwordx4 %1, %4, %3 offset:16"
               : "=&s"(rm), "=&s"(rn), "=&s"(ka), "=&s"(kz) : "s"(__builtin_amdgcn_kernarg_segment_ptr()), "s"(blockIdx.x) : "scc", "memory");
  SB();
  const int tid = threadIdx.x, lane = tid & 63;
  const int wid = __builtin_amdgcn_readfirstlane(tid >> 6);
  const int g = blockIdx.x ^ 6, xcd = g & 7, j = g >> 3;
  if (wid < 4) {
    unsigned co = ((j * 4 + wid) * 4 + (lane & 3)) * 128;
    asm volatile(".subsection 1\nk_main_end:\n.subsection 0\n\tv_min_u32 %0, k_main_end-k_main-132, %0\n\ts_getpc_b64 s[40:41]\n\ts_mov_b32 m0, %1\n\ts_mov_b64 exec, 15\n\t"
                 "global_load_lds_dword %0, s[40:41]\n\ts_mov_b64 exec, -1"
                 : "+v"(co) : "i"(LDS_DUMMY + 768) : "s40", "s41", "memory");
  }
  const int pw = wid - 4;
  const int row0 = (wid & 3) * 8 + (lane >> 3);
  const int cm = ((lane & 7) ^ (((row0 >> 1) & 1) | ((wid & 3) << 1))) * 16;
  int vm = row0 * (D_ * 2) + cm;
  asm volatile("" : "+v"(vm));
  const int kq = xcd >> 1;
  const int tt = (xcd & 1) * 4 + (j >> 3);
  const int et = j & 7;
  const int z = kq >> 1, st = (kq & 1) * 8 + tt;
  unsigned o_m = st * 128 * D_ * 2, o_n = et * 128 * D_ * 2, mb0 = lds_addr(smem) + pw * 1024;
  asm volatile("" : "+s"(o_m), "+s"(o_n), "+s"(mb0));
#define G1_X(kt, so, q) DMA_(rm, vm, mb0, (so) + (q) * 4096, o_m, (kt) * 128 + (((q) & 1) * 64 + ((q) >> 1) * 32) * D_ * 2, "")
#define WO(q) ((((q) >> 1) * 64 + ((q) & 1) * 2) * D_ * 2)
#define G1_W(kt, so, q) DMA_(rn, vw1, mb0, (so) + 16384 + (q) * 4096, o_n, (kt) * 128 + WO(q), "")
#define G1_STAGE_TILE(kt, so, W) do { SB(); G1_X(kt, so, 0); W(0); G1_X(kt, so, 1); W(1); G1_X(kt, so, 2); W(2); G1_X(kt, so, 3); W(3); \
    G1_W(kt, so, 0); W(4); G1_W(kt, so, 1); W(5); G1_W(kt, so, 2); W(6); G1_W(kt, so, 3); W(7); SB(); } while (0)
#define G1_STAGE_A(kt, so, W) do { SB(); G1_X(kt, so, 0); W(0); G1_X(kt, so, 2); W(1); \
    G1_W(kt, so, 0); W(2); G1_W(kt, so, 1); W(3); G1_W(kt, so, 2); W(4); G1_W(kt, so, 3); W(5); W(6); W(7); SB(); } while (0)
#define G1_STAGE_B(kt, so) do { SB(); G1_X(kt, so, 1); G1_X(kt, so, 3); SB(); } while (0)
#define NOW(i)
  asm volatile("s_waitcnt lgkmcnt(0)" : "+s"(ka), "+s"(rm), "+s"(rn) :: "memory");
  int lrow, drow, l16, vw1;
  unsigned o_l, o_d;
  auto rows = [&](int i) __attribute__((always_inline)) {
    int g_ = g, l_ = lane;
    if (i == 0) { asm volatile("" : "+s"(g_)); drow = (g_ & ~7) + pw * 2; lrow = drow + (g_ & 3) * 512 + (g_ & 4) * 64; asm volatile("" : "+s"(drow), "+s"(lrow)); }
    if (i == 1) { asm volatile("" : "+s"(g_)); drow += (g_ & 3) * 256; o_l = lrow * D_ * 2; o_d = drow * D_ * 2; asm volatile("" : "+s"(drow), "+s"(o_l), "+s"(o_d)); }
    if (i == 2) { asm volatile("" : "+v"(l_)); vw1 = ((pw * 4 + ((l_ >> 3) & 3)) * 4 + (l_ >> 5)) * (D_ * 2) + (vm & 127); asm volatile("" : "+v"(vw1)); }
    if (i == 4) { asm volatile("" : "+v"(l_)); l16 = l_ * 16; asm volatile("" : "+v"(l16)); }
  };
  if (__builtin_expect(wid >= 4, 1)) {
    G1_STAGE_TILE(0, 0, rows);
    __builtin_amdgcn_s_setprio(3);
  }
  x = (const u16*)(gptr)((u64)rm[0] | (u64)rm[1] << 32);
  const u16* wdz = (const u16*)(gptr)((u64)rn[0] | (u64)rn[1] << 32);
  w_r = (const u16*)(gptr)ka[0]; wsp = (char*)(gptr)ka[1]; ws_re = (const u16*)(gptr)ka[2]; ws_im = (const u16*)(gptr)ka[3];
  w_gate = (const u16*)(gptr)ka[4]; w_up = (const u16*)(gptr)ka[5]; w_down = (const u16*)(gptr)ka[6]; out = (u16*)(gptr)ka[7];
  int* flags = (int*)(wsp + WS_FLAGS);
  u64* stat = (u64*)(flags + FL_ST);
  const int fr = lane & 15, fq = lane >> 4;
  const int ha = wid >> 2;
  int trow, xo, so_, po;
  const char *prs, *prc;
  auto epi_setup = [&]() __attribute__((always_inline)) {
    int t_ = tt, l_ = lane;
    asm volatile("" : "+s"(t_));
    asm volatile("" : "+v"(l_));
    trow = t_ * 256 + ((wid >> 1) & 1) * 128 + kq * 32 + ha * 16;
    xo = ((trow + (l_ >> 4) * 4 + 2) * D_ + et * 128 + (wid & 1) * 64 + (l_ & 15) * 4) * 2;
    so_ = (trow + (l_ >> 4) * 4 + (l_ & 3)) * 8;
    prs = wsp + WS_SLAB + (size_t)((t_ * 8 + et) * 64 + (wid & 3)) * MSG;
    prc = (const char*)(flags + FL_X + ((t_ * 8 + et) * 64 + (wid & 3)) * 32);
    asm volatile("" : "+s"(trow), "+s"(prs), "+s"(prc));
    po = l_ < 3 ? (kq * 4 + ((kq + 1 + l_) & 3)) * 512 : (int)0x80000000;
    asm volatile("" : "+v"(xo), "+v"(po), "+v"(so_));
  };
#define rs mk_rsrc(prs)
#define rc mk_rsrc(prc)
  lds_vi sig = (lds_vi)(smem + LDS_SIG + (wid & 3) * 4);
  unsigned tag;
  u32x2 xq[4];
  u32x2 sq;
  f32x4 ya[4];
  unsigned key[2];
  float nf[16], la0, la1, lb0, lb1;
#define PINV(a) asm volatile("" : "+v"(a))
#define X_LOADS() do { xq[0] = ld8_s<-4096>(x, xo); xq[1] = ld8_s<-2048>(x, xo); xq[2] = ld8_s<0>(x, xo); xq[3] = ld8_s<2048>(x, xo); sq = ld8_s_sc1(stat, so_); } while (0)
  auto nf_pre = [&]() __attribute__((always_inline)) {
    if (__builtin_expect(!__all(sq.y == tag), 0)) sq = granule_wait(stat + (so_ >> 3), tag);
    const float ri = __builtin_amdgcn_rcpf(hi_bf(sq.x));
    la0 = -(lo_bf(sq.x) * ri); la1 = 1.f + ri;
    PINV(la0); PINV(la1);
  };
  auto nf_step = [&](int n) __attribute__((always_inline)) {
    const int r = n / 5, i = n % 5 - 1;
    if (i < 0) {
      if (r == 0) { lb0 = dpp_f<0x00>(la0); lb1 = dpp_f<0x00>(la1); } else if (r == 1) { lb0 = dpp_f<0x55>(la0); lb1 = dpp_f<0x55>(la1); }
      else if (r == 2) { lb0 = dpp_f<0xaa>(la0); lb1 = dpp_f<0xaa>(la1); } else { lb0 = dpp_f<0xff>(la0); lb1 = dpp_f<0xff>(la1); }
      PINV(lb0); PINV(lb1);
    } else {
      float t = (i & 1) ? hi_bf(xq[r][i >> 1]) : lo_bf(xq[r][i >> 1]);
      PINV(t);
      nf[i * 4 + r] = __builtin_fmaf(t, lb1, lb0);
      PINV(nf[i * 4 + r]);
    }
  };
#define MSG_IDX(m) (kq * 4 + ((kq + 1 + (m)) & 3))
  unsigned seen, fx;

  if (__builtin_expect(wid >= 4, 1)) {
    __builtin_amdgcn_s_waitcnt(0xc07f);
    constexpr int O_ST = (FL_ST - FL_AB) * 4, O_CE = (FL_CE - FL_AB) * 4, O_KEY = (FL_KEY - FL_AB) * 4, O_EP = (FL_EPOCH - FL_AB) * 4;
    const __amdgpu_buffer_rsrc_t rf = mk_rsrc(wsp + WS_FLAGS + FL_AB * 4);
    u32x4 lv[2], lv2[2], lw[2], ld[2], ld2[2];
    unsigned epv;
    {
      const char* xrow = (const char*)x + o_l;
      const char* wdrow = (const char*)wdz + o_d;
      lv[0] = ld16_s<0>(xrow, l16); lv[1] = ld16_s<1024>(xrow, l16);
      lv2[0] = ld16_s<2048>(xrow, l16); lv2[1] = ld16_s<3072>(xrow, l16);
      ld[0] = ld16_s<0>(wdrow, l16); ld[1] = ld16_s<1024>(wdrow, l16);
      ld2[0] = ld16_s<2048>(wdrow, l16); ld2[1] = ld16_s<3072>(wdrow, l16);
      lw[0] = ld16_s<0>(w_r, l16); lw[1] = ld16_s<1024>(w_r, l16);
      asm volatile("buffer_load_dword %0, off, %1, %2 sc1" : "=v"(epv) : "s"(rf), "s"(O_EP) : "memory");
    }
    SB();
    const unsigned long long did = llvm_dispatch_id();
    const unsigned dmix = (unsigned)did * 0x85EBCA6Bu + (unsigned)(did >> 32) * 0xC2B2AE35u + 0x27D4EB2Fu;
    const bool moe = et != 7;
    unsigned epoch;
    const int ci = lane & 15, rq = lane >> 4, tk = (lane >> 3) & 1;
    int mat, cg, dq, d0;
    u32x4 wv[16];
    const u16 *pgu, *pn2;
    const char *pm2, *spb;
#define rw __builtin_amdgcn_make_buffer_rsrc((void*)pgu, 0, moe ? 0x7fffffff : 0, 0x00020000)
#define rn2 mk_rsrc(pn2)
#define rm2 mk_rsrc(pm2)
    int vw, vn2, vm2;
    int ord2;
    auto k_blk = [&](int k) __attribute__((always_inline)) { return k >= 9 ? (ord2 >> (4 * (k - 9))) & 7 : tt ^ (k_ord(k) >> 1); };
#define KROT(kk_) (k_blk(kk_) * 256 + (k_ord(kk_) & 1) * 128)
    int tv2;
    unsigned sd = 0;
    auto touches = [&](auto P) __attribute__((always_inline)) {
      constexpr int p = decltype(P)::value;
      int e = et;
      asm volatile("" : "+s"(e));
#define TCH(k) if constexpr (tpos[k] == p) { if (e == twho[k]) { const int kb = KROT(k); touch1(rn2, tv2, mb0, kb); } }
      TCH(0) TCH(1) TCH(2) TCH(3) TCH(4) TCH(5) TCH(6) TCH(7) TCH(8) TCH(9) TCH(10) TCH(11) TCH(12) TCH(13) TCH(14) TCH(15)
    };
    unsigned mq0, mq1, mq2, mq3;
    const int cg8 = g & 7, rgb = g >> 3;
    const __amdgpu_buffer_rsrc_t rdn = mk_rsrc(w_down);
    const __amdgpu_buffer_rsrc_t rgu = mk_rsrc(wsp + WS_GU);
    int vdn, vgu;
    u32x4 dv[7], gv[4];
    int abo, abq[3];
    unsigned lma;
    unsigned sel[4];
    float g0, g1;
    u32x4 kk[4];
    unsigned k1 = 0, k2 = 0;
    const int ko_ = O_KEY + pw * 4096;
#define MM(a, b, c) __builtin_amdgcn_mfma_f32_16x16x32_bf16(__builtin_bit_cast(bf16x8, a), __builtin_bit_cast(bf16x8, b), c, 0, 0, 0)
#define PIN1(a) asm volatile("" : "+v"(a))
#define PIN2(a, b) asm volatile("" : "+v"(a), "+v"(b))
#define PIN3(a, b, c) asm volatile("" : "+v"(a), "+v"(b), "+v"(c))
    {
      int gro, go;
      u32x2 gr;
      u32x4 oh[5];
      unsigned dk[4], kl;
#pragma unroll
      for (int l = 0; l < WSN; ++l) asm volatile("" : "=v"(wv[l]));
      auto diag = [&](const f32x4& m) __attribute__((always_inline)) {
        return __uint_as_float((__float_as_uint(m[0]) & dk[0]) | (__float_as_uint(m[1]) & dk[1]) | (__float_as_uint(m[2]) & dk[2]) | (__float_as_uint(m[3]) & dk[3]));
      };
      auto setup = [&](auto JB, auto GP) __attribute__((always_inline)) {
        constexpr int jb = decltype(JB)::value, gp = decltype(GP)::value;
        SB();
        {
          int ln = lane, jj = j, p_ = pw;
          launder(ln, jj, p_);
          const int c_ = ln & 15, r_ = ln >> 4, e_ = jj & 7, t_ = (xcd & 1) * 4 + (jj >> 3);
          if constexpr (jb == 1 && gp == 0) {
            pn2 = ((kq >> 1) ? ws_im : ws_re) + (long)t_ * 256 * S_ + (kq & 1) * 1024;
            spb = (const char*)(pn2 + (e_ * 4 + p_) * 8 * S_);
            pins(pn2, spb);
          }
          if constexpr (jb == 1 && gp >= 1 && gp < 6) {
            const unsigned o = c_ == gp - 1 ? 0x3f803f80u : 0u;
            oh[gp - 1] = (u32x4){o, o, o, o};
            PIN1(oh[gp - 1]);
          }
          if constexpr (jb == 1 && gp == 6) {
            const int gm = moe ? (jj - (jj >> 3)) * 8 + xcd : 0;
            mat = gm / 112; cg = (gm % 112) >> 2; dq = gm & 3; d0 = dq * 256 + p_ * 64;
            pin4(mat, cg, dq, d0);
          }
          if constexpr (jb == 2 && gp == 0) {
            pgu = mat ? w_up : w_gate;
            pm2 = wsp + WS_ABT + ((long)e_ * 128 * 4096 + kq * 1024) * 2;
            pins(pgu, pm2);
            vw = ((d0 + r_ * 16) * F_ + cg * 128 + c_ * 8) * 2;
            PIN1(vw);
          }
          if constexpr (jb == 2 && (gp == 1 || gp == 2)) {
            dk[gp * 2 - 2] = c_ == r_ * 4 + gp * 2 - 2 ? ~0u : 0u; dk[gp * 2 - 1] = c_ == r_ * 4 + gp * 2 - 1 ? ~0u : 0u;
            PIN2(dk[gp * 2 - 2], dk[gp * 2 - 1]);
          }
          if constexpr (jb == 2 && gp == 3) { kl = lrow + (ln >> 5); go = (ln & 30) ? (int)0x80000000 : ((ln & 1) ? O_KEY : O_ST) + (int)kl * 8; PIN2(kl, go); }
          if constexpr (jb == 2 && gp == 4) { kl = 0xffff - kl; go = (ln & ~1) == 2 ? O_CE + (z * D_ + drow + (ln & 1)) * 8 : go; PIN2(kl, go); }
          if constexpr (jb == 2 && gp == 5) {
            gro = (p_ < 2 ? O_ST + (st * 128 + p_ * 64) * 8 : O_CE + (z * D_ + e_ * 128 + (p_ - 2) * 64) * 8) + ln * 8;
            tv2 = (p_ * 64 + ln) * 4096;
            lma = lds_addr(smem + LDS_MB) + (p_ < 2 ? p_ * 256 : 512 + p_ * 256) + ln * 4;
            PIN3(gro, tv2, lma);
          }
          if constexpr (jb == 2 && gp == 6) {
            const u64 lo = (t_ & 2) ? ((t_ & 4) ? 0x0150234601503247ull : 0x0514670205147603ull) : ((t_ & 4) ? 0x0123067401532076ull : 0x0567423005176432ull);
            ord2 = (int)(lo >> ((t_ & 1) * 32));
            pins(ord2, p_);
          }
          if constexpr (jb == 7 && gp == 0) {
            const int r0 = p_ * 8 + (ln >> 3);
            vn2 = r0 * (S_ * 2) + ((ln & 7) ^ ((r0 >> 1) & 7)) * 16; vm2 = r0 * 8192 + (vm & 127);
            PIN2(vn2, vm2);
          }
          if constexpr (jb == 7 && gp == 1) { vdn = jj * 112 + p_ * 28 + r_ * 7; vgu = (((ln >> 3) & 1) * 4 * F_ + vdn + min(ln & 7, 6)) * 16; PIN2(vdn, vgu); }
          if constexpr (jb == 7 && gp == 2) { vdn = (vdn * D_ + cg8 * 128 + c_ * 8) * 2; PIN1(vdn); }
          if constexpr (jb == 7 && gp == 3) {
            abo = (((kq * 8 + e_) * 4 + p_) * 16 + c_) * AB_STRIDE * 4;
            abq[2] = ln >= 16 || (ln & 7) == t_ ? (int)0x80000000 : abo;
            PIN2(abo, abq[2]);
          }
          if constexpr (jb == 7 && gp == 4) {
            abq[1] = ln >= 8 ? (int)0x80000000 : abq[2];
            abq[0] = ((ln ^ t_) & 4) ? (int)0x80000000 : abq[1];
            PIN2(abq[0], abq[1]);
          }
          if constexpr (jb == 7 && gp == 5) {
            const unsigned mb = lds_addr(smem) + p_ * 1024;
            mq0 = mb + ((3 - kq) & 3) * 4096; mq1 = mb + ((4 - kq) & 3) * 4096; mq2 = mb + ((5 - kq) & 3) * 4096; mq3 = mb + ((6 - kq) & 3) * 4096;
            pin4(mq0, mq1, mq2, mq3);
          }
          if constexpr (jb == 8 && gp < 4) { sel[gp] = ((ln & 7) >> 1) == gp ? ((ln & 1) ? 0xffff0000u : 0xffffu) : 0u; PIN1(sel[gp]); }
        }
        SB();
      };
#define WS1(i) setup(IC<1>{}, IC<i>{})
#define WS2(i) setup(IC<2>{}, IC<i>{})
#define WS3(i) do { setup(IC<7>{}, IC<i>{}); if (i >= 4) setup(IC<8>{}, IC<(i) & 3>{}); } while (0)
      G1_STAGE_TILE(1, G1_STAGE, WS1);
      WAIT_VM(19); BAR();
      G1_STAGE_TILE(2, 2 * G1_STAGE, WS2);
      G1_STAGE_TILE(3, 3 * G1_STAGE, WS3);
      auto work1 = [&](auto JB, auto GP) __attribute__((always_inline)) {
        constexpr int jb = decltype(JB)::value, gp = decltype(GP)::value;
        SB();
        if constexpr (jb == ST_JB + 3) {
          if constexpr (gp == 0) { if (!__all(gr.y == tag)) gr = granule_wait((const u64*)((const char*)(flags + FL_AB) + gro), tag); PIN1(gr); }
          if constexpr (gp == 1) { g1 = __builtin_amdgcn_rcpf(__uint_as_float((gr.x & 0xffff0000u) ^ (z << 31))); PIN1(g1); }
          if constexpr (gp == 2) { g0 = pw < 2 ? -(lo_bf(gr.x) * g1) : __uint_as_float(gr.x); PIN1(g0); }
        }
        SB();
      };
      auto step1 = [&](auto JB) __attribute__((always_inline)) {
        constexpr int jb = decltype(JB)::value;
        constexpr int n16 = jb == 12 ? 12 : jb == 11 ? 14 : 16;
        constexpr auto wl = [](int q) { return q == ST_JB ? 1 : 0; };
        if constexpr (jb == ST_JB + 3) {
          asm volatile("s_waitcnt vmcnt(%1)" : "+v"(gr) : "i"(n16 + wl(jb - 2) + wl(jb - 1)) : "memory");
        } else wait_vm<n16 + wl(jb - 3) + wl(jb - 2) + wl(jb - 1)>();
        BAR();
#define W1(i) work1(JB, IC<i>{})
        if constexpr (jb < 10) G1_STAGE_TILE(jb - 1 + G1_NST, ((jb - 1) % G1_NST) * G1_STAGE, W1);
        else G1_STAGE_A(jb - 1 + G1_NST, ((jb - 1) % G1_NST) * G1_STAGE, W1);
        if constexpr (jb == 12) { G1_STAGE_B(13, G1_STAGE); G1_STAGE_B(14, 2 * G1_STAGE); }
        if constexpr (jb == ST_JB) {
          asm volatile("buffer_load_dwordx2 %0, %1, %2, 0 offen sc1" : "=v"(gr) : "v"(gro), "s"(rf) : "memory");
        }
        touches(IC<100 + jb>{});
        SB();
      };
      static_assert(ST_JB == 9, "schedule");
      {
        PAGE(1);
        const int bs = (BST >> (4 * et)) & 15;
        unsigned rm_ = o_m + 512, rn_ = o_n + 512 - 128, ln_ = mb0;
        int jb = 0, sn = tt * 256;
        auto stepr = [&]() __attribute__((always_inline)) {
          const int so = sn;
          const unsigned rl = ln_;
          WAIT_VM(16); BAR();
          SB();
          DMA_(rm, vm, rl, 0, rm_, 0, ""); sld<0>(sd, spb, so); DMA_(rm, vm, rl, 4096, rm_, 64 * D_ * 2, ""); sld<64>(sd, spb, so);
          DMA_(rm, vm, rl, 8192, rm_, 32 * D_ * 2, ""); sld<4096>(sd, spb, so); DMA_(rm, vm, rl, 12288, rm_, 96 * D_ * 2, ""); sld<4096 + 64>(sd, spb, so);
          rm_ += 128; rn_ += 128; ++jb;
          sn = (((0x4210 >> ((jb >> 1) * 4)) & 7) ^ (2 * tt)) * 128 + (jb & 1) * 16384; ln_ = mb0 + (jb & 3) * G1_STAGE;
          asm volatile("" : "+s"(rm_), "+s"(jb), "+s"(rn_), "+s"(sn), "+s"(ln_));
          DMA_(rn, vw1, rl, 16384, rn_, WO(0), ""); sld<8192>(sd, spb, so); DMA_(rn, vw1, rl, 16384 + 4096, rn_, WO(1), ""); sld<8192 + 64>(sd, spb, so);
          DMA_(rn, vw1, rl, 16384 + 8192, rn_, WO(2), ""); sld<12288>(sd, spb, so); sld<12288 + 64>(sd, spb, so); DMA_(rn, vw1, rl, 16384 + 12288, rn_, WO(3), "");
          SB();
        };
#pragma unroll 1
        do stepr(); while (jb < bs);
        if (moe) {
#pragma unroll
          for (int l = 0; l < WSN; ++l) bld16_nt_io(wv[l], rw, vw, l * F_ * 2);
        }
        touches(IC<100>{});
        asm volatile("" : "+v"(lv[0]), "+v"(lv[1]), "+v"(lw[0]), "+v"(lw[1]), "+v"(ld[0]), "+v"(ld[1]), "+v"(lv2[0]), "+v"(lv2[1]), "+v"(ld2[0]), "+v"(ld2[1]), "+v"(epv));
        SB();
        epoch = __builtin_amdgcn_readfirstlane(epv); tag = ((epoch + 1u) * 0x9E3779B1u ^ dmix) | 1u;
        const f32x4 z4 = {0.f, 0.f, 0.f, 0.f};
        f32x4 gw[2], gx[2];
        gw[0] = MM(lv[1], lw[1], MM(lv[0], lw[0], z4)); gw[1] = MM(lv2[1], lw[1], MM(lv2[0], lw[0], z4));
        gx[0] = MM(lv[1], lv[1], MM(lv[0], lv[0], z4)); gx[1] = MM(lv2[1], lv2[1], MM(lv2[0], lv2[0], z4));
        SB();
        f32x4 sa4 = MM(lv[0], oh[0], z4);
        sa4 = MM(lv2[0], oh[1], sa4); sa4 = MM(lv[1], oh[0], sa4); sa4 = MM(lv2[1], oh[1], sa4);
        sa4 = MM(ld[0], oh[2], sa4); sa4 = MM(ld[1], oh[2], sa4); sa4 = MM(ld2[0], oh[3], sa4); sa4 = MM(ld2[1], oh[3], sa4);
        sa4 = MM(lw[0], oh[4], sa4); sa4 = MM(lw[1], oh[4], sa4);
        float gs = row_sum(fold16(fold32(diag(gw[0]), diag(gw[1])), fold32(diag(gx[0]), diag(gx[1]))));
        float su = (sa4[0] + sa4[1]) + (sa4[2] + sa4[3]);
        su = fold32(su, su); su = fold16(su, su);
        const float sx = __int_as_float(__builtin_amdgcn_update_dpp(__builtin_amdgcn_update_dpp(0, __float_as_int(su), 0x150, 0xf, 0xf, true), __float_as_int(su), 0x151, 0xc, 0xf, false));
        const float mean = sx * (1.0f / D_), mbv = rbf(mean);
        float ta = __uint_as_float(__builtin_amdgcn_permlane16_swap(__float_as_uint(gs), __float_as_uint(gs), false, false)[1]) - sx * mean;
        if (__builtin_expect(__any(!(sx * mean <= 16.f * ta)), 0)) {
          float q = fold32(sqdev16(lv, rdl(mbv, 0)), sqdev16(lv2, rdl(mbv, 32)));
          q = row_sum(fold16(q, q));
          const float dm = mean - mbv;
          ta = fmaxf(q - (float)D_ * dm * dm, 0.f);
        }
        ta = rbf(__builtin_amdgcn_sqrtf(rbf(rbf(ta * (1.0f / D_)) + 1e-5f)));
        gs -= mbv * rdl(su, 4);
        const unsigned rb = __float_as_uint(rbf(gs * __builtin_amdgcn_rcpf(ta)));
        kl |= (rb ^ ((unsigned)((int)rb >> 31) | 0x80000000u)) & 0xffff0000u;
        unsigned k0 = (__float_as_uint(mbv) >> 16) | __float_as_uint(ta);
        PIN2(kl, k0);
        kl = (lane & 1) ? kl : k0;
        PIN1(kl);
        kl = (lane & 2) ? __float_as_uint(su) : kl;
        __builtin_amdgcn_raw_buffer_store_b64((u32x2){kl, tag}, rf, go, 0, 16);
#pragma unroll 1
        while (jb < 8) stepr();
      }
      step1(IC<9>{}); step1(IC<10>{}); step1(IC<11>{}); step1(IC<12>{});
    }
#define G2_STAGE_N(kk_, W) do { const int kb = KROT(kk_); constexpr int so = n_off(kk_); SB(); \
    DMA_(rn2, vn2, mq0, so, kb, 0, ""); W(0); DMA_(rn2, vn2, mq1, so, kb, 32 * S_ * 2, ""); W(1); \
    DMA_(rn2, vn2, mq2, so, kb, 64 * S_ * 2, ""); W(2); DMA_(rn2, vn2, mq3, so, kb, 96 * S_ * 2, ""); W(3); \
    DMA_(rn2, vn2, mq0, so + 16384, kb, 128 * S_ * 2, ""); W(4); DMA_(rn2, vn2, mq1, so + 16384, kb, 160 * S_ * 2, ""); W(5); \
    DMA_(rn2, vn2, mq2, so + 16384, kb, 192 * S_ * 2, ""); W(6); DMA_(rn2, vn2, mq3, so + 16384, kb, 224 * S_ * 2, ""); W(7); SB(); } while (0)
#define G2_STAGE_NH(kk_, h) do { const int kb = KROT(kk_); constexpr int so = n_off(kk_) + (h) * 16384; SB(); \
    DMA_(rn2, vn2, mq0, so, kb, (h) * 128 * S_ * 2, ""); DMA_(rn2, vn2, mq1, so, kb, ((h) * 128 + 32) * S_ * 2, ""); \
    DMA_(rn2, vn2, mq2, so, kb, ((h) * 128 + 64) * S_ * 2, ""); DMA_(rn2, vn2, mq3, so, kb, ((h) * 128 + 96) * S_ * 2, ""); SB(); } while (0)
#define G2_STAGE_M(kk_, W) do { const int kb = KROT(kk_); constexpr int so = G2_MRING + m_off(kk_); SB(); \
    DMA_(rm2, vm2, mb0, so, kb, 0, " sc1"); W(8); DMA_(rm2, vm2, mb0, so + 4096, kb, 32 * 8192, " sc1"); W(9); \
    DMA_(rm2, vm2, mb0, so + 8192, kb, 64 * 8192, " sc1"); W(10); DMA_(rm2, vm2, mb0, so + 12288, kb, 96 * 8192, " sc1"); W(11); SB(); } while (0)
    WAIT_VM(16); BAR();
    asm volatile("ds_write_b32 %0, %1" :: "v"(lma), "v"(g0) : "memory");
    if (pw < 2) asm volatile("ds_write_b32 %0, %1 offset:512" :: "v"(lma), "v"(g1) : "memory");
    u32x4 tv[4];
    unsigned ready = 0;
    auto need = [&](int k) __attribute__((always_inline)) {
      const int b = k_blk(k) + (k_ord(k) & 1) * 8;
#pragma unroll 1
      for (int it = 0; !((ready >> b) & 1) && it < SPIN_MAX; ++it) {
        ready |= (unsigned)__ballot(__builtin_amdgcn_raw_buffer_load_b32(rf, abq[k < 5 ? 0 : k < 9 ? 1 : 2], 0, 16) == tag);
        asm volatile("" ::: "memory");
      }
    };
    unsigned ksel, bad = 0;
    int isel;
    unsigned xw;
#define QP(p) " quad_perm:[" #p "," #p "," #p "," #p "] row_mask:0xf bank_mask:0xf\n\t"
#define GSTEP_(w, v, p) \
    asm volatile("v_and_b32_dpp v228, %2, %3" QP(p) "v_and_b32_dpp v229, %2, %4" QP(p) "v_and_b32_dpp v230, %2, %5" QP(p) "v_and_b32_dpp v231, %2, %6" QP(p) \
                 "s_nop 1\n\tv_mfma_f32_16x16x32_bf16 %0, %1, v[228:231], %0" \
                 : "+v"(macc) : "v"(w), "v"(v), "v"(sel[0]), "v"(sel[1]), "v"(sel[2]), "v"(sel[3]) : "v228", "v229", "v230", "v231")
    f32x4 macc = {0.f, 0.f, 0.f, 0.f};
    auto gstep = [&](const u32x4& w, unsigned v, int p) __attribute__((always_inline)) {
      if (p == 0) GSTEP_(w, v, 0); else if (p == 1) GSTEP_(w, v, 1); else if (p == 2) GSTEP_(w, v, 2); else GSTEP_(w, v, 3);
    };
#define QLO(v) (unsigned)__builtin_amdgcn_update_dpp((int)(v), (int)(v), 0x114, 0xf, 0xa, false)
#define QHI(v) (unsigned)__builtin_amdgcn_update_dpp((int)(v), (int)(v), 0x104, 0xf, 0x5, false)
    unsigned xa[4];
    float* lg = (float*)(smem + LDS_GU);
    u32x2 sv;
    unsigned gbad;
    float s0, s1;
    auto work2 = [&](auto JB, auto GP) __attribute__((always_inline)) {
      constexpr int jb = decltype(JB)::value, gp = decltype(GP)::value;
      SB();
      if constexpr (jb == -1 && gp < 4) {
        constexpr int c = gp == 0 ? 0xB1 : gp == 1 ? 0x4E : gp == 2 ? 0x141 : 0x140;
        merge2(k1, k2, dpp_u<c>(k1), dpp_u<c>(k2));
        PIN2(k1, k2);
      }
      if constexpr (jb == -1 && gp == 4) {
        ksel = tk ? k2 : k1;
        isel = (0xffff - (int)(ksel & 0xffffu)) & (S_ - 1);
        k1 = (isel * D_ + d0 + rq * 16 + (lane & 7) * 2) * 2; k2 = O_ST + isel * 8;
        PIN1(ksel);
        xw = gld4_s(x, k1);
        sv = bld8_sc1(rf, k2);
      }
      if constexpr (jb == 0 && gp == 1) wait_xs<3 + 1 + 2>(xw, sv);
      if constexpr (jb == 0) {
        if constexpr (gp == 1) { if (!__all(sv.y == tag)) sv = granule_wait(stat + isel, tag); s0 = __builtin_amdgcn_rcpf(hi_bf(sv.x)); PIN1(s0); }
        if constexpr (gp == 2) { s1 = ln_elem(lo_bf(xw), lo_bf(sv.x), hi_bf(sv.x), s0); PIN1(s1); }
        if constexpr (gp == 3) { s0 = ln_elem(hi_bf(xw), lo_bf(sv.x), hi_bf(sv.x), s0); PIN1(s0); }
        if constexpr (gp == 4) {
          const unsigned a = pk2(s1, s1), b = pk2(s0, s0);
          xa[0] = QLO(a); xa[1] = QLO(b); xa[2] = QHI(a); xa[3] = QHI(b);
          PIN2(xa[0], xa[1]); PIN2(xa[2], xa[3]);
        }
      }
      if constexpr (jb == DG && gp < 7) gstep(dv[gp], xa[gp >> 2], gp & 3);
      if constexpr ((jb == 0 && gp >= 5) || jb == 1 || (jb == 2 && gp == 0)) {
        constexpr int l = jb == 0 ? gp - 5 : jb == 1 ? gp + 3 : 15;
        gstep(wv[l], xa[(l >> 3) * 2 + (l & 1)], (l >> 1) & 3);
      }
      if constexpr (jb == GV_JB + 2) {
        if constexpr (gp == 0) { gbad = (gv[0].y ^ tag) | (gv[0].w ^ tag) | (gv[1].y ^ tag) | (gv[1].w ^ tag); PIN1(gbad); }
        if constexpr (gp == 1) { gbad |= (gv[2].y ^ tag) | (gv[2].w ^ tag) | (gv[3].y ^ tag) | (gv[3].w ^ tag); PIN1(gbad); }
        if constexpr (gp == 2) {
          s0 = ((__uint_as_float(gv[0].x) + __uint_as_float(gv[1].x)) + __uint_as_float(gv[2].x)) + __uint_as_float(gv[3].x);
          s1 = ((__uint_as_float(gv[0].z) + __uint_as_float(gv[1].z)) + __uint_as_float(gv[2].z)) + __uint_as_float(gv[3].z);
          PIN2(s0, s1);
        }
        if constexpr (gp == 3) {
#pragma unroll 1
          for (int it = 0; !__all(gbad == 0) && it < SPIN_MAX; ++it) {
            u32x4 q[4];
            gbad = 0;
#pragma unroll
            for (int i = 0; i < 4; ++i) { q[i] = __builtin_amdgcn_raw_buffer_load_b128(rgu, vgu, i * F_ * 16, 16); gbad |= (q[i].y ^ tag) | (q[i].w ^ tag); }
            s0 = ((__uint_as_float(q[0].x) + __uint_as_float(q[1].x)) + __uint_as_float(q[2].x)) + __uint_as_float(q[3].x);
            s1 = ((__uint_as_float(q[0].z) + __uint_as_float(q[1].z)) + __uint_as_float(q[2].z)) + __uint_as_float(q[3].z);
            asm volatile("" ::: "memory");
          }
          const unsigned gp2 = pk2(s0, s1);
          const unsigned up2 = dpp_u<0x128>(gp2);
          s0 = tk ? hi_bf(up2) : lo_bf(gp2); s1 = tk ? hi_bf(gp2) : lo_bf(up2);
          PIN2(s0, s1);
        }
        if constexpr (gp == 4) { s0 = rbf(s0 * __builtin_amdgcn_rcpf(1.f + __expf(-s0))); PIN1(s0); }
        if constexpr (gp == 5) { const unsigned h = pk2(s0 * s1, s0 * s1); xa[0] = QLO(h); xa[1] = QHI(h); PIN2(xa[0], xa[1]); }
      }
      if constexpr (jb >= 3 && jb <= 10 && gp >= 4) {
        constexpr int n = (jb - 3) * 8 + gp - 4;
        sld<((n >> 1) & 7) * 4096 + (n & 1) * 64>(sd, spb, KROT(12 + (n >> 4)));
      }
      if constexpr (jb == 10 && gp == 2) epi_setup();
      if constexpr (jb == 10 && gp == 4) code_touch(sd, (j * 4 + pw) * 256);
      SB();
    };
#define W2M(i) work2(IC<-1>{}, IC<i>{})
#define W20(i) work2(IC<0>{}, IC<i>{})
#define W21(i) work2(IC<1>{}, IC<i>{})
    unsigned fs;
    {
      G1_STAGE_B(15, 3 * G1_STAGE);
      G2_STAGE_NH(0, 0);
      lgkm0(sd);
      WAIT_VM(16); BAR();
      G2_STAGE_NH(0, 1);
      WAIT_VM(10); BAR();
#pragma unroll
      for (int i = 0; i < 4; ++i) kk[i] = bld16_sc1(rf, l16, ko_ + i * 1024);
#pragma unroll
      for (int l = WSN; l < 16; ++l) wv[l] = bld16_nt(rw, vw, l * F_ * 2);
      asm volatile("s_waitcnt vmcnt(%4)" : "+v"(kk[0]), "+v"(kk[1]), "+v"(kk[2]), "+v"(kk[3]) : "i"(16 - WSN) : "memory");
      touches(IC<115>{});
#pragma unroll
      for (int i = 0; i < 4; ++i) {
        k2 = max(k2, min(k1, kk[i].x)); k1 = max(k1, kk[i].x);
        k2 = max(k2, min(k1, kk[i].z)); k1 = max(k1, kk[i].z);
        bad |= (kk[i].y ^ tag) | (kk[i].w ^ tag);
      }
#pragma unroll 1
      for (int it = 0; !__all(bad == 0) && it < SPIN_MAX; ++it) {
        k1 = 0; k2 = 0; bad = 0;
#pragma unroll
        for (int i = 0; i < 4; ++i) {
          const u32x4 q = __builtin_amdgcn_raw_buffer_load_b128(rf, l16, ko_ + i * 1024, 16);
          k2 = max(k2, min(k1, q.x)); k1 = max(k1, q.x);
          k2 = max(k2, min(k1, q.z)); k1 = max(k1, q.z);
          bad |= (q.y ^ tag) | (q.w ^ tag);
        }
        asm volatile("" ::: "memory");
      }
      merge2(k1, k2, dpp_u<0xB1>(k1), dpp_u<0xB1>(k2));
      merge2(k1, k2, dpp_u<0x4E>(k1), dpp_u<0x4E>(k2));
      merge2(k1, k2, dpp_u<0x141>(k1), dpp_u<0x141>(k2));
      merge2(k1, k2, dpp_u<0x140>(k1), dpp_u<0x140>(k2));
      if ((lane & 15) == 0) { *(u32x2*)(smem + LDS_KEY + (pw * 4 + rq) * 8) = (u32x2){k1, k2}; ((unsigned*)(smem + LDS_SEL))[5] = tag; }
      PAGE(3);
      BAR_LGKM();
#pragma unroll
      for (int jj = 0; jj < 4; ++jj) tv[jj] = *(const u32x4*)(smem + G2_MRING + m_off(0) + (jj * 4 + pw) * 1024 + lane * 16);
      { const u32x2 kp = *(const u32x2*)(smem + LDS_KEY + ci * 8); k1 = kp.x; k2 = kp.y; }
#pragma unroll
      for (int jj = 0; jj < 4; ++jj) __builtin_amdgcn_raw_buffer_store_b128(tv[jj], rm2, vm2 + jj * 32 * 8192 + 2 * tt * 128, 0, 16);
      G2_STAGE_N(1, W2M);
      WAIT_VM(10);
      if (lane == tt) __builtin_amdgcn_raw_buffer_store_b32(tag, rf, abo, 0, 16);
      G2_STAGE_N(2, W20);
      __builtin_amdgcn_s_sleep(2);
      WAIT_VM(9);
      fs = bld4_sc1(rf, abq[0]);
    }
    BAR();
#pragma unroll
    for (int jj = 0; jj < 4; ++jj) tv[jj] = *(const u32x4*)(smem + G2_MRING + m_off(1) + (jj * 4 + pw) * 1024 + lane * 16);
    G2_STAGE_N(3, NOW);
    asm volatile("s_waitcnt vmcnt(8)" : "+v"(fs) :: "memory");
    ready = (unsigned)__ballot(fs == tag);
    need(2);
    G2_STAGE_M(2, NOW);
    need(3);
    G2_STAGE_M(3, NOW);
    fs = bld4_sc1(rf, abq[1]);
    touches(IC<1>{});
    W21(0); W21(1); W21(2); W21(3); W21(4); W21(5); W21(6); W21(7); W21(8); W21(9); W21(10); W21(11);
    asm volatile("" : "+v"(tv[0]), "+v"(tv[1]), "+v"(tv[2]), "+v"(tv[3]));
    auto step2 = [&](auto JB) __attribute__((always_inline)) {
      constexpr int jb = decltype(JB)::value;
      if constexpr (jb == 3) PAGE(4);
      if constexpr (jb == 9) PAGE(5);
      constexpr auto must = [](int q) { return (q == 2 ? 4 : 0) + ((FSM >> q) & 1) + ((q == GV_JB || q == GV_JB - 1) ? 2 : 0); };
      if constexpr (jb == 2) wait_vm<4 + must(1)>();
      else wait_vm<dn_cnt(jb - 2) + (jb == 5 ? 1 : 0) + 12 + must(jb - 1) + dn_cnt(jb - 1)>();
      if constexpr (jb == 3) { if (pw == 0 && (lane & ~8) == 0) ((unsigned*)(smem + LDS_SEL))[tk] = ksel; }
      if constexpr ((jb == 3 || jb == DG + 1)) {
        *(f32x4*)(lg + pw * 256 + lane * 4) = macc;
        macc = (f32x4){0.f, 0.f, 0.f, 0.f};
        lgkm0(sd);
      }
      BAR();
      float p0[4], p1[4];
      const int col = (pw & 1) * 64 + lane;
      if constexpr (jb == 3) {
        if (g == 0 && pw == 0 && lane == 0) __hip_atomic_store((unsigned*)flags + FL_EPOCH, epoch + 1u, __ATOMIC_RELAXED, __HIP_MEMORY_SCOPE_AGENT);
      }
      if constexpr (jb == DG + 1) {
        const float* p = lg + (pw & 1) * 32 + (lane >> 4) * 64 + ((2 * lane) & 7) * 4 + ((lane >> 2) & 3);
#pragma unroll
        for (int i = 0; i < 4; ++i) { p0[i] = p[i * 256]; p1[i] = p[i * 256 + 4]; }
      }
      if constexpr (jb == 3) {
        const float* p = lg + (col >> 5) * 64 + (col & 7) * 4 + ((col >> 3) & 3);
#pragma unroll
        for (int i = 0; i < 4; ++i) { p0[i] = p[i * 256]; p1[i] = p[i * 256 + 32]; }
      }
      if constexpr (jb == 4) {
        if (lane == 8 + tt) __builtin_amdgcn_raw_buffer_store_b32(tag, rf, abo, 0, 16);
      }
      if constexpr ((FSM >> (jb - 2)) & 1) {
        asm volatile("" : "+v"(fs));
        ready |= (unsigned)__ballot(fs == tag);
      }
      if constexpr (jb == GV_JB + 2) asm volatile("" : "+v"(gv[0]), "+v"(gv[1]), "+v"(gv[2]), "+v"(gv[3]));
#define W2(i) do { if (jb == 3 && i == 2) asm volatile("" : "+v"(p0[0]), "+v"(p0[1]), "+v"(p0[2]), "+v"(p0[3]), "+v"(p1[0]), "+v"(p1[1]), "+v"(p1[2]), "+v"(p1[3])); work2(JB, IC<i>{}); } while (0)
      if constexpr (jb < 13) {
        G2_STAGE_N(jb + 2, W2);
        need(jb + 2);
        G2_STAGE_M(jb + 2, W2);
      } else {
#define W14(i) do { if (i == B14P - 1) { wait_vm<B14P>(); BAR(); } } while (0)
#define W14M(i) W14(i - 8)
        need(15);
        G2_STAGE_M(15, W14M);
#define LASTQ(q) do { const int kb = KROT(15) + ((q + kq + 1) & 3) * 32 * S_ * 2; \
        DMA_(rn2, vn2, mb0, n_off(15) + q * 4096, kb, 0, ""); W14(4 + 2 * q); DMA_(rn2, vn2, mb0, n_off(15) + 16384 + q * 4096, kb, 128 * S_ * 2, ""); W14(5 + 2 * q); } while (0)
        LASTQ(0); LASTQ(1); LASTQ(2); LASTQ(3);
        SB();
      }
      if constexpr (jb == 2) {
#pragma unroll
        for (int jj = 0; jj < 4; ++jj) __builtin_amdgcn_raw_buffer_store_b128(tv[jj], rm2, vm2 + jj * 32 * 8192 + (2 * tt + 1) * 128, 0, 16);
      }
      if constexpr ((FSM >> jb) & 1) fs = bld4_sc1(rf, abq[jb < 5 ? 1 : 2]);
      if constexpr ((jb == GV_JB || jb == GV_JB - 1)) {
#pragma unroll
        for (int i = (jb == GV_JB ? 2 : 0); i < (jb == GV_JB ? 4 : 2); ++i) gv[i] = bld16_sc1(rgu, vgu, i * F_ * 16);
      }
      touches(JB);
#pragma unroll
      for (int i = dn_first(jb); i < dn_first(jb) + dn_cnt(jb); ++i) dv[i] = bld16_nt(rdn, vdn, i * D_ * 2);
      if constexpr (jb == DG + 1) {
        if (pw < 2)
          __builtin_amdgcn_raw_buffer_store_b128((u32x4){__float_as_uint((p0[0] + p0[1]) + (p0[2] + p0[3])), tag, __float_as_uint((p1[0] + p1[1]) + (p1[2] + p1[3])), tag},
                                                 mk_rsrc(wsp + WS_DN), (((pw * 8 + cg8) * 32 + rgb) * 64 + lane) * 16, 0, 16);
      }
      if constexpr (jb == 3) {
        if (moe && pw < 2)
          __builtin_amdgcn_raw_buffer_store_b128((u32x4){__float_as_uint((p0[0] + p0[1]) + (p0[2] + p0[3])), tag, __float_as_uint((p1[0] + p1[1]) + (p1[2] + p1[3])), tag},
                                                 rgu, ((mat * 4 + dq) * F_ + cg * 128 + col) * 16, 0, 16);
      }
      SB();
    };
    static_assert(DG + 1 <= 12 && DG >= GV_JB + 3 && dn_first(DG - 2) == 7, "schedule");
    step2(IC<2>{}); step2(IC<3>{}); step2(IC<4>{}); step2(IC<5>{}); step2(IC<6>{}); step2(IC<7>{}); step2(IC<8>{}); step2(IC<9>{}); step2(IC<10>{}); step2(IC<11>{}); step2(IC<12>{}); step2(IC<13>{});
    lgkm0(sd);
    PAGE(7);
    WAIT_VM(4); BAR();
    X_LOADS();
    WAIT_VM(5); BAR();
    wait_xq<0>(xq, sq);
    key[0] = __builtin_amdgcn_readlane(ksel, 0); key[1] = __builtin_amdgcn_readlane(ksel, 8);
    nf_pre();
#pragma unroll
    for (int n = 0; n < 20; ++n) nf_step(n);
#pragma unroll 1
    for (int c = 0; c < 2; ++c) {
      const unsigned kc = __builtin_amdgcn_readlane(ksel, c * 8);
      const int ic = (0xffff - (int)(kc & 0xffffu)) & (S_ - 1);
      if ((ic >> 8) != tt || ((ic >> 5) & 3) != kq) continue;
      const __amdgpu_buffer_rsrc_t rd = mk_rsrc(wsp + WS_DN + (size_t)(c * 8 + et) * 32768);
#pragma unroll 1
      for (int it = 0; it < SPIN_MAX; ++it) {
        if (__all(__builtin_amdgcn_raw_buffer_load_b32(rd, (lane & 31) * 1024 + 1020, 0, 16) == tag)) break;
        __builtin_amdgcn_s_sleep(2);
        asm volatile("" ::: "memory");
      }
      float s0, s1;
      unsigned bad = 1;
#pragma unroll 1
      for (int it = 0; !__all(bad == 0) && it < SPIN_MAX; ++it) {
        bad = 0; s0 = 0.f; s1 = 0.f;
#pragma unroll
        for (int i = 0; i < 8; ++i) {
          const u32x4 q = __builtin_amdgcn_raw_buffer_load_b128(rd, rq * 1024 + pw * 256 + ci * 16, i * 4096, 16);
          bad |= (q.y ^ tag) | (q.w ^ tag); s0 += __uint_as_float(q.x); s1 += __uint_as_float(q.z);
        }
        asm volatile("" ::: "memory");
      }
      s0 = fold32(s0, s1);
      s0 = fold16(s0, s0);
      const float sg = rbf(__builtin_amdgcn_rcpf(1.f + __expf(-key_score(kc))));
      ((float*)(smem + LDS_FIN))[c * 128 + pw * 32 + ci * 2 + (lane >> 5)] = rbf(rbf(s0) * sg);
      asm volatile("s_waitcnt lgkmcnt(0)" ::: "memory");
      if (lane == 0) asm volatile("ds_add_u32 %0, %1" :: "v"(lds_addr(smem + LDS_SEL + 24 + c * 4)), "v"(1) : "memory");
    }
    asm volatile("v_mov_b32 %0, 0" : "=v"(fx), "=v"(ya[0]), "=v"(ya[1]), "=v"(ya[2]), "=v"(ya[3]));
  } else {
  {
    const int q = j * 4 + wid;
    constexpr int Q1 = WS_MB, Q2 = Q1 + 8, Q3 = Q2 + 8, Q4 = Q3 + 7, Q5 = Q4 + 7, Q6 = Q5 + 7, Q7 = Q6 + 4, Q8 = Q7 + 4, QC = Q8 + 1;
    static_assert(QC <= 128, "warm-up slots");
    unsigned dmy = 0;
    if (q < QC) {
      const char* base; int b0;
      if (q < Q1) { base = wsp; b0 = 0; }
      else if (q < Q2) { base = (const char*)ws_re; b0 = Q1; }
      else if (q < Q3) { base = (const char*)ws_im; b0 = Q2; }
      else if (q < Q4) { base = (const char*)w_gate; b0 = Q3; }
      else if (q < Q5) { base = (const char*)w_up; b0 = Q4; }
      else if (q < Q6) { base = (const char*)w_down; b0 = Q5; }
      else if (q < Q7) { base = (const char*)out; b0 = Q6; }
      else if (q < Q8) { base = (const char*)x; b0 = Q7; }
      else { base = (const char*)w_r; b0 = Q8; }
      base += (size_t)(q - b0) << 20;
      asm volatile("s_load_dword %0, %1, 0" : "+s"(dmy) : "s"(base) : "memory");
    }
    {
      const int w = g * 4 + wid;
      constexpr int R = (int)(WS_FLAGS - WS_DN);
      static_assert(FL_X == 0 && FL_ST == 6144 * 32 && FL_EPOCH == 6528 * 32, "");
      const int o = lane < 6 ? R + (lane * 1024 + w) * 128 : lane > 6 || w >= 896 ? (int)0x80000000 : w < 512 ? w * 1024 + 896 : R + (6144 - 512 + w) * 128;
      asm volatile("s_mov_b32 m0, %0\n\ts_nop 0\n\tbuffer_load_dword %1, %2, 0 offen sc1 lds"
                   :: "s"(lds_addr(smem + LDS_DUMMY) + wid * 1024 + 512), "v"(o), "s"(mk_rsrc(wsp + WS_DN)) : "memory");
    }
    if (tid < 24) ((int*)(smem + LDS_SEL))[tid] = 0;
    WAIT_VM(1);
    if (wid == 0) { CALL_PG(1); CALL_PG(8); CALL_PG(5); }
    else if (wid == 1) { CALL_PG(9); CALL_PG(6); }
    else if (wid == 2) { CALL_PG(3); CALL_PG(10); CALL_PG(7); }
    else { CALL_PG(4); CALL_PG(11); }
    asm volatile("s_barrier\n\ts_waitcnt lgkmcnt(0)" : "+s"(dmy) :: "memory");
  }
  PAGE(8);
  epi_setup();
  const int wr = wid >> 1, wc = wid & 1;
  const int sw = (fr >> 1) & 7;
  const int ko0 = ((0 + fq) ^ sw) * 16, ko1 = ((4 + fq) ^ sw) * 16;
  const int mrow = (fr >> 2) * 8 + (fr & 3);
  bf16x8 fm[2][4], fn[2][4];
  {
    const char* pm = smem + (wr * 64 + mrow) * 128;
    const char* pn = smem + 16384 + (wc * 64 + mrow) * 128;
    f32x4 acc[4][4];
#pragma unroll
    for (int i = 0; i < 4; ++i)
#pragma unroll
      for (int jn = 0; jn < 4; ++jn)
#pragma unroll
        for (int q = 0; q < 4; ++q) acc[i][jn][q] = 0.f;
#define TO(i) (((i) >> 1) * 4096 + ((i) & 1) * 512)
#define LDSP(T, a) ((__attribute__((address_space(3))) T*)(size_t)(a))
#define SLOTS(n) _Pragma("unroll") for (int q = 0; q < (n); ++q)
#define MFA(c, a, b) asm volatile("v_mfma_f32_16x16x32_bf16 %0, %1, %2, %0" : "+v"(c) : "v"(a), "v"(b))
#define MFA2(c, a, b, a2, b2) asm volatile("v_mfma_f32_16x16x32_bf16 %0, %1, %2, %0\n\tv_mfma_f32_16x16x32_bf16 %0, %3, %4, %0" : "+v"(c) : "v"(a), "v"(b), "v"(a2), "v"(b2))
    unsigned lm[2][2], ln[2][2];
#pragma unroll
    for (int u = 0; u < 2; ++u)
#pragma unroll
      for (int kk = 0; kk < 2; ++kk) {
        lm[u][kk] = lds_addr(pm) + u * 2 * G1_STAGE + (kk ? ko1 : ko0);
        ln[u][kk] = lds_addr(pn) + u * 2 * G1_STAGE + (kk ? ko1 : ko0);
        asm volatile("" : "+v"(lm[u][kk]), "+v"(ln[u][kk]));
      }
    auto ldm = [&](int stg, int i, int kk) __attribute__((always_inline)) { fm[kk][i] = *LDSP(const bf16x8, lm[stg >> 1][kk] + (stg & 1) * G1_STAGE + TO(i)); };
    auto ldn = [&](int stg, int jn, int kk) __attribute__((always_inline)) { fn[kk][jn] = *LDSP(const bf16x8, ln[stg >> 1][kk] + (stg & 1) * G1_STAGE + TO(jn)); };
    auto quad = [&](int a, int c, int d, int s) __attribute__((always_inline)) {
      const int i = (s < 2 || s >= 6) ? a : a + 1, jn = s < 4 ? c : d, kk = (s == 1 || s == 2 || s == 5 || s == 6);
      MFA(acc[i][jn], fm[kk][i], fn[kk][jn]);
    };
    auto quad2 = [&](int a, int c, int d, int s) __attribute__((always_inline)) {
      const int i = (s < 2 || s >= 6) ? a : a + 1, jn = s < 4 ? c : d, kk = (s == 2 || s == 6);
      MFA2(acc[i][jn], fm[kk][i], fn[kk][jn], fm[1 - kk][i], fn[1 - kk][jn]);
    };
    auto half1 = [&](int stg) __attribute__((always_inline)) {
      const int ja = (stg & 1) * 2, jb = 2 - ja;
      SLOTS(8) {
        quad(0, ja, ja + 1, q);
        if (q < 4) ldn(stg, jb + (q >> 1), q & 1); else ldm(stg, 2 + ((q - 4) >> 1), q == 5 || q == 6);
        SB();
      }
      SLOTS(4) { quad2(0, jb, jb + 1, 2 * q); SB(); }
    };
    auto half2 = [&](int stg) __attribute__((always_inline)) {
      const int ja = (stg & 1) * 2, jb = 2 - ja, nx = (stg + 1) & 3;
      SLOTS(8) {
        quad(2, jb + 1, jb, q);
        if (!(q & 1)) ldm(nx, q >> 2, q == 2 || q == 4);
        SB();
      }
      SLOTS(8) {
        quad(2, ja, ja + 1, q);
        if (!(q & 1)) ldn(nx, jb + (q >> 2), (q >> 1) & 1);
        SB();
      }
    };
    SLOTS(16) asm volatile("" : "+v"(acc[q >> 2][q & 3]));
    SB();
    SLOTS(4) { ldm(0, q >> 1, q & 1); ldn(0, q >> 1, q & 1); }
    SB();
#pragma unroll 1
    for (int k4 = 0; k4 < G1_NKT - 4; k4 += 4) {
      half1(0); BAR_LGKM();
      SB(); half2(0);
      half1(1); BAR_LGKM();
      SB(); half2(1);
      half1(2); BAR_LGKM();
      SB(); half2(2);
      half1(3); BAR_LGKM();
      SB(); half2(3);
    }
    bf16x8 gm[2][4], gn[2][8];
    unsigned am[2][2], an[2][2];
#pragma unroll
    for (int u = 0; u < 2; ++u)
#pragma unroll
      for (int kk = 0; kk < 2; ++kk) {
        am[u][kk] = lds_addr(pm) + (u ? 3 : 1) * G1_STAGE + (kk ? ko1 : ko0);
        an[u][kk] = lds_addr(pn) + (u ? 3 : 1) * G1_STAGE + (kk ? ko1 : ko0);
        asm volatile("" : "+v"(am[u][kk]), "+v"(an[u][kk]));
      }
    unsigned al = lds_addr(smem + LDS_MB) + (wr * 32 + fq * 8) * 4, ac = lds_addr(smem + LDS_MB) + (256 + wc * 64 + fr * 4) * 4;
    unsigned ae = lds_addr(smem + G2_MRING) + (wc * 64 + mrow) * 128 + (((wr * 4 + fq) ^ sw) * 16);
    asm volatile("" : "+v"(al), "+v"(ac), "+v"(ae));
    auto rdg = [&](int set, int t, int h, int r) __attribute__((always_inline)) {
      const int u = t == 15, o = t == 14 ? G1_STAGE : 0;
      if (r == 0 || r == 2 || r == 4 || r == 5) {
        const int i = r >= 4, kk = r == 2 || r == 4;
        gm[set][kk * 2 + i] = *LDSP(const bf16x8, am[u][kk] + o + h * 4096 + i * 512);
      } else {
        const int jn = r < 4 ? 0 : (r - 4) >> 1, kk = r < 4 ? r >> 1 : r & 1;
        gn[set][kk * 4 + jn] = *LDSP(const bf16x8, an[u][kk] + o + TO(jn));
      }
    };
    auto mmg = [&](int set, int h, int q) __attribute__((always_inline)) {
      const int jn = q >> 2, r = q & 3, i = (r >> 1) ^ (jn & 1), kk = r == 1 || r == 2;
      MFA(acc[h * 2 + i][jn], gm[set][kk * 2 + i], gn[set][kk * 4 + jn]);
    };
    f32x4 rsv[2], mrv[2];
    float cev[4], tc[32];
    auto rds = [&](int h, int r) __attribute__((always_inline)) {
      if (r < 2) mrv[r] = *LDSP(const f32x4, al + h * 256 + r * 16); else rsv[r - 2] = *LDSP(const f32x4, al + 512 + h * 256 + (r - 2) * 16);
    };
    auto prep = [&](int n) __attribute__((always_inline)) {
      tc[n] = llvm_fmul_legacy(mrv[(n >> 2) & 1][n & 3], cev[n >> 3]);
      asm volatile("" : "+v"(tc[n]));
    };
    u32x4 ep;
    auto epi = [&](int h, int q) __attribute__((always_inline)) {
      const int jn = q >> 1, i = q & 1;
      asm volatile("v_fmac_f32 %0, %4, %8\n\tv_fmac_f32 %1, %5, %9\n\tv_fmac_f32 %2, %6, %10\n\tv_fmac_f32 %3, %7, %11"
                   : "+v"(tc[q * 4]), "+v"(tc[q * 4 + 1]), "+v"(tc[q * 4 + 2]), "+v"(tc[q * 4 + 3])
                   : "v"(acc[h * 2 + i][jn][0]), "v"(acc[h * 2 + i][jn][1]), "v"(acc[h * 2 + i][jn][2]), "v"(acc[h * 2 + i][jn][3]),
                     "v"(rsv[i][0]), "v"(rsv[i][1]), "v"(rsv[i][2]), "v"(rsv[i][3]));
      ep[i * 2] = pk2(tc[q * 4], tc[q * 4 + 1]); ep[i * 2 + 1] = pk2(tc[q * 4 + 2], tc[q * 4 + 3]);
      if (i) *LDSP(u32x4, ae + m_off(h) + TO(jn)) = ep;
    };
    SB();
    half1(0);
    BAR_LGKM();
    SB();
    SLOTS(16) {
      if (q < 8) quad(2, 3, 2, q); else quad(2, 0, 1, q - 8);
      if (q < 12) rdg(0, 13, 0, q);
      SB();
    }
    BAR();
    SB();
    SLOTS(16) {
      mmg(0, 0, q);
      if (q < 12) rdg(1, 14, 0, q);
      else { rds(0, q - 12); cev[q - 12] = *LDSP(const float, ac + (q - 12) * 4); }
      SB();
    }
    PAGE(9);
    BAR();
    SB();
    SLOTS(16) { mmg(1, 0, q); if ((q & 3) < 3) rdg(0, 15, 0, (q >> 2) * 3 + (q & 3)); if (q >= 8) { prep(2 * q - 16); prep(2 * q - 15); } SB(); }
    SLOTS(16) { mmg(0, 0, q); if ((q & 3) < 3) rdg(1, 13, 1, (q >> 2) * 3 + (q & 3)); prep(16 + q); SB(); if (q >= 9 && (q & 1)) { epi(0, (q - 9) >> 1); SB(); } }
    SLOTS(8) { mmg(1, 1, q); if (q < 6) { rdg(0, 14, 1, 2 * q); rdg(0, 14, 1, 2 * q + 1); } SB(); if (q & 1) { epi(0, 4 + (q >> 1)); SB(); } }
    BAR_LGKM();
    SB();
    SLOTS(8) { mmg(1, 1, 8 + q); if (q < 4) rdg(1, 15, 1, q == 0 ? 1 : q == 1 ? 3 : q + 4); else rds(1, q - 4); SB(); }
    SLOTS(16) { mmg(0, 1, q); if (q < 8) rdg(1, 15, 1, q == 0 ? 0 : q == 1 ? 2 : q < 4 ? q + 2 : q + 4); prep(q); SB(); }
    asm volatile("s_waitcnt lgkmcnt(0)" ::: "memory");
    if (lane == 0) asm volatile("ds_add_u32 %0, %1" :: "v"(lds_addr(smem + LDS_SEL + 12)), "v"(1) : "memory");
    SB();
    SLOTS(16) {
      mmg(1, 1, q);
      if (q < 4) fn[0][q] = *(const bf16x8*)(smem + (wr * 128 + fr) * 128 + (q >> 1) * 2048 + ((q & 1) ? ko1 : ko0));
      else if (q < 8) fm[q & 1][(q - 4) >> 1] = *(const bf16x8*)(smem + G2_MRING + m_off(0) + (wc * 64 + mrow) * 128 + TO((q - 4) >> 1) + ((q & 1) ? ko1 : ko0));
      prep(16 + q);
      SB();
    }
    {
      lds_vi rel = (lds_vi)(smem + LDS_SEL + 12);
      int it = 0;
      while (rel[0] != 4 && it < SPIN_MAX * 8) ++it;
      asm volatile("" ::: "memory");
    }
    asm volatile("s_nop 7");
    SB();
    SLOTS(8) { epi(1, q); SB(); }
  }

  const char* pn = smem + (wr * 128 + fr) * 128;
  const char* pm = smem + G2_MRING + (wc * 64 + mrow) * 128;
  f32x4 acc[8][4];
  auto mm = [&](bool imaj, int set, int q2, int s, int mode) __attribute__((always_inline)) {
    int i, jj, kk;
    if (imaj) { i = s >> 2; const int r = s & 3; jj = (r >> 1) ^ (i & 1); kk = (r == 1 || r == 2); }
    else { jj = s >> 3; const int r = s & 7; i = jj ? 3 - (r >> 1) : (r >> 1); kk = ((r + 1) >> 1) & 1; }
    if (mode == 1 && !(s & 1)) asm volatile("v_mfma_f32_16x16x32_bf16 %0, %2, %1, 0" : "=&v"(acc[q2 * 2 + jj][i]) : "v"(fm[kk][i]), "v"(fn[set][jj * 2 + kk]));
    else if (mode) asm volatile("v_mfma_f32_16x16x32_bf16 %0, %2, %1, %0" : "+v"(acc[q2 * 2 + jj][i]) : "v"(fm[kk][i]), "v"(fn[set][jj * 2 + kk]));
    else acc[q2 * 2 + jj][i] = __builtin_amdgcn_mfma_f32_16x16x32_bf16(fn[set][jj * 2 + kk], fm[kk][i], acc[q2 * 2 + jj][i], 0, 0, 0);
  };
  unsigned bn[2], bm[2], an[2], am[2];
#pragma unroll
  for (int kk = 0; kk < 2; ++kk) {
    bn[kk] = lds_addr(pn) + (kk ? ko1 : ko0); bm[kk] = lds_addr(pm) + (kk ? ko1 : ko0);
    asm volatile("" : "+v"(bn[kk]), "+v"(bm[kk]));
    an[kk] = bn[kk]; am[kk] = bm[kk] + m_off(0);
  }
#define RDN(jt, kk, o) (*LDSP(const bf16x8, an[kk] + (o) + (jt) * 2048))
#define RDM(i, kk, o) (*LDSP(const bf16x8, am[kk] + (o) + TO(i)))
  PAGE(10);
  tag = __builtin_amdgcn_readfirstlane(((const unsigned*)(smem + LDS_SEL))[5]);
  {
    int son = 0;
    auto tile = [&](int kt, int zero) __attribute__((always_inline)) {
      SLOTS(16) {
        mm(true, 0, 0, q, zero);
        if (!(q & 1)) {
          if (q < 8) fm[(q >> 1) & 1][2 + (q >> 2)] = RDM(2 + (q >> 2), (q >> 1) & 1, 0);
          else { const int r = (q - 8) >> 1; fn[1][r] = RDN(2 + (r >> 1), r & 1, 0); }
        }
        SB();
      }
      SLOTS(16) {
        mm(false, 1, 1, q, zero);
        if (!(q & 1) && q < 8) fn[0][q >> 1] = RDN(4 + (q >> 2), (q >> 1) & 1, 0);
        SB();
      }
      const int som = son >> 1;
      son = son == 65536 ? 0 : son + 32768;
      SLOTS(16) {
        mm(false, 0, 2, q, zero);
        if (!(q & 1) && q < 8) fn[1][q >> 1] = RDN(6 + (q >> 2), (q >> 1) & 1, 0);
        if (q == 9 || q == 11) { an[q == 11] = bn[q == 11] + son; asm volatile("" : "+v"(an[q == 11])); }
        if (q == 13 || q == 15) { am[q == 15] = bm[q == 15] + som; asm volatile("" : "+v"(am[q == 15])); }
        SB();
      }
      BAR_LGKM();
      SB();
      SLOTS(16) {
        mm(true, 1, 3, q, zero);
        if (!(q & 1)) {
          if (q < 8) fn[0][q >> 1] = RDN(q >> 2, (q >> 1) & 1, 0);
          else fm[(q >> 1) & 1][(q - 8) >> 2] = RDM((q - 8) >> 2, (q >> 1) & 1, 0);
        }
        SB();
      }
    };
    SB();
    tile(0, 1);
#pragma unroll 1
    for (int kt = 1; kt < G2_NKT - 3; ++kt) tile(kt, 0);
  }
  {
    constexpr int NG = 12;
    static constexpr int g_t[NG] = {0, 0, 1, 1, 2, 2, 2, 1, 0, 0, 1, 2};
    static constexpr int g_q[NG] = {0, 1, 0, 1, 0, 1, 2, 2, 2, 3, 3, 3};
    constexpr int G14 = 1, G15 = 3, GP0 = 5, GP1 = 6;
    constexpr auto chp = [](int g) { return g == 0 || g_t[g] != g_t[g - 1]; };
    constexpr auto chn = [](int g) { return g + 1 < NG && g_t[g + 1] != g_t[g]; };
    static_assert(n_off(13) == 32768 && n_off(14) == 65536 && n_off(15) == 0, "");
    unsigned an2[2];
#pragma unroll
    for (int kk = 0; kk < 2; ++kk) {
      an[kk] = bn[kk]; an2[kk] = bn[kk] + 65536; am[kk] = bm[kk];
      asm volatile("" : "+v"(an[kk]), "+v"(an2[kk]), "+v"(am[kk]));
    }
    int vo;
    u32x4 pk;
    u32x2 kv;
    constexpr int NF0 = 152, F0 = 160, F1 = 172, F2 = 188;
    auto publish = [&](int d, int h) __attribute__((always_inline)) {
      if (h == 0) { vo = lane; PINV(vo); vo = vo * 16 + (((d + kq + 1) & 3) * 4 + kq) * 4 * MSG; PINV(vo); }
      const f32x4 v = acc[2 * d + (h >> 2)][h & 3];
      pk[(h & 1) * 2] = pk2(v[0], v[1]); pk[(h & 1) * 2 + 1] = pk2(v[2], v[3]);
      if (h & 1) __builtin_amdgcn_raw_buffer_store_b128(pk, rs, vo + (h >> 1) * 1024, 0, 16);
    };
    int fo = lane == 0 ? kq * 512 : (int)0x80000000;
#define MSG_FLAG(d) __builtin_amdgcn_raw_buffer_store_b32(tag, rc, fo + (((d) + kq + 1) & 3) * 2048, 0, 16)
    auto rdn = [&](int g, int r) __attribute__((always_inline)) {
      const int t = g_t[g], jt = g_q[g] * 2 + (r >> 1);
      fn[g & 1][r] = *LDSP(const bf16x8, (t == 1 ? an2[r & 1] : an[r & 1]) + (t == 0 ? 32768 : 0) + jt * 2048);
    };
    auto rdm = [&](int t, int i, int kk) __attribute__((always_inline)) { fm[kk][i] = *LDSP(const bf16x8, am[kk] + m_off(13 + t) + TO(i)); };
    auto group = [&](auto GI) __attribute__((always_inline)) {
      constexpr int gi = decltype(GI)::value;
      if (gi == G14) { BAR();
        SB(); }
      if (gi == 5) { BAR(); SB(); }
      if (gi == G15) {
        PAGE(11);
        BAR();
        SB();
        X_LOADS();
        kv = *(const u32x2*)(smem + LDS_SEL);
        SB();
      }
#pragma unroll
      for (int q = 0; q < 16; ++q) {
        const int n = gi * 16 + q;
        if (n == NF0) {
          wait_xq<12>(xq, sq);
          key[0] = __builtin_amdgcn_readfirstlane(kv.x); key[1] = __builtin_amdgcn_readfirstlane(kv.y);
          nf_pre();
          SB();
        }
        if (n == 112) {
        }
        if (n == 152) {
        }
        if (n == F0) {
          WAIT_VM(8); MSG_FLAG(0); SB(); }
        if (n == F1) { WAIT_VM(5); MSG_FLAG(1); SB(); }
        if (n == F2) {
          WAIT_VM(2);
          MSG_FLAG(2); fx = bld4_sc1(rc, po); SB(); }
        mm(chp(gi) || chn(gi), gi & 1, g_q[gi], q, 2);
        constexpr bool ni = gi + 1 < NG && (chp(gi + 1 < NG ? gi + 1 : gi) || chn(gi + 1 < NG ? gi + 1 : gi));
        auto nr = [](int r) { return ni && r >= 2 ? 5 - r : r; };
        if constexpr (chp(gi) && chn(gi)) {
          if (q < 4) rdm(g_t[gi], 2 + (q >> 1), q & 1); else if (q < 8) rdn(gi + 1, nr(q - 4)); else if (q < 12) rdm(g_t[gi + 1], (q - 8) >> 1, q & 1);
        } else if constexpr (chp(gi)) {
          if (q < 4) rdm(g_t[gi], 2 + (q >> 1), q & 1);
          else if (gi + 1 < NG && q < 12 && !(q & 1)) rdn(gi + 1, nr((q - 4) >> 1));
        } else if constexpr (chn(gi)) {
          if (!(q & 1)) { if (q < 8) rdn(gi + 1, nr(q >> 1)); else rdm(g_t[gi + 1], (q - 8) >> 2, (q >> 1) & 1); }
        } else {
          if (!(q & 1) && q < 8) rdn(gi + 1, nr(q >> 1));
        }
        if (gi == GP0 && q < 8) publish(0, q);
        if (gi == GP1 && q < 8) publish(1, q);
        if (gi == 9 && q < 8) publish(2, q);
        if (n >= NF0 && n < NF0 + 20) nf_step(n - NF0);
        SB();
      }
    };
#define GR4(i) group(IC<i>{}); group(IC<i + 1>{}); group(IC<i + 2>{}); group(IC<i + 3>{});
    GR4(0) GR4(4) GR4(8)
    asm volatile("s_nop 7");
    SB();
  }
#pragma unroll
  for (int i = 0; i < 4; ++i) *(f32x4*)(smem + LDS_ACC + wr * 16384 + wc * 4096 + i * 1024 + lane * 16) = acc[7][i];
#pragma unroll
  for (int i = 0; i < 4; ++i) ya[i] = acc[6][i];
  asm volatile("s_waitcnt lgkmcnt(0)" ::: "memory");
  if (lane == 0) sig[0] = 2;
  }

  PAGE(6);
  const unsigned rel0 = 0xffff - (key[0] & 0xffffu) - trow, rel1 = 0xffff - (key[1] & 0xffffu) - trow;
  if (min(rel0, rel1) < 16) {
#pragma unroll 1
    for (int c = 0; c < 2; ++c) {
      const unsigned rel = c ? rel1 : rel0;
      if (rel >= 16) continue;
      lds_vi cnt = (lds_vi)(smem + LDS_SEL + 24 + c * 4);
      int it = 0;
      while (cnt[0] != 4 && it < SPIN_MAX * 8) { __builtin_amdgcn_s_sleep(1); ++it; }
      asm volatile("" ::: "memory");
      const f32x4 v = *(const f32x4*)(smem + LDS_FIN + c * 512 + (wid & 1) * 256 + fr * 16);
#pragma unroll
      for (int e = 0; e < 16; ++e) { float t = nf[e] + v[e >> 2]; PINV(t); nf[e] = rel == fq * 4 + (e & 3) ? t : nf[e]; }
    }
  }
  u32x4 pp[3][2];
  unsigned e_lo = 0x00003f80u, e_hi = 0x3f800000u;
  asm volatile("" : "+v"(e_lo), "+v"(e_hi));
  __amdgpu_buffer_rsrc_t rs_ = rs, rc_ = rc, ro_ = mk_rsrc(out);
  int mo[3] = {MSG_IDX(0) * 4 * MSG, MSG_IDX(1) * 4 * MSG, MSG_IDX(2) * 4 * MSG}, mv = lane * 16 + ha * 2048, mv2 = mv + 1024;
  asm volatile("" : "+s"(rs_), "+s"(rc_), "+s"(ro_), "+s"(mo[0]), "+s"(mo[1]), "+s"(mo[2]));
  unsigned as_ = lds_addr((const void*)smem) + LDS_SIG + (wid & 3) * 4, aa_ = lds_addr(smem + LDS_ACC) + (wid & 2) * 8192 + (wid & 1) * 4096 + lane * 16;
  int xo2 = xo - 4096;
  asm volatile("" : "+v"(mv), "+v"(mv2), "+v"(as_), "+v"(aa_), "+v"(xo2));
  {
    unsigned sv_, it_ = SPIN_MAX * 8;
    asm volatile("s_cmp_eq_u32 %9, 0\n\ts_cbranch_scc1 2f\n"
                 "1:\n\tds_read_b32 %0, %10\n\ts_waitcnt lgkmcnt(0)\n\tv_cmp_eq_u32 vcc, 2, %0\n\ts_cbranch_vccnz 3f\n\ts_sleep 1\n\ts_sub_u32 %1, %1, 1\n\ts_cbranch_scc0 1b\n"
                 "3:\n\tds_read_b128 %4, %11\n\tds_read_b128 %5, %11 offset:1024\n\tds_read_b128 %6, %11 offset:2048\n\tds_read_b128 %7, %11 offset:3072\n"
                 "2:\n\ts_waitcnt vmcnt(0)\n\tv_cmp_eq_u32 vcc, %8, %3\n\ts_mov_b32 %2, vcc_lo"
                 : "=&v"(sv_), "+s"(it_), "=&s"(seen), "+v"(fx), "+v"(ya[0]), "+v"(ya[1]), "+v"(ya[2]), "+v"(ya[3])
                 : "s"(tag), "s"(ha), "v"(as_), "v"(aa_) : "vcc", "scc", "memory");
  }
#define MSG_POLL(m) do { \
    if (!((seen >> (m)) & 1)) { \
      unsigned pv_, it_ = SPIN_MAX; \
      asm volatile("1:\n\tbuffer_load_dword %0, %3, %4, 0 offen sc1\n\ts_waitcnt vmcnt(0)\n\tv_cmp_eq_u32 vcc, %5, %0\n\ts_mov_b32 %1, vcc_lo\n\t" \
                   "s_bitcmp1_b32 %1, %6\n\ts_cbranch_scc1 2f\n\ts_sub_u32 %2, %2, 1\n\ts_cbranch_scc1 2f\n\ts_sleep 1\n\ts_branch 1b\n2:" \
                   : "=&v"(pv_), "+s"(seen), "+s"(it_) : "v"(po), "s"(rc_), "s"(tag), "i"(m) : "vcc", "scc", "memory"); \
    } \
    pp[m][0] = __builtin_amdgcn_raw_buffer_load_b128(rs_, mv, mo[m], 16); \
    pp[m][1] = __builtin_amdgcn_raw_buffer_load_b128(rs_, mv2, mo[m], 16); } while (0)
#define MSG_ADD(m) do { \
    _Pragma("unroll") for (int u = 0; u < 8; ++u) { \
      ya[u >> 1][(u & 1) * 2] = add_bf(pp[m][u >> 2][u & 3], e_lo, ya[u >> 1][(u & 1) * 2]); ya[u >> 1][(u & 1) * 2 + 1] = add_bf(pp[m][u >> 2][u & 3], e_hi, ya[u >> 1][(u & 1) * 2 + 1]); } } while (0)
  MSG_POLL(2); MSG_POLL(1); MSG_POLL(0);
  asm volatile("s_waitcnt lgkmcnt(0)" : "+v"(ya[0]), "+v"(ya[1]), "+v"(ya[2]), "+v"(ya[3]) :: "memory");
  SB();
#pragma unroll
  for (int e = 0; e < 16; ++e) { float t = ya[e >> 2][e & 3] + nf[e]; PINV(t); ya[e >> 2][e & 3] = t; }
  SB();
  MSG_ADD(2); MSG_ADD(1);
  asm volatile("" : "+v"(ya[0]), "+v"(ya[1]), "+v"(ya[2]), "+v"(ya[3]));
  SB();
  MSG_ADD(0);
  {
    unsigned ov[8];
    SB();
#pragma unroll
    for (int u = 0; u < 8; ++u) ov[u] = pk2(ya[(u & 1) * 2][u >> 1], ya[(u & 1) * 2 + 1][u >> 1]);
    SB();
    __builtin_amdgcn_raw_buffer_store_b64((u32x2){ov[0], ov[1]}, ro_, xo2, 0, 16);
    __builtin_amdgcn_raw_buffer_store_b64((u32x2){ov[2], ov[3]}, ro_, xo2 + 2048, 0, 16);
    __builtin_amdgcn_raw_buffer_store_b64((u32x2){ov[4], ov[5]}, ro_, xo, 0, 16);
    __builtin_amdgcn_raw_buffer_store_b64((u32x2){ov[6], ov[7]}, ro_, xo + 2048, 0, 16);
  }
}

extern "C" void kernel_launch(void* const* d_in, const int* in_sizes, int n_in, void* d_out, int out_size,
                              void* d_ws, size_t ws_size, hipStream_t stream) {
  const u16* x = (const u16*)d_in[0];
  const u16* ws_re = (const u16*)d_in[1];
  const u16* ws_im = (const u16*)d_in[2];
  const u16* wd_re = (const u16*)d_in[3];
  const u16* wd_im = (const u16*)d_in[4];
  const u16* w_r = (const u16*)d_in[6];
  const u16* w_gate = (const u16*)d_in[7];
  const u16* w_up = (const u16*)d_in[8];
  const u16* w_down = (const u16*)d_in[9];
  k_main<<<256, 512, 0, stream>>>(x, 0x7fffffffu, 0x00020000u, wd_re, 0x7fffffffu, 0x00020000u, wd_im, 0x7fffffffu, 0x00020000u, w_r, (char*)d_ws,
                                  ws_re, ws_im, w_gate, w_up, w_down, (u16*)d_out);
}
```
